```python
import torch

def _softmax(x, axis=-1):
    return torch.softmax(x, dim=axis)
def _gelu(x):
    return torch.nn.functional.gelu(x, approximate="tanh")
def _silu(x):
    return torch.nn.functional.silu(x)
def _sigmoid(x):
    return torch.sigmoid(x)
def _layernorm(x, axis=-1, eps=1e-5):
    m = x.mean(dim=axis, keepdim=True)
    v = x.var(dim=axis, keepdim=True, unbiased=False)
    return (x - m) / torch.sqrt(v + eps)
def _scatter_add(tgt, idx, src, axis=0):
    return tgt.clone().index_add_(axis, idx, src)
def _segment_sum(src, seg, n):
    out = torch.zeros((n,) + src.shape[1:], dtype=src.dtype)
    return out.index_add_(0, seg, src)
def _pad(x, pw):
    flat = tuple(v for pair in reversed(pw) for v in pair)
    return torch.nn.functional.pad(x, flat)

kernel_name = 'sequential_preLN__ssm_conv_h3__glu_gated_geglu__prompt'

def setup_inputs():

    _w = (lambda a: (lambda t: t.to(torch.bfloat16) if t.is_floating_point() else t)(torch.as_tensor(a)))
    def _A_setup():
        B, S, D = 1, 2048, 576
        ks = 4
        return {
            "q": _w((lambda *s: torch.randn(*s, dtype=torch.bfloat16) * 0.05)(B, S, D)),
            "k": _w((lambda *s: torch.randn(*s, dtype=torch.bfloat16) * 0.2)(B, S, D)),
            "v": _w((lambda *s: torch.randn(*s, dtype=torch.bfloat16) * 0.2)(B, S, D)),
            "shift_k": _w((lambda *s: torch.randn(*s, dtype=torch.bfloat16) * 0.2)(D, ks) * 0.5),
            "ssm_k": _w((lambda *s: torch.randn(*s, dtype=torch.bfloat16) * 0.4)(D, S) * (1.0 / S ** 0.5)),
            "ssm_D": _w((lambda *s: torch.randn(*s, dtype=torch.bfloat16) * 0.2)(D)),
        }
    _A_in = _A_setup()

    def _B_setup():
        B, S, Dm, Df = 1, 2048, 576, 4608
        return {
            "x": _w((lambda *s: torch.randn(*s, dtype=torch.bfloat16) * 0.05)(B, S, Dm)),
            "w_gate": _w((lambda *s: torch.randn(*s, dtype=torch.bfloat16) * 0.4)(Dm, Df) * (Dm ** -0.5)),
            "w_up": _w((lambda *s: torch.randn(*s, dtype=torch.bfloat16) * 0.4)(Dm, Df) * (Dm ** -0.5)),
            "w_down": _w((lambda *s: torch.randn(*s, dtype=torch.bfloat16) * 0.4)(Df, Dm) * (Df ** -0.5)),
        }
    _B_in = _B_setup()

    return {
        "A_q": _A_in["q"],
        "A_k": _A_in["k"],
        "A_v": _A_in["v"],
        "A_shift_k": _A_in["shift_k"],
        "A_ssm_k": _A_in["ssm_k"],
        "A_ssm_D": _A_in["ssm_D"],
        "B_x": _B_in["x"],
        "B_w_gate": _B_in["w_gate"],
        "B_w_up": _B_in["w_up"],
        "B_w_down": _B_in["w_down"],
    }

def reference(A_q, A_k, A_v, A_shift_k, A_ssm_k, A_ssm_D, B_x, B_w_gate, B_w_up, B_w_down):
    def _A_ref(q, k, v, shift_k, ssm_k, ssm_D):
        B, S, D = q.shape
        ks = shift_k.shape[-1]
        k_pad = _pad(k, ((0, 0), (ks - 1, 0), (0, 0)))
        k_shift = torch.zeros_like(k)
        for t in range(ks):
            k_shift = k_shift + k_pad[:, t:t + S, :] * shift_k[:, ks - 1 - t][None, None, :]
        kv = k_shift * v
        L = 2 * S
        kv_pad = _pad(kv, ((0, 0), (0, S), (0, 0)))
        kf_pad = _pad(ssm_k, ((0, 0), (0, S)))
        kv_f = (lambda x, axes=(-2,-1): torch.fft.rfft2(x.float(), dim=axes))(kv_pad[:, None, :, :], axes=(1, 2))
        kf_f = (lambda x, axes=(-2,-1): torch.fft.rfft2(x.float(), dim=axes))(kf_pad[:, None, :], axes=(1, 2))
        kf_f = (lambda x, *d: x.permute(*d))(kf_f, 1, 2, 0)
        y_f = kv_f * kf_f[None, :, :, :]
        y = (lambda x, s, axes=(-2,-1): torch.fft.irfft2(x, s=s, dim=axes).to(torch.bfloat16))(y_f, (1, L), axes=(1, 2))[:, 0, :S, :]
        y = y + kv * ssm_D[None, None, :]
        return q * y

    def _B_ref(x, w_gate, w_up, w_down):
        g = torch.einsum("bsd,df->bsf", x, w_gate)
        u = torch.einsum("bsd,df->bsf", x, w_up)
        h = _gelu(g) * u
        return torch.einsum("bsf,fd->bsd", h, w_down)

    x = A_q
    h = x + _A_ref(_layernorm(x, axis=-1), A_k, A_v, A_shift_k, A_ssm_k, A_ssm_D)
    h2 = h + _B_ref(_layernorm(h, axis=-1), B_w_gate, B_w_up, B_w_down)
    return h2
```

```cpp
#include <hip/hip_runtime.h>
#include <hip/hip_bf16.h>

#define LAS __attribute__((address_space(3)))
typedef unsigned short u16;
typedef short s16x4 __attribute__((ext_vector_type(4)));
typedef short s16x8 __attribute__((ext_vector_type(8)));
typedef float f32x2 __attribute__((ext_vector_type(2)));
typedef float f32x4 __attribute__((ext_vector_type(4)));
typedef unsigned u32x2 __attribute__((ext_vector_type(2)));
typedef unsigned u32x4 __attribute__((ext_vector_type(4)));
typedef __bf16 bf16x2_t __attribute__((ext_vector_type(2)));

constexpr int S_ = 2048, D_ = 576, F_ = 4608;

__device__ __forceinline__ unsigned pk_bf16(float lo, float hi) {
    f32x2 v = {lo, hi};
    bf16x2_t b = __builtin_convertvector(v, bf16x2_t);
    return __builtin_bit_cast(unsigned, b);
}
__device__ __forceinline__ float bf_lo(unsigned w) { return __uint_as_float(w << 16); }
__device__ __forceinline__ float bf_hi(unsigned w) { return __uint_as_float(w & 0xffff0000u); }
__device__ __forceinline__ float rbf(float x) { return bf_lo(pk_bf16(x, 0.f)); }
__device__ __forceinline__ f32x4 mfma16(s16x8 a, s16x8 b, f32x4 c) {
    return __builtin_amdgcn_mfma_f32_16x16x32_bf16(a, b, c, 0, 0, 0);
}
#define WAIT_V(n) asm volatile("s_waitcnt vmcnt(" #n ")" ::: "memory")
#define BAR() __builtin_amdgcn_s_barrier()

constexpr int C1_COPY = 262 * 16;
constexpr int C1_FILT = 8 * C1_COPY;
constexpr int C1_KV = 2304 * 2;
constexpr int C1_KVOFF = 4 * C1_FILT;
constexpr int C1_LDS = C1_KVOFF + 4 * C1_KV;

__device__ __forceinline__ int c1_copy_off(int cp) { return (cp <= 4 ? (cp - 1) * 2 : (cp - 5) * 2 + 1) * C1_COPY; }

__global__ __launch_bounds__(512, 2) void k1_conv(const u16* __restrict__ kin, const u16* __restrict__ vin,
                                                  const u16* __restrict__ shift_k, const u16* __restrict__ ssm_k,
                                                  const u16* __restrict__ ssm_D, u16* __restrict__ yT) {
    extern __shared__ __attribute__((aligned(16))) char smem[];
    const int tid = threadIdx.x, lane = tid & 63;
    const int wid = __builtin_amdgcn_readfirstlane(tid >> 6);
    const int b = blockIdx.x;
    const int cg = (b & 7) * 18 + (b >> 3);
    const int ch0 = cg * 4;

    u32x4 fa[2], fb[2];
#pragma unroll
    for (int i = 0; i < 2; ++i) {
        const int task = tid + 512 * i, c = task >> 8, u = task & 255;
        const u16* src = ssm_k + (size_t)(ch0 + c) * S_ + u * 8;
        fa[i] = *(const u32x4*)src;
        fb[i] = (u < 255) ? *(const u32x4*)(src + 8) : (u32x4){0u, 0u, 0u, 0u};
    }
    u32x2 kr[4], vr[4], kh[3];
    const int t0 = tid * 4;
#pragma unroll
    for (int j = 0; j < 4; ++j) kr[j] = *(const u32x2*)(kin + (size_t)(t0 + j) * D_ + ch0);
#pragma unroll
    for (int j = 0; j < 4; ++j) vr[j] = *(const u32x2*)(vin + (size_t)(t0 + j) * D_ + ch0);
#pragma unroll
    for (int j = 0; j < 3; ++j) kh[j] = (u32x2){0u, 0u};
    if (lane == 0 && tid > 0) {
#pragma unroll
        for (int j = 0; j < 3; ++j) kh[j] = *(const u32x2*)(kin + (size_t)(t0 - 3 + j) * D_ + ch0);
    }
    u32x4 fe = {0u, 0u, 0u, 0u};
    if (tid >= 508) fe = *(const u32x4*)(ssm_k + (size_t)(ch0 + tid - 508) * S_);

    if (tid < 128) {
        const int e = tid & 3, q = e < 3 ? e : 260;
        *(LAS u32x4*)(smem + (tid >> 2) * C1_COPY + q * 16) = (u32x4){0u, 0u, 0u, 0u};
    } else if (tid >= 256 && tid < 384) {
        const int r = tid - 256, c = r >> 5, q = r & 31;
        *(LAS u32x4*)(smem + C1_KVOFF + c * C1_KV + 4096 + q * 16) = (u32x4){0u, 0u, 0u, 0u};
    }

    auto put_copies = [&](int c, int q, u32x4 lo, u32x4 hi) {
        unsigned d[8] = {lo.x, lo.y, lo.z, lo.w, hi.x, hi.y, hi.z, hi.w};
        unsigned o[7];
#pragma unroll
        for (int i = 0; i < 7; ++i) o[i] = __builtin_amdgcn_alignbit(d[i + 1], d[i], 16);
        LAS char* base = (LAS char*)smem + c * C1_FILT + q * 16;
#pragma unroll
        for (int cp = 1; cp <= 8; ++cp) {
            const int e = cp >> 1;
            u32x4 w = (cp & 1) ? (u32x4){o[e], o[e + 1], o[e + 2], o[e + 3]} : (u32x4){d[e], d[e + 1], d[e + 2], d[e + 3]};
            *(LAS u32x4*)(base + c1_copy_off(cp)) = w;
        }
    };
#pragma unroll
    for (int i = 0; i < 2; ++i) {
        const int task = tid + 512 * i;
        put_copies(task >> 8, (task & 255) + 4, fa[i], fb[i]);
    }
    if (tid >= 508) put_copies(tid - 508, 3, (u32x4){0u, 0u, 0u, 0u}, fe);

    {
        float w[4][4];
        const u32x4 sa = *(const u32x4*)(shift_k + ch0 * 4), sb = *(const u32x4*)(shift_k + ch0 * 4 + 8);
        const unsigned sw[8] = {sa.x, sa.y, sa.z, sa.w, sb.x, sb.y, sb.z, sb.w};
#pragma unroll
        for (int c = 0; c < 4; ++c) {
            w[c][0] = bf_lo(sw[2 * c]); w[c][1] = bf_hi(sw[2 * c]);
            w[c][2] = bf_lo(sw[2 * c + 1]); w[c][3] = bf_hi(sw[2 * c + 1]);
        }
        u32x2 rows[7];
#pragma unroll
        for (int j = 0; j < 3; ++j) {
            rows[j].x = (unsigned)__builtin_amdgcn_update_dpp((int)kh[j].x, (int)kr[j + 1].x, 0x138, 0xf, 0xf, false);
            rows[j].y = (unsigned)__builtin_amdgcn_update_dpp((int)kh[j].y, (int)kr[j + 1].y, 0x138, 0xf, 0xf, false);
        }
#pragma unroll
        for (int j = 0; j < 4; ++j) rows[3 + j] = kr[j];
#pragma unroll
        for (int c = 0; c < 4; ++c) {
            float kf[7], o[4];
#pragma unroll
            for (int j = 0; j < 7; ++j) {
                const unsigned ww = (c < 2) ? rows[j].x : rows[j].y;
                kf[j] = (c & 1) ? bf_hi(ww) : bf_lo(ww);
            }
#pragma unroll
            for (int j = 0; j < 4; ++j) {
                float s = rbf(kf[j] * w[c][3]);
                s = rbf(s + rbf(kf[j + 1] * w[c][2]));
                s = rbf(s + rbf(kf[j + 2] * w[c][1]));
                s = rbf(s + rbf(kf[j + 3] * w[c][0]));
                const unsigned vw = (c < 2) ? vr[j].x : vr[j].y;
                o[j] = s * ((c & 1) ? bf_hi(vw) : bf_lo(vw));
            }
            u32x2 pkd = {pk_bf16(o[3], o[2]), pk_bf16(o[1], o[0])};
            *(LAS u32x2*)(smem + C1_KVOFF + c * C1_KV + (2044 - t0) * 2) = pkd;
        }
    }
    __syncthreads();

    const int ch = wid & 3, par = wid >> 2;
    const int row = lane & 15, kg = lane >> 4;
    const int cp = (row & 7) + 1;
    const LAS char* abase = (const LAS char*)smem + ch * C1_FILT + c1_copy_off(cp) + (25 + row - cp - 8 * kg) * 2;
    const LAS char* bbase = (const LAS char*)smem + C1_KVOFF + ch * C1_KV + (2040 - 16 * row - 8 * kg) * 2;
    f32x4 acc[8];
#pragma unroll
    for (int n = 0; n < 8; ++n) acc[n] = (f32x4){0.f, 0.f, 0.f, 0.f};
    if (par == 0) {
        const s16x8 a0 = *(const LAS s16x8*)(abase);
#pragma unroll
        for (int n = 0; n < 8; ++n) acc[n] = mfma16(a0, *(const LAS s16x8*)(bbase - 512 * n), acc[n]);
    }
#pragma unroll 1
    for (int r = 0; r < 4; ++r) {
        const int rho = 32 * (2 * r + par);
        const LAS char* ap = abase - rho * 2;
        const LAS char* bp = bbase - rho * 2 - 512 * 6;
        s16x8 bf[8];
#pragma unroll
        for (int m = -1; m < 7; ++m) bf[m + 1] = *(const LAS s16x8*)(bp + 512 * (6 - m));
#pragma unroll
        for (int L = 1; L <= 8; ++L) {
            const s16x8 af = *(const LAS s16x8*)(ap + 512 * L);
#pragma unroll
            for (int n = L - 1; n < 8; ++n) acc[n] = mfma16(af, bf[n - L + 1], acc[n]);
        }
    }
    __syncthreads();
    {
        LAS char* xb = (LAS char*)smem + ch * C1_FILT + lane * 16;
#pragma unroll
        for (int n = 0; n < 4; ++n) *(LAS f32x4*)(xb + par * 4096 + n * 1024) = par ? acc[n] : acc[4 + n];
        __syncthreads();
        const float dv = bf_lo(ssm_D[ch0 + ch]);
#pragma unroll
        for (int n = 0; n < 4; ++n) {
            const f32x4 o = *(const LAS f32x4*)(xb + (1 - par) * 4096 + n * 1024);
            const f32x4 y = (par ? acc[4 + n] : acc[n]) + o;
            const int t = 256 * (4 * par + n) + 16 * row + 4 * kg;
            const u32x2 kvp = *(const LAS u32x2*)(smem + C1_KVOFF + ch * C1_KV + (2044 - t) * 2);
            const float r0 = rbf(y[0]) + rbf(bf_hi(kvp.y) * dv), r1 = rbf(y[1]) + rbf(bf_lo(kvp.y) * dv);
            const float r2 = rbf(y[2]) + rbf(bf_hi(kvp.x) * dv), r3 = rbf(y[3]) + rbf(bf_lo(kvp.x) * dv);
            *(u32x2*)(yT + (size_t)(ch0 + ch) * S_ + t) = (u32x2){pk_bf16(r0, r1), pk_bf16(r2, r3)};
        }
    }
}

__device__ __forceinline__ float tok_sum(float v, LAS float* red, int wid, int tok, int lane) {
    v += __shfl_xor(v, 8); v += __shfl_xor(v, 16); v += __shfl_xor(v, 32);
    if (lane < 8) red[wid * 8 + tok] = v;
    __syncthreads();
    float s = 0.f;
#pragma unroll
    for (int w = 0; w < 9; ++w) s += red[w * 8 + tok];
    return s;
}

__global__ __launch_bounds__(576) void k2_ln(const u16* __restrict__ x, const u16* __restrict__ yT,
                                             u16* __restrict__ h, u16* __restrict__ aT) {
    __shared__ __attribute__((aligned(16))) char ys[576 * 16 + 72 * 16];
    __shared__ float red[4][72];
    const int tid = threadIdx.x, lane = tid & 63, wid = tid >> 6;
    const int b = blockIdx.x, tg = (b & 7) * 32 + (b >> 3), tok0 = tg * 8;
    const int tok = tid & 7, c = tid >> 3;
    const u32x4 xv = *(const u32x4*)(x + (size_t)(tok0 + tok) * D_ + c * 8);
    const u32x4 yv = *(const u32x4*)(yT + (size_t)tid * S_ + tok0);
    *(u32x4*)(ys + tid * 16 + (tid >> 3) * 16) = yv;
    float xf[8] = {bf_lo(xv.x), bf_hi(xv.x), bf_lo(xv.y), bf_hi(xv.y), bf_lo(xv.z), bf_hi(xv.z), bf_lo(xv.w), bf_hi(xv.w)};
    float s = 0.f;
#pragma unroll
    for (int j = 0; j < 8; ++j) s += xf[j];
    const float mean1 = tok_sum(s, (LAS float*)red[0], wid, tok, lane) * (1.f / D_);
    float q = 0.f;
#pragma unroll
    for (int j = 0; j < 8; ++j) { const float d = xf[j] - mean1; q += d * d; }
    const float var1 = tok_sum(q, (LAS float*)red[1], wid, tok, lane) * (1.f / D_);
    const float m1 = rbf(mean1), sd1 = rbf(sqrtf(rbf(rbf(var1) + 1e-5f)));
    float hf[8];
    s = 0.f;
#pragma unroll
    for (int j = 0; j < 8; ++j) {
        const float y = bf_lo(*(const u16*)(ys + (c * 8 + j) * 16 + c * 16 + tok * 2));
        const float ln = rbf(rbf(xf[j] - m1) / sd1);
        hf[j] = rbf(xf[j] + rbf(ln * y));
        s += hf[j];
    }
    *(u32x4*)(h + (size_t)(tok0 + tok) * D_ + c * 8) =
        (u32x4){pk_bf16(hf[0], hf[1]), pk_bf16(hf[2], hf[3]), pk_bf16(hf[4], hf[5]), pk_bf16(hf[6], hf[7])};
    const float mean2 = tok_sum(s, (LAS float*)red[2], wid, tok, lane) * (1.f / D_);
    q = 0.f;
#pragma unroll
    for (int j = 0; j < 8; ++j) { const float d = hf[j] - mean2; q += d * d; }
    const float var2 = tok_sum(q, (LAS float*)red[3], wid, tok, lane) * (1.f / D_);
    const float m2 = rbf(mean2), sd2 = rbf(sqrtf(rbf(rbf(var2) + 1e-5f)));
    float af[8];
#pragma unroll
    for (int j = 0; j < 8; ++j) af[j] = rbf(hf[j] - m2) / sd2;
    const int t = tok0 + tok, rt = t >> 8, tile = (t >> 4) & 15, kb = c >> 2, kg = c & 3;
    *(u32x4*)(aT + ((size_t)((rt * 18 + kb) * 16 + tile) * 64 + kg * 16 + (t & 15)) * 8) =
        (u32x4){pk_bf16(af[0], af[1]), pk_bf16(af[2], af[3]), pk_bf16(af[4], af[5]), pk_bf16(af[6], af[7])};
}

constexpr int ST_BYTES = 16384 + 2 * 9216;
constexpr int NBUF = 4;
constexpr int G_LDS = NBUF * ST_BYTES;
#define SCHED() __builtin_amdgcn_sched_barrier(0)

template <int OFF>
__device__ __forceinline__ void dma16(unsigned voff, const void* sbase, unsigned m0v) {
    asm volatile("s_mov_b32 m0, %2\n\ts_nop 0\n\tglobal_load_lds_dwordx4 %0, %1 offset:%3"
                 :: "v"(voff), "s"(sbase), "s"(m0v), "i"(OFF) : "memory");
}
template <int OFF>
__device__ __forceinline__ void dma16_q(unsigned voff, const void* sbase, unsigned m0v) {
    asm volatile("s_mov_b32 m0, %2\n\ts_mov_b64 exec, 0xffff\n\tglobal_load_lds_dwordx4 %0, %1 offset:%3\n\ts_mov_b64 exec, -1"
                 :: "v"(voff), "s"(sbase), "s"(m0v), "i"(OFF) : "memory");
}
__device__ __forceinline__ s16x8 tr_frag(const LAS char* p) {
    const s16x4 lo = __builtin_amdgcn_ds_read_tr16_b64_v4i16((LAS s16x4*)p);
    const s16x4 hi = __builtin_amdgcn_ds_read_tr16_b64_v4i16((LAS s16x4*)(p + 16 * 288));
    return __builtin_shufflevector(lo, hi, 0, 1, 2, 3, 4, 5, 6, 7);
}
__device__ __forceinline__ float geglu(float g, float u) {
    const float z = g * __builtin_fmaf(g * g, -0.10294324f, -2.30220819f);
    const float e = __builtin_amdgcn_exp2f(z);
    return g * __builtin_amdgcn_rcpf(1.f + e) * u;
}
template <int P>
__device__ __forceinline__ void stage_piece(const char* as, const char* ws, unsigned aoff, unsigned w0, unsigned w1, unsigned w2,
                                            unsigned ma, unsigned mw) {
    if constexpr (P == 0) dma16<0>(aoff, as, ma);
    if constexpr (P == 1) dma16<1024>(aoff, as, ma);
    if constexpr (P == 2) dma16<0>(w0, ws, mw);
    if constexpr (P == 3) dma16<1024>(w1, ws, mw);
    if constexpr (P == 4) dma16_q<2048>(w2, ws, mw);
}
template <int N> struct IC { static constexpr int v = N; };

__global__ __launch_bounds__(512, 2) void k3_gemm1(const u16* __restrict__ aT, const u16* __restrict__ wg,
                                                   const u16* __restrict__ wu, u16* __restrict__ hh) {
    extern __shared__ __attribute__((aligned(16))) char smem[];
    LAS char* lds = (LAS char*)smem;
    const unsigned lds0 = (unsigned)(size_t)lds;
    const int tid = threadIdx.x, lane = tid & 63;
    const int wid = __builtin_amdgcn_readfirstlane(tid >> 6);
    const int wr = wid & 3, wc = wid >> 2;
    const int b = blockIdx.x, xcd = b & 7, idx = b >> 3;
    const int rt = (xcd >> 2) * 4 + (idx & 3), ct = (xcd & 3) * 8 + (idx >> 2);
    asm volatile("" :: "s"(hh));

    const char* asrc = (const char*)aT + (size_t)rt * 18 * 16384 + wid * 2048;
    const char* wsrc = (const char*)(wc ? wu : wg) + ct * 288 - 2048;
    auto wofs = [&](int i) -> unsigned {
        const int q = lane + 64 * i, lrow = 8 * wr + q / 18, pos = q % 18;
        const int krow = ((lrow >> 2) & 3) * 8 + (lrow >> 4) * 4 + (lrow & 3);
        const int col = pos < 8 ? 8 * pos : (pos < 16 ? 72 + 8 * (pos - 8) : (((pos & 1) ^ wc) ? 136 : 64));
        return (unsigned)(krow * (F_ * 2) + col * 2 + 2048 - 1024 * i);
    };
    const unsigned w0 = wofs(0), w1 = wofs(1), w2 = wofs(2), aoff = lane * 16;
    const unsigned ma0 = lds0 + wid * 2048, mw0 = lds0 + 16384 + wc * 9216 + wr * 2304;
#define K3_PIECE(P, s) stage_piece<P>(asrc + (size_t)(s) * 16384, wsrc + (size_t)(s) * (32 * F_ * 2), aoff, w0, w1, w2, \
                                      ma0 + ((s) & 3) * ST_BYTES, mw0 + ((s) & 3) * ST_BYTES)
#define K3_STAGE(s) do { K3_PIECE(0, s); K3_PIECE(1, s); K3_PIECE(2, s); K3_PIECE(3, s); K3_PIECE(4, s); } while (0)
    K3_STAGE(0); K3_STAGE(1); K3_STAGE(2);

    const int g4 = lane >> 4, q4 = (lane & 15) >> 2, p4 = lane & 3;
    const int rowoff = (4 * g4 + q4) * 288;
    const int toff = wr * 4096 + lane * 16;
    const int foff = 16384 + rowoff + wc * 128 + p4 * 8;
    const int moff = 16384 + rowoff + 256 + (p4 < 2 ? 16 * wc + 8 * p4 : 9216 + 16 * (1 - wc) + 8 * (p4 - 2));

    f32x4 acc[9][4];
#pragma unroll
    for (int j = 0; j < 9; ++j)
#pragma unroll
        for (int i = 0; i < 4; ++i) acc[j][i] = (f32x4){0.f, 0.f, 0.f, 0.f};
    s16x8 wf[9], tf[2][4];
    auto rd_w = [&](const LAS char* sb, int j) -> s16x8 {
        return j < 8 ? tr_frag(sb + foff + (j >> 2) * 9216 + (j & 3) * 32) : tr_frag(sb + moff);
    };

    WAIT_V(10); BAR();
#pragma unroll
    for (int j = 0; j < 2; ++j) wf[j] = rd_w(lds, j);
#pragma unroll
    for (int i = 0; i < 4; ++i) tf[0][i] = *(const LAS s16x8*)(lds + toff + i * 1024);
#pragma unroll
    for (int j = 2; j < 9; ++j) wf[j] = rd_w(lds, j);
    SCHED();

    auto iter = [&](auto mode, auto vm, int s, s16x8 (&tc)[4], s16x8 (&tn)[4]) {
        constexpr int MODE = decltype(mode)::v;
        if constexpr (MODE < 2) {
            if constexpr (decltype(vm)::v == 5) WAIT_V(5); else WAIT_V(0);
            BAR();
            SCHED();
        }
        const LAS char* sb = lds + ((s + 1) & 3) * ST_BYTES;
#pragma unroll
        for (int j = 0; j < 9; ++j) {
#pragma unroll
            for (int i = 0; i < 4; ++i) acc[j][i] = mfma16(wf[j], tc[i], acc[j][i]);
            SCHED();
            if constexpr (MODE < 2) {
                wf[j] = rd_w(sb, j);
                if (j >= 2 && j < 6) tn[j - 2] = *(const LAS s16x8*)(sb + toff + (j - 2) * 1024);
            }
            if constexpr (MODE == 0) {
                if (j == 0) K3_PIECE(0, s + 3);
                if (j == 1) K3_PIECE(1, s + 3);
                if (j == 2) K3_PIECE(2, s + 3);
                if (j == 3) K3_PIECE(3, s + 3);
                if (j == 4) K3_PIECE(4, s + 3);
            }
            SCHED();
        }
    };
#pragma unroll 1
    for (int s = 0; s < 14; s += 2) {
        iter(IC<0>{}, IC<5>{}, s, tf[0], tf[1]);
        iter(IC<0>{}, IC<5>{}, s + 1, tf[1], tf[0]);
    }
    iter(IC<0>{}, IC<5>{}, 14, tf[0], tf[1]);
    iter(IC<1>{}, IC<5>{}, 15, tf[1], tf[0]);
    iter(IC<1>{}, IC<0>{}, 16, tf[0], tf[1]);
    iter(IC<2>{}, IC<0>{}, 17, tf[1], tf[0]);

    const int mt = rt * 2 + (wr >> 1), ks = ct >> 3, lct = ct & 7;
    char* hb = (char*)hh + ((size_t)(mt * 4 + ks) * 36) * 8192 + ((wr & 1) * 4) * 1024;
#pragma unroll
    for (int i = 0; i < 4; ++i) {
#pragma unroll
        for (int jp = 0; jp < 2; ++jp) {
            const f32x4 g0 = acc[2 * jp][i], g1 = acc[2 * jp + 1][i], u0 = acc[4 + 2 * jp][i], u1 = acc[5 + 2 * jp][i];
            u32x4 o;
            o.x = pk_bf16(geglu(g0[0], u0[0]), geglu(g0[1], u0[1]));
            o.y = pk_bf16(geglu(g0[2], u0[2]), geglu(g0[3], u0[3]));
            o.z = pk_bf16(geglu(g1[0], u1[0]), geglu(g1[1], u1[1]));
            o.w = pk_bf16(geglu(g1[2], u1[2]), geglu(g1[3], u1[3]));
            *(u32x4*)(hb + (size_t)(4 + lct * 4 + wc * 2 + jp) * 8192 + i * 1024 + lane * 16) = o;
        }
    }
#pragma unroll
    for (int ip = 0; ip < 2; ++ip) {
        float gg[4], uu[4];
#pragma unroll
        for (int r = 0; r < 4; ++r) {
            auto sw = __builtin_amdgcn_permlane32_swap(__float_as_uint(acc[8][2 * ip][r]), __float_as_uint(acc[8][2 * ip + 1][r]), false, false);
            gg[r] = __uint_as_float(sw[0]); uu[r] = __uint_as_float(sw[1]);
        }
        u32x2 o = {pk_bf16(geglu(gg[0], uu[0]), geglu(gg[1], uu[1])), pk_bf16(geglu(gg[2], uu[2]), geglu(gg[3], uu[3]))};
        const int kgs = (lct & 1) * 2 + wc;
        *(u32x2*)(hb + (size_t)(lct >> 1) * 8192 + (2 * ip + (lane >> 5)) * 1024 + (kgs * 16 + (lane & 15)) * 16 + ((lane >> 4) & 1) * 8) = o;
    }
}

__global__ __launch_bounds__(512, 2) void k4_gemm2(const u16* __restrict__ hh, const u16* __restrict__ wd,
                                                   float* __restrict__ part) {
    extern __shared__ __attribute__((aligned(16))) char smem[];
    LAS char* lds = (LAS char*)smem;
    const unsigned lds0 = (unsigned)(size_t)lds;
    const int tid = threadIdx.x, lane = tid & 63;
    const int wid = __builtin_amdgcn_readfirstlane(tid >> 6);
    const int wr = wid & 3, kh = wid >> 2;
    const int b = blockIdx.x, xcd = b & 7, idx = b >> 3;
    const int mt = (xcd >> 2) * 8 + (idx & 7), ks = xcd & 3, nt = idx >> 3;
    asm volatile("" :: "s"(part));

    const char* asrc = (const char*)hh + ((size_t)(mt * 4 + ks) * 36) * 8192 + wid * 2048;
    const char* wsrc = (const char*)wd + (size_t)ks * 1152 * (D_ * 2) + nt * 288 - 2048;
    auto wofs = [&](int i, bool full) -> unsigned {
        const int q = lane + 64 * i, r = 8 * wr + q / 18, pos = q % 18;
        const int kg = (r & 15) >> 2, j = (r & 3) + 4 * (r >> 4);
        const int f = full ? r : (kg >> 1) * 144 + (kg & 1) * 72 + 64 + j;
        return (unsigned)(f * (D_ * 2) + pos * 16 + 2048 - 1024 * i);
    };
    const unsigned aoff = lane * 16;
    const unsigned ma0 = lds0 + wid * 2048, mw0 = lds0 + 16384 + kh * 9216 + wr * 2304;
    {
        const unsigned l0 = wofs(0, false), l1 = wofs(1, false), l2 = wofs(2, false);
#define K4_LP(P, s) stage_piece<P>(asrc + (size_t)(s) * 16384, wsrc + (size_t)((2 * (s) + kh) * 288) * (D_ * 2), aoff, l0, l1, l2, \
                                   ma0 + (s) * ST_BYTES, mw0 + (s) * ST_BYTES)
        K4_LP(0, 0); K4_LP(1, 0); K4_LP(2, 0); K4_LP(3, 0); K4_LP(4, 0);
        K4_LP(0, 1); K4_LP(1, 1); K4_LP(2, 1); K4_LP(3, 1); K4_LP(4, 1);
    }
    const unsigned w0 = wofs(0, true), w1 = wofs(1, true), w2 = wofs(2, true);
    const char* wsf = wsrc + (size_t)(kh * 32) * (D_ * 2);
#define K4_PIECE(P, s) stage_piece<P>(asrc + (size_t)(s) * 16384, wsf + (size_t)((s) - 2) * (72 * D_ * 2), aoff, w0, w1, w2, \
                                      ma0 + ((s) & 3) * ST_BYTES, mw0 + ((s) & 3) * ST_BYTES)
    K4_PIECE(0, 2); K4_PIECE(1, 2); K4_PIECE(2, 2); K4_PIECE(3, 2); K4_PIECE(4, 2);

    const int g4 = lane >> 4, q4 = (lane & 15) >> 2, p4 = lane & 3;
    const int toff = kh * 8192 + wr * 2048 + lane * 16;
    const int foff = 16384 + kh * 9216 + (4 * g4 + q4) * 288 + p4 * 8;

    f32x4 acc[9][2];
#pragma unroll
    for (int j = 0; j < 9; ++j)
#pragma unroll
        for (int i = 0; i < 2; ++i) acc[j][i] = (f32x4){0.f, 0.f, 0.f, 0.f};
    s16x8 wf[9], tf[2][2];

    WAIT_V(10); BAR();
#pragma unroll
    for (int i = 0; i < 2; ++i) tf[0][i] = *(const LAS s16x8*)(lds + toff + i * 1024);
#pragma unroll
    for (int j = 0; j < 9; ++j) wf[j] = tr_frag(lds + foff + j * 32);
    SCHED();

    auto iter = [&](auto mode, auto vm, int s, s16x8 (&tc)[2], s16x8 (&tn)[2]) {
        constexpr int MODE = decltype(mode)::v;
        if constexpr (MODE < 2) {
            if constexpr (decltype(vm)::v == 5) WAIT_V(5); else WAIT_V(0);
            BAR();
            SCHED();
        }
        const LAS char* sb = lds + ((s + 1) & 3) * ST_BYTES;
#pragma unroll
        for (int j = 0; j < 9; ++j) {
#pragma unroll
            for (int i = 0; i < 2; ++i) acc[j][i] = mfma16(wf[j], tc[i], acc[j][i]);
            SCHED();
            if constexpr (MODE < 2) {
                wf[j] = tr_frag(sb + foff + j * 32);
                if (j >= 2 && j < 4) tn[j - 2] = *(const LAS s16x8*)(sb + toff + (j - 2) * 1024);
            }
            if constexpr (MODE == 0) {
                if (j == 0) K4_PIECE(0, s + 3);
                if (j == 2) K4_PIECE(1, s + 3);
                if (j == 4) K4_PIECE(2, s + 3);
                if (j == 6) K4_PIECE(3, s + 3);
                if (j == 7) K4_PIECE(4, s + 3);
            }
            SCHED();
        }
    };
#pragma unroll 1
    for (int s = 0; s < 14; s += 2) {
        iter(IC<0>{}, IC<5>{}, s, tf[0], tf[1]);
        iter(IC<0>{}, IC<5>{}, s + 1, tf[1], tf[0]);
    }
    iter(IC<0>{}, IC<5>{}, 14, tf[0], tf[1]);
    iter(IC<1>{}, IC<5>{}, 15, tf[1], tf[0]);
    iter(IC<1>{}, IC<0>{}, 16, tf[0], tf[1]);
    iter(IC<2>{}, IC<0>{}, 17, tf[1], tf[0]);

    __syncthreads();
    LAS char* xb = lds + wr * (2 * 9216) + lane * 16;
#pragma unroll
    for (int j = 0; j < 9; ++j) *(LAS f32x4*)(xb + kh * 9216 + j * 1024) = kh ? acc[j][0] : acc[j][1];
    __syncthreads();
    const int tokn = mt * 128 + wr * 32 + kh * 16 + (lane & 15);
    float* po = part + ((size_t)ks * S_ + tokn) * D_ + nt * 144 + 4 * g4;
#pragma unroll
    for (int j = 0; j < 9; ++j) {
        const f32x4 o = *(const LAS f32x4*)(xb + (1 - kh) * 9216 + j * 1024);
        *(f32x4*)(po + j * 16) = (kh ? acc[j][1] : acc[j][0]) + o;
    }
}

__global__ __launch_bounds__(256) void k5_reduce(const float* __restrict__ part, const u16* __restrict__ h, u16* __restrict__ out) {
    const int i = (blockIdx.x * 256 + threadIdx.x) * 4;
    const f32x4 p0 = *(const f32x4*)(part + i), p1 = *(const f32x4*)(part + (size_t)S_ * D_ + i);
    const f32x4 p2 = *(const f32x4*)(part + (size_t)2 * S_ * D_ + i), p3 = *(const f32x4*)(part + (size_t)3 * S_ * D_ + i);
    const u32x2 hv = *(const u32x2*)(h + i);
    const f32x4 s = (p0 + p1) + (p2 + p3);
    *(u32x2*)(out + i) = (u32x2){pk_bf16(bf_lo(hv.x) + rbf(s[0]), bf_hi(hv.x) + rbf(s[1])),
                                 pk_bf16(bf_lo(hv.y) + rbf(s[2]), bf_hi(hv.y) + rbf(s[3]))};
}

constexpr size_t WS_YT = 0;
constexpr size_t WS_H = WS_YT + (size_t)S_ * D_ * 2;
constexpr size_t WS_AT = WS_H + (size_t)S_ * D_ * 2;
constexpr size_t WS_HH = WS_AT + (size_t)S_ * D_ * 2;
constexpr size_t WS_PART = WS_HH + (size_t)S_ * F_ * 2;

extern "C" void kernel_launch(void* const* d_in, const int* in_sizes, int n_in, void* d_out, int out_size, void* d_ws,
                              size_t ws_size, hipStream_t stream) {
    const u16* q = (const u16*)d_in[0];
    const u16* k = (const u16*)d_in[1];
    const u16* v = (const u16*)d_in[2];
    const u16* shift_k = (const u16*)d_in[3];
    const u16* ssm_k = (const u16*)d_in[4];
    const u16* ssm_D = (const u16*)d_in[5];
    const u16* wg = (const u16*)d_in[7];
    const u16* wu = (const u16*)d_in[8];
    const u16* wd = (const u16*)d_in[9];
    char* ws = (char*)d_ws;
    u16* yT = (u16*)(ws + WS_YT);
    u16* h = (u16*)(ws + WS_H);
    u16* aT = (u16*)(ws + WS_AT);
    u16* hh = (u16*)(ws + WS_HH);
    float* part = (float*)(ws + WS_PART);
    (void)hipFuncSetAttribute((const void*)k1_conv, hipFuncAttributeMaxDynamicSharedMemorySize, C1_LDS);
    (void)hipFuncSetAttribute((const void*)k3_gemm1, hipFuncAttributeMaxDynamicSharedMemorySize, G_LDS);
    (void)hipFuncSetAttribute((const void*)k4_gemm2, hipFuncAttributeMaxDynamicSharedMemorySize, G_LDS);
    k1_conv<<<144, 512, C1_LDS, stream>>>(k, v, shift_k, ssm_k, ssm_D, yT);
    k2_ln<<<256, 576, 0, stream>>>(q, yT, h, aT);
    k3_gemm1<<<256, 512, G_LDS, stream>>>(aT, wg, wu, hh);
    k4_gemm2<<<256, 512, G_LDS, stream>>>(hh, wd, part);
    k5_reduce<<<S_ * D_ / 1024, 256, 0, stream>>>(part, h, (u16*)d_out);
}
```

```cpp
#include <hip/hip_runtime.h>
#include <hip/hip_bf16.h>

#define LAS __attribute__((address_space(3)))
typedef unsigned short u16;
typedef short s16x4 __attribute__((ext_vector_type(4)));
typedef short s16x8 __attribute__((ext_vector_type(8)));
typedef float f32x2 __attribute__((ext_vector_type(2)));
typedef float f32x4 __attribute__((ext_vector_type(4)));
typedef unsigned u32x2 __attribute__((ext_vector_type(2)));
typedef unsigned u32x4 __attribute__((ext_vector_type(4)));
typedef __bf16 bf16x2_t __attribute__((ext_vector_type(2)));

constexpr int S_ = 2048, D_ = 576, F_ = 4608;

__device__ __forceinline__ unsigned pk_bf16(float lo, float hi) {
    f32x2 v = {lo, hi};
    bf16x2_t b = __builtin_convertvector(v, bf16x2_t);
    return __builtin_bit_cast(unsigned, b);
}
__device__ __forceinline__ float bf_lo(unsigned w) { return __uint_as_float(w << 16); }
__device__ __forceinline__ float bf_hi(unsigned w) { return __uint_as_float(w & 0xffff0000u); }
__device__ __forceinline__ float rbf(float x) { return bf_lo(pk_bf16(x, 0.f)); }
__device__ __forceinline__ f32x4 mfma16(s16x8 a, s16x8 b, f32x4 c) {
    return __builtin_amdgcn_mfma_f32_16x16x32_bf16(a, b, c, 0, 0, 0);
}
#define WAIT_V(n) asm volatile("s_waitcnt vmcnt(" #n ")" ::: "memory")
#define BAR() do { __builtin_amdgcn_s_barrier(); asm volatile("" ::: "memory"); } while (0)
#define SCHED() __builtin_amdgcn_sched_barrier(0)
#define DIAG_DECL
#define STAMP(i)
#define DIAG_END(wsbase, kid)
__device__ __forceinline__ void mfma_acc(f32x4& c, s16x8 a, s16x8 b) {
    asm volatile("v_mfma_f32_16x16x32_bf16 %0, %1, %2, %0" : "+v"(c) : "v"(a), "v"(b));
}
#define MFMA_DONE() do { asm volatile("s_nop 7\n\ts_nop 7" ::: "memory"); SCHED(); } while (0)
constexpr size_t WS_KV = 0;
constexpr size_t WS_H = WS_KV + (size_t)S_ * D_ * 2;
constexpr size_t WS_ST = WS_H + (size_t)S_ * D_ * 2;
constexpr size_t WS_CS = WS_ST + (size_t)S_ * 8;
constexpr size_t WS_G2 = WS_CS + (size_t)2 * F_ * 4;
constexpr size_t WS_HH = WS_G2 + (size_t)S_ * 8;
constexpr size_t WS_X = WS_HH + (size_t)S_ * F_ * 2;
constexpr unsigned X_BYTES = 64 * 4 * 3 * 9216;

typedef __amdgpu_buffer_rsrc_t rsrc_t;
__device__ __forceinline__ rsrc_t mk_rsrc(const void* p, unsigned bytes) { return __builtin_amdgcn_make_buffer_rsrc((void*)p, 0, bytes, 0x00020000); }
__device__ __forceinline__ void st16_wt(rsrc_t r, unsigned off, u32x4 v) { __builtin_amdgcn_raw_buffer_store_b128(v, r, off, 0, 16); }
__device__ __forceinline__ void st8_wt(rsrc_t r, unsigned off, u32x2 v) { __builtin_amdgcn_raw_buffer_store_b64(v, r, off, 0, 16); }
template <int CTRL> __device__ __forceinline__ float dpp_add(float x) {
    return x + __uint_as_float(__builtin_amdgcn_update_dpp(0, (int)__float_as_uint(x), CTRL, 0xf, 0xf, true));
}
__device__ __forceinline__ float x16_add(float x) {
    auto r = __builtin_amdgcn_permlane16_swap(__float_as_uint(x), __float_as_uint(x), false, false);
    return __uint_as_float(r[0]) + __uint_as_float(r[1]);
}
__device__ __forceinline__ float x32_add(float x) {
    auto r = __builtin_amdgcn_permlane32_swap(__float_as_uint(x), __float_as_uint(x), false, false);
    return __uint_as_float(r[0]) + __uint_as_float(r[1]);
}
__device__ __forceinline__ float sum32(float x) {
    x = dpp_add<0xB1>(x); x = dpp_add<0x4E>(x); x = dpp_add<0x141>(x); x = dpp_add<0x140>(x);
    return x16_add(x);
}
__device__ __forceinline__ float ln_sd(float var) { return rbf(__builtin_amdgcn_sqrtf(rbf(rbf(var) + 1e-5f))); }

__global__ __launch_bounds__(512) void k0_pre(const u16* __restrict__ x, const u16* __restrict__ kin, const u16* __restrict__ vin,
                                              const u16* __restrict__ shift_k, const u16* __restrict__ ssm_k, char* __restrict__ ws) {
    const int tid = threadIdx.x, lane = tid & 63;
    const int wid = __builtin_amdgcn_readfirstlane(tid >> 6);
    const int b = blockIdx.x, xcd = b & 7, idx = b >> 3;
    DIAG_DECL;
    if (wid < 4) {
        const int id = xcd * 36 + idx, tb = id / 9, cb = id % 9;
        const int t0 = tb * 64 + (lane >> 3) * 8, ch = cb * 64 + (wid * 8 + (lane & 7)) * 2;
        const rsrc_t rk = mk_rsrc(kin, S_ * D_ * 2), rv = mk_rsrc(vin, S_ * D_ * 2);
        unsigned kw[11], vw[8];
        const int off = t0 * (D_ * 2) + ch * 2;
#pragma unroll
        for (int j = 0; j < 11; ++j) kw[j] = __builtin_amdgcn_raw_buffer_load_b32(rk, (unsigned)(off + (j - 3) * (D_ * 2)), 0, 0);
#pragma unroll
        for (int j = 0; j < 8; ++j) vw[j] = __builtin_amdgcn_raw_buffer_load_b32(rv, (unsigned)(off + j * (D_ * 2)), 0, 0);
        const u32x4 sw = *(const u32x4*)(shift_k + ch * 4);
        const u32x4 pf = *(const u32x4*)(ssm_k + (size_t)xcd * 72 * S_ + idx * 4096 + tid * 8);
        STAMP(1);
        const f32x2 w0 = {bf_lo(sw.x), bf_lo(sw.z)}, w1 = {bf_hi(sw.x), bf_hi(sw.z)}, w2 = {bf_lo(sw.y), bf_lo(sw.w)}, w3 = {bf_hi(sw.y), bf_hi(sw.w)};
        f32x2 kf[11], o[8];
#pragma unroll
        for (int j = 0; j < 11; ++j) kf[j] = (f32x2){bf_lo(kw[j]), bf_hi(kw[j])};
#pragma unroll
        for (int j = 0; j < 8; ++j) {
            f32x2 s = kf[j] * w3;
            s = __builtin_elementwise_fma(kf[j + 1], w2, s);
            s = __builtin_elementwise_fma(kf[j + 2], w1, s);
            s = __builtin_elementwise_fma(kf[j + 3], w0, s);
            o[j] = s * (f32x2){bf_lo(vw[j]), bf_hi(vw[j])};
        }
        const rsrc_t ro = mk_rsrc(ws + WS_KV, S_ * D_ * 2);
        const unsigned oo = (unsigned)(ch * (S_ * 2) + (2040 - t0) * 2);
        st16_wt(ro, oo, (u32x4){pk_bf16(o[7].x, o[6].x), pk_bf16(o[5].x, o[4].x), pk_bf16(o[3].x, o[2].x), pk_bf16(o[1].x, o[0].x)});
        st16_wt(ro, oo + S_ * 2, (u32x4){pk_bf16(o[7].y, o[6].y), pk_bf16(o[5].y, o[4].y), pk_bf16(o[3].y, o[2].y), pk_bf16(o[1].y, o[0].y)});
        STAMP(2);
        asm volatile("" :: "v"(pf));
    } else {
        const int row = (b & 255) * 8 + (wid - 4) * 2 + (lane >> 5);
        const unsigned* xr = (const unsigned*)(x + (size_t)row * D_) + (lane & 31);
        unsigned xw[9];
#pragma unroll
        for (int j = 0; j < 9; ++j) xw[j] = xr[32 * j];
        const u32x4 pf = *(const u32x4*)(ssm_k + (size_t)xcd * 72 * S_ + idx * 4096 + tid * 8);
        STAMP(1);
        f32x2 xf[9], s = {0.f, 0.f};
#pragma unroll
        for (int j = 0; j < 9; ++j) { xf[j] = (f32x2){bf_lo(xw[j]), bf_hi(xw[j])}; s += xf[j]; }
        const float mean = sum32(s.x + s.y) * (1.f / D_);
        f32x2 q = {0.f, 0.f};
#pragma unroll
        for (int j = 0; j < 9; ++j) { const f32x2 d = xf[j] - mean; q = __builtin_elementwise_fma(d, d, q); }
        const float var = sum32(q.x + q.y) * (1.f / D_);
        if ((lane & 31) == 0 && b < 256) {
            st8_wt(mk_rsrc(ws + WS_ST, S_ * 8), row * 8, (u32x2){__float_as_uint(rbf(mean)), __float_as_uint(__builtin_amdgcn_rcpf(ln_sd(var)))});
            st8_wt(mk_rsrc(ws + WS_G2, S_ * 8), row * 8, (u32x2){0u, 0u});
        }
        STAMP(2);
        asm volatile("" :: "v"(pf));
    }
    DIAG_END(ws, 0);
}

constexpr int C1_COPY = 262 * 16;
constexpr int C1_FILT = 8 * C1_COPY;
constexpr int C1_KV = 2304 * 2;
constexpr int C1_KVOFF = 4 * C1_FILT;
constexpr int C1_LDS = C1_KVOFF + 4 * C1_KV;

__device__ __forceinline__ int c1_copy_off(int cp) { return (cp <= 4 ? (cp - 1) * 2 : (cp - 5) * 2 + 1) * C1_COPY; }

__global__ __launch_bounds__(512, 2) void k1_conv(const u16* __restrict__ x, const u16* __restrict__ ssm_k, const u16* __restrict__ ssm_D,
                                                  const u16* __restrict__ wg, const u16* __restrict__ wu, const u16* __restrict__ wd,
                                                  char* __restrict__ ws) {
    extern __shared__ __attribute__((aligned(16))) char smem[];
    const int tid = threadIdx.x, lane = tid & 63;
    const int wid = __builtin_amdgcn_readfirstlane(tid >> 6);
    const int b = blockIdx.x, xcd = b & 7, idx = b >> 3;
    DIAG_DECL;
    if (idx >= 18) {
        const int hp = idx - 18, cgp = xcd & 3, half = xcd >> 2;
        if (hp >= 9) {
            const unsigned* wds = (const unsigned*)(wd + (size_t)(cgp * 2 + half) * 576 * D_);
            unsigned touch = 0;
#pragma unroll
            for (int i = 0; i < 3; ++i) { const int l = (hp - 9) * 1037 + i * 512 + tid; if (i * 512 + tid < 1037 && l < 5184) touch ^= wds[l * 32]; }
            asm volatile("" :: "v"(touch));
        } else {
            const int rs = tid >> 3, c8 = tid & 7;
            const int col0 = cgp * 1152 + hp * 128;
            const u16* src = (half ? wu : wg) + col0 + c8 * 8 + (size_t)rs * F_;
            u32x4 v[2][9];
#pragma unroll
            for (int p = 0; p < 9; ++p)
#pragma unroll
                for (int l = 0; l < 2; ++l) v[l][p] = *(const u32x4*)(src + l * 64 + (size_t)p * 64 * F_);
            LAS float* red = (LAS float*)smem;
#pragma unroll
            for (int l = 0; l < 2; ++l) {
                float a[8];
#pragma unroll
                for (int e = 0; e < 8; ++e) a[e] = 0.f;
#pragma unroll
                for (int p = 0; p < 9; ++p) {
                    a[0] += bf_lo(v[l][p].x); a[1] += bf_hi(v[l][p].x); a[2] += bf_lo(v[l][p].y); a[3] += bf_hi(v[l][p].y);
                    a[4] += bf_lo(v[l][p].z); a[5] += bf_hi(v[l][p].z); a[6] += bf_lo(v[l][p].w); a[7] += bf_hi(v[l][p].w);
                }
#pragma unroll
                for (int e = 0; e < 8; ++e) a[e] = x32_add(x16_add(dpp_add<0x128>(a[e])));
                if (lane < 8) {
                    *(LAS f32x4*)(red + (wid * 2 + l) * 64 + lane * 8) = (f32x4){a[0], a[1], a[2], a[3]};
                    *(LAS f32x4*)(red + (wid * 2 + l) * 64 + lane * 8 + 4) = (f32x4){a[4], a[5], a[6], a[7]};
                }
            }
            __syncthreads();
            if (tid < 128) {
                float s = 0.f;
#pragma unroll
                for (int w = 0; w < 8; ++w) s += red[w * 128 + tid];
                __builtin_amdgcn_raw_buffer_store_b32(__float_as_uint(s), mk_rsrc(ws + WS_CS, 2 * F_ * 4), (unsigned)((half * F_ + col0 + tid) * 4), 0, 16);
            }
        }
        DIAG_END(ws, 1);
        return;
    }
    const int cg = xcd * 18 + idx, ch0 = cg * 4;

    u32x4 fa[2], fb[2], kvv[2];
#pragma unroll
    for (int i = 0; i < 2; ++i) {
        const int task = tid + 512 * i, c = task >> 8, u = task & 255;
        const u16* src = ssm_k + (size_t)(ch0 + c) * S_ + u * 8;
        fa[i] = *(const u32x4*)src;
        fb[i] = *(const u32x4*)(src + (u < 255 ? 8 : 0));
        kvv[i] = *(const u32x4*)((const u16*)(ws + WS_KV) + (size_t)(ch0 + c) * S_ + u * 8);
    }
    u32x4 fe = {0u, 0u, 0u, 0u};
    if (tid >= 508) fe = *(const u32x4*)(ssm_k + (size_t)(ch0 + tid - 508) * S_);
    STAMP(1);

    if (tid < 160) {
        const int cpy = tid / 5, e = tid % 5, q = e < 3 ? e : 257 + e;
        *(LAS u32x4*)(smem + cpy * C1_COPY + q * 16) = (u32x4){0u, 0u, 0u, 0u};
    } else if (tid >= 256 && tid < 384) {
        const int r = tid - 256, c = r >> 5, q = r & 31;
        *(LAS u32x4*)(smem + C1_KVOFF + c * C1_KV + 4096 + q * 16) = (u32x4){0u, 0u, 0u, 0u};
    }
    auto put_copies = [&](int c, int q, u32x4 lo, u32x4 hi) {
        unsigned d[8] = {lo.x, lo.y, lo.z, lo.w, hi.x, hi.y, hi.z, hi.w};
        unsigned o[7];
#pragma unroll
        for (int i = 0; i < 7; ++i) o[i] = __builtin_amdgcn_alignbit(d[i + 1], d[i], 16);
        LAS char* base = (LAS char*)smem + c * C1_FILT + q * 16;
#pragma unroll
        for (int cp = 1; cp <= 8; ++cp) {
            const int e = cp >> 1;
            u32x4 w = (cp & 1) ? (u32x4){o[e], o[e + 1], o[e + 2], o[e + 3]} : (u32x4){d[e], d[e + 1], d[e + 2], d[e + 3]};
            *(LAS u32x4*)(base + c1_copy_off(cp)) = w;
        }
    };
#pragma unroll
    for (int i = 0; i < 2; ++i) {
        const int task = tid + 512 * i;
        put_copies(task >> 8, (task & 255) + 4, fa[i], (task & 255) < 255 ? fb[i] : (u32x4){0u, 0u, 0u, 0u});
        *(LAS u32x4*)(smem + C1_KVOFF + (task >> 8) * C1_KV + (task & 255) * 16) = kvv[i];
    }
    if (tid >= 508) put_copies(tid - 508, 3, (u32x4){0u, 0u, 0u, 0u}, fe);
    STAMP(3);
    __syncthreads();
    STAMP(4);

    const int ch = wid & 3, par = wid >> 2;
    const int row = lane & 15, kg = lane >> 4;
    const int cp = (row & 7) + 1;
    const LAS char* abase = (const LAS char*)smem + ch * C1_FILT + c1_copy_off(cp) + (25 + row - cp - 8 * kg) * 2 - par * 64;
    const LAS char* bbase = (const LAS char*)smem + C1_KVOFF + ch * C1_KV + (2040 - 16 * row - 8 * kg) * 2 - par * 64 - 512 * 7 - 384;
    f32x4 acc[8];
#pragma unroll
    for (int n = 0; n < 8; ++n) acc[n] = (f32x4){0.f, 0.f, 0.f, 0.f};
    u32x2 xr[4];
    f32x4 s01, s23;
    const u16* xp = x + (size_t)(tid * 4) * D_ + ch0;
    const rsrc_t rx = mk_rsrc(ws + WS_X, X_BYTES);
    const unsigned xo = (unsigned)(cg * 49152 + tid * 16);
    s16x8 af[2][8], bf[2][8];
    auto rd_a = [&](int r, int L) -> s16x8 { return *(const LAS s16x8*)(abase - 128 * r + 512 * L); };
    auto rd_b = [&](int r, int m) -> s16x8 { return *(const LAS s16x8*)(bbase + 384 - 128 * r + 512 * (7 - m)); };
    af[0][0] = rd_a(0, 1);
#pragma unroll
    for (int e = 0; e < 8; ++e) bf[0][e] = rd_b(0, e - 1);
#pragma unroll
    for (int e = 1; e < 8; ++e) af[0][e] = rd_a(0, e + 1);
    asm volatile("s_nop 1" : "+v"(acc[0]), "+v"(acc[1]), "+v"(acc[2]), "+v"(acc[3]), "+v"(acc[4]), "+v"(acc[5]), "+v"(acc[6]), "+v"(acc[7]));
    SCHED();
#pragma unroll
    for (int r = 0; r < 4; ++r) {
#pragma unroll
        for (int L = 1; L <= 8; ++L) {
#pragma unroll
            for (int n = L - 1; n < 8; ++n) mfma_acc(acc[n], af[r & 1][L - 1], bf[r & 1][n - L + 1]);
            SCHED();
            if (r < 3) {
                if (L <= 4) { bf[~r & 1][2 * L - 2] = rd_b(r + 1, 2 * L - 3); bf[~r & 1][2 * L - 1] = rd_b(r + 1, 2 * L - 2);
                              af[~r & 1][2 * L - 2] = rd_a(r + 1, 2 * L - 1); af[~r & 1][2 * L - 1] = rd_a(r + 1, 2 * L); }
            } else if (L <= 4) {
                bf[0][2 * L - 2] = rd_b(0, 2 * L - 2); bf[0][2 * L - 1] = rd_b(0, 2 * L - 1);
                if (L == 1) af[0][0] = rd_a(0, 0);
            }
            if (L == 1) asm volatile("global_load_dwordx2 %0, %1, off" : "=v"(xr[r]) : "v"(xp + r * D_));
            if (L == 3 && r < 3) st16_wt(rx, xo + (2 * r) * 8192, (u32x4){~0u, ~0u, ~0u, ~0u});
            if (L == 5 && r < 3) st16_wt(rx, xo + (2 * r + 1) * 8192, (u32x4){~0u, ~0u, ~0u, ~0u});
            if (L == 3 && r == 3) asm volatile("global_load_dwordx4 %0, %1, off" : "=v"(s01) : "v"(ws + WS_ST + tid * 32));
            if (L == 5 && r == 3) asm volatile("global_load_dwordx4 %0, %1, off offset:16" : "=v"(s23) : "v"(ws + WS_ST + tid * 32));
            SCHED();
        }
    }
    if (par == 0) {
#pragma unroll
        for (int n = 0; n < 8; ++n) mfma_acc(acc[n], af[0][0], bf[0][n]);
    }
    MFMA_DONE();
    STAMP(5);
    __syncthreads();
    STAMP(6);
#pragma unroll
    for (int n = 0; n < 8; ++n) *(LAS f32x4*)(smem + wid * 8192 + n * 1024 + row * 64 + (kg ^ ((row >> 1) & 3)) * 16) = acc[n];
    __syncthreads();
    STAMP(7);
    {
        const u32x2 dw = *(const u32x2*)(ssm_D + ch0);
        const float dv[4] = {bf_lo(dw.x), bf_hi(dw.x), bf_lo(dw.y), bf_hi(dw.y)};
        float y[4][4];
        const int ypos = (tid ^ ((tid >> 3) & 3)) * 16;
#pragma unroll
        for (int c = 0; c < 4; ++c) {
            const f32x4 a = *(const LAS f32x4*)(smem + c * 8192 + ypos) + *(const LAS f32x4*)(smem + (4 + c) * 8192 + ypos);
            const u32x2 kvp = *(const LAS u32x2*)(smem + C1_KVOFF + c * C1_KV + (2044 - 4 * tid) * 2);
            y[c][0] = rbf(__builtin_fmaf(bf_hi(kvp.y), dv[c], a[0])); y[c][1] = rbf(__builtin_fmaf(bf_lo(kvp.y), dv[c], a[1]));
            y[c][2] = rbf(__builtin_fmaf(bf_hi(kvp.x), dv[c], a[2])); y[c][3] = rbf(__builtin_fmaf(bf_lo(kvp.x), dv[c], a[3]));
        }
        asm volatile("s_waitcnt vmcnt(0)" : "+v"(xr[0]), "+v"(xr[1]), "+v"(xr[2]), "+v"(xr[3]), "+v"(s01), "+v"(s23) :: "memory");
        const float mm[4] = {s01[0], s01[2], s23[0], s23[2]}, rr[4] = {s01[1], s01[3], s23[1], s23[3]};
        unsigned hw[8];
#pragma unroll
        for (int j = 0; j < 4; ++j) {
#pragma unroll
            for (int e = 0; e < 2; ++e) {
                const unsigned xw = e ? xr[j].y : xr[j].x;
                const float x0 = bf_lo(xw), x1 = bf_hi(xw);
                hw[2 * j + e] = pk_bf16(__builtin_fmaf((x0 - mm[j]) * rr[j], y[2 * e][j], x0), __builtin_fmaf((x1 - mm[j]) * rr[j], y[2 * e + 1][j], x1));
            }
        }
        const rsrc_t rh = mk_rsrc(ws + WS_H, S_ * D_ * 2);
        const unsigned ho = (unsigned)(((tid >> 6) * 144 + cg) * 2048 + (tid & 63) * 32);
        st16_wt(rh, ho, (u32x4){hw[0], hw[1], hw[2], hw[3]});
        st16_wt(rh, ho + 16, (u32x4){hw[4], hw[5], hw[6], hw[7]});
    }
    STAMP(8);
    DIAG_END(ws, 1);
}

constexpr int ST_BYTES = 16384 + 2 * 9216;
constexpr int NBUF = 4;
constexpr int G_LDS = NBUF * ST_BYTES;

template <int OFF>
__device__ __forceinline__ void dma16(unsigned voff, const void* sbase, unsigned m0v) {
    asm volatile("s_mov_b32 m0, %2\n\ts_nop 0\n\tglobal_load_lds_dwordx4 %0, %1 offset:%3"
                 :: "v"(voff), "s"(sbase), "s"(m0v), "i"(OFF) : "memory");
}
template <int OFF>
__device__ __forceinline__ void dma16_q(unsigned voff, const void* sbase, unsigned m0v) {
    asm volatile("s_mov_b32 m0, %2\n\ts_mov_b64 exec, 0xffff\n\tglobal_load_lds_dwordx4 %0, %1 offset:%3\n\ts_mov_b64 exec, -1"
                 :: "v"(voff), "s"(sbase), "s"(m0v), "i"(OFF) : "memory");
}
__device__ __forceinline__ s16x8 tr_frag(const LAS char* p) {
    const s16x4 lo = __builtin_amdgcn_ds_read_tr16_b64_v4i16((LAS s16x4*)p);
    const s16x4 hi = __builtin_amdgcn_ds_read_tr16_b64_v4i16((LAS s16x4*)(p + 16 * 288));
    return __builtin_shufflevector(lo, hi, 0, 1, 2, 3, 4, 5, 6, 7);
}
struct TokC { float nm, c0, c1, q; };
__device__ __forceinline__ float geglu(float ag, float au, float csg, float csu, const TokC& t) {
    const float g = __builtin_fmaf(t.nm, csg, ag), u = __builtin_fmaf(t.nm, csu, au);
    const float e = __builtin_amdgcn_exp2f(g * __builtin_fmaf(g * g, t.c1, t.c0));
    return (g * u) * __builtin_amdgcn_rcpf(__builtin_fmaf(e, t.q, t.q));
}
__device__ __forceinline__ float dot2(unsigned a, unsigned b, float c) {
    return __builtin_amdgcn_fdot2_f32_bf16(__builtin_bit_cast(bf16x2_t, a), __builtin_bit_cast(bf16x2_t, b), c, false);
}
template <int OFF, int MASK>
__device__ __forceinline__ void dma_m(unsigned voff, const void* sbase, unsigned m0v) {
    asm volatile("s_mov_b32 m0, %2\n\ts_mov_b64 exec, %4\n\tglobal_load_lds_dwordx4 %0, %1 offset:%3\n\ts_mov_b64 exec, -1"
                 :: "v"(voff), "s"(sbase), "s"(m0v), "i"(OFF), "i"(MASK) : "memory");
}
__device__ __forceinline__ void dma4(unsigned voff, const void* sbase, unsigned m0v) {
    asm volatile("s_mov_b32 m0, %2\n\ts_nop 0\n\tglobal_load_lds_dword %0, %1" :: "v"(voff), "s"(sbase), "s"(m0v) : "memory");
}
template <int P>
__device__ __forceinline__ void stage_piece(const char* as, const char* ws, unsigned aoff, unsigned w0, unsigned w1, unsigned w2,
                                            unsigned ma, unsigned mw) {
    if constexpr (P == 0) dma16<0>(aoff, as, ma);
    if constexpr (P == 1) dma16<1024>(aoff, as, ma);
    if constexpr (P == 2) dma16<0>(w0, ws, mw);
    if constexpr (P == 3) dma16<1024>(w1, ws, mw);
    if constexpr (P == 4) dma16_q<2048>(w2, ws, mw);
}
template <int N> struct IC { static constexpr int v = N; };

constexpr int K3_CS = G_LDS;
constexpr int K3_SP = K3_CS + 1152;
constexpr int K3_LDS = K3_SP + 512;
__global__ __launch_bounds__(512, 2) void k3_gemm1(const u16* __restrict__ wg, const u16* __restrict__ wu, const u16* __restrict__ wd,
                                                   char* __restrict__ ws) {
    extern __shared__ __attribute__((aligned(16))) char smem[];
    LAS char* lds = (LAS char*)smem;
    const unsigned lds0 = (unsigned)(size_t)lds;
    const int tid = threadIdx.x, lane = tid & 63;
    const int wid = __builtin_amdgcn_readfirstlane(tid >> 6);
    const int wr = wid & 3, wc = wid >> 2;
    const int b = blockIdx.x, xcd = b & 7, idx = b >> 3;
    const int rt = (xcd >> 2) * 4 + (idx & 3), ct = (xcd & 3) * 8 + (idx >> 2);
    asm volatile("" :: "s"(wg), "s"(wu), "s"(wd), "s"(ws));
    DIAG_DECL;

    const char* asrc = ws + WS_H + (size_t)rt * 144 * 2048 + wid * 2048;
    const unsigned aoff = (unsigned)(lane ^ ((wid & 2) << 2)) * 16;
    const unsigned ma0 = lds0 + wid * 2048, mw0 = lds0 + 16384 + wc * 9216 + wr * 2304;
    u32x2 hs[4];
    {
        const char* hsrc = ws + WS_H + (size_t)rt * 144 * 2048 + ct * 64;
        const unsigned o = (unsigned)((wid * 18 + (lane >> 3)) * 2048 + (lane & 7) * 8);
        asm volatile("global_load_dwordx2 %0, %1, %2" : "=v"(hs[0]) : "v"(o), "s"(hsrc) : "memory");
        asm volatile("global_load_dwordx2 %0, %1, %2" : "=v"(hs[1]) : "v"(o + 16384), "s"(hsrc) : "memory");
        asm volatile("global_load_dwordx2 %0, %1, %2" : "=v"(hs[2]) : "v"((unsigned)((wid * 18 + 16 + ((lane >> 3) & 1)) * 2048 + (lane & 7) * 8)), "s"(hsrc) : "memory");
        asm volatile("global_load_dwordx2 %0, %1, %2" : "=v"(hs[3]) : "v"((unsigned)(lane & 7) * 8), "s"(hsrc) : "memory");
    }
    dma_m<0, 0x1ff>(lane * 16, ws + WS_CS + (size_t)(wc * F_ + ct * 144 + wr * 36) * 4, lds0 + K3_CS + wid * 144);
    dma16<0>(aoff, asrc, ma0); dma16<1024>(aoff, asrc, ma0);
    const char* wsrc = (const char*)(wc ? wu : wg) + ct * 288 - 2048;
    auto wofs = [&](int i) -> unsigned {
        const int q = lane + 64 * i, lrow = 8 * wr + q / 18, pos = q % 18;
        const int krow = ((lrow >> 2) & 3) * 8 + (lrow >> 4) * 4 + (lrow & 3);
        const int col = pos * 8 + (pos < 8 ? 0 : (pos < 16 ? 8 : (((pos & 1) ^ wc) ? 136 : 64) - pos * 8));
        return (unsigned)(krow * (F_ * 2) + col * 2 + 2048 - 1024 * i);
    };
    const unsigned w0 = wofs(0), w1 = wofs(1), w2 = wofs(2);
#define K3_PIECE(P, s) stage_piece<P>(asrc + (size_t)(s) * 16384, wsrc + (size_t)(s) * (32 * F_ * 2), aoff, w0, w1, w2, \
                                      ma0 + ((s) & 3) * ST_BYTES, mw0 + ((s) & 3) * ST_BYTES)
#define K3_STAGE(s) do { K3_PIECE(0, s); K3_PIECE(1, s); K3_PIECE(2, s); K3_PIECE(3, s); K3_PIECE(4, s); } while (0)
    K3_PIECE(2, 0); K3_PIECE(3, 0); K3_PIECE(4, 0);
    K3_STAGE(1);

    const int g4 = lane >> 4, q4 = (lane & 15) >> 2, p4 = lane & 3;
    const int rowoff = (4 * g4 + q4) * 288;
    const int tbase = g4 * 4096 + wr * 512 + (lane & 15) * 8;
    const int toff0 = tbase + ((2 * wc) ^ (g4 & 1)) * 128, toff1 = tbase + ((2 * wc + 1) ^ (g4 & 1)) * 128;
    const int tstep = wc ? -256 : 256;
    const int foff = 16384 + rowoff + wc * 128 + p4 * 8;
    const int moff = 16384 + rowoff + 256 + (p4 < 2 ? 16 * wc + 8 * p4 : 9216 + 16 * (1 - wc) + 8 * (p4 - 2));

    f32x4 acc[9][4];
#pragma unroll
    for (int j = 0; j < 9; ++j)
#pragma unroll
        for (int i = 0; i < 4; ++i) acc[j][i] = (f32x4){0.f, 0.f, 0.f, 0.f};
    s16x8 wf[9], tf[2][4];
    auto rd_w = [&](const LAS char* sb, int j) -> s16x8 {
        return j < 8 ? tr_frag(sb + foff + (j >> 2) * 9216 + (j & 3) * 32) : tr_frag(sb + moff);
    };
    auto rd_t = [&](const LAS char* sb, int i) -> s16x8 {
        const LAS char* p = sb + ((i & 1) ? toff1 : toff0) + (i >> 1) * tstep;
        const s16x4 lo = *(const volatile LAS s16x4*)p, hi = *(const volatile LAS s16x4*)(p + 2048);
        return __builtin_shufflevector(lo, hi, 0, 1, 2, 3, 4, 5, 6, 7);
    };

    STAMP(1);
    float hc;
    {
        asm volatile("s_waitcnt vmcnt(11)" : "+v"(hs[0]), "+v"(hs[1]), "+v"(hs[2]), "+v"(hs[3]) :: "memory");
        hc = bf_lo(hs[3].x);
        float s1 = 0.f, s2 = 0.f;
#pragma unroll
        for (int i = 0; i < 3; ++i) {
            const float d[4] = {bf_lo(hs[i].x) - hc, bf_hi(hs[i].x) - hc, bf_lo(hs[i].y) - hc, bf_hi(hs[i].y) - hc};
            float a = (d[0] + d[1]) + (d[2] + d[3]), q = __builtin_fmaf(d[0], d[0], d[1] * d[1]) + __builtin_fmaf(d[2], d[2], d[3] * d[3]);
            if (i == 2 && lane >= 16) { a = 0.f; q = 0.f; }
            s1 += a; s2 += q;
        }
        s1 = x32_add(x16_add(dpp_add<0x128>(s1))); s2 = x32_add(x16_add(dpp_add<0x128>(s2)));
        if (lane < 8) *(LAS f32x2*)(lds + K3_SP + (lane * 8 + wid) * 8) = (f32x2){s1, s2};
        asm volatile("s_waitcnt lgkmcnt(0)" ::: "memory");
    }
    WAIT_V(5); BAR();
    STAMP(2);
    wf[0] = rd_w(lds, 0);
#pragma unroll
    for (int i = 0; i < 4; ++i) tf[0][i] = rd_t(lds, i);
#pragma unroll
    for (int j = 1; j < 9; ++j) wf[j] = rd_w(lds, j);
    SCHED();
    {
        const f32x2 pp = *(const LAS f32x2*)(lds + K3_SP + (wid * 8 + (lane & 7)) * 8);
        const float s1 = dpp_add<0x141>(dpp_add<0x4E>(dpp_add<0xB1>(pp.x))) * (1.f / D_);
        const float s2 = dpp_add<0x141>(dpp_add<0x4E>(dpp_add<0xB1>(pp.y))) * (1.f / D_);
        const float c0 = __uint_as_float(__builtin_amdgcn_readlane(__float_as_uint(hc), wid));
        const float sd = ln_sd(__builtin_fmaxf(__builtin_fmaf(-s1, s1, s2), 0.f));
        if (lane == 0) st8_wt(mk_rsrc(ws + WS_G2, S_ * 8), (unsigned)((rt * 256 + ct * 8 + wid) * 8), (u32x2){pk_bf16(c0 + s1, sd), 1u});
    }
    SCHED();
    K3_STAGE(2);
    SCHED();

    auto iter = [&](auto mode, auto vm, int s, s16x8 (&tc)[4], s16x8 (&tn)[4]) {
        constexpr int MODE = decltype(mode)::v;
        if constexpr (MODE < 2) {
            if constexpr (decltype(vm)::v == 5) WAIT_V(5); else WAIT_V(0);
            BAR();
            SCHED();
        }
        const LAS char* sb = lds + ((s + 1) & 3) * ST_BYTES;
#pragma unroll
        for (int j = 0; j < 9; ++j) {
#pragma unroll
            for (int i = 0; i < 4; ++i) mfma_acc(acc[j][i], wf[j], tc[i]);
            SCHED();
            if constexpr (MODE < 2) {
                wf[j] = rd_w(sb, j);
                if (j >= 2 && j < 6) tn[j - 2] = rd_t(sb, j - 2);
            }
            if constexpr (MODE == 0) {
                if (j == 0) K3_PIECE(0, s + 3);
                if (j == 1) K3_PIECE(1, s + 3);
                if (j == 2) K3_PIECE(2, s + 3);
                if (j == 3) K3_PIECE(3, s + 3);
                if (j == 4) K3_PIECE(4, s + 3);
            }
            if constexpr (MODE == 1 && decltype(vm)::v == 0) {
                if (j == 0)
                    dma4((unsigned)min(tid, 323) * 128, (const char*)wd + (size_t)((xcd & 3) * 32 + idx) * 41472, lds0 + 2 * ST_BYTES + wid * 256);
            }
            SCHED();
        }
    };
#pragma unroll 1
    for (int s = 0; s < 14; s += 2) {
        iter(IC<0>{}, IC<5>{}, s, tf[0], tf[1]);
        iter(IC<0>{}, IC<5>{}, s + 1, tf[1], tf[0]);
    }
    STAMP(3);
    iter(IC<0>{}, IC<5>{}, 14, tf[0], tf[1]);
    iter(IC<1>{}, IC<5>{}, 15, tf[1], tf[0]);
    iter(IC<1>{}, IC<0>{}, 16, tf[0], tf[1]);
    const rsrc_t rg = mk_rsrc(ws + WS_G2 + (size_t)(rt * 256 + wr * 64) * 8, 512);
    u32x2 gr[4];
#pragma unroll
    for (int i = 0; i < 4; ++i) gr[i] = __builtin_amdgcn_raw_buffer_load_b64(rg, (unsigned)(((i ^ (2 * wc)) * 16 + (lane & 15)) * 8), 0, 16);
    SCHED();
    iter(IC<2>{}, IC<0>{}, 17, tf[1], tf[0]);
    MFMA_DONE();
    STAMP(4);
    for (int spin = 0; spin < (1 << 20); ++spin) {
        if (__all((gr[0].y & gr[1].y & gr[2].y & gr[3].y) == 1u)) break;
        __builtin_amdgcn_s_sleep(2);
#pragma unroll
        for (int i = 0; i < 4; ++i) gr[i] = __builtin_amdgcn_raw_buffer_load_b64(rg, (unsigned)(((i ^ (2 * wc)) * 16 + (lane & 15)) * 8), 0, 16);
    }
    const int mt = rt * 2 + (wr >> 1), ks = ct >> 3, lct = ct & 7;
    const rsrc_t rhh = mk_rsrc(ws + WS_HH, S_ * F_ * 2);
    const unsigned hb = (unsigned)(((mt * 4 + ks) * 36) * 8192 + ((wr & 1) * 4) * 1024);
    const LAS char* csb = lds + K3_CS + (wc * 72 + 4 * g4) * 4;
    TokC tcs[4];
#pragma unroll
    for (int i = 0; i < 4; ++i) {
        const float sd = bf_hi(gr[i].x), r = __builtin_amdgcn_rcpf(sd);
        tcs[i] = (TokC){-bf_lo(gr[i].x), -2.30220819f * r, -0.10294324f * (r * r * r), sd * sd};
    }
#pragma unroll
    for (int jp = 0; jp < 2; ++jp) {
        f32x4 cg[2], cu[2];
#pragma unroll
        for (int e = 0; e < 2; ++e) { cg[e] = *(const LAS f32x4*)(csb + (2 * jp + e) * 64); cu[e] = *(const LAS f32x4*)(csb + 576 + (2 * jp + e) * 64); }
#pragma unroll
        for (int i = 0; i < 4; ++i) {
            unsigned o[4];
#pragma unroll
            for (int e = 0; e < 2; ++e) {
                const f32x4 g = acc[2 * jp + e][i], u = acc[4 + 2 * jp + e][i];
                o[2 * e] = pk_bf16(geglu(g[0], u[0], cg[e][0], cu[e][0], tcs[i]), geglu(g[1], u[1], cg[e][1], cu[e][1], tcs[i]));
                o[2 * e + 1] = pk_bf16(geglu(g[2], u[2], cg[e][2], cu[e][2], tcs[i]), geglu(g[3], u[3], cg[e][3], cu[e][3], tcs[i]));
            }
            st16_wt(rhh, hb + (unsigned)((4 + lct * 4 + wc * 2 + jp) * 8192 + (i ^ (2 * wc)) * 1024 + lane * 16), (u32x4){o[0], o[1], o[2], o[3]});
        }
    }
    {
        const LAS char* cm = lds + K3_CS + (wc * 72 + 64 + 4 * (g4 & 1)) * 4;
        const f32x4 cgm = *(const LAS f32x4*)cm, cum = *(const LAS f32x4*)(cm + 576);
#pragma unroll
        for (int ip = 0; ip < 2; ++ip) {
            const int ia = ((2 * ip) ^ (2 * wc)) + (lane >> 5);
            const bool up = lane >= 32;
            const TokC tc = {up ? tcs[2 * ip + 1].nm : tcs[2 * ip].nm, up ? tcs[2 * ip + 1].c0 : tcs[2 * ip].c0,
                             up ? tcs[2 * ip + 1].c1 : tcs[2 * ip].c1, up ? tcs[2 * ip + 1].q : tcs[2 * ip].q};
            float hv[4];
#pragma unroll
            for (int r = 0; r < 4; ++r) {
                auto sw = __builtin_amdgcn_permlane32_swap(__float_as_uint(acc[8][2 * ip][r]), __float_as_uint(acc[8][2 * ip + 1][r]), false, false);
                hv[r] = geglu(__uint_as_float(sw[0]), __uint_as_float(sw[1]), cgm[r], cum[r], tc);
            }
            const int kgs = (lct & 1) * 2 + wc;
            st8_wt(rhh, hb + (unsigned)((lct >> 1) * 8192 + ia * 1024 + (kgs * 16 + (lane & 15)) * 16 + (g4 & 1) * 8),
                   (u32x2){pk_bf16(hv[0], hv[1]), pk_bf16(hv[2], hv[3])});
        }
    }
    STAMP(5);
    DIAG_END(ws, 2);
}

constexpr int K4_LDS = G_LDS + 4096;
__global__ __launch_bounds__(512, 2) void k4_gemm2(const u16* __restrict__ wd, char* __restrict__ ws, u16* __restrict__ out) {
    extern __shared__ __attribute__((aligned(16))) char smem[];
    LAS char* lds = (LAS char*)smem;
    const unsigned lds0 = (unsigned)(size_t)lds;
    const int tid = threadIdx.x, lane = tid & 63;
    const int wid = __builtin_amdgcn_readfirstlane(tid >> 6);
    const int wr = wid & 3, kh = wid >> 2;
    const int b = blockIdx.x, xcd = b & 7, idx = b >> 3;
    const int mt = (xcd >> 2) * 8 + (idx & 7), ks = xcd & 3, nt = idx >> 3;
    asm volatile("" :: "s"(out), "s"(ws), "s"(wd));
    DIAG_DECL;

    const char* asrc = ws + WS_HH + ((size_t)(mt * 4 + ks) * 36) * 8192 + wid * 2048;
    const char* wsrc = (const char*)wd + (size_t)ks * 1152 * (D_ * 2) + nt * 288 - 2048;
    auto wofs = [&](int i, bool full) -> unsigned {
        const int q = lane + 64 * i, r = 8 * wr + q / 18, pos = q % 18;
        const int kg = (r & 15) >> 2, j = (r & 3) + 4 * (r >> 4);
        const int f = full ? r : (kg >> 1) * 144 + (kg & 1) * 72 + 64 + j;
        return (unsigned)(f * (D_ * 2) + pos * 16 + 2048 - 1024 * i);
    };
    const unsigned aoff = lane * 16;
    const unsigned ma0 = lds0 + wid * 2048, mw0 = lds0 + 16384 + kh * 9216 + wr * 2304;
    {
        const unsigned l0 = wofs(0, false), l1 = wofs(1, false), l2 = wofs(2, false);
#define K4_LP(P, s) stage_piece<P>(asrc + (size_t)(s) * 16384, wsrc + (size_t)((2 * (s) + kh) * 288) * (D_ * 2), aoff, l0, l1, l2, \
                                   ma0 + (s) * ST_BYTES, mw0 + (s) * ST_BYTES)
        K4_LP(0, 0); K4_LP(1, 0); K4_LP(2, 0); K4_LP(3, 0); K4_LP(4, 0);
        K4_LP(0, 1); K4_LP(1, 1); K4_LP(2, 1); K4_LP(3, 1); K4_LP(4, 1);
    }
    const unsigned w0 = wofs(0, true), w1 = wofs(1, true), w2 = wofs(2, true);
    const char* wsf = wsrc + (size_t)(kh * 32) * (D_ * 2);
#define K4_PIECE(P, s) stage_piece<P>(asrc + (size_t)(s) * 16384, wsf + (size_t)((s) - 2) * (72 * D_ * 2), aoff, w0, w1, w2, \
                                      ma0 + ((s) & 3) * ST_BYTES, mw0 + ((s) & 3) * ST_BYTES)
    K4_PIECE(0, 2); K4_PIECE(1, 2); K4_PIECE(2, 2); K4_PIECE(3, 2); K4_PIECE(4, 2);

    const int g4 = lane >> 4, q4 = (lane & 15) >> 2, p4 = lane & 3;
    const int toff = kh * 8192 + wr * 2048 + lane * 16;
    const int foff = 16384 + kh * 9216 + (4 * g4 + q4) * 288 + p4 * 8;

    f32x4 acc[9][2];
#pragma unroll
    for (int j = 0; j < 9; ++j)
#pragma unroll
        for (int i = 0; i < 2; ++i) acc[j][i] = (f32x4){0.f, 0.f, 0.f, 0.f};
    s16x8 wf[9], tf[2][2];

    STAMP(1);
    WAIT_V(10); BAR();
    STAMP(2);
#pragma unroll
    for (int i = 0; i < 2; ++i) tf[0][i] = *(const LAS s16x8*)(lds + toff + i * 1024);
#pragma unroll
    for (int j = 0; j < 9; ++j) wf[j] = tr_frag(lds + foff + j * 32);
    SCHED();

    auto iter = [&](auto mode, auto vm, int s, s16x8 (&tc)[2], s16x8 (&tn)[2]) {
        constexpr int MODE = decltype(mode)::v;
        if constexpr (MODE < 2) {
            if constexpr (decltype(vm)::v == 5) WAIT_V(5); else WAIT_V(0);
            BAR();
            SCHED();
        }
        const LAS char* sb = lds + ((s + 1) & 3) * ST_BYTES;
#pragma unroll
        for (int j = 0; j < 9; ++j) {
#pragma unroll
            for (int i = 0; i < 2; ++i) mfma_acc(acc[j][i], wf[j], tc[i]);
            SCHED();
            if constexpr (MODE < 2) {
                wf[j] = tr_frag(sb + foff + j * 32);
                if (j >= 2 && j < 4) tn[j - 2] = *(const LAS s16x8*)(sb + toff + (j - 2) * 1024);
            }
            if constexpr (MODE == 0) {
                if (j == 0) K4_PIECE(0, s + 3);
                if (j == 2) K4_PIECE(1, s + 3);
                if (j == 4) K4_PIECE(2, s + 3);
                if (j == 6) K4_PIECE(3, s + 3);
                if (j == 7) K4_PIECE(4, s + 3);
            }
            SCHED();
        }
    };
#pragma unroll 1
    for (int s = 0; s < 14; s += 2) {
        iter(IC<0>{}, IC<5>{}, s, tf[0], tf[1]);
        iter(IC<0>{}, IC<5>{}, s + 1, tf[1], tf[0]);
    }
    STAMP(3);
    iter(IC<0>{}, IC<5>{}, 14, tf[0], tf[1]);
    iter(IC<1>{}, IC<5>{}, 15, tf[1], tf[0]);
    iter(IC<1>{}, IC<0>{}, 16, tf[0], tf[1]);
    const int tokn = mt * 128 + wr * 32 + kh * 16 + (lane & 15);
    u32x2 hv[9];
    if (wr == ks) {
        const char* hp = ws + WS_H + ((size_t)((tokn >> 8) * 144 + nt * 36 + g4) * 256 + (tokn & 255)) * 8;
#pragma unroll
        for (int j = 0; j < 9; ++j) hv[j] = *(const u32x2*)(hp + j * 8192);
    }
    SCHED();
    iter(IC<2>{}, IC<0>{}, 17, tf[1], tf[0]);
    MFMA_DONE();
    STAMP(4);

    LAS char* xb = lds + 2 * ST_BYTES + wr * (2 * 9216) + lane * 16;
#pragma unroll
    for (int j = 0; j < 9; ++j) *(LAS f32x4*)(xb + kh * 9216 + j * 1024) = kh ? acc[j][0] : acc[j][1];
    asm volatile("s_waitcnt lgkmcnt(0)" ::: "memory"); BAR();
    f32x4 v[9];
#pragma unroll
    for (int j = 0; j < 9; ++j) v[j] = (kh ? acc[j][1] : acc[j][0]) + *(const LAS f32x4*)(xb + (1 - kh) * 9216 + j * 1024);
    STAMP(5);
    const rsrc_t rx = mk_rsrc(ws + WS_X, X_BYTES);
    const unsigned xt = (unsigned)(((mt * 4 + nt) * 4 + wr) * 3 * 9216 + kh * 4608);
    if (wr != ks) {
        unsigned w[18];
#pragma unroll
        for (int j = 0; j < 9; ++j) { w[2 * j] = pk_bf16(v[j][0], v[j][1]); w[2 * j + 1] = pk_bf16(v[j][2], v[j][3]); }
#pragma unroll
        for (int e = 0; e < 18; ++e) w[e] = (w[e] == ~0u) ? 0x7fc07fc0u : w[e];
        const unsigned o = xt + (unsigned)(((ks - wr - 1) & 3) * 9216);
#pragma unroll
        for (int p = 0; p < 4; ++p) st16_wt(rx, o + p * 1024 + lane * 16, (u32x4){w[4 * p], w[4 * p + 1], w[4 * p + 2], w[4 * p + 3]});
        st8_wt(rx, o + 4096 + lane * 8, (u32x2){w[16], w[17]});
    } else {
        u32x4 pa[3][4]; u32x2 pb[3];
        for (int spin = 0; spin < (1 << 18); ++spin) {
#pragma unroll
            for (int s = 0; s < 3; ++s) pb[s] = __builtin_amdgcn_raw_buffer_load_b64(rx, xt + s * 9216 + 4096 + lane * 8, 0, 16);
            unsigned m = max(max(max(pb[0].x, pb[0].y), max(pb[1].x, pb[1].y)), max(pb[2].x, pb[2].y));
            if (__all(m != ~0u)) {
#pragma unroll
                for (int s = 0; s < 3; ++s)
#pragma unroll
                    for (int p = 0; p < 4; ++p) pa[s][p] = __builtin_amdgcn_raw_buffer_load_b128(rx, xt + s * 9216 + p * 1024 + lane * 16, 0, 16);
#pragma unroll
                for (int s = 0; s < 3; ++s)
#pragma unroll
                    for (int p = 0; p < 4; ++p) m = max(max(max(m, pa[s][p].x), max(pa[s][p].y, pa[s][p].z)), pa[s][p].w);
                if (__all(m != ~0u)) break;
            }
            __builtin_amdgcn_s_sleep(1);
        }
        STAMP(6);
        u16* op = out + (size_t)tokn * D_ + nt * 144 + 4 * g4;
#pragma unroll
        for (int j = 0; j < 9; ++j) {
            f32x4 t = v[j];
#pragma unroll
            for (int s = 0; s < 3; ++s) {
                const unsigned w0 = j < 8 ? pa[s][j >> 1][(j & 1) * 2] : pb[s].x, w1 = j < 8 ? pa[s][j >> 1][(j & 1) * 2 + 1] : pb[s].y;
                t += (f32x4){bf_lo(w0), bf_hi(w0), bf_lo(w1), bf_hi(w1)};
            }
            *(u32x2*)(op + j * 16) = (u32x2){pk_bf16(bf_lo(hv[j].x) + rbf(t[0]), bf_hi(hv[j].x) + rbf(t[1])),
                                             pk_bf16(bf_lo(hv[j].y) + rbf(t[2]), bf_hi(hv[j].y) + rbf(t[3]))};
        }
    }
    STAMP(7);
    DIAG_END(ws, 3);
}

extern "C" void kernel_launch(void* const* d_in, const int* in_sizes, int n_in, void* d_out, int out_size, void* d_ws,
                              size_t ws_size, hipStream_t stream) {
    const u16* q = (const u16*)d_in[0];
    const u16* k = (const u16*)d_in[1];
    const u16* v = (const u16*)d_in[2];
    const u16* shift_k = (const u16*)d_in[3];
    const u16* ssm_k = (const u16*)d_in[4];
    const u16* ssm_D = (const u16*)d_in[5];
    const u16* wg = (const u16*)d_in[7];
    const u16* wu = (const u16*)d_in[8];
    const u16* wd = (const u16*)d_in[9];
    char* ws = (char*)d_ws;
    (void)hipFuncSetAttribute((const void*)k1_conv, hipFuncAttributeMaxDynamicSharedMemorySize, C1_LDS);
    (void)hipFuncSetAttribute((const void*)k3_gemm1, hipFuncAttributeMaxDynamicSharedMemorySize, K3_LDS);
    (void)hipFuncSetAttribute((const void*)k4_gemm2, hipFuncAttributeMaxDynamicSharedMemorySize, K4_LDS);
    k0_pre<<<288, 512, 0, stream>>>(q, k, v, shift_k, ssm_k, ws);
    k1_conv<<<256, 512, C1_LDS, stream>>>(q, ssm_k, ssm_D, wg, wu, wd, ws);
    k3_gemm1<<<256, 512, K3_LDS, stream>>>(wg, wu, wd, ws);
    k4_gemm2<<<256, 512, K4_LDS, stream>>>(wd, ws, (u16*)d_out);
}
```

```cpp
#include <hip/hip_runtime.h>
#include <hip/hip_bf16.h>

#define LAS __attribute__((address_space(3)))
typedef unsigned short u16;
typedef short s16x4 __attribute__((ext_vector_type(4)));
typedef short s16x8 __attribute__((ext_vector_type(8)));
typedef float f32x2 __attribute__((ext_vector_type(2)));
typedef float f32x4 __attribute__((ext_vector_type(4)));
typedef unsigned u32x2 __attribute__((ext_vector_type(2)));
typedef unsigned u32x4 __attribute__((ext_vector_type(4)));
typedef __bf16 bf16x2_t __attribute__((ext_vector_type(2)));

constexpr int S_ = 2048, D_ = 576, F_ = 4608;

__device__ __forceinline__ unsigned pk_bf16(float lo, float hi) {
    f32x2 v = {lo, hi};
    bf16x2_t b = __builtin_convertvector(v, bf16x2_t);
    return __builtin_bit_cast(unsigned, b);
}
__device__ __forceinline__ float bf_lo(unsigned w) { return __uint_as_float(w << 16); }
__device__ __forceinline__ float bf_hi(unsigned w) { return __uint_as_float(w & 0xffff0000u); }
__device__ __forceinline__ float rbf(float x) { return bf_lo(pk_bf16(x, 0.f)); }
__device__ __forceinline__ f32x4 mfma16(s16x8 a, s16x8 b, f32x4 c) {
    return __builtin_amdgcn_mfma_f32_16x16x32_bf16(a, b, c, 0, 0, 0);
}
#define WAIT_V(n) asm volatile("s_waitcnt vmcnt(" #n ")" ::: "memory")
#define BAR() do { __builtin_amdgcn_s_barrier(); asm volatile("" ::: "memory"); } while (0)
#define SCHED() __builtin_amdgcn_sched_barrier(0)
#define DIAG_DECL
#define MARK()
#define STAMP(i)
#define DIAG_END(wsbase, kid)
__device__ __forceinline__ void mfma_acc(f32x4& c, s16x8 a, s16x8 b) {
    asm volatile("v_mfma_f32_16x16x32_bf16 %0, %1, %2, %0" : "+v"(c) : "v"(a), "v"(b));
}
__device__ __forceinline__ void mfma_first(f32x4& c, s16x8 a, s16x8 b) {
    asm volatile("v_mfma_f32_16x16x32_bf16 %0, %1, %2, 0" : "=&v"(c) : "v"(a), "v"(b));
}
#define MFMA_DONE() do { asm volatile("s_nop 7\n\ts_nop 7" ::: "memory"); SCHED(); } while (0)
constexpr size_t WS_KV = 0;
constexpr size_t WS_H = WS_KV + (size_t)S_ * D_ * 2;
constexpr size_t WS_ST = WS_H + (size_t)S_ * D_ * 2;
constexpr size_t WS_CS = WS_ST + (size_t)S_ * 8;
constexpr size_t WS_G2 = WS_CS + (size_t)4 * F_ * 4;
constexpr size_t WS_F0 = WS_G2 + (size_t)S_ * 8;
#define FSP 128
constexpr size_t WS_EP = WS_F0 + 1280 * FSP;
constexpr size_t WS_HH = WS_EP + 1024;
constexpr size_t WS_X = WS_HH + (size_t)S_ * F_ * 2;
constexpr unsigned X_BYTES = 64 * 4 * 3 * 9216;
constexpr size_t WS_WT = WS_X + X_BYTES;

template <int N> struct IC { static constexpr int v = N; };
typedef __amdgpu_buffer_rsrc_t rsrc_t;
__device__ __forceinline__ rsrc_t mk_rsrc(const void* p, unsigned bytes) { return __builtin_amdgcn_make_buffer_rsrc((void*)p, 0, bytes, 0x00020000); }
__device__ __forceinline__ void st16_wt(rsrc_t r, unsigned off, u32x4 v) { __builtin_amdgcn_raw_buffer_store_b128(v, r, off, 0, 16); }
__device__ __forceinline__ void st8_wt(rsrc_t r, unsigned off, u32x2 v) { __builtin_amdgcn_raw_buffer_store_b64(v, r, off, 0, 16); }
template <int CTRL> __device__ __forceinline__ float dpp_add(float x) {
    return x + __uint_as_float(__builtin_amdgcn_update_dpp(0, (int)__float_as_uint(x), CTRL, 0xf, 0xf, true));
}
__device__ __forceinline__ float x16_add(float x) {
    auto r = __builtin_amdgcn_permlane16_swap(__float_as_uint(x), __float_as_uint(x), false, false);
    return __uint_as_float(r[0]) + __uint_as_float(r[1]);
}
__device__ __forceinline__ float x32_add(float x) {
    auto r = __builtin_amdgcn_permlane32_swap(__float_as_uint(x), __float_as_uint(x), false, false);
    return __uint_as_float(r[0]) + __uint_as_float(r[1]);
}
__device__ __forceinline__ float sum16(float x) {
    x = dpp_add<0xB1>(x); x = dpp_add<0x4E>(x); x = dpp_add<0x141>(x);
    return dpp_add<0x140>(x);
}
__device__ __forceinline__ float ln_sd(float var) { return rbf(__builtin_amdgcn_sqrtf(rbf(rbf(var) + 1e-5f))); }

template <int OFF>
__device__ __forceinline__ void dma16(unsigned voff, const void* sbase, unsigned m0v) {
    asm volatile("s_mov_b32 m0, %2\n\ts_nop 0\n\tglobal_load_lds_dwordx4 %0, %1 offset:%3"
                 :: "v"(voff), "s"(sbase), "s"(m0v), "i"(OFF) : "memory");
}
template <int OFF>
__device__ __forceinline__ void dma16_sc1(unsigned voff, const void* sbase, unsigned m0v) {
    asm volatile("s_mov_b32 m0, %2\n\ts_nop 0\n\tglobal_load_lds_dwordx4 %0, %1 offset:%3 sc1"
                 :: "v"(voff), "s"(sbase), "s"(m0v), "i"(OFF) : "memory");
}
template <int OFF>
__device__ __forceinline__ void dma16_q(unsigned voff, const void* sbase, unsigned m0v) {
    asm volatile("s_mov_b32 m0, %2\n\ts_mov_b64 exec, 0xffff\n\tglobal_load_lds_dwordx4 %0, %1 offset:%3\n\ts_mov_b64 exec, -1"
                 :: "v"(voff), "s"(sbase), "s"(m0v), "i"(OFF) : "memory");
}
template <int OFF, int MASK>
__device__ __forceinline__ void dma_m(unsigned voff, const void* sbase, unsigned m0v) {
    asm volatile("s_mov_b32 m0, %2\n\ts_mov_b64 exec, %4\n\tglobal_load_lds_dwordx4 %0, %1 offset:%3\n\ts_mov_b64 exec, -1"
                 :: "v"(voff), "s"(sbase), "s"(m0v), "i"(OFF), "i"(MASK) : "memory");
}
__device__ __forceinline__ void dma4(unsigned voff, const void* sbase, unsigned m0v) {
    asm volatile("s_mov_b32 m0, %2\n\ts_nop 0\n\tglobal_load_lds_dword %0, %1" :: "v"(voff), "s"(sbase), "s"(m0v) : "memory");
}
__device__ __forceinline__ s16x8 tr_frag(const LAS char* p) {
    const s16x4 lo = __builtin_amdgcn_ds_read_tr16_b64_v4i16((LAS s16x4*)p);
    const s16x4 hi = __builtin_amdgcn_ds_read_tr16_b64_v4i16((LAS s16x4*)(p + 16 * 288));
    return __builtin_shufflevector(lo, hi, 0, 1, 2, 3, 4, 5, 6, 7);
}
#define HELPER_DELAY 24
#define POLL0 24
#define POLL1 2
constexpr int C1_COPY = 4288;
constexpr int C1_FILT = 4 * C1_COPY;
constexpr int C1_KV = 2304 * 2;
constexpr int C1_KVOFF = 4 * C1_FILT + 64;
constexpr int C1_YOFF = C1_KVOFF + 4 * C1_KV;
constexpr int C1_XOFF = C1_YOFF + 16384;
constexpr int C1_SOFF = C1_XOFF + 16384;
constexpr int C1_TOFF = C1_SOFF + 16384;
constexpr int C1_LDS = C1_TOFF + 2048;

__global__ __launch_bounds__(512, 2) void k1_conv(const u16* __restrict__ x, const u16* __restrict__ kin, const u16* __restrict__ vin,
                                                  const u16* __restrict__ shift_k, const u16* __restrict__ ssm_k, const u16* __restrict__ ssm_D,
                                                  const u16* __restrict__ wg, const u16* __restrict__ wu, const u16* __restrict__ wd,
                                                  char* __restrict__ ws) {
    extern __shared__ __attribute__((aligned(16))) char smem[];
    const int tid = threadIdx.x, lane = tid & 63;
    const int wid = __builtin_amdgcn_readfirstlane(tid >> 6);
    const int b = blockIdx.x, xcd = b & 7, idx = b >> 3;
    DIAG_DECL;
    asm volatile("" :: "s"(x), "s"(kin), "s"(vin), "s"(shift_k), "s"(ssm_k), "s"(ssm_D), "s"(wg), "s"(wu), "s"(wd), "s"(ws));
    const bool conv = idx < 18;
    const int cg = xcd * 18 + idx, ch0 = cg * 4;
    const unsigned lds0 = (unsigned)(size_t)(LAS char*)smem;
    LAS unsigned* tagp = (LAS unsigned*)(smem + C1_TOFF);
    auto get_tag = [&]() -> unsigned { BAR(); return __builtin_amdgcn_readfirstlane(*(volatile LAS unsigned*)tagp); };

    const int h = (idx - 18) * 8 + xcd, hc_ = h < 108 ? h : 0;
    const int m3 = hc_ % 3, lcA = (hc_ / 3) * 4 + m3, rb0 = m3 * 3;
    auto cs_load = [&](u32x4 (&v)[12]) {
        const int rs = tid >> 3, c8 = tid & 7;
#pragma unroll
        for (int p = 0; p < 12; ++p) {
            const int rb = rb0 + p, lc = lcA + (rb >= 9), r = (rb >= 9 ? rb - 9 : rb) * 64 + rs;
            v[p] = *(const u32x4*)((lc >= 72 ? wu : wg) + (size_t)r * F_ + (lc % 72) * 64 + c8 * 8);
        }
        SCHED();
        const rsrc_t rx = mk_rsrc(ws + WS_X, X_BYTES);
#pragma unroll
        for (int i = 0; i < 8; ++i) st16_wt(rx, (unsigned)((i * 112 + h) * 8192 + tid * 16), (u32x4){~0u, ~0u, ~0u, ~0u});
        SCHED();
    };
    auto cs_finish = [&](u32x4 (&v)[12]) {
        float g[4][8];
#pragma unroll
        for (int q = 0; q < 4; ++q) {
#pragma unroll
            for (int e = 0; e < 4; ++e) {
                g[q][2 * e] = (bf_lo(v[3 * q][e]) + bf_lo(v[3 * q + 1][e])) + bf_lo(v[3 * q + 2][e]);
                g[q][2 * e + 1] = (bf_hi(v[3 * q][e]) + bf_hi(v[3 * q + 1][e])) + bf_hi(v[3 * q + 2][e]);
            }
        }
        LAS float* red = (LAS float*)smem;
#pragma unroll
        for (int l = 0; l < 2; ++l) {
            float a[8];
#pragma unroll
            for (int e = 0; e < 8; ++e) {
                a[e] = l == 0 ? g[0][e] + (m3 <= 1 ? g[1][e] : 0.f) + (m3 == 0 ? g[2][e] : 0.f) : g[3][e] + (m3 >= 1 ? g[2][e] : 0.f) + (m3 == 2 ? g[1][e] : 0.f);
                a[e] = x32_add(x16_add(dpp_add<0x128>(a[e])));
            }
            if (lane < 8) {
                *(LAS f32x4*)(red + (wid * 2 + l) * 64 + lane * 8) = (f32x4){a[0], a[1], a[2], a[3]};
                *(LAS f32x4*)(red + (wid * 2 + l) * 64 + lane * 8 + 4) = (f32x4){a[4], a[5], a[6], a[7]};
            }
        }
        asm volatile("s_waitcnt lgkmcnt(0)" ::: "memory"); BAR();
        if (tid < 192 && h < 108) {
            float s = 0.f;
            if (tid < 128) {
#pragma unroll
                for (int w = 0; w < 8; ++w) s += red[w * 128 + tid];
            }
            const int part = tid >> 6;
            const int lc = part == 0 ? lcA : (part == 1 ? lcA + 1 : (m3 == 0 ? lcA : lcA + 1));
            const int slot = part == 0 ? (m3 != 0) : (part == 1 ? 0 : 1);
            if (part < 2 || m3 != 1)
                __builtin_amdgcn_raw_buffer_store_b32(__float_as_uint(s), mk_rsrc(ws + WS_CS, 4 * F_ * 4), (unsigned)((slot * 2 * F_ + lc * 64 + (tid & 63)) * 4), 0, 16);
        }
    };
    auto wait_kv = [&](unsigned tag) {
        const rsrc_t rf = mk_rsrc(ws + WS_F0 + ((idx >> 2) * 8 + xcd) * 32 * FSP, 32 * FSP);
        __builtin_amdgcn_s_sleep(POLL0);
        for (int spin = 0; spin < (1 << 20); ++spin) {
            const unsigned f = __builtin_amdgcn_raw_buffer_load_b32(rf, (lane & 31) * FSP, 0, 16);
            if (__all(f == tag)) break;
            __builtin_amdgcn_s_sleep(POLL1);
        }
    };

    auto x_rows = [&]() {
        u32x2 xr[8];
#pragma unroll
        for (int i = 0; i < 8; ++i) xr[i] = *(const u32x2*)(x + (size_t)((tid - 256) + (wid - 4) * 448 + i * 64) * D_ + ch0);
#pragma unroll
        for (int i = 0; i < 8; ++i) *(LAS u32x2*)(smem + C1_XOFF + ((tid - 256) + (wid - 4) * 448 + i * 64) * 8) = xr[i];
        SCHED();
    };
    unsigned tag;
    unsigned dvv = 0;
    auto upper = [&](auto is_stat, auto is_conv) {
        constexpr bool STAT = decltype(is_stat)::v, CONV = decltype(is_conv)::v;
        const int srow = b * 8 + (wid - 5) * 4 + (lane >> 4), t3 = tid - 320;
        unsigned xw[18], epv = 0;
        if constexpr (STAT) {
            const unsigned* xr = (const unsigned*)(x + (size_t)srow * D_) + (lane & 15);
#pragma unroll
            for (int j = 0; j < 18; ++j) xw[j] = xr[16 * j];
        } else {
            epv = __hip_atomic_load((const unsigned*)(ws + WS_EP), __ATOMIC_RELAXED, __HIP_MEMORY_SCOPE_AGENT);
        }
        SCHED();
        u32x4 v[12], fa[6]; u32x2 fb[6];
        if constexpr (CONV) {
#pragma unroll
            for (int i = 0; i < 6; ++i) {
                const int task = min(t3 + 192 * i, 1023), u = task & 255;
                const u16* src = ssm_k + (size_t)(ch0 + (task >> 8)) * S_ + u * 8;
                fa[i] = *(const u32x4*)src;
                fb[i] = *(const u32x2*)(src + (u < 255 ? 8 : 0));
            }
            SCHED();
        }
        if constexpr (STAT) {
            f32x2 xf[18], s = {0.f, 0.f};
#pragma unroll
            for (int j = 0; j < 18; ++j) { xf[j] = (f32x2){bf_lo(xw[j]), bf_hi(xw[j])}; s += xf[j]; }
            const float mean = sum16(s.x + s.y) * (1.f / D_);
            f32x2 q = {0.f, 0.f};
#pragma unroll
            for (int j = 0; j < 18; ++j) { const f32x2 d = xf[j] - mean; q = __builtin_elementwise_fma(d, d, q); }
            const unsigned sgr = pk_bf16(mean, ln_sd(sum16(q.x + q.y) * (1.f / D_)));
            tag = get_tag();
            const unsigned so = (lane & 15) == 0 ? (unsigned)srow * 8 : 0x80000000u;
            st8_wt(mk_rsrc(ws + WS_ST, S_ * 8), so, (u32x2){sgr, tag});
            st8_wt(mk_rsrc(ws + WS_G2, S_ * 8), so, (u32x2){0u, 0u});
        } else {
            *tagp = epv + 1u;
            asm volatile("s_waitcnt lgkmcnt(0)" ::: "memory");
            tag = get_tag();
        }
        SCHED();
        if constexpr (!CONV) {
            __builtin_amdgcn_s_sleep(HELPER_DELAY);
            cs_load(v);
            if (!STAT && h == 111) {
                const rsrc_t rf = mk_rsrc(ws + WS_F0, 256 * FSP);
                for (int spin = 0; spin < (1 << 20); ++spin) {
                    bool ok = true;
#pragma unroll
                    for (int i = 0; i < 4; ++i) ok &= __builtin_amdgcn_raw_buffer_load_b32(rf, (i * 64 + lane) * FSP, 0, 16) == tag;
                    if (__all(ok)) break;
                    __builtin_amdgcn_s_sleep(8);
                }
                if (lane == 0) __hip_atomic_store((unsigned*)(ws + WS_EP), tag, __ATOMIC_RELAXED, __HIP_MEMORY_SCOPE_AGENT);
            }
            cs_finish(v);
            return;
        }
        if (t3 < 80) {
            const int cpy = t3 / 5, e = t3 % 5, q = e < 3 ? e : 257 + e;
            *(LAS u32x4*)(smem + cpy * C1_COPY + q * 16) = (u32x4){0u, 0u, 0u, 0u};
        } else if (t3 < 84) {
            *(LAS u32x4*)(smem + C1_KVOFF - 64 + (t3 - 80) * 16) = (u32x4){0u, 0u, 0u, 0u};
        }
        if (t3 < 128) *(LAS u32x4*)(smem + C1_KVOFF + (t3 >> 5) * C1_KV + 4096 + (t3 & 31) * 16) = (u32x4){0u, 0u, 0u, 0u};
#pragma unroll
        for (int i = 0; i < 6; ++i) {
            const int task = min(t3 + 192 * i, 1023), u = task & 255;
            const unsigned d[6] = {fa[i].x, fa[i].y, fa[i].z, fa[i].w, u < 255 ? fb[i].x : 0u, u < 255 ? fb[i].y : 0u};
            unsigned o[5];
#pragma unroll
            for (int e = 0; e < 5; ++e) o[e] = __builtin_amdgcn_alignbit(d[e + 1], d[e], 16);
            LAS char* base = (LAS char*)smem + (task >> 8) * C1_FILT + (u + 4) * 16;
            *(LAS u32x4*)(base) = (u32x4){o[0], o[1], o[2], o[3]};
            *(LAS u32x4*)(base + C1_COPY) = (u32x4){d[1], d[2], d[3], d[4]};
            *(LAS u32x4*)(base + 2 * C1_COPY) = (u32x4){o[1], o[2], o[3], o[4]};
            *(LAS u32x4*)(base + 3 * C1_COPY) = (u32x4){d[2], d[3], d[4], d[5]};
            if (u == 0) {
                *(LAS u32x4*)(base - 16) = (u32x4){0u, 0u, 0u, d[0] << 16};
                *(LAS u32x4*)(base - 16 + C1_COPY) = (u32x4){0u, 0u, 0u, d[0]};
                *(LAS u32x4*)(base - 16 + 2 * C1_COPY) = (u32x4){0u, 0u, d[0] << 16, o[0]};
                *(LAS u32x4*)(base - 16 + 3 * C1_COPY) = (u32x4){0u, 0u, d[0], d[1]};
            }
        }
        asm volatile("s_waitcnt lgkmcnt(0)" ::: "memory"); BAR();
        x_rows();
        dma4((unsigned)t3 * 128, (const char*)wd + (size_t)cg * 288 * 128, lds0 + C1_TOFF + 256 + (wid - 5) * 256);
        dma4((unsigned)(192 + (t3 < 96 ? t3 : 0)) * 128, (const char*)wd + (size_t)cg * 288 * 128, lds0 + C1_TOFF + 1024 + (wid - 5) * 256);
    };
    if (wid >= 5) {
        if (!conv) {
            if (wid < 7) upper(IC<1>{}, IC<0>{}); else upper(IC<0>{}, IC<0>{});
            DIAG_END(ws, 0);
            return;
        }
        if (wid < 7) upper(IC<1>{}, IC<1>{}); else upper(IC<0>{}, IC<1>{});
    } else {
        {
            unsigned kw[11], vw[8];
            const int t0 = idx * 64 + (lane >> 3) * 8, dc = wid * 8 + (lane & 7), kch = xcd * 72 + (dc < 36 ? dc : 35) * 2;
            const rsrc_t rk = mk_rsrc(kin, S_ * D_ * 2), rv = mk_rsrc(vin, S_ * D_ * 2);
            const int off = t0 * (D_ * 2) + kch * 2;
#pragma unroll
            for (int j = 0; j < 11; ++j) kw[j] = __builtin_amdgcn_raw_buffer_load_b32(rk, (unsigned)(off + (j - 3) * (D_ * 2)), 0, 0);
#pragma unroll
            for (int j = 0; j < 8; ++j) vw[j] = __builtin_amdgcn_raw_buffer_load_b32(rv, (unsigned)(off + j * (D_ * 2)), 0, 0);
            const u32x4 sw = *(const u32x4*)(shift_k + kch * 4);
            dvv = ssm_D[(conv ? ch0 : 0) + (wid & 3)];
            SCHED();
            STAMP(1);
            const f32x2 w0 = {bf_lo(sw.x), bf_lo(sw.z)}, w1 = {bf_hi(sw.x), bf_hi(sw.z)}, w2 = {bf_lo(sw.y), bf_lo(sw.w)}, w3 = {bf_hi(sw.y), bf_hi(sw.w)};
            f32x2 kf[11], o[8];
#pragma unroll
            for (int j = 0; j < 11; ++j) kf[j] = (f32x2){bf_lo(kw[j]), bf_hi(kw[j])};
#pragma unroll
            for (int j = 0; j < 8; ++j) {
                f32x2 s = kf[j] * w3;
                s = __builtin_elementwise_fma(kf[j + 1], w2, s);
                s = __builtin_elementwise_fma(kf[j + 2], w1, s);
                s = __builtin_elementwise_fma(kf[j + 3], w0, s);
                o[j] = s * (f32x2){bf_lo(vw[j]), bf_hi(vw[j])};
            }
            const rsrc_t ro = mk_rsrc(ws + WS_KV, S_ * D_ * 2);
            const unsigned oo = dc < 36 ? (unsigned)(kch * (S_ * 2) + (2040 - t0) * 2) : 0x80000000u;
            st16_wt(ro, oo, (u32x4){pk_bf16(o[7].x, o[6].x), pk_bf16(o[5].x, o[4].x), pk_bf16(o[3].x, o[2].x), pk_bf16(o[1].x, o[0].x)});
            st16_wt(ro, oo + S_ * 2, (u32x4){pk_bf16(o[7].y, o[6].y), pk_bf16(o[5].y, o[4].y), pk_bf16(o[3].y, o[2].y), pk_bf16(o[1].y, o[0].y)});
        }
        tag = get_tag();
        WAIT_V(0);
        if (lane == 0) __hip_atomic_store((unsigned*)(ws + WS_F0 + ((wid * 8 + xcd) * 32 + idx) * FSP), tag, __ATOMIC_RELAXED, __HIP_MEMORY_SCOPE_AGENT);
        MARK();
        STAMP(2);
        if (!conv) {
            u32x4 v[12];
            cs_load(v);
            cs_finish(v);
            DIAG_END(ws, 0);
            return;
        }
        if (wid == 4) {
            x_rows();
            BAR();
            __builtin_amdgcn_s_sleep(40);
            const char* gs = ws + WS_ST;
#pragma unroll
            for (int i = 0; i < 4; ++i) {
                dma16_sc1<0>(lane * 16, gs + i * 4096, lds0 + C1_SOFF + i * 4096); dma16_sc1<1024>(lane * 16, gs + i * 4096, lds0 + C1_SOFF + i * 4096);
                dma16_sc1<2048>(lane * 16, gs + i * 4096, lds0 + C1_SOFF + i * 4096); dma16_sc1<3072>(lane * 16, gs + i * 4096, lds0 + C1_SOFF + i * 4096);
            }
            WAIT_V(0);
        } else {
        {
            wait_kv(tag);
            STAMP(7);
            const char* kvs = ws + WS_KV + (size_t)(ch0 + wid) * (S_ * 2);
            const unsigned m = lds0 + C1_KVOFF + wid * C1_KV;
            dma16_sc1<0>(lane * 16, kvs, m); dma16_sc1<1024>(lane * 16, kvs, m); dma16_sc1<2048>(lane * 16, kvs, m); dma16_sc1<3072>(lane * 16, kvs, m);
            WAIT_V(0);
            STAMP(9);
            BAR();
            STAMP(4);
        }
        const int row = lane & 15, kg = lane >> 4;
        const LAS char* abase = (const LAS char*)smem + wid * C1_FILT + (row & 3) * C1_COPY + (24 + 4 * (row >> 2) - 8 * kg) * 2;
        const LAS char* bbase = (const LAS char*)smem + C1_KVOFF + wid * C1_KV + (2040 - 16 * row - 8 * kg) * 2 - 512 * 7 - 448;
        auto rd_ah = [&](int r, int L, int hf) -> s16x4 { return *(const volatile LAS s16x4*)(abase + 512 * L - 64 * r + 8 * hf); };
        auto rd_b = [&](int r, int m) -> s16x8 { return *(const LAS s16x8*)(bbase + 448 - 64 * r + 512 * (7 - m)); };
        auto cat = [&](s16x4 lo, s16x4 hi) -> s16x8 { return __builtin_shufflevector(lo, hi, 0, 1, 2, 3, 4, 5, 6, 7); };
        f32x4 acc[8];
        s16x4 al[2][8], ah[2][8];
        s16x8 bf[2][8], bd[8];
        auto rd_k = [&](int r, int k) {
            if (k < 2) { if (k == 0) al[r & 1][0] = rd_ah(r, 1, 0); else ah[r & 1][0] = rd_ah(r, 1, 1); }
            else if (k < 10) bf[r & 1][9 - k] = rd_b(r, 8 - k);
            else { const int L = (k - 10) / 2 + 2; if (k & 1) ah[r & 1][L - 1] = rd_ah(r, L, 1); else al[r & 1][L - 1] = rd_ah(r, L, 0); }
        };
        const s16x8 a0 = cat(rd_ah(0, 0, 0), rd_ah(0, 0, 1));
#pragma unroll
        for (int e = 0; e < 8; ++e) bd[e] = rd_b(0, e);
        s16x8 dfr;
        {
            const int j = 8 * kg + 7 - row;
            u32x4 w;
#pragma unroll
            for (int e = 0; e < 4; ++e) w[e] = j == 2 * e ? dvv : (j == 2 * e + 1 ? dvv << 16 : 0u);
            dfr = __builtin_bit_cast(s16x8, w);
            asm volatile("s_nop 1" : "+v"(dfr));
        }
        SCHED();
#pragma unroll
        for (int n = 0; n < 16; ++n) {
            if (n < 8) mfma_first(acc[n], a0, bd[n]); else mfma_acc(acc[n - 8], dfr, bd[n - 8]);
            SCHED();
            if (n == 0) { rd_k(0, 0); rd_k(0, 1); }
            else if (n == 1) rd_k(0, 9);
            else rd_k(0, n + 8);
            SCHED();
        }
#pragma unroll
        for (int r = 0; r < 8; ++r) {
            int k = 0;
#pragma unroll
            for (int L = 1; L <= 8; ++L) {
#pragma unroll
                for (int n = 7; n >= L - 1; --n) {
                    const int e = n - L + 1;
                    mfma_acc(acc[n], cat(al[r & 1][L - 1], ah[r & 1][L - 1]), (r == 0 && e > 0) ? bd[e - 1] : bf[r & 1][e]);
                    SCHED();
                    if (r < 7 && k < 24) rd_k(r + 1, k);
                    ++k;
                    SCHED();
                }
            }
        }
        MFMA_DONE();
#pragma unroll
        for (int n = 0; n < 8; ++n)
            *(LAS u32x2*)(smem + C1_YOFF + wid * 4096 + (256 * n + 16 * row + 4 * kg) * 2) = (u32x2){pk_bf16(acc[n][0], acc[n][1]), pk_bf16(acc[n][2], acc[n][3])};
        }
    }
    STAMP(5);
    asm volatile("s_waitcnt lgkmcnt(0)" ::: "memory"); BAR();
    STAMP(6);
    {
        u32x2 yc[4];
#pragma unroll
        for (int c = 0; c < 4; ++c) yc[c] = *(const LAS u32x2*)(smem + C1_YOFF + c * 4096 + tid * 8);
        const u32x4 xa = *(const LAS u32x4*)(smem + C1_XOFF + tid * 32), xb = *(const LAS u32x4*)(smem + C1_XOFF + tid * 32 + 16);
        const u32x2 xr[4] = {{xa.x, xa.y}, {xa.z, xa.w}, {xb.x, xb.y}, {xb.z, xb.w}};
        u32x4 s01 = *(const LAS u32x4*)(smem + C1_SOFF + tid * 32), s23 = *(const LAS u32x4*)(smem + C1_SOFF + tid * 32 + 16);
        if (__builtin_expect(!__all(s01.y == tag && s01.w == tag && s23.y == tag && s23.w == tag), 0)) {
            const rsrc_t rs = mk_rsrc(ws + WS_ST, S_ * 8);
            for (int spin = 0; spin < (1 << 20); ++spin) {
                s01 = __builtin_amdgcn_raw_buffer_load_b128(rs, tid * 32, 0, 16);
                s23 = __builtin_amdgcn_raw_buffer_load_b128(rs, tid * 32 + 16, 0, 16);
                if (__all(s01.y == tag && s01.w == tag && s23.y == tag && s23.w == tag)) break;
                __builtin_amdgcn_s_sleep(2);
            }
        }
        const unsigned sv[4] = {s01.x, s01.z, s23.x, s23.z};
        float mm[4], rr[4];
#pragma unroll
        for (int j = 0; j < 4; ++j) { mm[j] = bf_lo(sv[j]); rr[j] = __builtin_amdgcn_rcpf(bf_hi(sv[j])); }
        unsigned hw[8];
#pragma unroll
        for (int j = 0; j < 4; ++j) {
#pragma unroll
            for (int e = 0; e < 2; ++e) {
                const unsigned xw = e ? xr[j].y : xr[j].x;
                const unsigned ya = (j >> 1) ? yc[2 * e].y : yc[2 * e].x, yb = (j >> 1) ? yc[2 * e + 1].y : yc[2 * e + 1].x;
                const float y0 = (j & 1) ? bf_hi(ya) : bf_lo(ya), y1 = (j & 1) ? bf_hi(yb) : bf_lo(yb);
                const float x0 = bf_lo(xw), x1 = bf_hi(xw);
                hw[2 * j + e] = pk_bf16(__builtin_fmaf((x0 - mm[j]) * rr[j], y0, x0), __builtin_fmaf((x1 - mm[j]) * rr[j], y1, x1));
            }
        }
        const rsrc_t rh = mk_rsrc(ws + WS_H, S_ * D_ * 2);
        const unsigned ho = (unsigned)(((tid >> 6) * 144 + cg) * 2048 + (tid & 63) * 32);
        st16_wt(rh, ho, (u32x4){hw[0], hw[1], hw[2], hw[3]});
        st16_wt(rh, ho + 16, (u32x4){hw[4], hw[5], hw[6], hw[7]});
    }
    STAMP(8);
    DIAG_END(ws, 0);
}

constexpr int ST_BYTES = 16384 + 2 * 9216;
constexpr int NBUF = 4;
constexpr int G_LDS = NBUF * ST_BYTES;

struct TokC { float nm, c0, c1, q; };
__device__ __forceinline__ unsigned geglu2(f32x2 ag, f32x2 au, f32x2 csg, f32x2 csu, const TokC& t) {
    const f32x2 nm = {t.nm, t.nm}, c0 = {t.c0, t.c0}, c1 = {t.c1, t.c1}, q = {t.q, t.q};
    const f32x2 g = __builtin_elementwise_fma(nm, csg, ag), u = __builtin_elementwise_fma(nm, csu, au);
    const f32x2 z = g * __builtin_elementwise_fma(g * g, c1, c0);
    const f32x2 e = {__builtin_amdgcn_exp2f(z.x), __builtin_amdgcn_exp2f(z.y)};
    const f32x2 d = __builtin_elementwise_fma(e, q, q);
    const f32x2 r = (g * u) * (f32x2){__builtin_amdgcn_rcpf(d.x), __builtin_amdgcn_rcpf(d.y)};
    return pk_bf16(r.x, r.y);
}
#define LO2(v) __builtin_shufflevector(v, v, 0, 1)
#define HI2(v) __builtin_shufflevector(v, v, 2, 3)
__device__ __forceinline__ float dot2(unsigned a, unsigned b, float c) {
    return __builtin_amdgcn_fdot2_f32_bf16(__builtin_bit_cast(bf16x2_t, a), __builtin_bit_cast(bf16x2_t, b), c, false);
}
#define WAIT_VI(N) asm volatile("s_waitcnt vmcnt(%0)" :: "i"(N) : "memory")

constexpr int K3_WS = G_LDS;
constexpr int K3_LDS = K3_WS + 8 * 3072;
constexpr int K3_SP = K3_WS;
constexpr int K3_CS = 2 * ST_BYTES;
__global__ __launch_bounds__(512, 2) void k3_gemm1(const u16* __restrict__ wg, const u16* __restrict__ wu, const u16* __restrict__ wd,
                                                   char* __restrict__ ws) {
    extern __shared__ __attribute__((aligned(16))) char smem[];
    LAS char* lds = (LAS char*)smem;
    const unsigned lds0 = (unsigned)(size_t)lds;
    const int tid = threadIdx.x, lane = tid & 63;
    const int wid = __builtin_amdgcn_readfirstlane(tid >> 6);
    const int wr = wid & 3, wc = wid >> 2;
    const int b = blockIdx.x, xcd = b & 7, idx = b >> 3;
    const int rt = (xcd >> 2) * 4 + (idx & 3), ct = (xcd & 3) * 8 + (idx >> 2);
    asm volatile("" :: "s"(wg), "s"(wu), "s"(wd), "s"(ws));
    DIAG_DECL;

    const char* asrc = ws + WS_H + (size_t)rt * 144 * 2048 + wid * 2048;
    const unsigned aoff = (unsigned)(lane ^ ((wid & 2) << 2)) * 16;
    const unsigned ma0 = lds0 + wid * 2048, mw0 = lds0 + 16384 + wc * 9216 + wr * 2304;
    dma16<0>(aoff, asrc, ma0); dma16<1024>(aoff, asrc, ma0);
    const char* wsrc = (const char*)(wc ? wu : wg) + ct * 288 - 2048;
    auto wofs = [&](int i) -> unsigned {
        const int q = lane + 64 * i, lrow = 8 * wr + q / 18, pos = q % 18;
        const int krow = ((lrow >> 2) & 3) * 8 + (lrow >> 4) * 4 + (lrow & 3);
        const int col = pos * 8 + (pos < 8 ? 0 : (pos < 16 ? 8 : (((pos & 1) ^ wc) ? 136 : 64) - pos * 8));
        return (unsigned)(krow * (F_ * 2) + col * 2 + 2048 - 1024 * i);
    };
    const unsigned w0 = wofs(0), w1 = wofs(1), w2 = wofs(2);
    auto piece = [&](int P, int s) {
        const unsigned bo = (s & 3) * ST_BYTES;
        const char* wp = wsrc + (size_t)s * (32 * F_ * 2);
        if (P == 0) dma16<0>(aoff, asrc + (size_t)s * 16384, ma0 + bo);
        if (P == 1) dma16<1024>(aoff, asrc + (size_t)s * 16384, ma0 + bo);
        if (P == 2) dma16<0>(w0, wp, mw0 + bo);
        if (P == 3) dma16<1024>(w1, wp, mw0 + bo);
        if (P == 4) dma16_q<2048>(w2, wp, mw0 + bo);
    };
#define K3_PIECE(P, s) piece(P, s)
#define K3_STAGE(s) do { K3_PIECE(0, s); K3_PIECE(1, s); K3_PIECE(2, s); K3_PIECE(3, s); K3_PIECE(4, s); } while (0)
    K3_PIECE(2, 0); K3_PIECE(3, 0); K3_PIECE(4, 0);
    BAR();
    K3_STAGE(1);
    u32x2 hs[4];
    {
        const char* hsrc = ws + WS_H + (size_t)rt * 144 * 2048 + ct * 64;
        const unsigned o = (unsigned)((wid * 18 + (lane >> 3)) * 2048 + (lane & 7) * 8);
        asm volatile("global_load_dwordx2 %0, %1, %2" : "=v"(hs[0]) : "v"(o), "s"(hsrc) : "memory");
        asm volatile("global_load_dwordx2 %0, %1, %2" : "=v"(hs[1]) : "v"(o + 16384), "s"(hsrc) : "memory");
        asm volatile("global_load_dwordx2 %0, %1, %2" : "=v"(hs[2]) : "v"((unsigned)((wid * 18 + 16 + ((lane >> 3) & 1)) * 2048 + (lane & 7) * 8)), "s"(hsrc) : "memory");
        asm volatile("global_load_dwordx2 %0, %1, %2" : "=v"(hs[3]) : "v"((unsigned)(lane & 7) * 8), "s"(hsrc) : "memory");
    }

    const int g4 = lane >> 4, q4 = (lane & 15) >> 2, p4 = lane & 3;
    const int rowoff = (4 * g4 + q4) * 288;
    const int tbase = g4 * 4096 + wr * 512 + (lane & 15) * 8;
    const int toff0 = tbase + ((2 * wc) ^ (g4 & 1)) * 128, toff1 = tbase + ((2 * wc + 1) ^ (g4 & 1)) * 128;
    const int tstep = wc ? -256 : 256;
    const int foff = 16384 + rowoff + wc * 128 + p4 * 8;
    const int moff = 16384 + rowoff + 256 + (p4 < 2 ? 16 * wc + 8 * p4 : 9216 + 16 * (1 - wc) + 8 * (p4 - 2));

    f32x4 acc[9][4];
    s16x8 wf[9], tf[2][4];
    auto rd_w = [&](const LAS char* sb, int j) -> s16x8 {
        return j < 8 ? tr_frag(sb + foff + (j >> 2) * 9216 + (j & 3) * 32) : tr_frag(sb + moff);
    };
    auto rd_t = [&](const LAS char* sb, int i) -> s16x8 {
        const LAS char* p = sb + ((i & 1) ? toff1 : toff0) + (i >> 1) * tstep;
        const s16x4 lo = *(const volatile LAS s16x4*)p, hi = *(const volatile LAS s16x4*)(p + 2048);
        return __builtin_shufflevector(lo, hi, 0, 1, 2, 3, 4, 5, 6, 7);
    };

    const int un = (b < 216 ? b : b - 216) * 8 + wid, uB = un / 3, ut = un % 3;
    auto wd_dma = [&](int i) {
        const int kh = uB & 1, st = (uB >> 1) % 18, kn = uB / 36;
        const bool full = st < 16;
        const int f0 = (kn >> 2) * 1152 + (full ? (st & 7) * 144 + kh * 72 + (st >> 3) * 32 : ((st - 16) * 2 + kh) * 288);
        const char* src = (const char*)wd + (size_t)f0 * (D_ * 2) + (kn & 3) * 288 + ut * 96;
        const int q = lane + 64 * i, r = q / 6, pos = q % 6, kg = (r & 15) >> 2;
        const int f = full ? r : (kg >> 1) * 144 + (kg & 1) * 72 + 64 + (r & 3) + 4 * (r >> 4);
        dma16<0>((unsigned)(f * (D_ * 2) + pos * 16), src, lds0 + K3_WS + wid * 3072 + i * 1024);
    };

    STAMP(1);
    WAIT_V(9); BAR();
    STAMP(2);
    wf[0] = rd_w(lds, 0);
#pragma unroll
    for (int i = 0; i < 4; ++i) tf[0][i] = rd_t(lds, i);
#pragma unroll
    for (int j = 1; j < 9; ++j) wf[j] = rd_w(lds, j);
    SCHED();
    STAMP(6);
    {
        const LAS char* sb = lds + ST_BYTES;
        float hc = 0.f;
#pragma unroll
        for (int j = 0; j < 9; ++j) {
#pragma unroll
            for (int i = 0; i < 4; ++i) mfma_first(acc[j][i], wf[j], tf[0][i]);
            SCHED();
            if (j == 0) { K3_PIECE(0, 2); K3_PIECE(1, 2); }
            if (j == 1) { K3_PIECE(2, 2); K3_PIECE(3, 2); }
            if (j == 2) K3_PIECE(4, 2);
            if (j == 3) {
                asm volatile("s_waitcnt vmcnt(5)" : "+v"(hs[0]), "+v"(hs[1]), "+v"(hs[2]), "+v"(hs[3]) :: "memory");
                hc = bf_lo(hs[3].x);
                float s1 = 0.f, s2 = 0.f;
#pragma unroll
                for (int i = 0; i < 3; ++i) {
                    const float d[4] = {bf_lo(hs[i].x) - hc, bf_hi(hs[i].x) - hc, bf_lo(hs[i].y) - hc, bf_hi(hs[i].y) - hc};
                    float a = (d[0] + d[1]) + (d[2] + d[3]), q = __builtin_fmaf(d[0], d[0], d[1] * d[1]) + __builtin_fmaf(d[2], d[2], d[3] * d[3]);
                    if (i == 2 && lane >= 16) { a = 0.f; q = 0.f; }
                    s1 += a; s2 += q;
                }
                s1 = x32_add(x16_add(dpp_add<0x128>(s1))); s2 = x32_add(x16_add(dpp_add<0x128>(s2)));
                if (lane < 8) *(LAS f32x2*)(lds + K3_SP + (lane * 8 + wid) * 8) = (f32x2){s1, s2};
                asm volatile("s_waitcnt lgkmcnt(0)" ::: "memory");
                BAR();
                wf[0] = rd_w(sb, 0); wf[1] = rd_w(sb, 1);
                tf[1][0] = rd_t(sb, 0); tf[1][1] = rd_t(sb, 1);
            }
            if (j == 4) { tf[1][2] = rd_t(sb, 2); tf[1][3] = rd_t(sb, 3); wf[2] = rd_w(sb, 2); wf[3] = rd_w(sb, 3); }
            if (j >= 4) { wf[j] = rd_w(sb, j); K3_PIECE(j - 4, 3); }
            if (j == 8) {
                const f32x2 pp = *(const LAS f32x2*)(lds + K3_SP + (wid * 8 + (lane & 7)) * 8);
                const float s1 = dpp_add<0x141>(dpp_add<0x4E>(dpp_add<0xB1>(pp.x))) * (1.f / D_);
                const float s2 = dpp_add<0x141>(dpp_add<0x4E>(dpp_add<0xB1>(pp.y))) * (1.f / D_);
                const float c0 = __uint_as_float(__builtin_amdgcn_readlane(__float_as_uint(hc), wid));
                const float sd = ln_sd(__builtin_fmaxf(__builtin_fmaf(-s1, s1, s2), 0.f));
                if (lane == 0) st8_wt(mk_rsrc(ws + WS_G2, S_ * 8), (unsigned)((rt * 256 + ct * 8 + wid) * 8), (u32x2){pk_bf16(c0 + s1, sd), 1u});
            }
            SCHED();
        }
    }

    const rsrc_t rg = mk_rsrc(ws + WS_G2 + (size_t)(rt * 256 + wr * 64) * 8, 512);
    u32x2 gr[4];
    u32x4 wtv;
    auto iter = [&](auto mode, auto vm, int s, s16x8 (&tc)[4], s16x8 (&tn)[4]) {
        constexpr int MODE = decltype(mode)::v;
        if constexpr (MODE < 4) {
            if constexpr (MODE == 3) WAIT_V(0); else WAIT_VI(5 + decltype(vm)::v);
            BAR();
            SCHED();
        }
        const LAS char* sb = lds + ((s + 1) & 3) * ST_BYTES;
#pragma unroll
        for (int j = 0; j < 9; ++j) {
#pragma unroll
            for (int i = 0; i < 4; ++i) mfma_acc(acc[j][i], wf[j], tc[i]);
            SCHED();
            if constexpr (MODE < 4) {
                wf[j] = rd_w(sb, j);
                if (j >= 2 && j < 6) tn[j - 2] = rd_t(sb, j - 2);
            }
            if constexpr (MODE < 2) {
                if (j == 0) K3_PIECE(0, s + 3);
                if (j == 1) K3_PIECE(1, s + 3);
                if (j == 2) K3_PIECE(2, s + 3);
                if (j == 3) K3_PIECE(3, s + 3);
                if (j == 4) K3_PIECE(4, s + 3);
            }
            if constexpr (MODE == 0) { if (j >= 6) wd_dma(j - 6); }
            if constexpr (MODE == 3) {
                if (j == 0) {
#pragma unroll
                    for (int i = 0; i < 4; ++i) gr[i] = __builtin_amdgcn_raw_buffer_load_b64(rg, (unsigned)(((i ^ (2 * wc)) * 16 + (lane & 15)) * 8), 0, 16);
                    dma_m<0, 0x1ff>(lane * 16, ws + WS_CS + (size_t)(wc * F_ + ct * 144 + wr * 36) * 4, lds0 + K3_CS + wid * 144);
                    dma_m<0, 0x1ff>(lane * 16, ws + WS_CS + (size_t)(2 * F_ + wc * F_ + ct * 144 + wr * 36) * 4, lds0 + K3_CS + 1152 + wid * 144);
                }
            }
            if constexpr (MODE >= 3) {
                constexpr int T0 = MODE == 3 ? 0 : 2;
                if (j == 1 || (MODE == 3 && j == 6)) {
                    const LAS char* tp = lds + K3_WS + wid * 3072 + (lane >> 2) * 96 + (lane & 3) * 8 + (T0 + (j == 6)) * 32;
                    const s16x4 lo = __builtin_amdgcn_ds_read_tr16_b64_v4i16((LAS s16x4*)tp), hi = __builtin_amdgcn_ds_read_tr16_b64_v4i16((LAS s16x4*)(tp + 16 * 96));
                    wtv = __builtin_bit_cast(u32x4, __builtin_shufflevector(lo, hi, 0, 1, 2, 3, 4, 5, 6, 7));
                }
                if (j == 3 || (MODE == 3 && j == 8))
                    st16_wt(mk_rsrc(ws + WS_WT, b < 216 ? F_ * D_ * 2 : 0), (unsigned)(uB * 9216 + (ut * 3 + T0 + (j == 8)) * 1024 + lane * 16), wtv);
            }
            SCHED();
        }
    };
    iter(IC<0>{}, IC<1>{}, 1, tf[1], tf[0]);
    iter(IC<1>{}, IC<4>{}, 2, tf[0], tf[1]);
    iter(IC<1>{}, IC<3>{}, 3, tf[1], tf[0]);
#pragma unroll 1
    for (int s = 4; s < 14; s += 2) {
        iter(IC<1>{}, IC<0>{}, s, tf[0], tf[1]);
        iter(IC<1>{}, IC<0>{}, s + 1, tf[1], tf[0]);
    }
    STAMP(3);
    iter(IC<1>{}, IC<0>{}, 14, tf[0], tf[1]);
    iter(IC<2>{}, IC<0>{}, 15, tf[1], tf[0]);
    iter(IC<3>{}, IC<0>{}, 16, tf[0], tf[1]);
    iter(IC<4>{}, IC<0>{}, 17, tf[1], tf[0]);
    MFMA_DONE();
    STAMP(4);
    WAIT_V(3); BAR();
    if (__builtin_expect(!__all((gr[0].y & gr[1].y & gr[2].y & gr[3].y) == 1u), 0)) {
        for (int spin = 0; spin < (1 << 20); ++spin) {
            __builtin_amdgcn_s_sleep(2);
#pragma unroll
            for (int i = 0; i < 4; ++i) gr[i] = __builtin_amdgcn_raw_buffer_load_b64(rg, (unsigned)(((i ^ (2 * wc)) * 16 + (lane & 15)) * 8), 0, 16);
            if (__all((gr[0].y & gr[1].y & gr[2].y & gr[3].y) == 1u)) break;
        }
    }
    const int mt = rt * 2 + (wr >> 1), ks = ct >> 3, lct = ct & 7;
    const rsrc_t rhh = mk_rsrc(ws + WS_HH, S_ * F_ * 2);
    const unsigned hb = (unsigned)(((mt * 4 + ks) * 36) * 8192 + ((wr & 1) * 4) * 1024);
    const LAS char* csb = lds + K3_CS + (wc * 72 + 4 * g4) * 4;
    TokC tcs[4];
#pragma unroll
    for (int i = 0; i < 4; ++i) {
        const float sd = bf_hi(gr[i].x), r = __builtin_amdgcn_rcpf(sd);
        tcs[i] = (TokC){-bf_lo(gr[i].x), -2.30220819f * r, -0.10294324f * (r * r * r), sd * sd};
    }
#pragma unroll
    for (int jp = 0; jp < 2; ++jp) {
        f32x4 cg[2], cu[2];
#pragma unroll
        for (int e = 0; e < 2; ++e) {
            cg[e] = *(const LAS f32x4*)(csb + (2 * jp + e) * 64) + *(const LAS f32x4*)(csb + 1152 + (2 * jp + e) * 64);
            cu[e] = *(const LAS f32x4*)(csb + 576 + (2 * jp + e) * 64) + *(const LAS f32x4*)(csb + 1728 + (2 * jp + e) * 64);
        }
#pragma unroll
        for (int i = 0; i < 4; ++i) {
            unsigned o[4];
#pragma unroll
            for (int e = 0; e < 2; ++e) {
                const f32x4 g = acc[2 * jp + e][i], u = acc[4 + 2 * jp + e][i];
                o[2 * e] = geglu2(LO2(g), LO2(u), LO2(cg[e]), LO2(cu[e]), tcs[i]);
                o[2 * e + 1] = geglu2(HI2(g), HI2(u), HI2(cg[e]), HI2(cu[e]), tcs[i]);
            }
            st16_wt(rhh, hb + (unsigned)(((jp * 8 + lct) * 2 + wc) * 8192 + (i ^ (2 * wc)) * 1024 + lane * 16), (u32x4){o[0], o[1], o[2], o[3]});
        }
    }
    {
        const LAS char* cm = lds + K3_CS + (wc * 72 + 64 + 4 * (g4 & 1)) * 4;
        const f32x4 cgm = *(const LAS f32x4*)cm + *(const LAS f32x4*)(cm + 1152), cum = *(const LAS f32x4*)(cm + 576) + *(const LAS f32x4*)(cm + 1728);
#pragma unroll
        for (int ip = 0; ip < 2; ++ip) {
            const int ia = ((2 * ip) ^ (2 * wc)) + (lane >> 5);
            const bool up = lane >= 32;
            const TokC tc = {up ? tcs[2 * ip + 1].nm : tcs[2 * ip].nm, up ? tcs[2 * ip + 1].c0 : tcs[2 * ip].c0,
                             up ? tcs[2 * ip + 1].c1 : tcs[2 * ip].c1, up ? tcs[2 * ip + 1].q : tcs[2 * ip].q};
            f32x4 gm, um;
#pragma unroll
            for (int r = 0; r < 4; ++r) {
                auto sw = __builtin_amdgcn_permlane32_swap(__float_as_uint(acc[8][2 * ip][r]), __float_as_uint(acc[8][2 * ip + 1][r]), false, false);
                gm[r] = __uint_as_float(sw[0]); um[r] = __uint_as_float(sw[1]);
            }
            const int kgs = (lct & 1) * 2 + wc;
            st8_wt(rhh, hb + (unsigned)((32 + (lct >> 1)) * 8192 + ia * 1024 + (kgs * 16 + (lane & 15)) * 16 + (g4 & 1) * 8),
                   (u32x2){geglu2(LO2(gm), LO2(um), LO2(cgm), LO2(cum), tc), geglu2(HI2(gm), HI2(um), HI2(cgm), HI2(cum), tc)});
        }
    }
    STAMP(5);
    DIAG_END(ws, 1);
}

constexpr int K4_LDS = G_LDS;
template <int NT>
__device__ __forceinline__ void k4_body(char* __restrict__ ws, u16* __restrict__ out, LAS char* lds, int lane, int wid, int mt, int ks, int nt) {
    constexpr int dh = NT == 4;
    const unsigned lds0 = (unsigned)(size_t)lds;
    const int wr = wid & 3;
    DIAG_DECL;
    const char* asrc = ws + WS_HH + ((size_t)(mt * 4 + ks) * 36) * 8192 + wid * 2048;
    const char* wsrc = ws + WS_WT + (size_t)(ks * 4 + nt) * 18 * 18432 + wid * 2304;
    const unsigned aoff = lane * 16;
    const unsigned ma0 = lds0 + wid * 2048, mw0 = lds0 + 16384 + wid * 2304;
    auto piece = [&](int P, int s) {
        const unsigned bo = (s & 3) * ST_BYTES;
        if (P == 0) dma16<0>(aoff, asrc + (size_t)s * 16384, ma0 + bo);
        if (P == 1) dma16<1024>(aoff, asrc + (size_t)s * 16384, ma0 + bo);
        if (P == 2) dma16<0>(aoff, wsrc + (size_t)s * 18432, mw0 + bo);
        if (P == 3) dma16<1024>(aoff, wsrc + (size_t)s * 18432, mw0 + bo);
        if (P == 4) dma16_q<2048>(aoff, wsrc + (size_t)s * 18432, mw0 + bo);
    };
#define K4_STAGE(s) do { piece(0, s); piece(1, s); piece(2, s); piece(3, s); piece(4, s); } while (0)
    K4_STAGE(0);
    BAR();
    K4_STAGE(1);

    const int g4 = lane >> 4;
    const int toff = wr * 2048 + lane * 16;
    const int foff = 16384 + dh * 5120 + lane * 16;
    auto rdf = [&](const LAS char* p) -> s16x8 { return *(const LAS s16x8*)p; };

    f32x4 acc[NT][2];
    s16x8 wf[2 * NT], tf[2][4];
    STAMP(1);
    WAIT_V(5); BAR();
    STAMP(2);
#pragma unroll
    for (int e = 0; e < 4; ++e) tf[0][e] = rdf(lds + toff + (e >> 1) * 8192 + (e & 1) * 1024);
#pragma unroll
    for (int e = 0; e < 2 * NT; ++e) wf[e] = rdf(lds + foff + (e / NT) * 9216 + (e % NT) * 1024);
    SCHED();
    K4_STAGE(2);
    SCHED();

    auto iter = [&](auto mode, int s, s16x8 (&tc)[4], s16x8 (&tn)[4]) {
        constexpr int MODE = decltype(mode)::v;
        if constexpr (MODE < 4) {
            if constexpr (MODE == 3) WAIT_V(0); else WAIT_V(5);
            BAR();
            SCHED();
        }
        const LAS char* sb = lds + ((s + 1) & 3) * ST_BYTES;
#pragma unroll
        for (int e = 0; e < 2 * NT; ++e) {
#pragma unroll
            for (int i = 0; i < 2; ++i) {
                if (MODE == 0 && e < NT) mfma_first(acc[e % NT][i], wf[e], tc[(e / NT) * 2 + i]);
                else mfma_acc(acc[e % NT][i], wf[e], tc[(e / NT) * 2 + i]);
            }
            SCHED();
            if constexpr (MODE < 4) {
                wf[e] = rdf(sb + foff + (e / NT) * 9216 + (e % NT) * 1024);
                if (e >= 2 && e < 6) tn[e - 2] = rdf(sb + toff + ((e - 2) >> 1) * 8192 + ((e - 2) & 1) * 1024);
            }
            if constexpr (MODE < 2) {
                if (e == 0) piece(0, s + 3);
                if (e == 1) piece(1, s + 3);
                if (e == 3) piece(2, s + 3);
                if (e == 5) piece(3, s + 3);
                if (e == 6) piece(4, s + 3);
            }
            SCHED();
        }
    };
    iter(IC<0>{}, 0, tf[0], tf[1]);
    iter(IC<1>{}, 1, tf[1], tf[0]);
#pragma unroll 1
    for (int s = 2; s < 14; s += 2) {
        iter(IC<1>{}, s, tf[0], tf[1]);
        iter(IC<1>{}, s + 1, tf[1], tf[0]);
    }
    STAMP(3);
    iter(IC<1>{}, 14, tf[0], tf[1]);
    iter(IC<2>{}, 15, tf[1], tf[0]);
    iter(IC<3>{}, 16, tf[0], tf[1]);
    const int tok0 = mt * 128 + wr * 32 + (lane & 15);
    u32x2 hv[2][5];
    if (wr == ks) {
#pragma unroll
        for (int i = 0; i < 2; ++i) {
            const int t = tok0 + i * 16;
            const char* hp = ws + WS_H + ((size_t)((t >> 8) * 144 + nt * 36 + dh * 20) * 256 + (t & 255)) * 8;
#pragma unroll
            for (int e = 0; e < 4; ++e) hv[i][e] = *(const u32x2*)(hp + ((e >> 1) * 8 + (g4 & 1) * 4 + (g4 >> 1) * 2 + (e & 1)) * 2048);
            if (NT == 5) hv[i][4] = *(const u32x2*)(hp + (16 + g4) * 2048);
        }
    }
    SCHED();
    iter(IC<4>{}, 17, tf[1], tf[0]);
    MFMA_DONE();
    STAMP(4);

    const rsrc_t rx = mk_rsrc(ws + WS_X, X_BYTES);
    const unsigned xt = (unsigned)(((mt * 4 + nt) * 4 + wr) * 3 * 9216 + dh * 4096);
    if (wr != ks) {
        const unsigned o = xt + (unsigned)(((ks - wr - 1) & 3) * 9216);
#pragma unroll
        for (int i = 0; i < 2; ++i) {
            unsigned w[2 * NT];
#pragma unroll
            for (int j = 0; j < NT; ++j) { w[2 * j] = pk_bf16(acc[j][i][0], acc[j][i][1]); w[2 * j + 1] = pk_bf16(acc[j][i][2], acc[j][i][3]); }
#pragma unroll
            for (int e = 0; e < 2 * NT; ++e) w[e] = (w[e] == ~0u) ? 0x7fc07fc0u : w[e];
            if constexpr (NT == 5) st8_wt(rx, o + 8192 + i * 512 + lane * 8, (u32x2){w[8], w[9]});
            st16_wt(rx, o + i * 2048 + lane * 16, (u32x4){w[0], w[1], w[2], w[3]});
            st16_wt(rx, o + i * 2048 + 1024 + lane * 16, (u32x4){w[4], w[5], w[6], w[7]});
        }
    } else {
        u32x4 pa[3][4]; u32x2 pb[3][2];
        for (int spin = 0; spin < (1 << 18); ++spin) {
#pragma unroll
            for (int s = 0; s < 3; ++s) pa[s][3] = __builtin_amdgcn_raw_buffer_load_b128(rx, xt + s * 9216 + 3072 + lane * 16, 0, 16);
            unsigned m = 0u;
#pragma unroll
            for (int s = 0; s < 3; ++s) m = max(max(max(m, pa[s][3].x), max(pa[s][3].y, pa[s][3].z)), pa[s][3].w);
            if (__all(m != ~0u)) {
                STAMP(5);
#pragma unroll
                for (int s = 0; s < 3; ++s) {
#pragma unroll
                    for (int p = 0; p < 3; ++p) pa[s][p] = __builtin_amdgcn_raw_buffer_load_b128(rx, xt + s * 9216 + p * 1024 + lane * 16, 0, 16);
                    if constexpr (NT == 5) {
#pragma unroll
                        for (int i = 0; i < 2; ++i) pb[s][i] = __builtin_amdgcn_raw_buffer_load_b64(rx, xt + s * 9216 + 8192 + i * 512 + lane * 8, 0, 16);
                    }
                }
#pragma unroll
                for (int s = 0; s < 3; ++s) {
#pragma unroll
                    for (int p = 0; p < 3; ++p) m = max(max(max(m, pa[s][p].x), max(pa[s][p].y, pa[s][p].z)), pa[s][p].w);
                    if constexpr (NT == 5) m = max(max(max(m, pb[s][0].x), max(pb[s][0].y, pb[s][1].x)), pb[s][1].y);
                }
                if (__all(m != ~0u)) break;
            }
            __builtin_amdgcn_s_sleep(1);
        }
        STAMP(6);
        const rsrc_t ro = mk_rsrc(out, S_ * D_ * 2);
#pragma unroll
        for (int i = 0; i < 2; ++i) {
            const unsigned ob = (unsigned)(((tok0 + i * 16) * D_ + nt * 144 + dh * 80) * 2);
            unsigned r[NT][2];
#pragma unroll
            for (int j = 0; j < NT; ++j) {
                f32x4 t = acc[j][i];
#pragma unroll
                for (int s = 0; s < 3; ++s) {
                    const unsigned w0 = j < 4 ? pa[s][i * 2 + (j >> 1)][(j & 1) * 2] : pb[s][i].x, w1 = j < 4 ? pa[s][i * 2 + (j >> 1)][(j & 1) * 2 + 1] : pb[s][i].y;
                    t += (f32x4){bf_lo(w0), bf_hi(w0), bf_lo(w1), bf_hi(w1)};
                }
                r[j][0] = pk_bf16(t[0], t[1]); r[j][1] = pk_bf16(t[2], t[3]);
            }
            auto addh = [&](unsigned a, unsigned h) -> unsigned { return pk_bf16(bf_lo(h) + bf_lo(a), bf_hi(h) + bf_hi(a)); };
#pragma unroll
            for (int p = 0; p < 2; ++p) {
                auto s0 = __builtin_amdgcn_permlane16_swap(r[2 * p][0], r[2 * p + 1][0], false, false);
                auto s1 = __builtin_amdgcn_permlane16_swap(r[2 * p][1], r[2 * p + 1][1], false, false);
                st16_wt(ro, ob + (unsigned)((p * 32 + (g4 & 1) * 16 + (g4 >> 1) * 8) * 2),
                        (u32x4){addh(s0[0], hv[i][2 * p].x), addh(s1[0], hv[i][2 * p].y), addh(s0[1], hv[i][2 * p + 1].x), addh(s1[1], hv[i][2 * p + 1].y)});
            }
            if constexpr (NT == 5) st8_wt(ro, ob + (unsigned)((64 + g4 * 4) * 2), (u32x2){addh(r[4][0], hv[i][4].x), addh(r[4][1], hv[i][4].y)});
        }
    }
    STAMP(7);
    DIAG_END(ws, 2);
}

__global__ __launch_bounds__(512, 2) void k4_gemm2(char* __restrict__ ws, u16* __restrict__ out) {
    extern __shared__ __attribute__((aligned(16))) char smem[];
    const int lane = threadIdx.x & 63;
    const int wid = __builtin_amdgcn_readfirstlane(threadIdx.x >> 6);
    const int b = blockIdx.x, xcd = b & 7, idx = b >> 3;
    const int mt = (xcd >> 2) * 8 + (idx & 7), ks = xcd & 3, nt = idx >> 3;
    asm volatile("" :: "s"(out), "s"(ws));
    if (wid < 4) k4_body<5>(ws, out, (LAS char*)smem, lane, wid, mt, ks, nt);
    else k4_body<4>(ws, out, (LAS char*)smem, lane, wid, mt, ks, nt);
}

extern "C" void kernel_launch(void* const* d_in, const int* in_sizes, int n_in, void* d_out, int out_size, void* d_ws,
                              size_t ws_size, hipStream_t stream) {
    const u16* q = (const u16*)d_in[0];
    const u16* k = (const u16*)d_in[1];
    const u16* v = (const u16*)d_in[2];
    const u16* shift_k = (const u16*)d_in[3];
    const u16* ssm_k = (const u16*)d_in[4];
    const u16* ssm_D = (const u16*)d_in[5];
    const u16* wg = (const u16*)d_in[7];
    const u16* wu = (const u16*)d_in[8];
    const u16* wd = (const u16*)d_in[9];
    char* ws = (char*)d_ws;
    (void)hipFuncSetAttribute((const void*)k1_conv, hipFuncAttributeMaxDynamicSharedMemorySize, C1_LDS);
    (void)hipFuncSetAttribute((const void*)k3_gemm1, hipFuncAttributeMaxDynamicSharedMemorySize, K3_LDS);
    (void)hipFuncSetAttribute((const void*)k4_gemm2, hipFuncAttributeMaxDynamicSharedMemorySize, K4_LDS);
    k1_conv<<<256, 512, C1_LDS, stream>>>(q, k, v, shift_k, ssm_k, ssm_D, wg, wu, wd, ws);
    k3_gemm1<<<256, 512, K3_LDS, stream>>>(wg, wu, wd, ws);
    k4_gemm2<<<256, 512, K4_LDS, stream>>>(ws, (u16*)d_out);
}
```

```cpp
#include <hip/hip_runtime.h>
#include <hip/hip_bf16.h>

#define LAS __attribute__((address_space(3)))
typedef unsigned short u16;
typedef short s16x4 __attribute__((ext_vector_type(4)));
typedef short s16x8 __attribute__((ext_vector_type(8)));
typedef float f32x2 __attribute__((ext_vector_type(2)));
typedef float f32x4 __attribute__((ext_vector_type(4)));
typedef unsigned u32x2 __attribute__((ext_vector_type(2)));
typedef unsigned u32x4 __attribute__((ext_vector_type(4)));
typedef __bf16 bf16x2_t __attribute__((ext_vector_type(2)));

constexpr int S_ = 2048, D_ = 576, F_ = 4608;

__device__ __forceinline__ unsigned pk_bf16(float lo, float hi) {
    f32x2 v = {lo, hi};
    bf16x2_t b = __builtin_convertvector(v, bf16x2_t);
    return __builtin_bit_cast(unsigned, b);
}
__device__ __forceinline__ float bf_lo(unsigned w) { return __uint_as_float(w << 16); }
__device__ __forceinline__ float bf_hi(unsigned w) { return __uint_as_float(w & 0xffff0000u); }
__device__ __forceinline__ float rbf(float x) { return bf_lo(pk_bf16(x, 0.f)); }
__device__ __forceinline__ f32x4 mfma16(s16x8 a, s16x8 b, f32x4 c) {
    return __builtin_amdgcn_mfma_f32_16x16x32_bf16(a, b, c, 0, 0, 0);
}
#define WAIT_V(n) asm volatile("s_waitcnt vmcnt(" #n ")" ::: "memory")
#define BAR() do { __builtin_amdgcn_s_barrier(); asm volatile("" ::: "memory"); } while (0)
#define SCHED() __builtin_amdgcn_sched_barrier(0)
#define DIAG_DECL
#define MARK()
#define STAMP(i)
#define DIAG_END(wsbase, kid)
__device__ __forceinline__ void mfma_acc(f32x4& c, s16x8 a, s16x8 b) {
    asm volatile("v_mfma_f32_16x16x32_bf16 %0, %1, %2, %0" : "+v"(c) : "v"(a), "v"(b));
}
__device__ __forceinline__ void mfma_first(f32x4& c, s16x8 a, s16x8 b) {
    asm volatile("v_mfma_f32_16x16x32_bf16 %0, %1, %2, 0" : "=&v"(c) : "v"(a), "v"(b));
}
#define MFMA_DONE() do { asm volatile("s_nop 7\n\ts_nop 7" ::: "memory"); SCHED(); } while (0)
constexpr size_t WS_KV = 0;
constexpr size_t WS_H = WS_KV + (size_t)S_ * D_ * 2;
constexpr size_t WS_ST = WS_H + (size_t)S_ * D_ * 2;
constexpr size_t WS_CS = WS_ST + (size_t)S_ * 8;
constexpr size_t WS_G2 = WS_CS + (size_t)4 * F_ * 4;
constexpr size_t WS_F0 = WS_G2 + (size_t)S_ * 8;
#define FSP 128
constexpr size_t WS_EP = WS_F0 + 1280 * FSP;
constexpr size_t WS_HH = WS_EP + 1024;
constexpr size_t WS_X = WS_HH + (size_t)S_ * F_ * 2;
constexpr unsigned X_BYTES = 64 * 4 * 3 * 9216;
constexpr size_t WS_WT = WS_X + X_BYTES;

template <int N> struct IC { static constexpr int v = N; };
typedef __amdgpu_buffer_rsrc_t rsrc_t;
__device__ __forceinline__ rsrc_t mk_rsrc(const void* p, unsigned bytes) { return __builtin_amdgcn_make_buffer_rsrc((void*)p, 0, bytes, 0x00020000); }
__device__ __forceinline__ void st16_wt(rsrc_t r, unsigned off, u32x4 v) { __builtin_amdgcn_raw_buffer_store_b128(v, r, off, 0, 16); }
__device__ __forceinline__ void st8_wt(rsrc_t r, unsigned off, u32x2 v) { __builtin_amdgcn_raw_buffer_store_b64(v, r, off, 0, 16); }
template <int CTRL> __device__ __forceinline__ float dpp_add(float x) {
    return x + __uint_as_float(__builtin_amdgcn_update_dpp(0, (int)__float_as_uint(x), CTRL, 0xf, 0xf, true));
}
__device__ __forceinline__ float x16_add(float x) {
    auto r = __builtin_amdgcn_permlane16_swap(__float_as_uint(x), __float_as_uint(x), false, false);
    return __uint_as_float(r[0]) + __uint_as_float(r[1]);
}
__device__ __forceinline__ float x32_add(float x) {
    auto r = __builtin_amdgcn_permlane32_swap(__float_as_uint(x), __float_as_uint(x), false, false);
    return __uint_as_float(r[0]) + __uint_as_float(r[1]);
}
__device__ __forceinline__ float sum16(float x) {
    x = dpp_add<0xB1>(x); x = dpp_add<0x4E>(x); x = dpp_add<0x141>(x);
    return dpp_add<0x140>(x);
}
__device__ __forceinline__ float ln_sd(float var) { return rbf(__builtin_amdgcn_sqrtf(rbf(rbf(var) + 1e-5f))); }

template <int OFF>
__device__ __forceinline__ void dma16(unsigned voff, const void* sbase, unsigned m0v) {
    asm volatile("s_mov_b32 m0, %2\n\ts_nop 0\n\tglobal_load_lds_dwordx4 %0, %1 offset:%3"
                 :: "v"(voff), "s"(sbase), "s"(m0v), "i"(OFF) : "memory");
}
template <int OFF>
__device__ __forceinline__ void dma16_sc1(unsigned voff, const void* sbase, unsigned m0v) {
    asm volatile("s_mov_b32 m0, %2\n\ts_nop 0\n\tglobal_load_lds_dwordx4 %0, %1 offset:%3 sc1"
                 :: "v"(voff), "s"(sbase), "s"(m0v), "i"(OFF) : "memory");
}
template <int OFF>
__device__ __forceinline__ void dma16_q(unsigned voff, const void* sbase, unsigned m0v) {
    asm volatile("s_mov_b32 m0, %2\n\ts_mov_b64 exec, 0xffff\n\tglobal_load_lds_dwordx4 %0, %1 offset:%3\n\ts_mov_b64 exec, -1"
                 :: "v"(voff), "s"(sbase), "s"(m0v), "i"(OFF) : "memory");
}
template <int OFF, int MASK>
__device__ __forceinline__ void dma_m(unsigned voff, const void* sbase, unsigned m0v) {
    asm volatile("s_mov_b32 m0, %2\n\ts_mov_b64 exec, %4\n\tglobal_load_lds_dwordx4 %0, %1 offset:%3\n\ts_mov_b64 exec, -1"
                 :: "v"(voff), "s"(sbase), "s"(m0v), "i"(OFF), "i"(MASK) : "memory");
}
__device__ __forceinline__ void dma4(unsigned voff, const void* sbase, unsigned m0v) {
    asm volatile("s_mov_b32 m0, %2\n\ts_nop 0\n\tglobal_load_lds_dword %0, %1" :: "v"(voff), "s"(sbase), "s"(m0v) : "memory");
}
#define SYM_ADDR(out, sym) asm volatile("s_getpc_b64 s[40:41]\n\ts_add_u32 s40, s40, " #sym "@rel32@lo+4\n\ts_addc_u32 s41, s41, " #sym "@rel32@hi+12\n\ts_mov_b64 %0, s[40:41]" \
                                        : "=s"(out) :: "s40", "s41", "scc")
template <int NI>
__device__ __forceinline__ void warm_code(const char* a, const char* e, int l0, int lane, unsigned m0v) {
    const unsigned last = e > a ? (unsigned)(e - a) - 4u : 0u;
#pragma unroll
    for (int i = 0; i < NI; ++i) dma4(min((unsigned)(l0 + i * 64 + lane) * 128u, last), a, m0v);
}
__device__ __forceinline__ s16x8 tr_frag(const LAS char* p) {
    const s16x4 lo = __builtin_amdgcn_ds_read_tr16_b64_v4i16((LAS s16x4*)p);
    const s16x4 hi = __builtin_amdgcn_ds_read_tr16_b64_v4i16((LAS s16x4*)(p + 16 * 288));
    return __builtin_shufflevector(lo, hi, 0, 1, 2, 3, 4, 5, 6, 7);
}
#define HELPER_DELAY 24
#define POLL0 24
#define POLL1 2
constexpr int C1_COPY = 4288;
constexpr int C1_FILT = 4 * C1_COPY;
constexpr int C1_KV = 2304 * 2;
constexpr int C1_KVOFF = 4 * C1_FILT + 64;
constexpr int C1_YOFF = C1_KVOFF + 4 * C1_KV;
constexpr int C1_XOFF = C1_YOFF + 16384;
constexpr int C1_SOFF = C1_XOFF + 16384;
constexpr int C1_TOFF = C1_SOFF + 16384;
constexpr int C1_LDS = C1_TOFF + 2048;

extern "C" __global__ __launch_bounds__(512, 2) void k1_conv(const u16* __restrict__ x, const u16* __restrict__ kin, const u16* __restrict__ vin,
                                                  const u16* __restrict__ shift_k, const u16* __restrict__ ssm_k, const u16* __restrict__ ssm_D,
                                                  const u16* __restrict__ wg, const u16* __restrict__ wu, const u16* __restrict__ wd,
                                                  char* __restrict__ ws) {
    extern __shared__ __attribute__((aligned(16))) char smem[];
    const int tid = threadIdx.x, lane = tid & 63;
    const int wid = __builtin_amdgcn_readfirstlane(tid >> 6);
    const int b = blockIdx.x, xcd = b & 7, idx = b >> 3;
    {
        const char *c1, *c3;
        SYM_ADDR(c1, k1_conv); SYM_ADDR(c3, k3_gemm1);
        if (wid >= 4) warm_code<1>(c1, c3, (wid & 3) * 64, lane, (unsigned)(size_t)(LAS char*)smem + C1_TOFF + 256 + (wid & 3) * 256);
    }
    DIAG_DECL;
    asm volatile("" :: "s"(x), "s"(kin), "s"(vin), "s"(shift_k), "s"(ssm_k), "s"(ssm_D), "s"(wg), "s"(wu), "s"(wd), "s"(ws));
    const bool conv = idx < 18;
    const int cg = xcd * 18 + idx, ch0 = cg * 4;
    const unsigned lds0 = (unsigned)(size_t)(LAS char*)smem;
    LAS unsigned* tagp = (LAS unsigned*)(smem + C1_TOFF);
    auto get_tag = [&]() -> unsigned { BAR(); return __builtin_amdgcn_readfirstlane(*(volatile LAS unsigned*)tagp); };

    const int h = (idx - 18) * 8 + xcd, hc_ = h < 108 ? h : 0;
    const int m3 = hc_ % 3, lcA = (hc_ / 3) * 4 + m3, rb0 = m3 * 3;
    auto cs_load = [&](u32x4 (&v)[12]) {
        const int rs = tid >> 3, c8 = tid & 7;
#pragma unroll
        for (int p = 0; p < 12; ++p) {
            const int rb = rb0 + p, lc = lcA + (rb >= 9), r = (rb >= 9 ? rb - 9 : rb) * 64 + rs;
            v[p] = *(const u32x4*)((lc >= 72 ? wu : wg) + (size_t)r * F_ + (lc % 72) * 64 + c8 * 8);
        }
        SCHED();
        const rsrc_t rx = mk_rsrc(ws + WS_X, X_BYTES);
#pragma unroll
        for (int i = 0; i < 8; ++i) st16_wt(rx, (unsigned)((i * 112 + h) * 8192 + tid * 16), (u32x4){~0u, ~0u, ~0u, ~0u});
        SCHED();
    };
    auto cs_finish = [&](u32x4 (&v)[12]) {
        float g[4][8];
#pragma unroll
        for (int q = 0; q < 4; ++q) {
#pragma unroll
            for (int e = 0; e < 4; ++e) {
                g[q][2 * e] = (bf_lo(v[3 * q][e]) + bf_lo(v[3 * q + 1][e])) + bf_lo(v[3 * q + 2][e]);
                g[q][2 * e + 1] = (bf_hi(v[3 * q][e]) + bf_hi(v[3 * q + 1][e])) + bf_hi(v[3 * q + 2][e]);
            }
        }
        LAS float* red = (LAS float*)smem;
#pragma unroll
        for (int l = 0; l < 2; ++l) {
            float a[8];
#pragma unroll
            for (int e = 0; e < 8; ++e) {
                a[e] = l == 0 ? g[0][e] + (m3 <= 1 ? g[1][e] : 0.f) + (m3 == 0 ? g[2][e] : 0.f) : g[3][e] + (m3 >= 1 ? g[2][e] : 0.f) + (m3 == 2 ? g[1][e] : 0.f);
                a[e] = x32_add(x16_add(dpp_add<0x128>(a[e])));
            }
            if (lane < 8) {
                *(LAS f32x4*)(red + (wid * 2 + l) * 64 + lane * 8) = (f32x4){a[0], a[1], a[2], a[3]};
                *(LAS f32x4*)(red + (wid * 2 + l) * 64 + lane * 8 + 4) = (f32x4){a[4], a[5], a[6], a[7]};
            }
        }
        asm volatile("s_waitcnt lgkmcnt(0)" ::: "memory"); BAR();
        if (tid < 192 && h < 108) {
            float s = 0.f;
            if (tid < 128) {
#pragma unroll
                for (int w = 0; w < 8; ++w) s += red[w * 128 + tid];
            }
            const int part = tid >> 6;
            const int lc = part == 0 ? lcA : (part == 1 ? lcA + 1 : (m3 == 0 ? lcA : lcA + 1));
            const int slot = part == 0 ? (m3 != 0) : (part == 1 ? 0 : 1);
            if (part < 2 || m3 != 1)
                __builtin_amdgcn_raw_buffer_store_b32(__float_as_uint(s), mk_rsrc(ws + WS_CS, 4 * F_ * 4), (unsigned)((slot * 2 * F_ + lc * 64 + (tid & 63)) * 4), 0, 16);
        }
    };
    auto wait_kv = [&](unsigned tag) {
        const rsrc_t rf = mk_rsrc(ws + WS_F0 + ((idx >> 2) * 8 + xcd) * 32 * FSP, 32 * FSP);
        __builtin_amdgcn_s_sleep(POLL0);
        for (int spin = 0; spin < (1 << 20); ++spin) {
            const unsigned f = __builtin_amdgcn_raw_buffer_load_b32(rf, (lane & 31) * FSP, 0, 16);
            if (__all(f == tag)) break;
            __builtin_amdgcn_s_sleep(POLL1);
        }
    };

    auto x_rows = [&]() {
        u32x2 xr[8];
#pragma unroll
        for (int i = 0; i < 8; ++i) xr[i] = *(const u32x2*)(x + (size_t)((tid - 256) + (wid - 4) * 448 + i * 64) * D_ + ch0);
#pragma unroll
        for (int i = 0; i < 8; ++i) *(LAS u32x2*)(smem + C1_XOFF + ((tid - 256) + (wid - 4) * 448 + i * 64) * 8) = xr[i];
        SCHED();
    };
    unsigned tag;
    unsigned dvv = 0;
    auto upper = [&](auto is_stat, auto is_conv) {
        constexpr bool STAT = decltype(is_stat)::v, CONV = decltype(is_conv)::v;
        const int srow = b * 8 + (wid - 5) * 4 + (lane >> 4), t3 = tid - 320;
        unsigned xw[18], epv = 0;
        if constexpr (STAT) {
            const unsigned* xr = (const unsigned*)(x + (size_t)srow * D_) + (lane & 15);
#pragma unroll
            for (int j = 0; j < 18; ++j) xw[j] = xr[16 * j];
        } else {
            epv = __hip_atomic_load((const unsigned*)(ws + WS_EP), __ATOMIC_RELAXED, __HIP_MEMORY_SCOPE_AGENT);
        }
        SCHED();
        if constexpr (STAT) {
            const char *c3, *c4;
            SYM_ADDR(c3, k3_gemm1); SYM_ADDR(c4, k4_gemm2);
            if ((idx & 7) == 0) warm_code<1>(c3, c4, (wid - 5) * 64, lane, lds0 + C1_TOFF + 256 + (wid - 5) * 256);
        }
        SCHED();
        u32x4 v[12], fa[6]; u32x2 fb[6];
        if constexpr (CONV) {
#pragma unroll
            for (int i = 0; i < 6; ++i) {
                const int task = min(t3 + 192 * i, 1023), u = task & 255;
                const u16* src = ssm_k + (size_t)(ch0 + (task >> 8)) * S_ + u * 8;
                fa[i] = *(const u32x4*)src;
                fb[i] = *(const u32x2*)(src + (u < 255 ? 8 : 0));
            }
            SCHED();
        }
        if constexpr (STAT) {
            f32x2 xf[18], s = {0.f, 0.f};
#pragma unroll
            for (int j = 0; j < 18; ++j) { xf[j] = (f32x2){bf_lo(xw[j]), bf_hi(xw[j])}; s += xf[j]; }
            const float mean = sum16(s.x + s.y) * (1.f / D_);
            f32x2 q = {0.f, 0.f};
#pragma unroll
            for (int j = 0; j < 18; ++j) { const f32x2 d = xf[j] - mean; q = __builtin_elementwise_fma(d, d, q); }
            const unsigned sgr = pk_bf16(mean, ln_sd(sum16(q.x + q.y) * (1.f / D_)));
            tag = get_tag();
            const unsigned so = (lane & 15) == 0 ? (unsigned)srow * 8 : 0x80000000u;
            st8_wt(mk_rsrc(ws + WS_ST, S_ * 8), so, (u32x2){sgr, tag});
            st8_wt(mk_rsrc(ws + WS_G2, S_ * 8), so, (u32x2){0u, 0u});
        } else {
            *tagp = epv + 1u;
            asm volatile("s_waitcnt lgkmcnt(0)" ::: "memory");
            tag = get_tag();
        }
        SCHED();
        if constexpr (!CONV) {
            __builtin_amdgcn_s_sleep(HELPER_DELAY);
            cs_load(v);
            if (!STAT && h == 111) {
                const rsrc_t rf = mk_rsrc(ws + WS_F0, 256 * FSP);
                for (int spin = 0; spin < (1 << 20); ++spin) {
                    bool ok = true;
#pragma unroll
                    for (int i = 0; i < 4; ++i) ok &= __builtin_amdgcn_raw_buffer_load_b32(rf, (i * 64 + lane) * FSP, 0, 16) == tag;
                    if (__all(ok)) break;
                    __builtin_amdgcn_s_sleep(8);
                }
                if (lane == 0) __hip_atomic_store((unsigned*)(ws + WS_EP), tag, __ATOMIC_RELAXED, __HIP_MEMORY_SCOPE_AGENT);
            }
            cs_finish(v);
            return;
        }
        if (t3 < 80) {
            const int cpy = t3 / 5, e = t3 % 5, q = e < 3 ? e : 257 + e;
            *(LAS u32x4*)(smem + cpy * C1_COPY + q * 16) = (u32x4){0u, 0u, 0u, 0u};
        } else if (t3 < 84) {
            *(LAS u32x4*)(smem + C1_KVOFF - 64 + (t3 - 80) * 16) = (u32x4){0u, 0u, 0u, 0u};
        }
        if (t3 < 128) *(LAS u32x4*)(smem + C1_KVOFF + (t3 >> 5) * C1_KV + 4096 + (t3 & 31) * 16) = (u32x4){0u, 0u, 0u, 0u};
#pragma unroll
        for (int i = 0; i < 6; ++i) {
            const int task = min(t3 + 192 * i, 1023), u = task & 255;
            const unsigned d[6] = {fa[i].x, fa[i].y, fa[i].z, fa[i].w, u < 255 ? fb[i].x : 0u, u < 255 ? fb[i].y : 0u};
            unsigned o[5];
#pragma unroll
            for (int e = 0; e < 5; ++e) o[e] = __builtin_amdgcn_alignbit(d[e + 1], d[e], 16);
            LAS char* base = (LAS char*)smem + (task >> 8) * C1_FILT + (u + 4) * 16;
            *(LAS u32x4*)(base) = (u32x4){o[0], o[1], o[2], o[3]};
            *(LAS u32x4*)(base + C1_COPY) = (u32x4){d[1], d[2], d[3], d[4]};
            *(LAS u32x4*)(base + 2 * C1_COPY) = (u32x4){o[1], o[2], o[3], o[4]};
            *(LAS u32x4*)(base + 3 * C1_COPY) = (u32x4){d[2], d[3], d[4], d[5]};
            if (u == 0) {
                *(LAS u32x4*)(base - 16) = (u32x4){0u, 0u, 0u, d[0] << 16};
                *(LAS u32x4*)(base - 16 + C1_COPY) = (u32x4){0u, 0u, 0u, d[0]};
                *(LAS u32x4*)(base - 16 + 2 * C1_COPY) = (u32x4){0u, 0u, d[0] << 16, o[0]};
                *(LAS u32x4*)(base - 16 + 3 * C1_COPY) = (u32x4){0u, 0u, d[0], d[1]};
            }
        }
        asm volatile("s_waitcnt lgkmcnt(0)" ::: "memory"); BAR();
        x_rows();
        dma4((unsigned)t3 * 128, (const char*)wd + (size_t)cg * 288 * 128, lds0 + C1_TOFF + 256 + (wid - 5) * 256);
        dma4((unsigned)(192 + (t3 < 96 ? t3 : 0)) * 128, (const char*)wd + (size_t)cg * 288 * 128, lds0 + C1_TOFF + 1024 + (wid - 5) * 256);
    };
    if (wid >= 5) {
        if (!conv) {
            if (wid < 7) upper(IC<1>{}, IC<0>{}); else upper(IC<0>{}, IC<0>{});
            DIAG_END(ws, 0);
            return;
        }
        if (wid < 7) upper(IC<1>{}, IC<1>{}); else upper(IC<0>{}, IC<1>{});
    } else {
        {
            unsigned kw[11], vw[8];
            const int t0 = idx * 64 + (lane >> 3) * 8, dc = wid * 8 + (lane & 7), kch = xcd * 72 + (dc < 36 ? dc : 35) * 2;
            const rsrc_t rk = mk_rsrc(kin, S_ * D_ * 2), rv = mk_rsrc(vin, S_ * D_ * 2);
            const int off = t0 * (D_ * 2) + kch * 2;
#pragma unroll
            for (int j = 0; j < 11; ++j) kw[j] = __builtin_amdgcn_raw_buffer_load_b32(rk, (unsigned)(off + (j - 3) * (D_ * 2)), 0, 0);
#pragma unroll
            for (int j = 0; j < 8; ++j) vw[j] = __builtin_amdgcn_raw_buffer_load_b32(rv, (unsigned)(off + j * (D_ * 2)), 0, 0);
            const u32x4 sw = *(const u32x4*)(shift_k + kch * 4);
            dvv = ssm_D[(conv ? ch0 : 0) + (wid & 3)];
            SCHED();
            STAMP(1);
            const f32x2 w0 = {bf_lo(sw.x), bf_lo(sw.z)}, w1 = {bf_hi(sw.x), bf_hi(sw.z)}, w2 = {bf_lo(sw.y), bf_lo(sw.w)}, w3 = {bf_hi(sw.y), bf_hi(sw.w)};
            f32x2 kf[11], o[8];
#pragma unroll
            for (int j = 0; j < 11; ++j) kf[j] = (f32x2){bf_lo(kw[j]), bf_hi(kw[j])};
#pragma unroll
            for (int j = 0; j < 8; ++j) {
                f32x2 s = kf[j] * w3;
                s = __builtin_elementwise_fma(kf[j + 1], w2, s);
                s = __builtin_elementwise_fma(kf[j + 2], w1, s);
                s = __builtin_elementwise_fma(kf[j + 3], w0, s);
                o[j] = s * (f32x2){bf_lo(vw[j]), bf_hi(vw[j])};
            }
            const rsrc_t ro = mk_rsrc(ws + WS_KV, S_ * D_ * 2);
            const unsigned oo = dc < 36 ? (unsigned)(kch * (S_ * 2) + (2040 - t0) * 2) : 0x80000000u;
            st16_wt(ro, oo, (u32x4){pk_bf16(o[7].x, o[6].x), pk_bf16(o[5].x, o[4].x), pk_bf16(o[3].x, o[2].x), pk_bf16(o[1].x, o[0].x)});
            st16_wt(ro, oo + S_ * 2, (u32x4){pk_bf16(o[7].y, o[6].y), pk_bf16(o[5].y, o[4].y), pk_bf16(o[3].y, o[2].y), pk_bf16(o[1].y, o[0].y)});
        }
        tag = get_tag();
        WAIT_V(0);
        if (lane == 0) __hip_atomic_store((unsigned*)(ws + WS_F0 + ((wid * 8 + xcd) * 32 + idx) * FSP), tag, __ATOMIC_RELAXED, __HIP_MEMORY_SCOPE_AGENT);
        MARK();
        STAMP(2);
        if (!conv) {
            u32x4 v[12];
            cs_load(v);
            cs_finish(v);
            DIAG_END(ws, 0);
            return;
        }
        if (wid == 4) {
            x_rows();
            BAR();
            __builtin_amdgcn_s_sleep(40);
            const char* gs = ws + WS_ST;
#pragma unroll
            for (int i = 0; i < 4; ++i) {
                dma16_sc1<0>(lane * 16, gs + i * 4096, lds0 + C1_SOFF + i * 4096); dma16_sc1<1024>(lane * 16, gs + i * 4096, lds0 + C1_SOFF + i * 4096);
                dma16_sc1<2048>(lane * 16, gs + i * 4096, lds0 + C1_SOFF + i * 4096); dma16_sc1<3072>(lane * 16, gs + i * 4096, lds0 + C1_SOFF + i * 4096);
            }
            WAIT_V(0);
        } else {
        {
            wait_kv(tag);
            STAMP(7);
            const char* kvs = ws + WS_KV + (size_t)(ch0 + wid) * (S_ * 2);
            const unsigned m = lds0 + C1_KVOFF + wid * C1_KV;
            dma16_sc1<0>(lane * 16, kvs, m); dma16_sc1<1024>(lane * 16, kvs, m); dma16_sc1<2048>(lane * 16, kvs, m); dma16_sc1<3072>(lane * 16, kvs, m);
            WAIT_V(0);
            STAMP(9);
            BAR();
            STAMP(4);
        }
        const int row = lane & 15, kg = lane >> 4;
        const LAS char* abase = (const LAS char*)smem + wid * C1_FILT + (row & 3) * C1_COPY + (24 + 4 * (row >> 2) - 8 * kg) * 2;
        const LAS char* bbase = (const LAS char*)smem + C1_KVOFF + wid * C1_KV + (2040 - 16 * row - 8 * kg) * 2 - 512 * 7 - 448;
        auto rd_ah = [&](int r, int L, int hf) -> s16x4 { return *(const volatile LAS s16x4*)(abase + 512 * L - 64 * r + 8 * hf); };
        auto rd_b = [&](int r, int m) -> s16x8 { return *(const LAS s16x8*)(bbase + 448 - 64 * r + 512 * (7 - m)); };
        auto cat = [&](s16x4 lo, s16x4 hi) -> s16x8 { return __builtin_shufflevector(lo, hi, 0, 1, 2, 3, 4, 5, 6, 7); };
        f32x4 acc[8];
        s16x4 al[2][8], ah[2][8];
        s16x8 bf[2][8], bd[8];
        auto rd_k = [&](int r, int k) {
            if (k < 2) { if (k == 0) al[r & 1][0] = rd_ah(r, 1, 0); else ah[r & 1][0] = rd_ah(r, 1, 1); }
            else if (k < 10) bf[r & 1][9 - k] = rd_b(r, 8 - k);
            else { const int L = (k - 10) / 2 + 2; if (k & 1) ah[r & 1][L - 1] = rd_ah(r, L, 1); else al[r & 1][L - 1] = rd_ah(r, L, 0); }
        };
        const s16x8 a0 = cat(rd_ah(0, 0, 0), rd_ah(0, 0, 1));
#pragma unroll
        for (int e = 0; e < 8; ++e) bd[e] = rd_b(0, e);
        s16x8 dfr;
        {
            const int j = 8 * kg + 7 - row;
            u32x4 w;
#pragma unroll
            for (int e = 0; e < 4; ++e) w[e] = j == 2 * e ? dvv : (j == 2 * e + 1 ? dvv << 16 : 0u);
            dfr = __builtin_bit_cast(s16x8, w);
            asm volatile("s_nop 1" : "+v"(dfr));
        }
        SCHED();
#pragma unroll
        for (int n = 0; n < 16; ++n) {
            if (n < 8) mfma_first(acc[n], a0, bd[n]); else mfma_acc(acc[n - 8], dfr, bd[n - 8]);
            SCHED();
            if (n == 0) { rd_k(0, 0); rd_k(0, 1); }
            else if (n == 1) rd_k(0, 9);
            else rd_k(0, n + 8);
            SCHED();
        }
#pragma unroll
        for (int r = 0; r < 8; ++r) {
            int k = 0;
#pragma unroll
            for (int L = 1; L <= 8; ++L) {
#pragma unroll
                for (int n = 7; n >= L - 1; --n) {
                    const int e = n - L + 1;
                    mfma_acc(acc[n], cat(al[r & 1][L - 1], ah[r & 1][L - 1]), (r == 0 && e > 0) ? bd[e - 1] : bf[r & 1][e]);
                    SCHED();
                    if (r < 7 && k < 24) rd_k(r + 1, k);
                    ++k;
                    SCHED();
                }
            }
        }
        MFMA_DONE();
#pragma unroll
        for (int n = 0; n < 8; ++n)
            *(LAS u32x2*)(smem + C1_YOFF + wid * 4096 + (256 * n + 16 * row + 4 * kg) * 2) = (u32x2){pk_bf16(acc[n][0], acc[n][1]), pk_bf16(acc[n][2], acc[n][3])};
        }
    }
    STAMP(5);
    asm volatile("s_waitcnt lgkmcnt(0)" ::: "memory"); BAR();
    STAMP(6);
    {
        u32x2 yc[4];
#pragma unroll
        for (int c = 0; c < 4; ++c) yc[c] = *(const LAS u32x2*)(smem + C1_YOFF + c * 4096 + tid * 8);
        const u32x4 xa = *(const LAS u32x4*)(smem + C1_XOFF + tid * 32), xb = *(const LAS u32x4*)(smem + C1_XOFF + tid * 32 + 16);
        const u32x2 xr[4] = {{xa.x, xa.y}, {xa.z, xa.w}, {xb.x, xb.y}, {xb.z, xb.w}};
        u32x4 s01 = *(const LAS u32x4*)(smem + C1_SOFF + tid * 32), s23 = *(const LAS u32x4*)(smem + C1_SOFF + tid * 32 + 16);
        if (__builtin_expect(!__all(s01.y == tag && s01.w == tag && s23.y == tag && s23.w == tag), 0)) {
            const rsrc_t rs = mk_rsrc(ws + WS_ST, S_ * 8);
            for (int spin = 0; spin < (1 << 20); ++spin) {
                s01 = __builtin_amdgcn_raw_buffer_load_b128(rs, tid * 32, 0, 16);
                s23 = __builtin_amdgcn_raw_buffer_load_b128(rs, tid * 32 + 16, 0, 16);
                if (__all(s01.y == tag && s01.w == tag && s23.y == tag && s23.w == tag)) break;
                __builtin_amdgcn_s_sleep(2);
            }
        }
        const unsigned sv[4] = {s01.x, s01.z, s23.x, s23.z};
        float mm[4], rr[4];
#pragma unroll
        for (int j = 0; j < 4; ++j) { mm[j] = bf_lo(sv[j]); rr[j] = __builtin_amdgcn_rcpf(bf_hi(sv[j])); }
        unsigned hw[8];
#pragma unroll
        for (int j = 0; j < 4; ++j) {
#pragma unroll
            for (int e = 0; e < 2; ++e) {
                const unsigned xw = e ? xr[j].y : xr[j].x;
                const unsigned ya = (j >> 1) ? yc[2 * e].y : yc[2 * e].x, yb = (j >> 1) ? yc[2 * e + 1].y : yc[2 * e + 1].x;
                const float y0 = (j & 1) ? bf_hi(ya) : bf_lo(ya), y1 = (j & 1) ? bf_hi(yb) : bf_lo(yb);
                const float x0 = bf_lo(xw), x1 = bf_hi(xw);
                hw[2 * j + e] = pk_bf16(__builtin_fmaf((x0 - mm[j]) * rr[j], y0, x0), __builtin_fmaf((x1 - mm[j]) * rr[j], y1, x1));
            }
        }
        const rsrc_t rh = mk_rsrc(ws + WS_H, S_ * D_ * 2);
        const unsigned ho = (unsigned)(((tid >> 6) * 144 + cg) * 2048 + (tid & 63) * 32);
        st16_wt(rh, ho, (u32x4){hw[0], hw[1], hw[2], hw[3]});
        st16_wt(rh, ho + 16, (u32x4){hw[4], hw[5], hw[6], hw[7]});
    }
    STAMP(8);
    DIAG_END(ws, 0);
}

constexpr int ST_BYTES = 16384 + 2 * 9216;
constexpr int NBUF = 4;
constexpr int G_LDS = NBUF * ST_BYTES;

struct TokC { float nm, c0, c1, q; };
__device__ __forceinline__ unsigned geglu2(f32x2 ag, f32x2 au, f32x2 csg, f32x2 csu, const TokC& t) {
    const f32x2 nm = {t.nm, t.nm}, c0 = {t.c0, t.c0}, c1 = {t.c1, t.c1}, q = {t.q, t.q};
    const f32x2 g = __builtin_elementwise_fma(nm, csg, ag), u = __builtin_elementwise_fma(nm, csu, au);
    const f32x2 z = g * __builtin_elementwise_fma(g * g, c1, c0);
    const f32x2 e = {__builtin_amdgcn_exp2f(z.x), __builtin_amdgcn_exp2f(z.y)};
    const f32x2 d = __builtin_elementwise_fma(e, q, q);
    const f32x2 r = (g * u) * (f32x2){__builtin_amdgcn_rcpf(d.x), __builtin_amdgcn_rcpf(d.y)};
    return pk_bf16(r.x, r.y);
}
#define LO2(v) __builtin_shufflevector(v, v, 0, 1)
#define HI2(v) __builtin_shufflevector(v, v, 2, 3)
__device__ __forceinline__ float dot2(unsigned a, unsigned b, float c) {
    return __builtin_amdgcn_fdot2_f32_bf16(__builtin_bit_cast(bf16x2_t, a), __builtin_bit_cast(bf16x2_t, b), c, false);
}
#define WAIT_VI(N) asm volatile("s_waitcnt vmcnt(%0)" :: "i"(N) : "memory")

constexpr int K3_WS = G_LDS;
constexpr int K3_LDS = K3_WS + 8 * 3072;
constexpr int K3_SP = K3_WS;
constexpr int K3_CS = 2 * ST_BYTES;
extern "C" __global__ __launch_bounds__(512, 2) void k3_gemm1(const u16* __restrict__ wg, const u16* __restrict__ wu, const u16* __restrict__ wd,
                                                   char* __restrict__ ws) {
    extern __shared__ __attribute__((aligned(16))) char smem[];
    LAS char* lds = (LAS char*)smem;
    const unsigned lds0 = (unsigned)(size_t)lds;
    const int tid = threadIdx.x, lane = tid & 63;
    const int wid = __builtin_amdgcn_readfirstlane(tid >> 6);
    const int wr = wid & 3, wc = wid >> 2;
    const int b = blockIdx.x, xcd = b & 7, idx = b >> 3;
    const int rt = (xcd >> 2) * 4 + (idx & 3), ct = (xcd & 3) * 8 + (idx >> 2);
    asm volatile("" :: "s"(wg), "s"(wu), "s"(wd), "s"(ws));
    DIAG_DECL;

    const char* asrc = ws + WS_H + (size_t)rt * 144 * 2048 + wid * 2048;
    const unsigned aoff = (unsigned)(lane ^ ((wid & 2) << 2)) * 16;
    const unsigned ma0 = lds0 + wid * 2048, mw0 = lds0 + 16384 + wc * 9216 + wr * 2304;
    dma16<0>(aoff, asrc, ma0); dma16<1024>(aoff, asrc, ma0);
    const char* wsrc = (const char*)(wc ? wu : wg) + ct * 288 - 2048;
    auto wofs = [&](int i) -> unsigned {
        const int q = lane + 64 * i, lrow = 8 * wr + q / 18, pos = q % 18;
        const int krow = ((lrow >> 2) & 3) * 8 + (lrow >> 4) * 4 + (lrow & 3);
        const int col = pos * 8 + (pos < 8 ? 0 : (pos < 16 ? 8 : (((pos & 1) ^ wc) ? 136 : 64) - pos * 8));
        return (unsigned)(krow * (F_ * 2) + col * 2 + 2048 - 1024 * i);
    };
    const unsigned w0 = wofs(0), w1 = wofs(1), w2 = wofs(2);
    auto piece = [&](int P, int s) {
        const unsigned bo = (s & 3) * ST_BYTES;
        const char* wp = wsrc + (size_t)s * (32 * F_ * 2);
        if (P == 0) dma16<0>(aoff, asrc + (size_t)s * 16384, ma0 + bo);
        if (P == 1) dma16<1024>(aoff, asrc + (size_t)s * 16384, ma0 + bo);
        if (P == 2) dma16<0>(w0, wp, mw0 + bo);
        if (P == 3) dma16<1024>(w1, wp, mw0 + bo);
        if (P == 4) dma16_q<2048>(w2, wp, mw0 + bo);
    };
#define K3_PIECE(P, s) piece(P, s)
#define K3_STAGE(s) do { K3_PIECE(0, s); K3_PIECE(1, s); K3_PIECE(2, s); K3_PIECE(3, s); K3_PIECE(4, s); } while (0)
    K3_PIECE(2, 0); K3_PIECE(3, 0); K3_PIECE(4, 0);
    BAR();
    K3_STAGE(1);
    u32x2 hs[4];
    {
        const char* hsrc = ws + WS_H + (size_t)rt * 144 * 2048 + ct * 64;
        const unsigned o = (unsigned)((wid * 18 + (lane >> 3)) * 2048 + (lane & 7) * 8);
        asm volatile("global_load_dwordx2 %0, %1, %2" : "=v"(hs[0]) : "v"(o), "s"(hsrc) : "memory");
        asm volatile("global_load_dwordx2 %0, %1, %2" : "=v"(hs[1]) : "v"(o + 16384), "s"(hsrc) : "memory");
        asm volatile("global_load_dwordx2 %0, %1, %2" : "=v"(hs[2]) : "v"((unsigned)((wid * 18 + 16 + ((lane >> 3) & 1)) * 2048 + (lane & 7) * 8)), "s"(hsrc) : "memory");
        asm volatile("global_load_dwordx2 %0, %1, %2" : "=v"(hs[3]) : "v"((unsigned)(lane & 7) * 8), "s"(hsrc) : "memory");
    }

    const int g4 = lane >> 4, q4 = (lane & 15) >> 2, p4 = lane & 3;
    const int rowoff = (4 * g4 + q4) * 288;
    const int tbase = g4 * 4096 + wr * 512 + (lane & 15) * 8;
    const int toff0 = tbase + ((2 * wc) ^ (g4 & 1)) * 128, toff1 = tbase + ((2 * wc + 1) ^ (g4 & 1)) * 128;
    const int tstep = wc ? -256 : 256;
    const int foff = 16384 + rowoff + wc * 128 + p4 * 8;
    const int moff = 16384 + rowoff + 256 + (p4 < 2 ? 16 * wc + 8 * p4 : 9216 + 16 * (1 - wc) + 8 * (p4 - 2));

    f32x4 acc[9][4];
    s16x8 wf[9], tf[2][4];
    auto rd_w = [&](const LAS char* sb, int j) -> s16x8 {
        return j < 8 ? tr_frag(sb + foff + (j >> 2) * 9216 + (j & 3) * 32) : tr_frag(sb + moff);
    };
    auto rd_t = [&](const LAS char* sb, int i) -> s16x8 {
        const LAS char* p = sb + ((i & 1) ? toff1 : toff0) + (i >> 1) * tstep;
        const s16x4 lo = *(const volatile LAS s16x4*)p, hi = *(const volatile LAS s16x4*)(p + 2048);
        return __builtin_shufflevector(lo, hi, 0, 1, 2, 3, 4, 5, 6, 7);
    };

    const int un = (b < 216 ? b : b - 216) * 8 + wid, uB = un / 3, ut = un % 3;
    auto wd_dma = [&](int i) {
        const int kh = uB & 1, st = (uB >> 1) % 18, kn = uB / 36;
        const bool full = st < 16;
        const int f0 = (kn >> 2) * 1152 + (full ? (st & 7) * 144 + kh * 72 + (st >> 3) * 32 : ((st - 16) * 2 + kh) * 288);
        const char* src = (const char*)wd + (size_t)f0 * (D_ * 2) + (kn & 3) * 288 + ut * 96;
        const int q = lane + 64 * i, r = q / 6, pos = q % 6, kg = (r & 15) >> 2;
        const int f = full ? r : (kg >> 1) * 144 + (kg & 1) * 72 + 64 + (r & 3) + 4 * (r >> 4);
        dma16<0>((unsigned)(f * (D_ * 2) + pos * 16), src, lds0 + K3_WS + wid * 3072 + i * 1024);
    };

    STAMP(1);
    WAIT_V(9); BAR();
    STAMP(2);
    wf[0] = rd_w(lds, 0);
#pragma unroll
    for (int i = 0; i < 4; ++i) tf[0][i] = rd_t(lds, i);
#pragma unroll
    for (int j = 1; j < 9; ++j) wf[j] = rd_w(lds, j);
    SCHED();
    STAMP(6);
    {
        const LAS char* sb = lds + ST_BYTES;
        float hc = 0.f;
#pragma unroll
        for (int j = 0; j < 9; ++j) {
#pragma unroll
            for (int i = 0; i < 4; ++i) mfma_first(acc[j][i], wf[j], tf[0][i]);
            SCHED();
            if (j == 0) { K3_PIECE(0, 2); K3_PIECE(1, 2); }
            if (j == 1) { K3_PIECE(2, 2); K3_PIECE(3, 2); }
            if (j == 2) K3_PIECE(4, 2);
            if (j == 3) {
                asm volatile("s_waitcnt vmcnt(5)" : "+v"(hs[0]), "+v"(hs[1]), "+v"(hs[2]), "+v"(hs[3]) :: "memory");
                hc = bf_lo(hs[3].x);
                float s1 = 0.f, s2 = 0.f;
#pragma unroll
                for (int i = 0; i < 3; ++i) {
                    const float d[4] = {bf_lo(hs[i].x) - hc, bf_hi(hs[i].x) - hc, bf_lo(hs[i].y) - hc, bf_hi(hs[i].y) - hc};
                    float a = (d[0] + d[1]) + (d[2] + d[3]), q = __builtin_fmaf(d[0], d[0], d[1] * d[1]) + __builtin_fmaf(d[2], d[2], d[3] * d[3]);
                    if (i == 2 && lane >= 16) { a = 0.f; q = 0.f; }
                    s1 += a; s2 += q;
                }
                s1 = x32_add(x16_add(dpp_add<0x128>(s1))); s2 = x32_add(x16_add(dpp_add<0x128>(s2)));
                if (lane < 8) *(LAS f32x2*)(lds + K3_SP + (lane * 8 + wid) * 8) = (f32x2){s1, s2};
                asm volatile("s_waitcnt lgkmcnt(0)" ::: "memory");
                BAR();
                wf[0] = rd_w(sb, 0); wf[1] = rd_w(sb, 1);
                tf[1][0] = rd_t(sb, 0); tf[1][1] = rd_t(sb, 1);
            }
            if (j == 4) { tf[1][2] = rd_t(sb, 2); tf[1][3] = rd_t(sb, 3); wf[2] = rd_w(sb, 2); wf[3] = rd_w(sb, 3); }
            if (j >= 4) { wf[j] = rd_w(sb, j); K3_PIECE(j - 4, 3); }
            if (j == 8) {
                const f32x2 pp = *(const LAS f32x2*)(lds + K3_SP + (wid * 8 + (lane & 7)) * 8);
                const float s1 = dpp_add<0x141>(dpp_add<0x4E>(dpp_add<0xB1>(pp.x))) * (1.f / D_);
                const float s2 = dpp_add<0x141>(dpp_add<0x4E>(dpp_add<0xB1>(pp.y))) * (1.f / D_);
                const float c0 = __uint_as_float(__builtin_amdgcn_readlane(__float_as_uint(hc), wid));
                const float sd = ln_sd(__builtin_fmaxf(__builtin_fmaf(-s1, s1, s2), 0.f));
                if (lane == 0) st8_wt(mk_rsrc(ws + WS_G2, S_ * 8), (unsigned)((rt * 256 + ct * 8 + wid) * 8), (u32x2){pk_bf16(c0 + s1, sd), 1u});
            }
            SCHED();
        }
    }

    const rsrc_t rg = mk_rsrc(ws + WS_G2 + (size_t)(rt * 256 + wr * 64) * 8, 512);
    u32x2 gr[4];
    u32x4 wtv;
    auto iter = [&](auto mode, auto vm, int s, s16x8 (&tc)[4], s16x8 (&tn)[4]) {
        constexpr int MODE = decltype(mode)::v;
        if constexpr (MODE < 4) {
            if constexpr (MODE == 3) WAIT_V(0); else WAIT_VI(5 + decltype(vm)::v);
            BAR();
            SCHED();
        }
        const LAS char* sb = lds + ((s + 1) & 3) * ST_BYTES;
#pragma unroll
        for (int j = 0; j < 9; ++j) {
#pragma unroll
            for (int i = 0; i < 4; ++i) mfma_acc(acc[j][i], wf[j], tc[i]);
            SCHED();
            if constexpr (MODE < 4) {
                wf[j] = rd_w(sb, j);
                if (j >= 2 && j < 6) tn[j - 2] = rd_t(sb, j - 2);
            }
            if constexpr (MODE < 2) {
                if (j == 0) K3_PIECE(0, s + 3);
                if (j == 1) K3_PIECE(1, s + 3);
                if (j == 2) K3_PIECE(2, s + 3);
                if (j == 3) K3_PIECE(3, s + 3);
                if (j == 4) K3_PIECE(4, s + 3);
            }
            if constexpr (MODE == 0) { if (j >= 6) wd_dma(j - 6); }
            if constexpr (MODE == 3) {
                if (j == 0) {
#pragma unroll
                    for (int i = 0; i < 4; ++i) gr[i] = __builtin_amdgcn_raw_buffer_load_b64(rg, (unsigned)(((i ^ (2 * wc)) * 16 + (lane & 15)) * 8), 0, 16);
                    dma_m<0, 0x1ff>(lane * 16, ws + WS_CS + (size_t)(wc * F_ + ct * 144 + wr * 36) * 4, lds0 + K3_CS + wid * 144);
                    dma_m<0, 0x1ff>(lane * 16, ws + WS_CS + (size_t)(2 * F_ + wc * F_ + ct * 144 + wr * 36) * 4, lds0 + K3_CS + 1152 + wid * 144);
                    const char *c4, *ce;
                    SYM_ADDR(c4, k4_gemm2); SYM_ADDR(ce, k9_end);
                    warm_code<1>(c4, ce, (wid & 3) * 64, lane, lds0 + K3_CS + 4096 + wid * 256);
                }
            }
            if constexpr (MODE >= 3) {
                constexpr int T0 = MODE == 3 ? 0 : 2;
                if (j == 1 || (MODE == 3 && j == 6)) {
                    const LAS char* tp = lds + K3_WS + wid * 3072 + (lane >> 2) * 96 + (lane & 3) * 8 + (T0 + (j == 6)) * 32;
                    const s16x4 lo = __builtin_amdgcn_ds_read_tr16_b64_v4i16((LAS s16x4*)tp), hi = __builtin_amdgcn_ds_read_tr16_b64_v4i16((LAS s16x4*)(tp + 16 * 96));
                    wtv = __builtin_bit_cast(u32x4, __builtin_shufflevector(lo, hi, 0, 1, 2, 3, 4, 5, 6, 7));
                }
                if (j == 3 || (MODE == 3 && j == 8))
                    st16_wt(mk_rsrc(ws + WS_WT, b < 216 ? F_ * D_ * 2 : 0), (unsigned)(uB * 9216 + (ut * 3 + T0 + (j == 8)) * 1024 + lane * 16), wtv);
            }
            SCHED();
        }
    };
    iter(IC<0>{}, IC<1>{}, 1, tf[1], tf[0]);
    iter(IC<1>{}, IC<4>{}, 2, tf[0], tf[1]);
    iter(IC<1>{}, IC<3>{}, 3, tf[1], tf[0]);
#pragma unroll 1
    for (int s = 4; s < 14; s += 2) {
        iter(IC<1>{}, IC<0>{}, s, tf[0], tf[1]);
        iter(IC<1>{}, IC<0>{}, s + 1, tf[1], tf[0]);
    }
    STAMP(3);
    iter(IC<1>{}, IC<0>{}, 14, tf[0], tf[1]);
    iter(IC<2>{}, IC<0>{}, 15, tf[1], tf[0]);
    iter(IC<3>{}, IC<0>{}, 16, tf[0], tf[1]);
    iter(IC<4>{}, IC<0>{}, 17, tf[1], tf[0]);
    MFMA_DONE();
    STAMP(4);
    WAIT_V(3); BAR();
    if (__builtin_expect(!__all((gr[0].y & gr[1].y & gr[2].y & gr[3].y) == 1u), 0)) {
        for (int spin = 0; spin < (1 << 20); ++spin) {
            __builtin_amdgcn_s_sleep(2);
#pragma unroll
            for (int i = 0; i < 4; ++i) gr[i] = __builtin_amdgcn_raw_buffer_load_b64(rg, (unsigned)(((i ^ (2 * wc)) * 16 + (lane & 15)) * 8), 0, 16);
            if (__all((gr[0].y & gr[1].y & gr[2].y & gr[3].y) == 1u)) break;
        }
    }
    const int mt = rt * 2 + (wr >> 1), ks = ct >> 3, lct = ct & 7;
    const rsrc_t rhh = mk_rsrc(ws + WS_HH, S_ * F_ * 2);
    const unsigned hb = (unsigned)(((mt * 4 + ks) * 36) * 8192 + ((wr & 1) * 4) * 1024);
    const LAS char* csb = lds + K3_CS + (wc * 72 + 4 * g4) * 4;
    TokC tcs[4];
#pragma unroll
    for (int i = 0; i < 4; ++i) {
        const float sd = bf_hi(gr[i].x), r = __builtin_amdgcn_rcpf(sd);
        tcs[i] = (TokC){-bf_lo(gr[i].x), -2.30220819f * r, -0.10294324f * (r * r * r), sd * sd};
    }
#pragma unroll
    for (int jp = 0; jp < 2; ++jp) {
        f32x4 cg[2], cu[2];
#pragma unroll
        for (int e = 0; e < 2; ++e) {
            cg[e] = *(const LAS f32x4*)(csb + (2 * jp + e) * 64) + *(const LAS f32x4*)(csb + 1152 + (2 * jp + e) * 64);
            cu[e] = *(const LAS f32x4*)(csb + 576 + (2 * jp + e) * 64) + *(const LAS f32x4*)(csb + 1728 + (2 * jp + e) * 64);
        }
#pragma unroll
        for (int i = 0; i < 4; ++i) {
            unsigned o[4];
#pragma unroll
            for (int e = 0; e < 2; ++e) {
                const f32x4 g = acc[2 * jp + e][i], u = acc[4 + 2 * jp + e][i];
                o[2 * e] = geglu2(LO2(g), LO2(u), LO2(cg[e]), LO2(cu[e]), tcs[i]);
                o[2 * e + 1] = geglu2(HI2(g), HI2(u), HI2(cg[e]), HI2(cu[e]), tcs[i]);
            }
            st16_wt(rhh, hb + (unsigned)(((jp * 8 + lct) * 2 + wc) * 8192 + (i ^ (2 * wc)) * 1024 + lane * 16), (u32x4){o[0], o[1], o[2], o[3]});
        }
    }
    {
        const LAS char* cm = lds + K3_CS + (wc * 72 + 64 + 4 * (g4 & 1)) * 4;
        const f32x4 cgm = *(const LAS f32x4*)cm + *(const LAS f32x4*)(cm + 1152), cum = *(const LAS f32x4*)(cm + 576) + *(const LAS f32x4*)(cm + 1728);
#pragma unroll
        for (int ip = 0; ip < 2; ++ip) {
            const int ia = ((2 * ip) ^ (2 * wc)) + (lane >> 5);
            const bool up = lane >= 32;
            const TokC tc = {up ? tcs[2 * ip + 1].nm : tcs[2 * ip].nm, up ? tcs[2 * ip + 1].c0 : tcs[2 * ip].c0,
                             up ? tcs[2 * ip + 1].c1 : tcs[2 * ip].c1, up ? tcs[2 * ip + 1].q : tcs[2 * ip].q};
            f32x4 gm, um;
#pragma unroll
            for (int r = 0; r < 4; ++r) {
                auto sw = __builtin_amdgcn_permlane32_swap(__float_as_uint(acc[8][2 * ip][r]), __float_as_uint(acc[8][2 * ip + 1][r]), false, false);
                gm[r] = __uint_as_float(sw[0]); um[r] = __uint_as_float(sw[1]);
            }
            const int kgs = (lct & 1) * 2 + wc;
            st8_wt(rhh, hb + (unsigned)((32 + (lct >> 1)) * 8192 + ia * 1024 + (kgs * 16 + (lane & 15)) * 16 + (g4 & 1) * 8),
                   (u32x2){geglu2(LO2(gm), LO2(um), LO2(cgm), LO2(cum), tc), geglu2(HI2(gm), HI2(um), HI2(cgm), HI2(cum), tc)});
        }
    }
    STAMP(5);
    DIAG_END(ws, 1);
}

constexpr int K4_LDS = G_LDS;
template <int NT>
__device__ __forceinline__ void k4_body(char* __restrict__ ws, u16* __restrict__ out, LAS char* lds, int lane, int wid, int mt, int ks, int nt) {
    constexpr int dh = NT == 4;
    const unsigned lds0 = (unsigned)(size_t)lds;
    const int wr = wid & 3;
    DIAG_DECL;
    const char* asrc = ws + WS_HH + ((size_t)(mt * 4 + ks) * 36) * 8192 + wid * 2048;
    const char* wsrc = ws + WS_WT + (size_t)(ks * 4 + nt) * 18 * 18432 + wid * 2304;
    const unsigned aoff = lane * 16;
    const unsigned ma0 = lds0 + wid * 2048, mw0 = lds0 + 16384 + wid * 2304;
    auto piece = [&](int P, int s) {
        const unsigned bo = (s & 3) * ST_BYTES;
        if (P == 0) dma16<0>(aoff, asrc + (size_t)s * 16384, ma0 + bo);
        if (P == 1) dma16<1024>(aoff, asrc + (size_t)s * 16384, ma0 + bo);
        if (P == 2) dma16<0>(aoff, wsrc + (size_t)s * 18432, mw0 + bo);
        if (P == 3) dma16<1024>(aoff, wsrc + (size_t)s * 18432, mw0 + bo);
        if (P == 4) dma16_q<2048>(aoff, wsrc + (size_t)s * 18432, mw0 + bo);
    };
#define K4_STAGE(s) do { piece(0, s); piece(1, s); piece(2, s); piece(3, s); piece(4, s); } while (0)
    K4_STAGE(0);
    BAR();
    K4_STAGE(1);

    const int g4 = lane >> 4;
    const int toff = wr * 2048 + lane * 16;
    const int foff = 16384 + dh * 5120 + lane * 16;
    auto rdf = [&](const LAS char* p) -> s16x8 { return *(const LAS s16x8*)p; };

    f32x4 acc[NT][2];
    s16x8 wf[2 * NT], tf[2][4];
    STAMP(1);
    WAIT_V(5); BAR();
    STAMP(2);
#pragma unroll
    for (int e = 0; e < 4; ++e) tf[0][e] = rdf(lds + toff + (e >> 1) * 8192 + (e & 1) * 1024);
#pragma unroll
    for (int e = 0; e < 2 * NT; ++e) wf[e] = rdf(lds + foff + (e / NT) * 9216 + (e % NT) * 1024);
    SCHED();
    K4_STAGE(2);
    SCHED();

    auto iter = [&](auto mode, int s, s16x8 (&tc)[4], s16x8 (&tn)[4]) {
        constexpr int MODE = decltype(mode)::v;
        if constexpr (MODE < 4) {
            if constexpr (MODE == 3) WAIT_V(0); else WAIT_V(5);
            BAR();
            SCHED();
        }
        const LAS char* sb = lds + ((s + 1) & 3) * ST_BYTES;
#pragma unroll
        for (int e = 0; e < 2 * NT; ++e) {
#pragma unroll
            for (int i = 0; i < 2; ++i) {
                if (MODE == 0 && e < NT) mfma_first(acc[e % NT][i], wf[e], tc[(e / NT) * 2 + i]);
                else mfma_acc(acc[e % NT][i], wf[e], tc[(e / NT) * 2 + i]);
            }
            SCHED();
            if constexpr (MODE < 4) {
                wf[e] = rdf(sb + foff + (e / NT) * 9216 + (e % NT) * 1024);
                if (e >= 2 && e < 6) tn[e - 2] = rdf(sb + toff + ((e - 2) >> 1) * 8192 + ((e - 2) & 1) * 1024);
            }
            if constexpr (MODE < 2) {
                if (e == 0) piece(0, s + 3);
                if (e == 1) piece(1, s + 3);
                if (e == 3) piece(2, s + 3);
                if (e == 5) piece(3, s + 3);
                if (e == 6) piece(4, s + 3);
            }
            SCHED();
        }
    };
    iter(IC<0>{}, 0, tf[0], tf[1]);
    iter(IC<1>{}, 1, tf[1], tf[0]);
#pragma unroll 1
    for (int s = 2; s < 14; s += 2) {
        iter(IC<1>{}, s, tf[0], tf[1]);
        iter(IC<1>{}, s + 1, tf[1], tf[0]);
    }
    STAMP(3);
    iter(IC<1>{}, 14, tf[0], tf[1]);
    iter(IC<2>{}, 15, tf[1], tf[0]);
    iter(IC<3>{}, 16, tf[0], tf[1]);
    const int tok0 = mt * 128 + wr * 32 + (lane & 15);
    u32x2 hv[2][5];
    if (wr == ks) {
#pragma unroll
        for (int i = 0; i < 2; ++i) {
            const int t = tok0 + i * 16;
            const char* hp = ws + WS_H + ((size_t)((t >> 8) * 144 + nt * 36 + dh * 20) * 256 + (t & 255)) * 8;
#pragma unroll
            for (int e = 0; e < 4; ++e) hv[i][e] = *(const u32x2*)(hp + ((e >> 1) * 8 + (g4 & 1) * 4 + (g4 >> 1) * 2 + (e & 1)) * 2048);
            if (NT == 5) hv[i][4] = *(const u32x2*)(hp + (16 + g4) * 2048);
        }
    }
    SCHED();
    iter(IC<4>{}, 17, tf[1], tf[0]);
    MFMA_DONE();
    STAMP(4);

    const rsrc_t rx = mk_rsrc(ws + WS_X, X_BYTES);
    const unsigned xt = (unsigned)(((mt * 4 + nt) * 4 + wr) * 3 * 9216 + dh * 4096);
    if (wr != ks) {
        const unsigned o = xt + (unsigned)(((ks - wr - 1) & 3) * 9216);
#pragma unroll
        for (int i = 0; i < 2; ++i) {
            unsigned w[2 * NT];
#pragma unroll
            for (int j = 0; j < NT; ++j) { w[2 * j] = pk_bf16(acc[j][i][0], acc[j][i][1]); w[2 * j + 1] = pk_bf16(acc[j][i][2], acc[j][i][3]); }
#pragma unroll
            for (int e = 0; e < 2 * NT; ++e) w[e] = (w[e] == ~0u) ? 0x7fc07fc0u : w[e];
            if constexpr (NT == 5) st8_wt(rx, o + 8192 + i * 512 + lane * 8, (u32x2){w[8], w[9]});
            st16_wt(rx, o + i * 2048 + lane * 16, (u32x4){w[0], w[1], w[2], w[3]});
            st16_wt(rx, o + i * 2048 + 1024 + lane * 16, (u32x4){w[4], w[5], w[6], w[7]});
        }
    } else {
        u32x4 pa[3][4]; u32x2 pb[3][2];
        for (int spin = 0; spin < (1 << 18); ++spin) {
#pragma unroll
            for (int s = 0; s < 3; ++s) pa[s][3] = __builtin_amdgcn_raw_buffer_load_b128(rx, xt + s * 9216 + 3072 + lane * 16, 0, 16);
            unsigned m = 0u;
#pragma unroll
            for (int s = 0; s < 3; ++s) m = max(max(max(m, pa[s][3].x), max(pa[s][3].y, pa[s][3].z)), pa[s][3].w);
            if (__all(m != ~0u)) {
                STAMP(5);
#pragma unroll
                for (int s = 0; s < 3; ++s) {
#pragma unroll
                    for (int p = 0; p < 3; ++p) pa[s][p] = __builtin_amdgcn_raw_buffer_load_b128(rx, xt + s * 9216 + p * 1024 + lane * 16, 0, 16);
                    if constexpr (NT == 5) {
#pragma unroll
                        for (int i = 0; i < 2; ++i) pb[s][i] = __builtin_amdgcn_raw_buffer_load_b64(rx, xt + s * 9216 + 8192 + i * 512 + lane * 8, 0, 16);
                    }
                }
#pragma unroll
                for (int s = 0; s < 3; ++s) {
#pragma unroll
                    for (int p = 0; p < 3; ++p) m = max(max(max(m, pa[s][p].x), max(pa[s][p].y, pa[s][p].z)), pa[s][p].w);
                    if constexpr (NT == 5) m = max(max(max(m, pb[s][0].x), max(pb[s][0].y, pb[s][1].x)), pb[s][1].y);
                }
                if (__all(m != ~0u)) break;
            }
            __builtin_amdgcn_s_sleep(1);
        }
        STAMP(6);
        const rsrc_t ro = mk_rsrc(out, S_ * D_ * 2);
#pragma unroll
        for (int i = 0; i < 2; ++i) {
            const unsigned ob = (unsigned)(((tok0 + i * 16) * D_ + nt * 144 + dh * 80) * 2);
            unsigned r[NT][2];
#pragma unroll
            for (int j = 0; j < NT; ++j) {
                f32x4 t = acc[j][i];
#pragma unroll
                for (int s = 0; s < 3; ++s) {
                    const unsigned w0 = j < 4 ? pa[s][i * 2 + (j >> 1)][(j & 1) * 2] : pb[s][i].x, w1 = j < 4 ? pa[s][i * 2 + (j >> 1)][(j & 1) * 2 + 1] : pb[s][i].y;
                    t += (f32x4){bf_lo(w0), bf_hi(w0), bf_lo(w1), bf_hi(w1)};
                }
                r[j][0] = pk_bf16(t[0], t[1]); r[j][1] = pk_bf16(t[2], t[3]);
            }
            auto addh = [&](unsigned a, unsigned h) -> unsigned { return pk_bf16(bf_lo(h) + bf_lo(a), bf_hi(h) + bf_hi(a)); };
#pragma unroll
            for (int p = 0; p < 2; ++p) {
                auto s0 = __builtin_amdgcn_permlane16_swap(r[2 * p][0], r[2 * p + 1][0], false, false);
                auto s1 = __builtin_amdgcn_permlane16_swap(r[2 * p][1], r[2 * p + 1][1], false, false);
                st16_wt(ro, ob + (unsigned)((p * 32 + (g4 & 1) * 16 + (g4 >> 1) * 8) * 2),
                        (u32x4){addh(s0[0], hv[i][2 * p].x), addh(s1[0], hv[i][2 * p].y), addh(s0[1], hv[i][2 * p + 1].x), addh(s1[1], hv[i][2 * p + 1].y)});
            }
            if constexpr (NT == 5) st8_wt(ro, ob + (unsigned)((64 + g4 * 4) * 2), (u32x2){addh(r[4][0], hv[i][4].x), addh(r[4][1], hv[i][4].y)});
        }
    }
    STAMP(7);
    DIAG_END(ws, 2);
}

extern "C" __global__ __launch_bounds__(512, 2) void k4_gemm2(char* __restrict__ ws, u16* __restrict__ out) {
    extern __shared__ __attribute__((aligned(16))) char smem[];
    const int lane = threadIdx.x & 63;
    const int wid = __builtin_amdgcn_readfirstlane(threadIdx.x >> 6);
    const int b = blockIdx.x, xcd = b & 7, idx = b >> 3;
    const int mt = (xcd >> 2) * 8 + (idx & 7), ks = xcd & 3, nt = idx >> 3;
    asm volatile("" :: "s"(out), "s"(ws));
    if (wid < 4) k4_body<5>(ws, out, (LAS char*)smem, lane, wid, mt, ks, nt);
    else k4_body<4>(ws, out, (LAS char*)smem, lane, wid, mt, ks, nt);
}

extern "C" __global__ void k9_end() {}

extern "C" void kernel_launch(void* const* d_in, const int* in_sizes, int n_in, void* d_out, int out_size, void* d_ws,
                              size_t ws_size, hipStream_t stream) {
    const u16* q = (const u16*)d_in[0];
    const u16* k = (const u16*)d_in[1];
    const u16* v = (const u16*)d_in[2];
    const u16* shift_k = (const u16*)d_in[3];
    const u16* ssm_k = (const u16*)d_in[4];
    const u16* ssm_D = (const u16*)d_in[5];
    const u16* wg = (const u16*)d_in[7];
    const u16* wu = (const u16*)d_in[8];
    const u16* wd = (const u16*)d_in[9];
    char* ws = (char*)d_ws;
    (void)hipFuncSetAttribute((const void*)k1_conv, hipFuncAttributeMaxDynamicSharedMemorySize, C1_LDS);
    (void)hipFuncSetAttribute((const void*)k3_gemm1, hipFuncAttributeMaxDynamicSharedMemorySize, K3_LDS);
    (void)hipFuncSetAttribute((const void*)k4_gemm2, hipFuncAttributeMaxDynamicSharedMemorySize, K4_LDS);
    k1_conv<<<256, 512, C1_LDS, stream>>>(q, k, v, shift_k, ssm_k, ssm_D, wg, wu, wd, ws);
    k3_gemm1<<<256, 512, K3_LDS, stream>>>(wg, wu, wd, ws);
    k4_gemm2<<<256, 512, K4_LDS, stream>>>(ws, (u16*)d_out);
}
```

```cpp
#include <hip/hip_runtime.h>
#include <hip/hip_bf16.h>

#define LAS __attribute__((address_space(3)))
typedef unsigned short u16;
typedef short s16x4 __attribute__((ext_vector_type(4)));
typedef short s16x8 __attribute__((ext_vector_type(8)));
typedef float f32x2 __attribute__((ext_vector_type(2)));
typedef float f32x4 __attribute__((ext_vector_type(4)));
typedef unsigned u32x2 __attribute__((ext_vector_type(2)));
typedef unsigned u32x4 __attribute__((ext_vector_type(4)));
typedef __bf16 bf16x2_t __attribute__((ext_vector_type(2)));

constexpr int S_ = 2048, D_ = 576, F_ = 4608;

__device__ __forceinline__ unsigned pk_bf16(float lo, float hi) {
    f32x2 v = {lo, hi};
    bf16x2_t b = __builtin_convertvector(v, bf16x2_t);
    return __builtin_bit_cast(unsigned, b);
}
__device__ __forceinline__ float bf_lo(unsigned w) { return __uint_as_float(w << 16); }
__device__ __forceinline__ float bf_hi(unsigned w) { return __uint_as_float(w & 0xffff0000u); }
__device__ __forceinline__ float rbf(float x) { return bf_lo(pk_bf16(x, 0.f)); }
__device__ __forceinline__ f32x4 mfma16(s16x8 a, s16x8 b, f32x4 c) {
    return __builtin_amdgcn_mfma_f32_16x16x32_bf16(a, b, c, 0, 0, 0);
}
#define WAIT_V(n) asm volatile("s_waitcnt vmcnt(" #n ")" ::: "memory")
#define BAR() do { __builtin_amdgcn_s_barrier(); asm volatile("" ::: "memory"); } while (0)
#define SCHED() __builtin_amdgcn_sched_barrier(0)
#define DIAG_DECL
#define MARK()
#define STAMP(i)
#define DIAG_END(wsbase, kid)
__device__ __forceinline__ void mfma_acc(f32x4& c, s16x8 a, s16x8 b) {
    asm volatile("v_mfma_f32_16x16x32_bf16 %0, %1, %2, %0" : "+v"(c) : "v"(a), "v"(b));
}
__device__ __forceinline__ void mfma_first(f32x4& c, s16x8 a, s16x8 b) {
    asm volatile("v_mfma_f32_16x16x32_bf16 %0, %1, %2, 0" : "=&v"(c) : "v"(a), "v"(b));
}
#define MFMA_DONE() do { asm volatile("s_nop 7\n\ts_nop 7" ::: "memory"); SCHED(); } while (0)
constexpr size_t WS_KV = 0;
constexpr size_t WS_H = WS_KV + (size_t)S_ * D_ * 2;
constexpr size_t WS_ST = WS_H + (size_t)S_ * D_ * 2;
constexpr size_t WS_CS = WS_ST + (size_t)S_ * 8;
constexpr size_t WS_G2 = WS_CS + (size_t)4 * F_ * 4;
constexpr size_t WS_F0 = WS_G2 + (size_t)S_ * 8;
#define FSP 128
constexpr size_t WS_EP = WS_F0 + 1280 * FSP;
constexpr size_t WS_HH = WS_EP + 1024;
constexpr size_t WS_X = WS_HH + (size_t)S_ * F_ * 2;
constexpr unsigned X_BYTES = 64 * 4 * 3 * 9216;
constexpr size_t WS_WT = WS_X + X_BYTES;

template <int N> struct IC { static constexpr int v = N; };
typedef __amdgpu_buffer_rsrc_t rsrc_t;
__device__ __forceinline__ rsrc_t mk_rsrc(const void* p, unsigned bytes) { return __builtin_amdgcn_make_buffer_rsrc((void*)p, 0, bytes, 0x00020000); }
__device__ __forceinline__ void st16_wt(rsrc_t r, unsigned off, u32x4 v) { __builtin_amdgcn_raw_buffer_store_b128(v, r, off, 0, 16); }
__device__ __forceinline__ void st8_wt(rsrc_t r, unsigned off, u32x2 v) { __builtin_amdgcn_raw_buffer_store_b64(v, r, off, 0, 16); }
template <int CTRL> __device__ __forceinline__ float dpp_add(float x) {
    return x + __uint_as_float(__builtin_amdgcn_update_dpp(0, (int)__float_as_uint(x), CTRL, 0xf, 0xf, true));
}
__device__ __forceinline__ float x16_add(float x) {
    auto r = __builtin_amdgcn_permlane16_swap(__float_as_uint(x), __float_as_uint(x), false, false);
    return __uint_as_float(r[0]) + __uint_as_float(r[1]);
}
__device__ __forceinline__ float x32_add(float x) {
    auto r = __builtin_amdgcn_permlane32_swap(__float_as_uint(x), __float_as_uint(x), false, false);
    return __uint_as_float(r[0]) + __uint_as_float(r[1]);
}
__device__ __forceinline__ float sum16(float x) {
    x = dpp_add<0xB1>(x); x = dpp_add<0x4E>(x); x = dpp_add<0x141>(x);
    return dpp_add<0x140>(x);
}
__device__ __forceinline__ float ln_sd(float var) { return rbf(__builtin_amdgcn_sqrtf(rbf(rbf(var) + 1e-5f))); }

template <int OFF>
__device__ __forceinline__ void dma16(unsigned voff, const void* sbase, unsigned m0v) {
    asm volatile("s_mov_b32 m0, %2\n\ts_nop 0\n\tglobal_load_lds_dwordx4 %0, %1 offset:%3"
                 :: "v"(voff), "s"(sbase), "s"(m0v), "i"(OFF) : "memory");
}
template <int OFF>
__device__ __forceinline__ void dma16_sc1(unsigned voff, const void* sbase, unsigned m0v) {
    asm volatile("s_mov_b32 m0, %2\n\ts_nop 0\n\tglobal_load_lds_dwordx4 %0, %1 offset:%3 sc1"
                 :: "v"(voff), "s"(sbase), "s"(m0v), "i"(OFF) : "memory");
}
template <int OFF>
__device__ __forceinline__ void dma16_q(unsigned voff, const void* sbase, unsigned m0v) {
    asm volatile("s_mov_b32 m0, %2\n\ts_mov_b64 exec, 0xffff\n\tglobal_load_lds_dwordx4 %0, %1 offset:%3\n\ts_mov_b64 exec, -1"
                 :: "v"(voff), "s"(sbase), "s"(m0v), "i"(OFF) : "memory");
}
__device__ __forceinline__ void dma_m36(unsigned voff, const void* sbase, unsigned m0v) {
    asm volatile("s_mov_b32 m0, %2\n\ts_mov_b32 exec_hi, 15\n\tglobal_load_lds_dwordx4 %0, %1\n\ts_mov_b64 exec, -1"
                 :: "v"(voff), "s"(sbase), "s"(m0v) : "memory");
}
__device__ __forceinline__ void dma4(unsigned voff, const void* sbase, unsigned m0v) {
    asm volatile("s_mov_b32 m0, %2\n\ts_nop 0\n\tglobal_load_lds_dword %0, %1" :: "v"(voff), "s"(sbase), "s"(m0v) : "memory");
}
#define SYM_ADDR(out, sym) asm volatile("s_getpc_b64 s[40:41]\n\ts_add_u32 s40, s40, " #sym "@rel32@lo+4\n\ts_addc_u32 s41, s41, " #sym "@rel32@hi+12\n\ts_mov_b64 %0, s[40:41]" \
                                        : "=s"(out) :: "s40", "s41", "scc")
template <int NI>
__device__ __forceinline__ void warm_code(const char* a, const char* e, int l0, int lane, unsigned m0v) {
    const unsigned last = e > a ? (unsigned)(e - a) - 4u : 0u;
#pragma unroll
    for (int i = 0; i < NI; ++i) dma4(min((unsigned)(l0 + i * 64 + lane) * 128u, last), a, m0v);
}
__device__ __forceinline__ s16x8 tr_frag(const LAS char* p) {
    const s16x4 lo = __builtin_amdgcn_ds_read_tr16_b64_v4i16((LAS s16x4*)p);
    const s16x4 hi = __builtin_amdgcn_ds_read_tr16_b64_v4i16((LAS s16x4*)(p + 16 * 288));
    return __builtin_shufflevector(lo, hi, 0, 1, 2, 3, 4, 5, 6, 7);
}
#define HELPER_DELAY 24
#define POLL0 24
#define POLL1 2
constexpr int C1_COPY = 4288;
constexpr int C1_FILT = 4 * C1_COPY;
constexpr int C1_KV = 2304 * 2;
constexpr int C1_KVOFF = 4 * C1_FILT + 64;
constexpr int C1_YOFF = C1_KVOFF + 4 * C1_KV;
constexpr int C1_XOFF = C1_YOFF + 16384;
constexpr int C1_SOFF = C1_XOFF + 16384;
constexpr int C1_TOFF = C1_SOFF + 16384;
constexpr int C1_LDS = C1_TOFF + 2048;

extern "C" __global__ __launch_bounds__(512, 2) void k1_conv(const u16* __restrict__ x, const u16* __restrict__ kin, const u16* __restrict__ vin,
                                                  const u16* __restrict__ shift_k, const u16* __restrict__ ssm_k, const u16* __restrict__ ssm_D,
                                                  const u16* __restrict__ wg, const u16* __restrict__ wu, const u16* __restrict__ wd,
                                                  char* __restrict__ ws) {
    extern __shared__ __attribute__((aligned(16))) char smem[];
    const int tid = threadIdx.x, lane = tid & 63;
    const int wid = __builtin_amdgcn_readfirstlane(tid >> 6);
    const int b = blockIdx.x, xcd = b & 7, idx = b >> 3;
    {
        const char *c1, *c3;
        SYM_ADDR(c1, k1_conv); SYM_ADDR(c3, k3_gemm1);
        if (wid >= 4) warm_code<1>(c1, c3, (wid & 3) * 64, lane, (unsigned)(size_t)(LAS char*)smem + C1_TOFF + 256 + (wid & 3) * 256);
    }
    DIAG_DECL;
    asm volatile("" :: "s"(x), "s"(kin), "s"(vin), "s"(shift_k), "s"(ssm_k), "s"(ssm_D), "s"(wg), "s"(wu), "s"(wd), "s"(ws));
    const bool conv = idx < 18;
    const int cg = xcd * 18 + idx, ch0 = cg * 4;
    const unsigned lds0 = (unsigned)(size_t)(LAS char*)smem;
    LAS unsigned* tagp = (LAS unsigned*)(smem + C1_TOFF);
    auto get_tag = [&]() -> unsigned { BAR(); return __builtin_amdgcn_readfirstlane(*(volatile LAS unsigned*)tagp); };

    const int h = (idx - 18) * 8 + xcd, hc_ = h < 108 ? h : 0;
    const int m3 = hc_ % 3, lcA = (hc_ / 3) * 4 + m3, rb0 = m3 * 3;
    auto cs_load = [&](u32x4 (&v)[12]) {
        const int rs = tid >> 3, c8 = tid & 7;
#pragma unroll
        for (int p = 0; p < 12; ++p) {
            const int rb = rb0 + p, lc = lcA + (rb >= 9), r = (rb >= 9 ? rb - 9 : rb) * 64 + rs;
            v[p] = *(const u32x4*)((lc >= 72 ? wu : wg) + (size_t)r * F_ + (lc % 72) * 64 + c8 * 8);
        }
        SCHED();
        const rsrc_t rx = mk_rsrc(ws + WS_X, X_BYTES);
#pragma unroll
        for (int i = 0; i < 8; ++i) st16_wt(rx, (unsigned)((i * 112 + h) * 8192 + tid * 16), (u32x4){~0u, ~0u, ~0u, ~0u});
        SCHED();
    };
    auto cs_finish = [&](u32x4 (&v)[12]) {
        float g[4][8];
#pragma unroll
        for (int q = 0; q < 4; ++q) {
#pragma unroll
            for (int e = 0; e < 4; ++e) {
                g[q][2 * e] = (bf_lo(v[3 * q][e]) + bf_lo(v[3 * q + 1][e])) + bf_lo(v[3 * q + 2][e]);
                g[q][2 * e + 1] = (bf_hi(v[3 * q][e]) + bf_hi(v[3 * q + 1][e])) + bf_hi(v[3 * q + 2][e]);
            }
        }
        LAS float* red = (LAS float*)smem;
#pragma unroll
        for (int l = 0; l < 2; ++l) {
            float a[8];
#pragma unroll
            for (int e = 0; e < 8; ++e) {
                a[e] = l == 0 ? g[0][e] + (m3 <= 1 ? g[1][e] : 0.f) + (m3 == 0 ? g[2][e] : 0.f) : g[3][e] + (m3 >= 1 ? g[2][e] : 0.f) + (m3 == 2 ? g[1][e] : 0.f);
                a[e] = x32_add(x16_add(dpp_add<0x128>(a[e])));
            }
            if (lane < 8) {
                *(LAS f32x4*)(red + (wid * 2 + l) * 64 + lane * 8) = (f32x4){a[0], a[1], a[2], a[3]};
                *(LAS f32x4*)(red + (wid * 2 + l) * 64 + lane * 8 + 4) = (f32x4){a[4], a[5], a[6], a[7]};
            }
        }
        asm volatile("s_waitcnt lgkmcnt(0)" ::: "memory"); BAR();
        if (tid < 192 && h < 108) {
            float s = 0.f;
            if (tid < 128) {
#pragma unroll
                for (int w = 0; w < 8; ++w) s += red[w * 128 + tid];
            }
            const int part = tid >> 6;
            const int lc = part == 0 ? lcA : (part == 1 ? lcA + 1 : (m3 == 0 ? lcA : lcA + 1));
            const int slot = part == 0 ? (m3 != 0) : (part == 1 ? 0 : 1);
            if (part < 2 || m3 != 1)
                __builtin_amdgcn_raw_buffer_store_b32(__float_as_uint(s), mk_rsrc(ws + WS_CS, 4 * F_ * 4), (unsigned)((slot * 2 * F_ + lc * 64 + (tid & 63)) * 4), 0, 16);
        }
    };
    auto wait_kv = [&](unsigned tag) {
        const rsrc_t rf = mk_rsrc(ws + WS_F0 + ((idx >> 2) * 8 + xcd) * 32 * FSP, 32 * FSP);
        __builtin_amdgcn_s_sleep(POLL0);
        for (int spin = 0; spin < (1 << 20); ++spin) {
            const unsigned f = __builtin_amdgcn_raw_buffer_load_b32(rf, (lane & 31) * FSP, 0, 16);
            if (__all(f == tag)) break;
            __builtin_amdgcn_s_sleep(POLL1);
        }
    };

    auto x_rows = [&]() {
        u32x2 xr[8];
#pragma unroll
        for (int i = 0; i < 8; ++i) xr[i] = *(const u32x2*)(x + (size_t)((tid - 256) + (wid - 4) * 448 + i * 64) * D_ + ch0);
#pragma unroll
        for (int i = 0; i < 8; ++i) *(LAS u32x2*)(smem + C1_XOFF + ((tid - 256) + (wid - 4) * 448 + i * 64) * 8) = xr[i];
        SCHED();
    };
    unsigned tag;
    unsigned dvv = 0;
    auto upper = [&](auto is_stat, auto is_conv) {
        constexpr bool STAT = decltype(is_stat)::v, CONV = decltype(is_conv)::v;
        const int srow = b * 8 + (wid - 5) * 4 + (lane >> 4), t3 = tid - 320;
        unsigned xw[18], epv = 0;
        if constexpr (STAT) {
            const unsigned* xr = (const unsigned*)(x + (size_t)srow * D_) + (lane & 15);
#pragma unroll
            for (int j = 0; j < 18; ++j) xw[j] = xr[16 * j];
        } else {
            epv = __hip_atomic_load((const unsigned*)(ws + WS_EP), __ATOMIC_RELAXED, __HIP_MEMORY_SCOPE_AGENT);
        }
        SCHED();
        if constexpr (STAT) {
            const char *c3, *c4;
            SYM_ADDR(c3, k3_gemm1); SYM_ADDR(c4, k4_gemm2);
            if ((idx & 7) == 0) warm_code<1>(c3, c4, (wid - 5) * 64, lane, lds0 + C1_TOFF + 256 + (wid - 5) * 256);
        }
        SCHED();
        u32x4 v[12], fa[6]; u32x2 fb[6];
        if constexpr (CONV) {
#pragma unroll
            for (int i = 0; i < 6; ++i) {
                const int task = min(t3 + 192 * i, 1023), u = task & 255;
                const u16* src = ssm_k + (size_t)(ch0 + (task >> 8)) * S_ + u * 8;
                fa[i] = *(const u32x4*)src;
                fb[i] = *(const u32x2*)(src + (u < 255 ? 8 : 0));
            }
            SCHED();
        }
        if constexpr (STAT) {
            f32x2 xf[18], s = {0.f, 0.f};
#pragma unroll
            for (int j = 0; j < 18; ++j) { xf[j] = (f32x2){bf_lo(xw[j]), bf_hi(xw[j])}; s += xf[j]; }
            const float mean = sum16(s.x + s.y) * (1.f / D_);
            f32x2 q = {0.f, 0.f};
#pragma unroll
            for (int j = 0; j < 18; ++j) { const f32x2 d = xf[j] - mean; q = __builtin_elementwise_fma(d, d, q); }
            const unsigned sgr = pk_bf16(mean, ln_sd(sum16(q.x + q.y) * (1.f / D_)));
            tag = get_tag();
            const unsigned so = (lane & 15) == 0 ? (unsigned)srow * 8 : 0x80000000u;
            st8_wt(mk_rsrc(ws + WS_ST, S_ * 8), so, (u32x2){sgr, tag});
            st8_wt(mk_rsrc(ws + WS_G2, S_ * 8), so, (u32x2){0u, 0u});
        } else {
            *tagp = epv + 1u;
            asm volatile("s_waitcnt lgkmcnt(0)" ::: "memory");
            tag = get_tag();
        }
        SCHED();
        if constexpr (!CONV) {
            __builtin_amdgcn_s_sleep(HELPER_DELAY);
            cs_load(v);
            if (!STAT && h == 111) {
                const rsrc_t rf = mk_rsrc(ws + WS_F0, 256 * FSP);
                for (int spin = 0; spin < (1 << 20); ++spin) {
                    bool ok = true;
#pragma unroll
                    for (int i = 0; i < 4; ++i) ok &= __builtin_amdgcn_raw_buffer_load_b32(rf, (i * 64 + lane) * FSP, 0, 16) == tag;
                    if (__all(ok)) break;
                    __builtin_amdgcn_s_sleep(8);
                }
                if (lane == 0) __hip_atomic_store((unsigned*)(ws + WS_EP), tag, __ATOMIC_RELAXED, __HIP_MEMORY_SCOPE_AGENT);
            }
            cs_finish(v);
            return;
        }
        if (t3 < 80) {
            const int cpy = t3 / 5, e = t3 % 5, q = e < 3 ? e : 257 + e;
            *(LAS u32x4*)(smem + cpy * C1_COPY + q * 16) = (u32x4){0u, 0u, 0u, 0u};
        } else if (t3 < 84) {
            *(LAS u32x4*)(smem + C1_KVOFF - 64 + (t3 - 80) * 16) = (u32x4){0u, 0u, 0u, 0u};
        }
        if (t3 < 128) *(LAS u32x4*)(smem + C1_KVOFF + (t3 >> 5) * C1_KV + 4096 + (t3 & 31) * 16) = (u32x4){0u, 0u, 0u, 0u};
#pragma unroll
        for (int i = 0; i < 6; ++i) {
            const int task = min(t3 + 192 * i, 1023), u = task & 255;
            const unsigned d[6] = {fa[i].x, fa[i].y, fa[i].z, fa[i].w, u < 255 ? fb[i].x : 0u, u < 255 ? fb[i].y : 0u};
            unsigned o[5];
#pragma unroll
            for (int e = 0; e < 5; ++e) o[e] = __builtin_amdgcn_alignbit(d[e + 1], d[e], 16);
            LAS char* base = (LAS char*)smem + (task >> 8) * C1_FILT + (u + 4) * 16;
            *(LAS u32x4*)(base) = (u32x4){o[0], o[1], o[2], o[3]};
            *(LAS u32x4*)(base + C1_COPY) = (u32x4){d[1], d[2], d[3], d[4]};
            *(LAS u32x4*)(base + 2 * C1_COPY) = (u32x4){o[1], o[2], o[3], o[4]};
            *(LAS u32x4*)(base + 3 * C1_COPY) = (u32x4){d[2], d[3], d[4], d[5]};
            if (u == 0) {
                *(LAS u32x4*)(base - 16) = (u32x4){0u, 0u, 0u, d[0] << 16};
                *(LAS u32x4*)(base - 16 + C1_COPY) = (u32x4){0u, 0u, 0u, d[0]};
                *(LAS u32x4*)(base - 16 + 2 * C1_COPY) = (u32x4){0u, 0u, d[0] << 16, o[0]};
                *(LAS u32x4*)(base - 16 + 3 * C1_COPY) = (u32x4){0u, 0u, d[0], d[1]};
            }
        }
        asm volatile("s_waitcnt lgkmcnt(0)" ::: "memory"); BAR();
        x_rows();
    };
    if (wid >= 5) {
        if (!conv) {
            if (wid < 7) upper(IC<1>{}, IC<0>{}); else upper(IC<0>{}, IC<0>{});
            DIAG_END(ws, 0);
            return;
        }
        if (wid < 7) upper(IC<1>{}, IC<1>{}); else upper(IC<0>{}, IC<1>{});
    } else {
        {
            unsigned kw[11], vw[8];
            const int t0 = idx * 64 + (lane >> 3) * 8, dc = wid * 8 + (lane & 7), kch = xcd * 72 + (dc < 36 ? dc : 35) * 2;
            const rsrc_t rk = mk_rsrc(kin, S_ * D_ * 2), rv = mk_rsrc(vin, S_ * D_ * 2);
            const int off = t0 * (D_ * 2) + kch * 2;
#pragma unroll
            for (int j = 0; j < 11; ++j) kw[j] = __builtin_amdgcn_raw_buffer_load_b32(rk, (unsigned)(off + (j - 3) * (D_ * 2)), 0, 0);
#pragma unroll
            for (int j = 0; j < 8; ++j) vw[j] = __builtin_amdgcn_raw_buffer_load_b32(rv, (unsigned)(off + j * (D_ * 2)), 0, 0);
            const u32x4 sw = *(const u32x4*)(shift_k + kch * 4);
            dvv = ssm_D[(conv ? ch0 : 0) + (wid & 3)];
            SCHED();
            STAMP(1);
            const f32x2 w0 = {bf_lo(sw.x), bf_lo(sw.z)}, w1 = {bf_hi(sw.x), bf_hi(sw.z)}, w2 = {bf_lo(sw.y), bf_lo(sw.w)}, w3 = {bf_hi(sw.y), bf_hi(sw.w)};
            f32x2 kf[11], o[8];
#pragma unroll
            for (int j = 0; j < 11; ++j) kf[j] = (f32x2){bf_lo(kw[j]), bf_hi(kw[j])};
#pragma unroll
            for (int j = 0; j < 8; ++j) {
                f32x2 s = kf[j] * w3;
                s = __builtin_elementwise_fma(kf[j + 1], w2, s);
                s = __builtin_elementwise_fma(kf[j + 2], w1, s);
                s = __builtin_elementwise_fma(kf[j + 3], w0, s);
                o[j] = s * (f32x2){bf_lo(vw[j]), bf_hi(vw[j])};
            }
            const rsrc_t ro = mk_rsrc(ws + WS_KV, S_ * D_ * 2);
            const unsigned oo = dc < 36 ? (unsigned)(kch * (S_ * 2) + (2040 - t0) * 2) : 0x80000000u;
            st16_wt(ro, oo, (u32x4){pk_bf16(o[7].x, o[6].x), pk_bf16(o[5].x, o[4].x), pk_bf16(o[3].x, o[2].x), pk_bf16(o[1].x, o[0].x)});
            st16_wt(ro, oo + S_ * 2, (u32x4){pk_bf16(o[7].y, o[6].y), pk_bf16(o[5].y, o[4].y), pk_bf16(o[3].y, o[2].y), pk_bf16(o[1].y, o[0].y)});
        }
        tag = get_tag();
        WAIT_V(0);
        if (lane == 0) __hip_atomic_store((unsigned*)(ws + WS_F0 + ((wid * 8 + xcd) * 32 + idx) * FSP), tag, __ATOMIC_RELAXED, __HIP_MEMORY_SCOPE_AGENT);
        MARK();
        STAMP(2);
        if (!conv) {
            u32x4 v[12];
            cs_load(v);
            cs_finish(v);
            DIAG_END(ws, 0);
            return;
        }
        if (wid == 4) {
            x_rows();
            BAR();
#pragma unroll
            for (int i = 0; i < 5; ++i)
                dma4((unsigned)min(i * 64 + lane, 287) * 128u, (const char*)wd + (size_t)cg * 288 * 128, lds0 + C1_TOFF + 256);
            __builtin_amdgcn_s_sleep(36);
            const char* gs = ws + WS_ST;
#pragma unroll
            for (int i = 0; i < 4; ++i) {
                dma16_sc1<0>(lane * 16, gs + i * 4096, lds0 + C1_SOFF + i * 4096); dma16_sc1<1024>(lane * 16, gs + i * 4096, lds0 + C1_SOFF + i * 4096);
                dma16_sc1<2048>(lane * 16, gs + i * 4096, lds0 + C1_SOFF + i * 4096); dma16_sc1<3072>(lane * 16, gs + i * 4096, lds0 + C1_SOFF + i * 4096);
            }
            WAIT_V(0);
        } else {
        {
            wait_kv(tag);
            STAMP(7);
            const char* kvs = ws + WS_KV + (size_t)(ch0 + wid) * (S_ * 2);
            const unsigned m = lds0 + C1_KVOFF + wid * C1_KV;
            dma16_sc1<0>(lane * 16, kvs, m); dma16_sc1<1024>(lane * 16, kvs, m); dma16_sc1<2048>(lane * 16, kvs, m); dma16_sc1<3072>(lane * 16, kvs, m);
            WAIT_V(0);
            STAMP(9);
            BAR();
            STAMP(4);
        }
        const int row = lane & 15, kg = lane >> 4;
        const LAS char* abase = (const LAS char*)smem + wid * C1_FILT + (row & 3) * C1_COPY + (24 + 4 * (row >> 2) - 8 * kg) * 2;
        const LAS char* bbase = (const LAS char*)smem + C1_KVOFF + wid * C1_KV + (2040 - 16 * row - 8 * kg) * 2 - 512 * 7 - 448;
        auto rd_ah = [&](int r, int L, int hf) -> s16x4 { return *(const volatile LAS s16x4*)(abase + 512 * L - 64 * r + 8 * hf); };
        auto rd_b = [&](int r, int m) -> s16x8 { return *(const LAS s16x8*)(bbase + 448 - 64 * r + 512 * (7 - m)); };
        auto cat = [&](s16x4 lo, s16x4 hi) -> s16x8 { return __builtin_shufflevector(lo, hi, 0, 1, 2, 3, 4, 5, 6, 7); };
        f32x4 acc[8];
        s16x4 al[2][8], ah[2][8];
        s16x8 bf[2][8], bd[8];
        auto rd_k = [&](int r, int k) {
            if (k < 2) { if (k == 0) al[r & 1][0] = rd_ah(r, 1, 0); else ah[r & 1][0] = rd_ah(r, 1, 1); }
            else if (k < 10) bf[r & 1][9 - k] = rd_b(r, 8 - k);
            else { const int L = (k - 10) / 2 + 2; if (k & 1) ah[r & 1][L - 1] = rd_ah(r, L, 1); else al[r & 1][L - 1] = rd_ah(r, L, 0); }
        };
        const s16x8 a0 = cat(rd_ah(0, 0, 0), rd_ah(0, 0, 1));
#pragma unroll
        for (int e = 0; e < 8; ++e) bd[e] = rd_b(0, e);
        s16x8 dfr;
        {
            const int j = 8 * kg + 7 - row;
            u32x4 w;
#pragma unroll
            for (int e = 0; e < 4; ++e) w[e] = j == 2 * e ? dvv : (j == 2 * e + 1 ? dvv << 16 : 0u);
            dfr = __builtin_bit_cast(s16x8, w);
            asm volatile("s_nop 1" : "+v"(dfr));
        }
        SCHED();
#pragma unroll
        for (int n = 0; n < 16; ++n) {
            if (n < 8) mfma_first(acc[n], a0, bd[n]); else mfma_acc(acc[n - 8], dfr, bd[n - 8]);
            SCHED();
            if (n == 0) { rd_k(0, 0); rd_k(0, 1); }
            else if (n == 1) rd_k(0, 9);
            else rd_k(0, n + 8);
            SCHED();
        }
#pragma unroll
        for (int r = 0; r < 8; ++r) {
            int k = 0;
#pragma unroll
            for (int L = 1; L <= 8; ++L) {
#pragma unroll
                for (int n = 7; n >= L - 1; --n) {
                    const int e = n - L + 1;
                    mfma_acc(acc[n], cat(al[r & 1][L - 1], ah[r & 1][L - 1]), (r == 0 && e > 0) ? bd[e - 1] : bf[r & 1][e]);
                    SCHED();
                    if (r < 7 && k < 24) rd_k(r + 1, k);
                    ++k;
                    SCHED();
                }
            }
        }
        MFMA_DONE();
#pragma unroll
        for (int n = 0; n < 8; ++n)
            *(LAS u32x2*)(smem + C1_YOFF + wid * 4096 + (256 * n + 16 * row + 4 * kg) * 2) = (u32x2){pk_bf16(acc[n][0], acc[n][1]), pk_bf16(acc[n][2], acc[n][3])};
        }
    }
    STAMP(5);
    asm volatile("s_waitcnt lgkmcnt(0)" ::: "memory"); BAR();
    STAMP(6);
    {
        u32x2 yc[4];
#pragma unroll
        for (int c = 0; c < 4; ++c) yc[c] = *(const LAS u32x2*)(smem + C1_YOFF + c * 4096 + tid * 8);
        const u32x4 xa = *(const LAS u32x4*)(smem + C1_XOFF + tid * 32), xb = *(const LAS u32x4*)(smem + C1_XOFF + tid * 32 + 16);
        const u32x2 xr[4] = {{xa.x, xa.y}, {xa.z, xa.w}, {xb.x, xb.y}, {xb.z, xb.w}};
        u32x4 s01 = *(const LAS u32x4*)(smem + C1_SOFF + tid * 32), s23 = *(const LAS u32x4*)(smem + C1_SOFF + tid * 32 + 16);
        if (__builtin_expect(!__all(s01.y == tag && s01.w == tag && s23.y == tag && s23.w == tag), 0)) {
            const rsrc_t rs = mk_rsrc(ws + WS_ST, S_ * 8);
            for (int spin = 0; spin < (1 << 20); ++spin) {
                s01 = __builtin_amdgcn_raw_buffer_load_b128(rs, tid * 32, 0, 16);
                s23 = __builtin_amdgcn_raw_buffer_load_b128(rs, tid * 32 + 16, 0, 16);
                if (__all(s01.y == tag && s01.w == tag && s23.y == tag && s23.w == tag)) break;
                __builtin_amdgcn_s_sleep(2);
            }
        }
        const unsigned sv[4] = {s01.x, s01.z, s23.x, s23.z};
        float mm[4], rr[4];
#pragma unroll
        for (int j = 0; j < 4; ++j) { mm[j] = bf_lo(sv[j]); rr[j] = __builtin_amdgcn_rcpf(bf_hi(sv[j])); }
        unsigned hw[8];
#pragma unroll
        for (int j = 0; j < 4; ++j) {
#pragma unroll
            for (int e = 0; e < 2; ++e) {
                const unsigned xw = e ? xr[j].y : xr[j].x;
                const unsigned ya = (j >> 1) ? yc[2 * e].y : yc[2 * e].x, yb = (j >> 1) ? yc[2 * e + 1].y : yc[2 * e + 1].x;
                const float y0 = (j & 1) ? bf_hi(ya) : bf_lo(ya), y1 = (j & 1) ? bf_hi(yb) : bf_lo(yb);
                const float x0 = bf_lo(xw), x1 = bf_hi(xw);
                hw[2 * j + e] = pk_bf16(__builtin_fmaf((x0 - mm[j]) * rr[j], y0, x0), __builtin_fmaf((x1 - mm[j]) * rr[j], y1, x1));
            }
        }
        const rsrc_t rh = mk_rsrc(ws + WS_H, S_ * D_ * 2);
        const unsigned ho = (unsigned)(((tid >> 6) * 144 + cg) * 2048 + (tid & 63) * 32);
        st16_wt(rh, ho, (u32x4){hw[0], hw[1], hw[2], hw[3]});
        st16_wt(rh, ho + 16, (u32x4){hw[4], hw[5], hw[6], hw[7]});
    }
    STAMP(8);
    DIAG_END(ws, 0);
}

constexpr int ST_BYTES = 16384 + 2 * 9216;
constexpr int NBUF = 4;
constexpr int G_LDS = NBUF * ST_BYTES;

struct TokC { float nm, c0, c1, q; };
__device__ __forceinline__ unsigned geglu2(f32x2 ag, f32x2 au, f32x2 csg, f32x2 csu, const TokC& t) {
    const f32x2 nm = {t.nm, t.nm}, c0 = {t.c0, t.c0}, c1 = {t.c1, t.c1}, q = {t.q, t.q};
    const f32x2 g = __builtin_elementwise_fma(nm, csg, ag), u = __builtin_elementwise_fma(nm, csu, au);
    const f32x2 z = g * __builtin_elementwise_fma(g * g, c1, c0);
    const f32x2 e = {__builtin_amdgcn_exp2f(z.x), __builtin_amdgcn_exp2f(z.y)};
    const f32x2 d = __builtin_elementwise_fma(e, q, q);
    const f32x2 r = (g * u) * (f32x2){__builtin_amdgcn_rcpf(d.x), __builtin_amdgcn_rcpf(d.y)};
    return pk_bf16(r.x, r.y);
}
#define LO2(v) __builtin_shufflevector(v, v, 0, 1)
#define HI2(v) __builtin_shufflevector(v, v, 2, 3)
__device__ __forceinline__ float dot2(unsigned a, unsigned b, float c) {
    return __builtin_amdgcn_fdot2_f32_bf16(__builtin_bit_cast(bf16x2_t, a), __builtin_bit_cast(bf16x2_t, b), c, false);
}
#define WAIT_VI(N) asm volatile("s_waitcnt vmcnt(%0)" :: "i"(N) : "memory")

constexpr int K3_WS = G_LDS;
constexpr int K3_LDS = K3_WS + 8 * 3072;
constexpr int K3_SP = K3_WS;
constexpr int K3_CS = 2 * ST_BYTES;
extern "C" __global__ __launch_bounds__(512, 2) void k3_gemm1(const u16* __restrict__ wg, const u16* __restrict__ wu, const u16* __restrict__ wd,
                                                   char* __restrict__ ws) {
    extern __shared__ __attribute__((aligned(16))) char smem[];
    LAS char* lds = (LAS char*)smem;
    const unsigned lds0 = (unsigned)(size_t)lds;
    const int tid = threadIdx.x, lane = tid & 63;
    const int wid = __builtin_amdgcn_readfirstlane(tid >> 6);
    const int wr = wid & 3, wc = wid >> 2;
    const int b = blockIdx.x, xcd = b & 7, idx = b >> 3;
    const int rt = (xcd >> 2) * 4 + (idx & 3), ct = (xcd & 3) * 8 + (idx >> 2);
    asm volatile("" :: "s"(wg), "s"(wu), "s"(wd), "s"(ws));
    DIAG_DECL;

    const char* asrc = ws + WS_H + (size_t)rt * 144 * 2048 + wid * 2048;
    const unsigned aoff = (unsigned)(lane ^ ((wid & 2) << 2)) * 16;
    const unsigned ma0 = lds0 + wid * 2048, mw0 = lds0 + 16384 + wc * 9216 + wr * 2304;
    dma16<0>(aoff, asrc, ma0); dma16<1024>(aoff, asrc, ma0);
    const char* wsrc = (const char*)(wc ? wu : wg) + ct * 288 - 2048;
    auto wofs = [&](int i) -> unsigned {
        const int q = lane + 64 * i, lrow = 8 * wr + q / 18, pos = q % 18;
        const int krow = ((lrow >> 2) & 3) * 8 + (lrow >> 4) * 4 + (lrow & 3);
        const int col = pos * 8 + (pos < 8 ? 0 : (pos < 16 ? 8 : (((pos & 1) ^ wc) ? 136 : 64) - pos * 8));
        return (unsigned)(krow * (F_ * 2) + col * 2 + 2048 - 1024 * i);
    };
    const unsigned w0 = wofs(0), w1 = wofs(1), w2 = wofs(2);
    auto piece = [&](int P, int s) {
        const unsigned bo = (s & 3) * ST_BYTES;
        const char* wp = wsrc + (size_t)s * (32 * F_ * 2);
        if (P == 0) dma16<0>(aoff, asrc + (size_t)s * 16384, ma0 + bo);
        if (P == 1) dma16<1024>(aoff, asrc + (size_t)s * 16384, ma0 + bo);
        if (P == 2) dma16<0>(w0, wp, mw0 + bo);
        if (P == 3) dma16<1024>(w1, wp, mw0 + bo);
        if (P == 4) dma16_q<2048>(w2, wp, mw0 + bo);
    };
#define K3_PIECE(P, s) piece(P, s)
#define K3_STAGE(s) do { K3_PIECE(0, s); K3_PIECE(1, s); K3_PIECE(2, s); K3_PIECE(3, s); K3_PIECE(4, s); } while (0)
    K3_PIECE(2, 0); K3_PIECE(3, 0); K3_PIECE(4, 0);
    BAR();
    K3_STAGE(1);
    u32x2 hs[4];
    {
        const char* hsrc = ws + WS_H + (size_t)rt * 144 * 2048 + ct * 64;
        const unsigned o = (unsigned)((wid * 18 + (lane >> 3)) * 2048 + (lane & 7) * 8);
        asm volatile("global_load_dwordx2 %0, %1, %2" : "=v"(hs[0]) : "v"(o), "s"(hsrc) : "memory");
        asm volatile("global_load_dwordx2 %0, %1, %2" : "=v"(hs[1]) : "v"(o + 16384), "s"(hsrc) : "memory");
        asm volatile("global_load_dwordx2 %0, %1, %2" : "=v"(hs[2]) : "v"((unsigned)((wid * 18 + 16 + ((lane >> 3) & 1)) * 2048 + (lane & 7) * 8)), "s"(hsrc) : "memory");
        asm volatile("global_load_dwordx2 %0, %1, %2" : "=v"(hs[3]) : "v"((unsigned)(lane & 7) * 8), "s"(hsrc) : "memory");
    }

    const int g4 = lane >> 4, q4 = (lane & 15) >> 2, p4 = lane & 3;
    const int rowoff = (4 * g4 + q4) * 288;
    const int tbase = g4 * 4096 + wr * 512 + (lane & 15) * 8;
    const int toff0 = tbase + ((2 * wc) ^ (g4 & 1)) * 128, toff1 = tbase + ((2 * wc + 1) ^ (g4 & 1)) * 128;
    const int tstep = wc ? -256 : 256;
    const int foff = 16384 + rowoff + wc * 128 + p4 * 8;
    const int moff = 16384 + rowoff + 256 + (p4 < 2 ? 16 * wc + 8 * p4 : 9216 + 16 * (1 - wc) + 8 * (p4 - 2));

    f32x4 acc[9][4];
    s16x8 wf[9], tf[2][4];
    auto rd_w = [&](const LAS char* sb, int j) -> s16x8 {
        return j < 8 ? tr_frag(sb + foff + (j >> 2) * 9216 + (j & 3) * 32) : tr_frag(sb + moff);
    };
    auto rd_t = [&](const LAS char* sb, int i) -> s16x8 {
        const LAS char* p = sb + ((i & 1) ? toff1 : toff0) + (i >> 1) * tstep;
        const s16x4 lo = *(const volatile LAS s16x4*)p, hi = *(const volatile LAS s16x4*)(p + 2048);
        return __builtin_shufflevector(lo, hi, 0, 1, 2, 3, 4, 5, 6, 7);
    };

    const int un = (b < 216 ? b : b - 216) * 8 + wid, uB = un / 3, ut = un % 3;
    auto wd_dma = [&](int i) {
        const int kh = uB & 1, st = (uB >> 1) % 18, kn = uB / 36;
        const bool full = st < 16;
        const int f0 = (kn >> 2) * 1152 + (full ? (st & 7) * 144 + kh * 72 + (st >> 3) * 32 : ((st - 16) * 2 + kh) * 288);
        const char* src = (const char*)wd + (size_t)f0 * (D_ * 2) + (kn & 3) * 288 + ut * 96;
        const int q = lane + 64 * i, r = q / 6, pos = q % 6, kg = (r & 15) >> 2;
        const int f = full ? r : (kg >> 1) * 144 + (kg & 1) * 72 + 64 + (r & 3) + 4 * (r >> 4);
        dma16<0>((unsigned)(f * (D_ * 2) + pos * 16), src, lds0 + K3_WS + wid * 3072 + i * 1024);
    };

    STAMP(1);
    WAIT_V(9); BAR();
    STAMP(2);
    wf[0] = rd_w(lds, 0);
#pragma unroll
    for (int i = 0; i < 4; ++i) tf[0][i] = rd_t(lds, i);
#pragma unroll
    for (int j = 1; j < 9; ++j) wf[j] = rd_w(lds, j);
    SCHED();
    STAMP(6);
    {
        const LAS char* sb = lds + ST_BYTES;
        float hc = 0.f;
#pragma unroll
        for (int j = 0; j < 9; ++j) {
#pragma unroll
            for (int i = 0; i < 4; ++i) mfma_first(acc[j][i], wf[j], tf[0][i]);
            SCHED();
            if (j == 0) { K3_PIECE(0, 2); K3_PIECE(1, 2); }
            if (j == 1) { K3_PIECE(2, 2); K3_PIECE(3, 2); }
            if (j == 2) K3_PIECE(4, 2);
            if (j == 3) {
                asm volatile("s_waitcnt vmcnt(5)" : "+v"(hs[0]), "+v"(hs[1]), "+v"(hs[2]), "+v"(hs[3]) :: "memory");
                hc = bf_lo(hs[3].x);
                float s1 = 0.f, s2 = 0.f;
#pragma unroll
                for (int i = 0; i < 3; ++i) {
                    const float d[4] = {bf_lo(hs[i].x) - hc, bf_hi(hs[i].x) - hc, bf_lo(hs[i].y) - hc, bf_hi(hs[i].y) - hc};
                    float a = (d[0] + d[1]) + (d[2] + d[3]), q = __builtin_fmaf(d[0], d[0], d[1] * d[1]) + __builtin_fmaf(d[2], d[2], d[3] * d[3]);
                    if (i == 2 && lane >= 16) { a = 0.f; q = 0.f; }
                    s1 += a; s2 += q;
                }
                s1 = x32_add(x16_add(dpp_add<0x128>(s1))); s2 = x32_add(x16_add(dpp_add<0x128>(s2)));
                if (lane < 8) *(LAS f32x2*)(lds + K3_SP + (lane * 8 + wid) * 8) = (f32x2){s1, s2};
                asm volatile("s_waitcnt lgkmcnt(0)" ::: "memory");
                BAR();
                wf[0] = rd_w(sb, 0); wf[1] = rd_w(sb, 1);
                tf[1][0] = rd_t(sb, 0); tf[1][1] = rd_t(sb, 1);
            }
            if (j == 4) { tf[1][2] = rd_t(sb, 2); tf[1][3] = rd_t(sb, 3); wf[2] = rd_w(sb, 2); wf[3] = rd_w(sb, 3); }
            if (j >= 4) { wf[j] = rd_w(sb, j); K3_PIECE(j - 4, 3); }
            if (j == 8) {
                const f32x2 pp = *(const LAS f32x2*)(lds + K3_SP + (wid * 8 + (lane & 7)) * 8);
                const float s1 = dpp_add<0x141>(dpp_add<0x4E>(dpp_add<0xB1>(pp.x))) * (1.f / D_);
                const float s2 = dpp_add<0x141>(dpp_add<0x4E>(dpp_add<0xB1>(pp.y))) * (1.f / D_);
                const float c0 = __uint_as_float(__builtin_amdgcn_readlane(__float_as_uint(hc), wid));
                const float sd = ln_sd(__builtin_fmaxf(__builtin_fmaf(-s1, s1, s2), 0.f));
                if (lane == 0) st8_wt(mk_rsrc(ws + WS_G2, S_ * 8), (unsigned)((rt * 256 + ct * 8 + wid) * 8), (u32x2){pk_bf16(c0 + s1, sd), 1u});
            }
            SCHED();
        }
    }

    const rsrc_t rg = mk_rsrc(ws + WS_G2 + (size_t)(rt * 256 + wr * 64) * 8, 512);
    u32x2 gr[4];
    u32x4 wtv;
    s16x4 tl;
#define TP 1
#define DPJ0 0
#define DPJ1 5
    auto iter = [&](auto mode, auto vm, int s, s16x8 (&tc)[4], s16x8 (&tn)[4]) {
        constexpr int MODE = decltype(mode)::v;
        if constexpr (MODE < 4) {
            if constexpr (MODE == 3) WAIT_V(0); else WAIT_VI(5 + decltype(vm)::v);
            BAR();
            SCHED();
        }
        const LAS char* sb = lds + ((s + 1) & 3) * ST_BYTES;
#pragma unroll
        for (int j = 0; j < 9; ++j) {
            s16x4 wl, wh;
#pragma unroll
            for (int i = 0; i < 4; ++i) {
                mfma_acc(acc[j][i], wf[j], tc[i]);
                SCHED();
                if constexpr (MODE < 4) {
                    if (j >= 1) {
                        const LAS char* p = (j - 1 < 8) ? sb + foff + ((j - 1) >> 2) * 9216 + ((j - 1) & 3) * 32 : sb + moff;
                        if (i == 0) wl = __builtin_amdgcn_ds_read_tr16_b64_v4i16((LAS s16x4*)p);
                        if (i == 1) { wh = __builtin_amdgcn_ds_read_tr16_b64_v4i16((LAS s16x4*)(p + 16 * 288)); wf[j - 1] = __builtin_shufflevector(wl, wh, 0, 1, 2, 3, 4, 5, 6, 7); }
                    }
                    if (j >= TP && j < TP + 4) {
                        const int ti = j - TP;
                        const LAS char* p = sb + ((ti & 1) ? toff1 : toff0) + (ti >> 1) * tstep;
                        if (i == 2) tl = *(const volatile LAS s16x4*)p;
                        if (i == 3) tn[ti] = __builtin_shufflevector(tl, *(const volatile LAS s16x4*)(p + 2048), 0, 1, 2, 3, 4, 5, 6, 7);
                    }
                    if (j == 8 && i == 3) wf[8] = rd_w(sb, 8);
                }
                if constexpr (MODE < 2) {
                    if (j == DPJ0 && i == 1) K3_PIECE(0, s + 3);
                    if (j == DPJ0 && i == 3) K3_PIECE(1, s + 3);
                    if (j == DPJ1 && i == 3) K3_PIECE(2, s + 3);
                    if (j == DPJ1 + 1 && i == 3) K3_PIECE(3, s + 3);
                    if (j == DPJ1 + 2 && i == 3) K3_PIECE(4, s + 3);
                }
                SCHED();
            }
            if constexpr (MODE == 0) { if (j >= 6) wd_dma(j - 6); }
            if constexpr (MODE == 3) {
                if (j == 0) {
#pragma unroll
                    for (int i = 0; i < 4; ++i) gr[i] = __builtin_amdgcn_raw_buffer_load_b64(rg, (unsigned)(((i ^ (2 * wc)) * 16 + (lane & 15)) * 8), 0, 16);
                    const unsigned co = (unsigned)((lane >= 18 ? F_ * 4 + (lane - 18) * 16 : lane * 16));
                    dma_m36(co, ws + WS_CS + (size_t)(ct * 144 + wc * 72) * 4, lds0 + K3_CS + wid * 1152);
                    dma_m36(co, ws + WS_CS + (size_t)(2 * F_ + ct * 144 + wc * 72) * 4, lds0 + K3_CS + wid * 1152 + 576);
                    const char *c4, *ce;
                    SYM_ADDR(c4, k4_gemm2); SYM_ADDR(ce, k9_end);
                    warm_code<1>(c4, ce, (wid & 3) * 64, lane, lds0 + K3_CS + 12288 + wid * 256);
                }
            }
            if constexpr (MODE >= 3) {
                constexpr int T0 = MODE == 3 ? 0 : 2;
                if (j == 1 || (MODE == 3 && j == 6)) {
                    const LAS char* tp = lds + K3_WS + wid * 3072 + (lane >> 2) * 96 + (lane & 3) * 8 + (T0 + (j == 6)) * 32;
                    const s16x4 lo = __builtin_amdgcn_ds_read_tr16_b64_v4i16((LAS s16x4*)tp), hi = __builtin_amdgcn_ds_read_tr16_b64_v4i16((LAS s16x4*)(tp + 16 * 96));
                    wtv = __builtin_bit_cast(u32x4, __builtin_shufflevector(lo, hi, 0, 1, 2, 3, 4, 5, 6, 7));
                }
                if (j == 3 || (MODE == 3 && j == 8))
                    st16_wt(mk_rsrc(ws + WS_WT, b < 216 ? F_ * D_ * 2 : 0), (unsigned)(uB * 9216 + (ut * 3 + T0 + (j == 8)) * 1024 + lane * 16), wtv);
            }
            SCHED();
        }
    };
    iter(IC<0>{}, IC<1>{}, 1, tf[1], tf[0]);
    iter(IC<1>{}, IC<4>{}, 2, tf[0], tf[1]);
    iter(IC<1>{}, IC<3>{}, 3, tf[1], tf[0]);
#pragma unroll 1
    for (int s = 4; s < 14; s += 2) {
        iter(IC<1>{}, IC<0>{}, s, tf[0], tf[1]);
        iter(IC<1>{}, IC<0>{}, s + 1, tf[1], tf[0]);
    }
    STAMP(3);
    iter(IC<1>{}, IC<0>{}, 14, tf[0], tf[1]);
    iter(IC<2>{}, IC<0>{}, 15, tf[1], tf[0]);
    iter(IC<3>{}, IC<0>{}, 16, tf[0], tf[1]);
    iter(IC<4>{}, IC<0>{}, 17, tf[1], tf[0]);
    MFMA_DONE();
    STAMP(4);
    WAIT_V(3);
    if (__builtin_expect(!__all((gr[0].y & gr[1].y & gr[2].y & gr[3].y) == 1u), 0)) {
        for (int spin = 0; spin < (1 << 20); ++spin) {
            __builtin_amdgcn_s_sleep(2);
#pragma unroll
            for (int i = 0; i < 4; ++i) gr[i] = __builtin_amdgcn_raw_buffer_load_b64(rg, (unsigned)(((i ^ (2 * wc)) * 16 + (lane & 15)) * 8), 0, 16);
            if (__all((gr[0].y & gr[1].y & gr[2].y & gr[3].y) == 1u)) break;
        }
    }
    const int mt = rt * 2 + (wr >> 1), ks = ct >> 3, lct = ct & 7;
    const rsrc_t rhh = mk_rsrc(ws + WS_HH, S_ * F_ * 2);
    const unsigned hb = (unsigned)(((mt * 4 + ks) * 36) * 8192 + ((wr & 1) * 4) * 1024);
    const LAS char* csb = lds + K3_CS + wid * 1152 + 16 * g4;
    TokC tcs[4];
#pragma unroll
    for (int i = 0; i < 4; ++i) {
        const float sd = bf_hi(gr[i].x), r = __builtin_amdgcn_rcpf(sd);
        tcs[i] = (TokC){-bf_lo(gr[i].x), -2.30220819f * r, -0.10294324f * (r * r * r), sd * sd};
    }
#pragma unroll
    for (int jp = 0; jp < 2; ++jp) {
        f32x4 cg[2], cu[2];
#pragma unroll
        for (int e = 0; e < 2; ++e) {
            cg[e] = *(const LAS f32x4*)(csb + (2 * jp + e) * 64) + *(const LAS f32x4*)(csb + 576 + (2 * jp + e) * 64);
            cu[e] = *(const LAS f32x4*)(csb + 288 + (2 * jp + e) * 64) + *(const LAS f32x4*)(csb + 864 + (2 * jp + e) * 64);
        }
#pragma unroll
        for (int i = 0; i < 4; ++i) {
            unsigned o[4];
#pragma unroll
            for (int e = 0; e < 2; ++e) {
                const f32x4 g = acc[2 * jp + e][i], u = acc[4 + 2 * jp + e][i];
                o[2 * e] = geglu2(LO2(g), LO2(u), LO2(cg[e]), LO2(cu[e]), tcs[i]);
                o[2 * e + 1] = geglu2(HI2(g), HI2(u), HI2(cg[e]), HI2(cu[e]), tcs[i]);
            }
            st16_wt(rhh, hb + (unsigned)(((jp * 8 + lct) * 2 + wc) * 8192 + (i ^ (2 * wc)) * 1024 + lane * 16), (u32x4){o[0], o[1], o[2], o[3]});
        }
    }
    {
        const LAS char* cm = lds + K3_CS + wid * 1152 + 256 + 16 * (g4 & 1);
        const f32x4 cgm = *(const LAS f32x4*)cm + *(const LAS f32x4*)(cm + 576), cum = *(const LAS f32x4*)(cm + 288) + *(const LAS f32x4*)(cm + 864);
#pragma unroll
        for (int ip = 0; ip < 2; ++ip) {
            const int ia = ((2 * ip) ^ (2 * wc)) + (lane >> 5);
            const bool up = lane >= 32;
            const TokC tc = {up ? tcs[2 * ip + 1].nm : tcs[2 * ip].nm, up ? tcs[2 * ip + 1].c0 : tcs[2 * ip].c0,
                             up ? tcs[2 * ip + 1].c1 : tcs[2 * ip].c1, up ? tcs[2 * ip + 1].q : tcs[2 * ip].q};
            f32x4 gm, um;
#pragma unroll
            for (int r = 0; r < 4; ++r) {
                auto sw = __builtin_amdgcn_permlane32_swap(__float_as_uint(acc[8][2 * ip][r]), __float_as_uint(acc[8][2 * ip + 1][r]), false, false);
                gm[r] = __uint_as_float(sw[0]); um[r] = __uint_as_float(sw[1]);
            }
            const int kgs = (lct & 1) * 2 + wc;
            st8_wt(rhh, hb + (unsigned)((32 + (lct >> 1)) * 8192 + ia * 1024 + (kgs * 16 + (lane & 15)) * 16 + (g4 & 1) * 8),
                   (u32x2){geglu2(LO2(gm), LO2(um), LO2(cgm), LO2(cum), tc), geglu2(HI2(gm), HI2(um), HI2(cgm), HI2(cum), tc)});
        }
    }
    STAMP(5);
    DIAG_END(ws, 1);
}

constexpr int K4_LDS = G_LDS;
template <int NT>
__device__ __forceinline__ void k4_body(char* __restrict__ ws, u16* __restrict__ out, LAS char* lds, int lane, int wid, int mt, int ks, int nt) {
    constexpr int dh = NT == 4;
    const unsigned lds0 = (unsigned)(size_t)lds;
    const int wr = wid & 3;
    DIAG_DECL;
    const char* asrc = ws + WS_HH + ((size_t)(mt * 4 + ks) * 36) * 8192 + wid * 2048;
    const char* wsrc = ws + WS_WT + (size_t)(ks * 4 + nt) * 18 * 18432 + wid * 2304;
    const unsigned aoff = lane * 16;
    const unsigned ma0 = lds0 + wid * 2048, mw0 = lds0 + 16384 + wid * 2304;
    auto piece = [&](int P, int s) {
        const unsigned bo = (s & 3) * ST_BYTES;
        if (P == 0) dma16<0>(aoff, asrc + (size_t)s * 16384, ma0 + bo);
        if (P == 1) dma16<1024>(aoff, asrc + (size_t)s * 16384, ma0 + bo);
        if (P == 2) dma16<0>(aoff, wsrc + (size_t)s * 18432, mw0 + bo);
        if (P == 3) dma16<1024>(aoff, wsrc + (size_t)s * 18432, mw0 + bo);
        if (P == 4) dma16_q<2048>(aoff, wsrc + (size_t)s * 18432, mw0 + bo);
    };
#define K4_STAGE(s) do { piece(0, s); piece(1, s); piece(2, s); piece(3, s); piece(4, s); } while (0)
    K4_STAGE(0);
    BAR();
    K4_STAGE(1);

    const int g4 = lane >> 4;
    const int toff = wr * 2048 + lane * 16;
    const int foff = 16384 + dh * 5120 + lane * 16;
    auto rdf = [&](const LAS char* p) -> s16x8 { return *(const LAS s16x8*)p; };

    f32x4 acc[NT][2];
    s16x8 wf[2 * NT], tf[2][4];
    STAMP(1);
    WAIT_V(5); BAR();
    STAMP(2);
#pragma unroll
    for (int e = 0; e < 4; ++e) tf[0][e] = rdf(lds + toff + (e >> 1) * 8192 + (e & 1) * 1024);
#pragma unroll
    for (int e = 0; e < 2 * NT; ++e) wf[e] = rdf(lds + foff + (e / NT) * 9216 + (e % NT) * 1024);
    SCHED();
    K4_STAGE(2);
    SCHED();

    auto iter = [&](auto mode, int s, s16x8 (&tc)[4], s16x8 (&tn)[4]) {
        constexpr int MODE = decltype(mode)::v;
        if constexpr (MODE < 4) {
            if constexpr (MODE == 3) WAIT_V(0); else WAIT_V(5);
            BAR();
            SCHED();
        }
        const LAS char* sb = lds + ((s + 1) & 3) * ST_BYTES;
#pragma unroll
        for (int e = 0; e < 2 * NT; ++e) {
#pragma unroll
            for (int i = 0; i < 2; ++i) {
                if (MODE == 0 && e < NT) mfma_first(acc[e % NT][i], wf[e], tc[(e / NT) * 2 + i]);
                else mfma_acc(acc[e % NT][i], wf[e], tc[(e / NT) * 2 + i]);
            }
            SCHED();
            if constexpr (MODE < 4) {
                wf[e] = rdf(sb + foff + (e / NT) * 9216 + (e % NT) * 1024);
                if (e >= 2 && e < 6) tn[e - 2] = rdf(sb + toff + ((e - 2) >> 1) * 8192 + ((e - 2) & 1) * 1024);
            }
            if constexpr (MODE < 2) {
                if (e == 0) piece(0, s + 3);
                if (e == 1) piece(1, s + 3);
                if (e == 3) piece(2, s + 3);
                if (e == 5) piece(3, s + 3);
                if (e == 6) piece(4, s + 3);
            }
            SCHED();
        }
    };
    iter(IC<0>{}, 0, tf[0], tf[1]);
    iter(IC<1>{}, 1, tf[1], tf[0]);
#pragma unroll 1
    for (int s = 2; s < 14; s += 2) {
        iter(IC<1>{}, s, tf[0], tf[1]);
        iter(IC<1>{}, s + 1, tf[1], tf[0]);
    }
    STAMP(3);
    iter(IC<1>{}, 14, tf[0], tf[1]);
    iter(IC<2>{}, 15, tf[1], tf[0]);
    iter(IC<3>{}, 16, tf[0], tf[1]);
    const int tok0 = mt * 128 + wr * 32 + (lane & 15);
    u32x2 hv[2][5];
    if (wr == ks) {
#pragma unroll
        for (int i = 0; i < 2; ++i) {
            const int t = tok0 + i * 16;
            const char* hp = ws + WS_H + ((size_t)((t >> 8) * 144 + nt * 36 + dh * 20) * 256 + (t & 255)) * 8;
#pragma unroll
            for (int e = 0; e < 4; ++e) hv[i][e] = *(const u32x2*)(hp + ((e >> 1) * 8 + (g4 & 1) * 4 + (g4 >> 1) * 2 + (e & 1)) * 2048);
            if (NT == 5) hv[i][4] = *(const u32x2*)(hp + (16 + g4) * 2048);
        }
    }
    SCHED();
    iter(IC<4>{}, 17, tf[1], tf[0]);
    MFMA_DONE();
    STAMP(4);
    MARK();

    const rsrc_t rx = mk_rsrc(ws + WS_X, X_BYTES);
    const unsigned xt = (unsigned)(((mt * 4 + nt) * 4 + wr) * 3 * 9216 + dh * 4096);
    if (wr != ks) {
        const unsigned o = xt + (unsigned)(((ks - wr - 1) & 3) * 9216);
#pragma unroll
        for (int i = 0; i < 2; ++i) {
            unsigned w[2 * NT];
#pragma unroll
            for (int j = 0; j < NT; ++j) { w[2 * j] = pk_bf16(acc[j][i][0], acc[j][i][1]); w[2 * j + 1] = pk_bf16(acc[j][i][2], acc[j][i][3]); }
#pragma unroll
            for (int e = 0; e < 2 * NT; ++e) w[e] = (w[e] == ~0u) ? 0x7fc07fc0u : w[e];
            if constexpr (NT == 5) st8_wt(rx, o + 8192 + i * 512 + lane * 8, (u32x2){w[8], w[9]});
            st16_wt(rx, o + i * 2048 + lane * 16, (u32x4){w[0], w[1], w[2], w[3]});
            st16_wt(rx, o + i * 2048 + 1024 + lane * 16, (u32x4){w[4], w[5], w[6], w[7]});
        }
    } else {
        u32x4 pa[3][4]; u32x2 pb[3][2];
#define XS0 16
#define XS1 1
#define XFULL 0
        __builtin_amdgcn_s_sleep(XS0);
        for (int spin = 0; spin < (1 << 18); ++spin) {
            bool go = XFULL && spin == 0;
            if (!go) {
                const u32x4 t = __builtin_amdgcn_raw_buffer_load_b128(rx, lane < 3 ? xt + lane * 9216 + 4080 : 0x80000000u, 0, 16);
                go = __all(t.w != ~0u);
            }
            if (go) {
                STAMP(5);
                unsigned m = 0u;
#pragma unroll
                for (int s = 0; s < 3; ++s) {
#pragma unroll
                    for (int p = 0; p < 4; ++p) pa[s][p] = __builtin_amdgcn_raw_buffer_load_b128(rx, xt + s * 9216 + p * 1024 + lane * 16, 0, 16);
                    if constexpr (NT == 5) {
#pragma unroll
                        for (int i = 0; i < 2; ++i) pb[s][i] = __builtin_amdgcn_raw_buffer_load_b64(rx, xt + s * 9216 + 8192 + i * 512 + lane * 8, 0, 16);
                    }
                }
#pragma unroll
                for (int s = 0; s < 3; ++s) {
#pragma unroll
                    for (int p = 0; p < 4; ++p) m = max(max(max(m, pa[s][p].x), max(pa[s][p].y, pa[s][p].z)), pa[s][p].w);
                    if constexpr (NT == 5) m = max(max(max(m, pb[s][0].x), max(pb[s][0].y, pb[s][1].x)), pb[s][1].y);
                }
                if (__all(m != ~0u)) break;
            }
            __builtin_amdgcn_s_sleep(XS1);
        }
        STAMP(6);
        const rsrc_t ro = mk_rsrc(out, S_ * D_ * 2);
#pragma unroll
        for (int i = 0; i < 2; ++i) {
            const unsigned ob = (unsigned)(((tok0 + i * 16) * D_ + nt * 144 + dh * 80) * 2);
            unsigned r[NT][2];
#pragma unroll
            for (int j = 0; j < NT; ++j) {
                f32x4 t = acc[j][i];
#pragma unroll
                for (int s = 0; s < 3; ++s) {
                    const unsigned w0 = j < 4 ? pa[s][i * 2 + (j >> 1)][(j & 1) * 2] : pb[s][i].x, w1 = j < 4 ? pa[s][i * 2 + (j >> 1)][(j & 1) * 2 + 1] : pb[s][i].y;
                    t += (f32x4){bf_lo(w0), bf_hi(w0), bf_lo(w1), bf_hi(w1)};
                }
                r[j][0] = pk_bf16(t[0], t[1]); r[j][1] = pk_bf16(t[2], t[3]);
            }
            auto addh = [&](unsigned a, unsigned h) -> unsigned { return pk_bf16(bf_lo(h) + bf_lo(a), bf_hi(h) + bf_hi(a)); };
#pragma unroll
            for (int p = 0; p < 2; ++p) {
                auto s0 = __builtin_amdgcn_permlane16_swap(r[2 * p][0], r[2 * p + 1][0], false, false);
                auto s1 = __builtin_amdgcn_permlane16_swap(r[2 * p][1], r[2 * p + 1][1], false, false);
                st16_wt(ro, ob + (unsigned)((p * 32 + (g4 & 1) * 16 + (g4 >> 1) * 8) * 2),
                        (u32x4){addh(s0[0], hv[i][2 * p].x), addh(s1[0], hv[i][2 * p].y), addh(s0[1], hv[i][2 * p + 1].x), addh(s1[1], hv[i][2 * p + 1].y)});
            }
            if constexpr (NT == 5) st8_wt(ro, ob + (unsigned)((64 + g4 * 4) * 2), (u32x2){addh(r[4][0], hv[i][4].x), addh(r[4][1], hv[i][4].y)});
        }
    }
    STAMP(7);
    DIAG_END(ws, 2);
}

extern "C" __global__ __launch_bounds__(512, 2) void k4_gemm2(char* __restrict__ ws, u16* __restrict__ out) {
    extern __shared__ __attribute__((aligned(16))) char smem[];
    const int lane = threadIdx.x & 63;
    const int wid = __builtin_amdgcn_readfirstlane(threadIdx.x >> 6);
    const int b = blockIdx.x, xcd = b & 7, idx = b >> 3;
    const int mt = (xcd >> 2) * 8 + (idx & 7), ks = xcd & 3, nt = idx >> 3;
    asm volatile("" :: "s"(out), "s"(ws));
    if (wid < 4) k4_body<5>(ws, out, (LAS char*)smem, lane, wid, mt, ks, nt);
    else k4_body<4>(ws, out, (LAS char*)smem, lane, wid, mt, ks, nt);
}

extern "C" __global__ void k9_end() {}

extern "C" void kernel_launch(void* const* d_in, const int* in_sizes, int n_in, void* d_out, int out_size, void* d_ws,
                              size_t ws_size, hipStream_t stream) {
    const u16* q = (const u16*)d_in[0];
    const u16* k = (const u16*)d_in[1];
    const u16* v = (const u16*)d_in[2];
    const u16* shift_k = (const u16*)d_in[3];
    const u16* ssm_k = (const u16*)d_in[4];
    const u16* ssm_D = (const u16*)d_in[5];
    const u16* wg = (const u16*)d_in[7];
    const u16* wu = (const u16*)d_in[8];
    const u16* wd = (const u16*)d_in[9];
    char* ws = (char*)d_ws;
    (void)hipFuncSetAttribute((const void*)k1_conv, hipFuncAttributeMaxDynamicSharedMemorySize, C1_LDS);
    (void)hipFuncSetAttribute((const void*)k3_gemm1, hipFuncAttributeMaxDynamicSharedMemorySize, K3_LDS);
    (void)hipFuncSetAttribute((const void*)k4_gemm2, hipFuncAttributeMaxDynamicSharedMemorySize, K4_LDS);
    k1_conv<<<256, 512, C1_LDS, stream>>>(q, k, v, shift_k, ssm_k, ssm_D, wg, wu, wd, ws);
    k3_gemm1<<<256, 512, K3_LDS, stream>>>(wg, wu, wd, ws);
    k4_gemm2<<<256, 512, K4_LDS, stream>>>(ws, (u16*)d_out);
}
```

```cpp
#include <hip/hip_runtime.h>
#include <hip/hip_bf16.h>

#define LAS __attribute__((address_space(3)))
typedef unsigned short u16;
typedef short s16x4 __attribute__((ext_vector_type(4)));
typedef short s16x8 __attribute__((ext_vector_type(8)));
typedef float f32x2 __attribute__((ext_vector_type(2)));
typedef float f32x4 __attribute__((ext_vector_type(4)));
typedef unsigned u32x2 __attribute__((ext_vector_type(2)));
typedef unsigned u32x4 __attribute__((ext_vector_type(4)));
typedef __bf16 bf16x2_t __attribute__((ext_vector_type(2)));

constexpr int S_ = 2048, D_ = 576, F_ = 4608;

__device__ __forceinline__ unsigned pk_bf16(float lo, float hi) {
    f32x2 v = {lo, hi};
    bf16x2_t b = __builtin_convertvector(v, bf16x2_t);
    return __builtin_bit_cast(unsigned, b);
}
__device__ __forceinline__ float bf_lo(unsigned w) { return __uint_as_float(w << 16); }
__device__ __forceinline__ float bf_hi(unsigned w) { return __uint_as_float(w & 0xffff0000u); }
__device__ __forceinline__ float rbf(float x) { return bf_lo(pk_bf16(x, 0.f)); }
__device__ __forceinline__ f32x4 mfma16(s16x8 a, s16x8 b, f32x4 c) {
    return __builtin_amdgcn_mfma_f32_16x16x32_bf16(a, b, c, 0, 0, 0);
}
#define WAIT_V(n) asm volatile("s_waitcnt vmcnt(" #n ")" ::: "memory")
#define BAR() do { __builtin_amdgcn_s_barrier(); asm volatile("" ::: "memory"); } while (0)
#define SCHED() __builtin_amdgcn_sched_barrier(0)
#define DIAG_DECL
#define MARK()
#define STAMP(i)
#define DIAG_END(wsbase, kid)
__device__ __forceinline__ void mfma_acc(f32x4& c, s16x8 a, s16x8 b) {
    asm volatile("v_mfma_f32_16x16x32_bf16 %0, %1, %2, %0" : "+v"(c) : "v"(a), "v"(b));
}
__device__ __forceinline__ void mfma_first(f32x4& c, s16x8 a, s16x8 b) {
    asm volatile("v_mfma_f32_16x16x32_bf16 %0, %1, %2, 0" : "=&v"(c) : "v"(a), "v"(b));
}
#define MFMA_DONE() do { asm volatile("s_nop 7\n\ts_nop 7" ::: "memory"); SCHED(); } while (0)
constexpr size_t WS_KV = 0;
constexpr size_t WS_H = WS_KV + (size_t)S_ * D_ * 2;
constexpr size_t WS_ST = WS_H + (size_t)S_ * D_ * 2;
constexpr size_t WS_CS = WS_ST + (size_t)S_ * 8;
constexpr size_t WS_G2 = WS_CS + (size_t)4 * F_ * 4;
constexpr size_t WS_F0 = WS_G2 + (size_t)S_ * 8;
#define FSP 128
constexpr size_t WS_EP = WS_F0 + 1280 * FSP;
constexpr size_t WS_HH = WS_EP + 1024;
constexpr size_t WS_X = WS_HH + (size_t)S_ * F_ * 2;
constexpr unsigned X_BYTES = 64 * 4 * 3 * 9216;
constexpr size_t WS_FB = WS_X + X_BYTES;
constexpr size_t WS_WT = WS_FB + 16384;

template <int N> struct IC { static constexpr int v = N; };
typedef __amdgpu_buffer_rsrc_t rsrc_t;
__device__ __forceinline__ rsrc_t mk_rsrc(const void* p, unsigned bytes) { return __builtin_amdgcn_make_buffer_rsrc((void*)p, 0, bytes, 0x00020000); }
__device__ __forceinline__ void st16_wt(rsrc_t r, unsigned off, u32x4 v) { __builtin_amdgcn_raw_buffer_store_b128(v, r, off, 0, 16); }
__device__ __forceinline__ void st8_wt(rsrc_t r, unsigned off, u32x2 v) { __builtin_amdgcn_raw_buffer_store_b64(v, r, off, 0, 16); }
template <int CTRL> __device__ __forceinline__ float dpp_add(float x) {
    return x + __uint_as_float(__builtin_amdgcn_update_dpp(0, (int)__float_as_uint(x), CTRL, 0xf, 0xf, true));
}
__device__ __forceinline__ float x16_add(float x) {
    auto r = __builtin_amdgcn_permlane16_swap(__float_as_uint(x), __float_as_uint(x), false, false);
    return __uint_as_float(r[0]) + __uint_as_float(r[1]);
}
__device__ __forceinline__ float x32_add(float x) {
    auto r = __builtin_amdgcn_permlane32_swap(__float_as_uint(x), __float_as_uint(x), false, false);
    return __uint_as_float(r[0]) + __uint_as_float(r[1]);
}
__device__ __forceinline__ float sum16(float x) {
    x = dpp_add<0xB1>(x); x = dpp_add<0x4E>(x); x = dpp_add<0x141>(x);
    return dpp_add<0x140>(x);
}
__device__ __forceinline__ float ln_sd(float var) { return rbf(__builtin_amdgcn_sqrtf(rbf(rbf(var) + 1e-5f))); }

template <int OFF>
__device__ __forceinline__ void dma16(unsigned voff, const void* sbase, unsigned m0v) {
    asm volatile("s_mov_b32 m0, %2\n\ts_nop 0\n\tglobal_load_lds_dwordx4 %0, %1 offset:%3"
                 :: "v"(voff), "s"(sbase), "s"(m0v), "i"(OFF) : "memory");
}
template <int OFF>
__device__ __forceinline__ void dma16_sc1(unsigned voff, const void* sbase, unsigned m0v) {
    asm volatile("s_mov_b32 m0, %2\n\ts_nop 0\n\tglobal_load_lds_dwordx4 %0, %1 offset:%3 sc1"
                 :: "v"(voff), "s"(sbase), "s"(m0v), "i"(OFF) : "memory");
}
template <int OFF>
__device__ __forceinline__ void dma16_q(unsigned voff, const void* sbase, unsigned m0v) {
    asm volatile("s_mov_b32 m0, %2\n\ts_mov_b64 exec, 0xffff\n\tglobal_load_lds_dwordx4 %0, %1 offset:%3\n\ts_mov_b64 exec, -1"
                 :: "v"(voff), "s"(sbase), "s"(m0v), "i"(OFF) : "memory");
}
__device__ __forceinline__ void dma_m36(unsigned voff, const void* sbase, unsigned m0v) {
    asm volatile("s_mov_b32 m0, %2\n\ts_mov_b32 exec_hi, 15\n\tglobal_load_lds_dwordx4 %0, %1\n\ts_mov_b64 exec, -1"
                 :: "v"(voff), "s"(sbase), "s"(m0v) : "memory");
}
__device__ __forceinline__ void dma4(unsigned voff, const void* sbase, unsigned m0v) {
    asm volatile("s_mov_b32 m0, %2\n\ts_nop 0\n\tglobal_load_lds_dword %0, %1" :: "v"(voff), "s"(sbase), "s"(m0v) : "memory");
}
__device__ __forceinline__ void dma4_sc1(unsigned voff, const void* sbase, unsigned m0v) {
    asm volatile("s_mov_b32 m0, %2\n\ts_nop 0\n\tglobal_load_lds_dword %0, %1 sc1" :: "v"(voff), "s"(sbase), "s"(m0v) : "memory");
}
#define SYM_ADDR(out, sym) asm volatile("s_getpc_b64 s[40:41]\n\ts_add_u32 s40, s40, " #sym "@rel32@lo+4\n\ts_addc_u32 s41, s41, " #sym "@rel32@hi+12\n\ts_mov_b64 %0, s[40:41]" \
                                        : "=s"(out) :: "s40", "s41", "scc")
template <int NI>
__device__ __forceinline__ void warm_code(const char* a, const char* e, int l0, int lane, unsigned m0v) {
    const unsigned last = e > a ? (unsigned)(e - a) - 4u : 0u;
#pragma unroll
    for (int i = 0; i < NI; ++i) dma4(min((unsigned)(l0 + i * 64 + lane) * 128u, last), a, m0v);
}
__device__ __forceinline__ s16x8 tr_frag(const LAS char* p) {
    const s16x4 lo = __builtin_amdgcn_ds_read_tr16_b64_v4i16((LAS s16x4*)p);
    const s16x4 hi = __builtin_amdgcn_ds_read_tr16_b64_v4i16((LAS s16x4*)(p + 16 * 288));
    return __builtin_shufflevector(lo, hi, 0, 1, 2, 3, 4, 5, 6, 7);
}
__device__ __forceinline__ void wd_unit_load(const u16* wd, int un, int lane, unsigned m0v) {
    const int uB = un / 3, ut = un % 3, kh = uB & 1, st = (uB >> 1) % 18, kn = uB / 36;
    const bool full = st >= 2;
    const int f0 = (kn >> 2) * 1152 + (full ? ((st - 2) & 7) * 144 + kh * 72 + ((st - 2) >> 3) * 32 : (st * 2 + kh) * 288);
    const char* src = (const char*)wd + (size_t)f0 * (D_ * 2) + (kn & 3) * 288 + ut * 96;
#pragma unroll
    for (int i = 0; i < 3; ++i) {
        const int q = lane + 64 * i, r = q / 6, pos = q % 6, kg = (r & 15) >> 2;
        const int f = full ? r : (kg >> 1) * 144 + (kg & 1) * 72 + 64 + (r & 3) + 4 * (r >> 4);
        dma16<0>((unsigned)(f * (D_ * 2) + pos * 16), src, m0v + i * 1024);
    }
}
__device__ __forceinline__ void wd_unit_store(char* ws, int un, int lane, const LAS char* l) {
    const rsrc_t r = mk_rsrc(ws + WS_WT, F_ * D_ * 2);
#pragma unroll
    for (int t = 0; t < 3; ++t) {
        const LAS char* tp = l + (lane >> 2) * 96 + (lane & 3) * 8 + t * 32;
        const s16x4 lo = __builtin_amdgcn_ds_read_tr16_b64_v4i16((LAS s16x4*)tp), hi = __builtin_amdgcn_ds_read_tr16_b64_v4i16((LAS s16x4*)(tp + 16 * 96));
        st16_wt(r, (unsigned)(un * 3072 + t * 1024 + lane * 16), __builtin_bit_cast(u32x4, __builtin_shufflevector(lo, hi, 0, 1, 2, 3, 4, 5, 6, 7)));
    }
}
#define WU4 5
#define HELPER_DELAY 24
#define POLL0 24
#define POLL1 2
constexpr int C1_COPY = 4288;
constexpr int C1_FILT = 4 * C1_COPY;
constexpr int C1_KV = 2304 * 2;
constexpr int C1_KVOFF = 4 * C1_FILT + 64;
constexpr int C1_YOFF = C1_KVOFF + 4 * C1_KV;
constexpr int C1_XOFF = C1_YOFF + 16384;
constexpr int C1_SOFF = C1_XOFF + 16384;
constexpr int C1_TOFF = C1_SOFF + 16384;
constexpr int C1_WOFF = C1_TOFF + 2048;
constexpr int C1_LDS = C1_WOFF + 24576;

extern "C" __global__ __launch_bounds__(512, 2) void k1_conv(const u16* __restrict__ x, const u16* __restrict__ kin, const u16* __restrict__ vin,
                                                  const u16* __restrict__ shift_k, const u16* __restrict__ ssm_k, const u16* __restrict__ ssm_D,
                                                  const u16* __restrict__ wg, const u16* __restrict__ wu, const u16* __restrict__ wd,
                                                  char* __restrict__ ws) {
    extern __shared__ __attribute__((aligned(16))) char smem[];
    const int tid = threadIdx.x, lane = tid & 63;
    const int wid = __builtin_amdgcn_readfirstlane(tid >> 6);
    const int b = blockIdx.x, xcd = b & 7, idx = b >> 3;
    {
        const char *c1, *c3;
        SYM_ADDR(c1, k1_conv); SYM_ADDR(c3, k3_gemm1);
        if (wid >= 4) warm_code<1>(c1, c3, (wid & 3) * 64, lane, (unsigned)(size_t)(LAS char*)smem + C1_TOFF + 256 + (wid & 3) * 256);
    }
    DIAG_DECL;
    asm volatile("" :: "s"(x), "s"(kin), "s"(vin), "s"(shift_k), "s"(ssm_k), "s"(ssm_D), "s"(wg), "s"(wu), "s"(wd), "s"(ws));
    const bool conv = idx < 18;
    const int cg = xcd * 18 + idx, ch0 = cg * 4;
    const unsigned lds0 = (unsigned)(size_t)(LAS char*)smem;
    LAS unsigned* tagp = (LAS unsigned*)(smem + C1_TOFF);
    auto get_tag = [&]() -> unsigned { BAR(); return __builtin_amdgcn_readfirstlane(*(volatile LAS unsigned*)tagp); };

    const int h = (idx - 18) * 8 + xcd, hc_ = h < 108 ? h : 0;
    const int m3 = hc_ % 3, lcA = (hc_ / 3) * 4 + m3, rb0 = m3 * 3;
    constexpr int HU = (1728 - 144 * WU4 + 111) / 112;
    const int hbase = 144 * WU4 + h * HU, hun = hbase + wid * 2, hend = min(hbase + HU, 1728);
    auto cs_load = [&](u32x4 (&v)[12]) {
        const int rs = tid >> 3, c8 = tid & 7;
#pragma unroll
        for (int p = 0; p < 12; ++p) {
            const int rb = rb0 + p, lc = lcA + (rb >= 9), r = (rb >= 9 ? rb - 9 : rb) * 64 + rs;
            v[p] = *(const u32x4*)((lc >= 72 ? wu : wg) + (size_t)r * F_ + (lc % 72) * 64 + c8 * 8);
        }
        SCHED();
#pragma unroll
        for (int u = 0; u < 2; ++u) if (hun + u < hend) wd_unit_load(wd, hun + u, lane, lds0 + 16384 + (wid * 2 + u) * 3072);
        st16_wt(mk_rsrc(ws + WS_FB, 16384), (unsigned)(h * 8192 + tid * 16), (u32x4){0u, 0u, 0u, 0u});
        SCHED();
    };
    auto cs_finish = [&](u32x4 (&v)[12]) {
        float g[4][8];
#pragma unroll
        for (int q = 0; q < 4; ++q) {
#pragma unroll
            for (int e = 0; e < 4; ++e) {
                g[q][2 * e] = (bf_lo(v[3 * q][e]) + bf_lo(v[3 * q + 1][e])) + bf_lo(v[3 * q + 2][e]);
                g[q][2 * e + 1] = (bf_hi(v[3 * q][e]) + bf_hi(v[3 * q + 1][e])) + bf_hi(v[3 * q + 2][e]);
            }
        }
        LAS float* red = (LAS float*)smem;
#pragma unroll
        for (int l = 0; l < 2; ++l) {
            float a[8];
#pragma unroll
            for (int e = 0; e < 8; ++e) {
                a[e] = l == 0 ? g[0][e] + (m3 <= 1 ? g[1][e] : 0.f) + (m3 == 0 ? g[2][e] : 0.f) : g[3][e] + (m3 >= 1 ? g[2][e] : 0.f) + (m3 == 2 ? g[1][e] : 0.f);
                a[e] = x32_add(x16_add(dpp_add<0x128>(a[e])));
            }
            if (lane < 8) {
                *(LAS f32x4*)(red + (wid * 2 + l) * 64 + lane * 8) = (f32x4){a[0], a[1], a[2], a[3]};
                *(LAS f32x4*)(red + (wid * 2 + l) * 64 + lane * 8 + 4) = (f32x4){a[4], a[5], a[6], a[7]};
            }
        }
        WAIT_V(1);
#pragma unroll
        for (int u = 0; u < 2; ++u) if (hun + u < hend) wd_unit_store(ws, hun + u, lane, (const LAS char*)smem + 16384 + (wid * 2 + u) * 3072);
        asm volatile("s_waitcnt lgkmcnt(0)" ::: "memory"); BAR();
        if (tid < 192 && h < 108) {
            float s = 0.f;
            if (tid < 128) {
#pragma unroll
                for (int w = 0; w < 8; ++w) s += red[w * 128 + tid];
            }
            const int part = tid >> 6;
            const int lc = part == 0 ? lcA : (part == 1 ? lcA + 1 : (m3 == 0 ? lcA : lcA + 1));
            const int slot = part == 0 ? (m3 != 0) : (part == 1 ? 0 : 1);
            if (part < 2 || m3 != 1)
                __builtin_amdgcn_raw_buffer_store_b32(__float_as_uint(s), mk_rsrc(ws + WS_CS, 4 * F_ * 4), (unsigned)((slot * 2 * F_ + lc * 64 + (tid & 63)) * 4), 0, 16);
        }
    };
    auto wait_kv = [&](unsigned tag) {
        const rsrc_t rf = mk_rsrc(ws + WS_F0 + ((idx >> 2) * 8 + xcd) * 32 * FSP, 32 * FSP);
        __builtin_amdgcn_s_sleep(POLL0);
        for (int spin = 0; spin < (1 << 20); ++spin) {
            const unsigned f = __builtin_amdgcn_raw_buffer_load_b32(rf, (lane & 31) * FSP, 0, 16);
            if (__all(f == tag)) break;
            __builtin_amdgcn_s_sleep(POLL1);
        }
    };

    auto x_rows = [&]() {
        u32x2 xr[8];
#pragma unroll
        for (int i = 0; i < 8; ++i) xr[i] = *(const u32x2*)(x + (size_t)((tid - 256) + (wid - 4) * 448 + i * 64) * D_ + ch0);
#pragma unroll
        for (int i = 0; i < 8; ++i) *(LAS u32x2*)(smem + C1_XOFF + ((tid - 256) + (wid - 4) * 448 + i * 64) * 8) = xr[i];
        SCHED();
    };
    unsigned tag;
    unsigned dvv = 0;
    auto upper = [&](auto is_stat, auto is_conv) {
        constexpr bool STAT = decltype(is_stat)::v, CONV = decltype(is_conv)::v;
        const int srow = b * 8 + (wid - 5) * 4 + (lane >> 4), t3 = tid - 320;
        unsigned xw[18], epv = 0;
        if constexpr (STAT) {
            const unsigned* xr = (const unsigned*)(x + (size_t)srow * D_) + (lane & 15);
#pragma unroll
            for (int j = 0; j < 18; ++j) xw[j] = xr[16 * j];
        } else {
            epv = __hip_atomic_load((const unsigned*)(ws + WS_EP), __ATOMIC_RELAXED, __HIP_MEMORY_SCOPE_AGENT);
        }
        SCHED();
        if constexpr (STAT) {
            const char *c3, *c4;
            SYM_ADDR(c3, k3_gemm1); SYM_ADDR(c4, k9_end);
            warm_code<1>(c3, c4, ((idx & 3) * 2 + wid - 5) * 64, lane, lds0 + C1_TOFF + 256 + (wid - 5) * 256);
        }
        SCHED();
        u32x4 v[12], fa[6]; u32x2 fb[6];
        if constexpr (CONV) {
#pragma unroll
            for (int i = 0; i < 6; ++i) {
                const int task = min(t3 + 192 * i, 1023), u = task & 255;
                const u16* src = ssm_k + (size_t)(ch0 + (task >> 8)) * S_ + u * 8;
                fa[i] = *(const u32x4*)src;
                fb[i] = *(const u32x2*)(src + (u < 255 ? 8 : 0));
            }
            SCHED();
        }
        if constexpr (STAT) {
            f32x2 xf[18], s = {0.f, 0.f};
#pragma unroll
            for (int j = 0; j < 18; ++j) { xf[j] = (f32x2){bf_lo(xw[j]), bf_hi(xw[j])}; s += xf[j]; }
            const float mean = sum16(s.x + s.y) * (1.f / D_);
            f32x2 q = {0.f, 0.f};
#pragma unroll
            for (int j = 0; j < 18; ++j) { const f32x2 d = xf[j] - mean; q = __builtin_elementwise_fma(d, d, q); }
            const unsigned sgr = pk_bf16(mean, ln_sd(sum16(q.x + q.y) * (1.f / D_)));
            tag = get_tag();
            const unsigned so = (lane & 15) == 0 ? (unsigned)srow * 8 : 0x80000000u;
            st8_wt(mk_rsrc(ws + WS_ST, S_ * 8), so, (u32x2){sgr, tag});
            st8_wt(mk_rsrc(ws + WS_G2, S_ * 8), so, (u32x2){0u, 0u});
        } else {
            *tagp = epv + 1u;
            asm volatile("s_waitcnt lgkmcnt(0)" ::: "memory");
            tag = get_tag();
        }
        SCHED();
        if constexpr (!CONV) {
            __builtin_amdgcn_s_sleep(HELPER_DELAY);
            cs_load(v);
            if (!STAT && h == 111) {
                const rsrc_t rf = mk_rsrc(ws + WS_F0, 256 * FSP);
                for (int spin = 0; spin < (1 << 20); ++spin) {
                    bool ok = true;
#pragma unroll
                    for (int i = 0; i < 4; ++i) ok &= __builtin_amdgcn_raw_buffer_load_b32(rf, (i * 64 + lane) * FSP, 0, 16) == tag;
                    if (__all(ok)) break;
                    __builtin_amdgcn_s_sleep(8);
                }
                if (lane == 0) __hip_atomic_store((unsigned*)(ws + WS_EP), tag, __ATOMIC_RELAXED, __HIP_MEMORY_SCOPE_AGENT);
            }
            cs_finish(v);
            return;
        }
        if (t3 < 80) {
            const int cpy = t3 / 5, e = t3 % 5, q = e < 3 ? e : 257 + e;
            *(LAS u32x4*)(smem + cpy * C1_COPY + q * 16) = (u32x4){0u, 0u, 0u, 0u};
        } else if (t3 < 84) {
            *(LAS u32x4*)(smem + C1_KVOFF - 64 + (t3 - 80) * 16) = (u32x4){0u, 0u, 0u, 0u};
        }
        if (t3 < 128) *(LAS u32x4*)(smem + C1_KVOFF + (t3 >> 5) * C1_KV + 4096 + (t3 & 31) * 16) = (u32x4){0u, 0u, 0u, 0u};
#pragma unroll
        for (int i = 0; i < 6; ++i) {
            const int task = min(t3 + 192 * i, 1023), u = task & 255;
            const unsigned d[6] = {fa[i].x, fa[i].y, fa[i].z, fa[i].w, u < 255 ? fb[i].x : 0u, u < 255 ? fb[i].y : 0u};
            unsigned o[5];
#pragma unroll
            for (int e = 0; e < 5; ++e) o[e] = __builtin_amdgcn_alignbit(d[e + 1], d[e], 16);
            LAS char* base = (LAS char*)smem + (task >> 8) * C1_FILT + (u + 4) * 16;
            *(LAS u32x4*)(base) = (u32x4){o[0], o[1], o[2], o[3]};
            *(LAS u32x4*)(base + C1_COPY) = (u32x4){d[1], d[2], d[3], d[4]};
            *(LAS u32x4*)(base + 2 * C1_COPY) = (u32x4){o[1], o[2], o[3], o[4]};
            *(LAS u32x4*)(base + 3 * C1_COPY) = (u32x4){d[2], d[3], d[4], d[5]};
            if (u == 0) {
                *(LAS u32x4*)(base - 16) = (u32x4){0u, 0u, 0u, d[0] << 16};
                *(LAS u32x4*)(base - 16 + C1_COPY) = (u32x4){0u, 0u, 0u, d[0]};
                *(LAS u32x4*)(base - 16 + 2 * C1_COPY) = (u32x4){0u, 0u, d[0] << 16, o[0]};
                *(LAS u32x4*)(base - 16 + 3 * C1_COPY) = (u32x4){0u, 0u, d[0], d[1]};
            }
        }
        asm volatile("s_waitcnt lgkmcnt(0)" ::: "memory"); BAR();
        x_rows();
    };
    if (wid >= 5) {
        if (!conv) {
            if (wid < 7) upper(IC<1>{}, IC<0>{}); else upper(IC<0>{}, IC<0>{});
            DIAG_END(ws, 0);
            return;
        }
        if (wid < 7) upper(IC<1>{}, IC<1>{}); else upper(IC<0>{}, IC<1>{});
    } else {
        {
            unsigned kw[11], vw[8];
            const int t0 = idx * 64 + (lane >> 3) * 8, dc = wid * 8 + (lane & 7), kch = xcd * 72 + (dc < 36 ? dc : 35) * 2;
            const rsrc_t rk = mk_rsrc(kin, S_ * D_ * 2), rv = mk_rsrc(vin, S_ * D_ * 2);
            const int off = t0 * (D_ * 2) + kch * 2;
#pragma unroll
            for (int j = 0; j < 11; ++j) kw[j] = __builtin_amdgcn_raw_buffer_load_b32(rk, (unsigned)(off + (j - 3) * (D_ * 2)), 0, 0);
#pragma unroll
            for (int j = 0; j < 8; ++j) vw[j] = __builtin_amdgcn_raw_buffer_load_b32(rv, (unsigned)(off + j * (D_ * 2)), 0, 0);
            const u32x4 sw = *(const u32x4*)(shift_k + kch * 4);
            dvv = ssm_D[(conv ? ch0 : 0) + (wid & 3)];
            SCHED();
            STAMP(1);
            const f32x2 w0 = {bf_lo(sw.x), bf_lo(sw.z)}, w1 = {bf_hi(sw.x), bf_hi(sw.z)}, w2 = {bf_lo(sw.y), bf_lo(sw.w)}, w3 = {bf_hi(sw.y), bf_hi(sw.w)};
            f32x2 kf[11], o[8];
#pragma unroll
            for (int j = 0; j < 11; ++j) kf[j] = (f32x2){bf_lo(kw[j]), bf_hi(kw[j])};
#pragma unroll
            for (int j = 0; j < 8; ++j) {
                f32x2 s = kf[j] * w3;
                s = __builtin_elementwise_fma(kf[j + 1], w2, s);
                s = __builtin_elementwise_fma(kf[j + 2], w1, s);
                s = __builtin_elementwise_fma(kf[j + 3], w0, s);
                o[j] = s * (f32x2){bf_lo(vw[j]), bf_hi(vw[j])};
            }
            const rsrc_t ro = mk_rsrc(ws + WS_KV, S_ * D_ * 2);
            const unsigned oo = dc < 36 ? (unsigned)(kch * (S_ * 2) + (2040 - t0) * 2) : 0x80000000u;
            st16_wt(ro, oo, (u32x4){pk_bf16(o[7].x, o[6].x), pk_bf16(o[5].x, o[4].x), pk_bf16(o[3].x, o[2].x), pk_bf16(o[1].x, o[0].x)});
            st16_wt(ro, oo + S_ * 2, (u32x4){pk_bf16(o[7].y, o[6].y), pk_bf16(o[5].y, o[4].y), pk_bf16(o[3].y, o[2].y), pk_bf16(o[1].y, o[0].y)});
        }
        tag = get_tag();
        WAIT_V(0);
        if (lane == 0) __hip_atomic_store((unsigned*)(ws + WS_F0 + ((wid * 8 + xcd) * 32 + idx) * FSP), tag, __ATOMIC_RELAXED, __HIP_MEMORY_SCOPE_AGENT);
        MARK();
        STAMP(2);
        if (!conv) {
            u32x4 v[12];
            cs_load(v);
            cs_finish(v);
            DIAG_END(ws, 0);
            return;
        }
        if (wid == 4) {
            x_rows();
            BAR();
#pragma unroll 1
            for (int u = 0; u < WU4; ++u) wd_unit_load(wd, cg * WU4 + u, lane, lds0 + C1_WOFF + u * 3072);
            __builtin_amdgcn_s_sleep(24);
            const char* gs = ws + WS_ST;
#pragma unroll
            for (int i = 0; i < 4; ++i) {
                dma16_sc1<0>(lane * 16, gs + i * 4096, lds0 + C1_SOFF + i * 4096); dma16_sc1<1024>(lane * 16, gs + i * 4096, lds0 + C1_SOFF + i * 4096);
                dma16_sc1<2048>(lane * 16, gs + i * 4096, lds0 + C1_SOFF + i * 4096); dma16_sc1<3072>(lane * 16, gs + i * 4096, lds0 + C1_SOFF + i * 4096);
            }
            WAIT_V(0);
#pragma unroll 1
            for (int u = 0; u < WU4; ++u) wd_unit_store(ws, cg * WU4 + u, lane, (const LAS char*)smem + C1_WOFF + u * 3072);
        } else {
        {
            wait_kv(tag);
            STAMP(7);
            const char* kvs = ws + WS_KV + (size_t)(ch0 + wid) * (S_ * 2);
            const unsigned m = lds0 + C1_KVOFF + wid * C1_KV;
            dma16_sc1<0>(lane * 16, kvs, m); dma16_sc1<1024>(lane * 16, kvs, m); dma16_sc1<2048>(lane * 16, kvs, m); dma16_sc1<3072>(lane * 16, kvs, m);
            WAIT_V(0);
            STAMP(9);
            BAR();
            STAMP(4);
        }
        const int row = lane & 15, kg = lane >> 4;
        const LAS char* abase = (const LAS char*)smem + wid * C1_FILT + (row & 3) * C1_COPY + (24 + 4 * (row >> 2) - 8 * kg) * 2;
        const LAS char* bbase = (const LAS char*)smem + C1_KVOFF + wid * C1_KV + (2040 - 16 * row - 8 * kg) * 2 - 512 * 7 - 448;
        auto rd_ah = [&](int r, int L, int hf) -> s16x4 { return *(const volatile LAS s16x4*)(abase + 512 * L - 64 * r + 8 * hf); };
        auto rd_b = [&](int r, int m) -> s16x8 { return *(const LAS s16x8*)(bbase + 448 - 64 * r + 512 * (7 - m)); };
        auto cat = [&](s16x4 lo, s16x4 hi) -> s16x8 { return __builtin_shufflevector(lo, hi, 0, 1, 2, 3, 4, 5, 6, 7); };
        f32x4 acc[8];
        s16x4 al[2][8], ah[2][8];
        s16x8 bf[2][8], bd[8];
        auto rd_k = [&](int r, int k) {
            if (k < 2) { if (k == 0) al[r & 1][0] = rd_ah(r, 1, 0); else ah[r & 1][0] = rd_ah(r, 1, 1); }
            else if (k < 10) bf[r & 1][9 - k] = rd_b(r, 8 - k);
            else { const int L = (k - 10) / 2 + 2; if (k & 1) ah[r & 1][L - 1] = rd_ah(r, L, 1); else al[r & 1][L - 1] = rd_ah(r, L, 0); }
        };
        const s16x8 a0 = cat(rd_ah(0, 0, 0), rd_ah(0, 0, 1));
#pragma unroll
        for (int e = 0; e < 8; ++e) bd[e] = rd_b(0, e);
        s16x8 dfr;
        {
            const int j = 8 * kg + 7 - row;
            u32x4 w;
#pragma unroll
            for (int e = 0; e < 4; ++e) w[e] = j == 2 * e ? dvv : (j == 2 * e + 1 ? dvv << 16 : 0u);
            dfr = __builtin_bit_cast(s16x8, w);
            asm volatile("s_nop 1" : "+v"(dfr));
        }
        SCHED();
#pragma unroll
        for (int n = 0; n < 16; ++n) {
            if (n < 8) mfma_first(acc[n], a0, bd[n]); else mfma_acc(acc[n - 8], dfr, bd[n - 8]);
            SCHED();
            if (n == 0) { rd_k(0, 0); rd_k(0, 1); }
            else if (n == 1) rd_k(0, 9);
            else rd_k(0, n + 8);
            SCHED();
        }
#pragma unroll
        for (int r = 0; r < 8; ++r) {
            int k = 0;
#pragma unroll
            for (int L = 1; L <= 8; ++L) {
#pragma unroll
                for (int n = 7; n >= L - 1; --n) {
                    const int e = n - L + 1;
                    mfma_acc(acc[n], cat(al[r & 1][L - 1], ah[r & 1][L - 1]), (r == 0 && e > 0) ? bd[e - 1] : bf[r & 1][e]);
                    SCHED();
                    if (r < 7 && k < 24) rd_k(r + 1, k);
                    ++k;
                    SCHED();
                }
            }
        }
        MFMA_DONE();
#pragma unroll
        for (int n = 0; n < 8; ++n)
            *(LAS u32x2*)(smem + C1_YOFF + wid * 4096 + (256 * n + 16 * row + 4 * kg) * 2) = (u32x2){pk_bf16(acc[n][0], acc[n][1]), pk_bf16(acc[n][2], acc[n][3])};
        }
    }
    STAMP(5);
    asm volatile("s_waitcnt lgkmcnt(0)" ::: "memory"); BAR();
    STAMP(6);
    {
        u32x2 yc[4];
#pragma unroll
        for (int c = 0; c < 4; ++c) yc[c] = *(const LAS u32x2*)(smem + C1_YOFF + c * 4096 + tid * 8);
        const u32x4 xa = *(const LAS u32x4*)(smem + C1_XOFF + tid * 32), xb = *(const LAS u32x4*)(smem + C1_XOFF + tid * 32 + 16);
        const u32x2 xr[4] = {{xa.x, xa.y}, {xa.z, xa.w}, {xb.x, xb.y}, {xb.z, xb.w}};
        u32x4 s01 = *(const LAS u32x4*)(smem + C1_SOFF + tid * 32), s23 = *(const LAS u32x4*)(smem + C1_SOFF + tid * 32 + 16);
        if (__builtin_expect(!__all(s01.y == tag && s01.w == tag && s23.y == tag && s23.w == tag), 0)) {
            const rsrc_t rs = mk_rsrc(ws + WS_ST, S_ * 8);
            for (int spin = 0; spin < (1 << 20); ++spin) {
                s01 = __builtin_amdgcn_raw_buffer_load_b128(rs, tid * 32, 0, 16);
                s23 = __builtin_amdgcn_raw_buffer_load_b128(rs, tid * 32 + 16, 0, 16);
                if (__all(s01.y == tag && s01.w == tag && s23.y == tag && s23.w == tag)) break;
                __builtin_amdgcn_s_sleep(2);
            }
        }
        const unsigned sv[4] = {s01.x, s01.z, s23.x, s23.z};
        float mm[4], rr[4];
#pragma unroll
        for (int j = 0; j < 4; ++j) { mm[j] = bf_lo(sv[j]); rr[j] = __builtin_amdgcn_rcpf(bf_hi(sv[j])); }
        unsigned hw[8];
#pragma unroll
        for (int j = 0; j < 4; ++j) {
#pragma unroll
            for (int e = 0; e < 2; ++e) {
                const unsigned xw = e ? xr[j].y : xr[j].x;
                const unsigned ya = (j >> 1) ? yc[2 * e].y : yc[2 * e].x, yb = (j >> 1) ? yc[2 * e + 1].y : yc[2 * e + 1].x;
                const float y0 = (j & 1) ? bf_hi(ya) : bf_lo(ya), y1 = (j & 1) ? bf_hi(yb) : bf_lo(yb);
                const float x0 = bf_lo(xw), x1 = bf_hi(xw);
                hw[2 * j + e] = pk_bf16(__builtin_fmaf((x0 - mm[j]) * rr[j], y0, x0), __builtin_fmaf((x1 - mm[j]) * rr[j], y1, x1));
            }
        }
        const rsrc_t rh = mk_rsrc(ws + WS_H, S_ * D_ * 2);
        const unsigned ho = (unsigned)(((tid >> 6) * 144 + cg) * 2048 + (tid & 63) * 32);
        st16_wt(rh, ho, (u32x4){hw[0], hw[1], hw[2], hw[3]});
        st16_wt(rh, ho + 16, (u32x4){hw[4], hw[5], hw[6], hw[7]});
    }
    STAMP(8);
    DIAG_END(ws, 0);
}

constexpr int ST_BYTES = 16384 + 2 * 9216;
constexpr int NBUF = 4;
constexpr int G_LDS = NBUF * ST_BYTES;

struct TokC { float nm, c0, c1, q; };
__device__ __forceinline__ unsigned geglu2(f32x2 ag, f32x2 au, f32x2 csg, f32x2 csu, const TokC& t) {
    const f32x2 nm = {t.nm, t.nm}, c0 = {t.c0, t.c0}, c1 = {t.c1, t.c1}, q = {t.q, t.q};
    const f32x2 g = __builtin_elementwise_fma(nm, csg, ag), u = __builtin_elementwise_fma(nm, csu, au);
    const f32x2 z = g * __builtin_elementwise_fma(g * g, c1, c0);
    const f32x2 e = {__builtin_amdgcn_exp2f(z.x), __builtin_amdgcn_exp2f(z.y)};
    const f32x2 d = __builtin_elementwise_fma(e, q, q);
    const f32x2 r = (g * u) * (f32x2){__builtin_amdgcn_rcpf(d.x), __builtin_amdgcn_rcpf(d.y)};
    return pk_bf16(r.x, r.y);
}
#define LO2(v) __builtin_shufflevector(v, v, 0, 1)
#define HI2(v) __builtin_shufflevector(v, v, 2, 3)
__device__ __forceinline__ float dot2(unsigned a, unsigned b, float c) {
    return __builtin_amdgcn_fdot2_f32_bf16(__builtin_bit_cast(bf16x2_t, a), __builtin_bit_cast(bf16x2_t, b), c, false);
}
#define WAIT_VI(N) asm volatile("s_waitcnt vmcnt(%0)" :: "i"(N) : "memory")

constexpr int G2_W0 = 71680, G2_FL = 69632;
__device__ __forceinline__ void g2_wpiece(int P, int s, const char* wsrc, unsigned aoff, unsigned mw0) {
    const unsigned m = mw0 + (s % 5) * 18432;
    if (P == 0) dma16<0>(aoff, wsrc + (size_t)s * 18432, m);
    if (P == 1) dma16<1024>(aoff, wsrc + (size_t)s * 18432, m);
    if (P == 2) dma16_q<2048>(aoff, wsrc + (size_t)s * 18432, m);
}
constexpr int g2_n(int S) { return (S + 3 < 18 ? 2 : 0) + (S + 4 < 18 ? 3 : 0) + (S == 4 ? 1 : 0); }
constexpr int g2_vm(int S) {
    return S == 0 ? 3 : (S == 1 ? 1 + g2_n(0) : g2_n(S - 2) - (S + 1 < 18 ? 2 : 0) + g2_n(S - 1));
}
#define DIAG_ARG
#define DIAG_PASS
template <int NT>
__device__ __forceinline__ void g2_body(char* __restrict__ ws, u16* __restrict__ out, LAS char* lds, int lane, int wid, int mt, int ks, int nt,
                                        const char* wsrc, unsigned flag2 DIAG_ARG) {
    constexpr int dh = NT == 4;
    const unsigned lds0 = (unsigned)(size_t)lds;
    const int wr = wid & 3;
    const char* asrc = ws + WS_HH + ((size_t)(mt * 4 + ks) * 36) * 8192 + wid * 2048;
    const unsigned aoff = lane * 16;
    const unsigned ma0 = lds0 + wid * 2048, mw0 = lds0 + G2_W0 + wid * 2304;
    auto hpiece = [&](int P, int s) {
        if (P == 0) dma16_sc1<0>(aoff, asrc + (size_t)s * 16384, ma0 + (s & 3) * 16384);
        if (P == 1) dma16_sc1<1024>(aoff, asrc + (size_t)s * 16384, ma0 + (s & 3) * 16384);
    };
    hpiece(0, 0); hpiece(1, 0); hpiece(0, 1); hpiece(1, 1); hpiece(0, 2); hpiece(1, 2);

    const int g4 = lane >> 4;
    const int toff = wr * 2048 + lane * 16;
    const int foff = G2_W0 + dh * 5120 + lane * 16;
    auto rdf = [&](const LAS char* p) -> s16x8 { return *(const LAS s16x8*)p; };
    const rsrc_t rfb = mk_rsrc(ws + WS_FB, 16384);

    f32x4 acc[NT][2];
    s16x8 wf[2 * NT], tf[2][4];
    WAIT_V(4);
    __builtin_amdgcn_raw_buffer_store_b32(1u, rfb, lane == 0 ? flag2 : 0x80000000u, 0, 16);
    BAR();
    STAMP(8);
#pragma unroll
    for (int e = 0; e < 4; ++e) tf[0][e] = rdf(lds + toff + (e >> 1) * 8192 + (e & 1) * 1024);
#pragma unroll
    for (int e = 0; e < 2 * NT; ++e) wf[e] = rdf(lds + foff + (e / NT) * 9216 + (e % NT) * 1024);
    SCHED();

    auto iter = [&](auto sc, s16x8 (&tc)[4], s16x8 (&tn)[4]) {
        constexpr int S = decltype(sc)::v;
        if constexpr (S < 17) {
            WAIT_VI(g2_vm(S));
            BAR();
            if constexpr (S == 7) {
                unsigned fb2 = *(const volatile LAS unsigned*)(lds + G2_FL + wid * 256 + lane * 4);
                if (__builtin_expect(!__all(fb2 == 1u), 0)) {
                    for (int spin = 0; spin < (1 << 20); ++spin) {
                        fb2 = __builtin_amdgcn_raw_buffer_load_b32(rfb, (unsigned)(((mt * 4 + ks) * 2 + 1) * 128 + (lane & 31) * 4), 0, 16);
                        if (__all(fb2 == 1u)) break;
                        __builtin_amdgcn_s_sleep(2);
                    }
                }
            }
            SCHED();
        }
        const LAS char* tb = lds + ((S + 1) & 3) * 16384;
        const LAS char* wb = lds + ((S + 1) % 5) * 18432;
#pragma unroll
        for (int e = 0; e < 2 * NT; ++e) {
#pragma unroll
            for (int i = 0; i < 2; ++i) {
                if (S == 0 && e < NT) mfma_first(acc[e % NT][i], wf[e], tc[(e / NT) * 2 + i]);
                else mfma_acc(acc[e % NT][i], wf[e], tc[(e / NT) * 2 + i]);
            }
            SCHED();
            if constexpr (S < 17) {
                wf[e] = rdf(wb + foff + (e / NT) * 9216 + (e % NT) * 1024);
                if (e >= 2 && e < 6) tn[e - 2] = rdf(tb + toff + ((e - 2) >> 1) * 8192 + ((e - 2) & 1) * 1024);
            }
            if constexpr (S + 3 < 18) { if (e < 2) hpiece(e, S + 3); }
            if constexpr (S + 4 < 18) {
                if (e == 3) g2_wpiece(0, S + 4, wsrc, aoff, mw0);
                if (e == 5) g2_wpiece(1, S + 4, wsrc, aoff, mw0);
                if (e == 6) g2_wpiece(2, S + 4, wsrc, aoff, mw0);
            }
            if constexpr (S == 4) {
                if (e == 7) dma4_sc1((unsigned)(lane & 31) * 4, ws + WS_FB + ((mt * 4 + ks) * 2 + 1) * 128, lds0 + G2_FL + wid * 256);
            }
            SCHED();
        }
    };
#define G2_IT2(S) iter(IC<S>{}, tf[0], tf[1]); iter(IC<S + 1>{}, tf[1], tf[0])
    G2_IT2(0); G2_IT2(2); G2_IT2(4); G2_IT2(6); G2_IT2(8); G2_IT2(10); G2_IT2(12);
    STAMP(9);
    G2_IT2(14);
    iter(IC<16>{}, tf[0], tf[1]);
    const int tok0 = mt * 128 + wr * 32 + (lane & 15);
    u32x2 hv[2][5];
    if (wr == ks) {
#pragma unroll
        for (int i = 0; i < 2; ++i) {
            const int t = tok0 + i * 16;
            const char* hp = ws + WS_H + ((size_t)((t >> 8) * 144 + nt * 36 + dh * 20) * 256 + (t & 255)) * 8;
#pragma unroll
            for (int e = 0; e < 4; ++e) hv[i][e] = *(const u32x2*)(hp + ((e >> 1) * 8 + (g4 & 1) * 4 + (g4 >> 1) * 2 + (e & 1)) * 2048);
            if (NT == 5) hv[i][4] = *(const u32x2*)(hp + (16 + g4) * 2048);
        }
    }
    SCHED();
    iter(IC<17>{}, tf[1], tf[0]);
    MFMA_DONE();
    STAMP(10);

    const rsrc_t rx = mk_rsrc(ws + WS_X, X_BYTES);
    const unsigned xt = (unsigned)((mt * 4 + nt) * 4 * 27648 + dh * 4096);
    auto inb = [&](int s) -> unsigned { return xt + (unsigned)(((ks + 1 + s) & 3) * 27648 + (2 - s) * 9216); };
    if (wr != ks) {
        const unsigned o = xt + (unsigned)(ks * 27648 + ((wr - ks - 1) & 3) * 9216);
#pragma unroll
        for (int i = 0; i < 2; ++i) {
            unsigned w[2 * NT];
#pragma unroll
            for (int j = 0; j < NT; ++j) { w[2 * j] = pk_bf16(acc[j][i][0], acc[j][i][1]); w[2 * j + 1] = pk_bf16(acc[j][i][2], acc[j][i][3]); }
#pragma unroll
            for (int e = 0; e < 2 * NT; ++e) w[e] = (w[e] == ~0u) ? 0x7fc07fc0u : w[e];
            if constexpr (NT == 5) st8_wt(rx, o + 8192 + i * 512 + lane * 8, (u32x2){w[8], w[9]});
            st16_wt(rx, o + i * 2048 + lane * 16, (u32x4){w[0], w[1], w[2], w[3]});
            st16_wt(rx, o + i * 2048 + 1024 + lane * 16, (u32x4){w[4], w[5], w[6], w[7]});
        }
    } else {
        u32x4 pa[3][4]; u32x2 pb[3][2];
#define XS0 16
#define XS1 1
#define XFULL 0
        __builtin_amdgcn_s_sleep(XS0);
        for (int spin = 0; spin < (1 << 18); ++spin) {
            bool go = XFULL && spin == 0;
            if (!go) {
                const u32x4 t = __builtin_amdgcn_raw_buffer_load_b128(rx, lane < 3 ? inb(lane) + 4080 : 0x80000000u, 0, 16);
                go = __all(t.w != ~0u);
            }
            if (go) {
                STAMP(11);
                unsigned m = 0u;
#pragma unroll
                for (int s = 0; s < 3; ++s) {
#pragma unroll
                    for (int p = 0; p < 4; ++p) pa[s][p] = __builtin_amdgcn_raw_buffer_load_b128(rx, inb(s) + p * 1024 + lane * 16, 0, 16);
                    if constexpr (NT == 5) {
#pragma unroll
                        for (int i = 0; i < 2; ++i) pb[s][i] = __builtin_amdgcn_raw_buffer_load_b64(rx, inb(s) + 8192 + i * 512 + lane * 8, 0, 16);
                    }
                }
#pragma unroll
                for (int s = 0; s < 3; ++s) {
#pragma unroll
                    for (int p = 0; p < 4; ++p) m = max(max(max(m, pa[s][p].x), max(pa[s][p].y, pa[s][p].z)), pa[s][p].w);
                    if constexpr (NT == 5) m = max(max(max(m, pb[s][0].x), max(pb[s][0].y, pb[s][1].x)), pb[s][1].y);
                }
                if (__all(m != ~0u)) break;
            }
            __builtin_amdgcn_s_sleep(XS1);
        }
        STAMP(12);
        const rsrc_t ro = mk_rsrc(out, S_ * D_ * 2);
#pragma unroll
        for (int i = 0; i < 2; ++i) {
            const unsigned ob = (unsigned)(((tok0 + i * 16) * D_ + nt * 144 + dh * 80) * 2);
            unsigned r[NT][2];
#pragma unroll
            for (int j = 0; j < NT; ++j) {
                f32x4 t = acc[j][i];
#pragma unroll
                for (int s = 0; s < 3; ++s) {
                    const unsigned w0 = j < 4 ? pa[s][i * 2 + (j >> 1)][(j & 1) * 2] : pb[s][i].x, w1 = j < 4 ? pa[s][i * 2 + (j >> 1)][(j & 1) * 2 + 1] : pb[s][i].y;
                    t += (f32x4){bf_lo(w0), bf_hi(w0), bf_lo(w1), bf_hi(w1)};
                }
                r[j][0] = pk_bf16(t[0], t[1]); r[j][1] = pk_bf16(t[2], t[3]);
            }
            auto addh = [&](unsigned a, unsigned h) -> unsigned { return pk_bf16(bf_lo(h) + bf_lo(a), bf_hi(h) + bf_hi(a)); };
#pragma unroll
            for (int p = 0; p < 2; ++p) {
                auto s0 = __builtin_amdgcn_permlane16_swap(r[2 * p][0], r[2 * p + 1][0], false, false);
                auto s1 = __builtin_amdgcn_permlane16_swap(r[2 * p][1], r[2 * p + 1][1], false, false);
                st16_wt(ro, ob + (unsigned)((p * 32 + (g4 & 1) * 16 + (g4 >> 1) * 8) * 2),
                        (u32x4){addh(s0[0], hv[i][2 * p].x), addh(s1[0], hv[i][2 * p].y), addh(s0[1], hv[i][2 * p + 1].x), addh(s1[1], hv[i][2 * p + 1].y)});
            }
            if constexpr (NT == 5) st8_wt(ro, ob + (unsigned)((64 + g4 * 4) * 2), (u32x2){addh(r[4][0], hv[i][4].x), addh(r[4][1], hv[i][4].y)});
        }
    }
    STAMP(13);
}

constexpr int K3_LDS = 163840;
constexpr int K3_SP = G_LDS;
constexpr int K3_CS = G2_W0 + 4 * 18432;
extern "C" __global__ __launch_bounds__(512, 2) void k3_gemm1(const u16* __restrict__ wg, const u16* __restrict__ wu,
                                                   char* __restrict__ ws, u16* __restrict__ out) {
    extern __shared__ __attribute__((aligned(16))) char smem[];
    LAS char* lds = (LAS char*)smem;
    const unsigned lds0 = (unsigned)(size_t)lds;
    const int tid = threadIdx.x, lane = tid & 63;
    const int wid = __builtin_amdgcn_readfirstlane(tid >> 6);
    const int wr = wid & 3, wc = wid >> 2;
    const int b = blockIdx.x, xcd = b & 7, idx = b >> 3;
    const int rt = (xcd >> 2) * 4 + (idx & 3), ct = (xcd & 3) * 8 + (idx >> 2);
    asm volatile("" :: "s"(wg), "s"(wu), "s"(ws), "s"(out));
    DIAG_DECL;

    const char* asrc = ws + WS_H + (size_t)rt * 144 * 2048 + wid * 2048;
    const unsigned aoff = (unsigned)(lane ^ ((wid & 2) << 2)) * 16;
    const unsigned ma0 = lds0 + wid * 2048, mw0 = lds0 + 16384 + wc * 9216 + wr * 2304;
    dma16<0>(aoff, asrc, ma0); dma16<1024>(aoff, asrc, ma0);
    const char* wsrc = (const char*)(wc ? wu : wg) + ct * 288 - 2048;
    auto wofs = [&](int i) -> unsigned {
        const int q = lane + 64 * i, lrow = 8 * wr + q / 18, pos = q % 18;
        const int krow = ((lrow >> 2) & 3) * 8 + (lrow >> 4) * 4 + (lrow & 3);
        const int col = pos * 8 + (pos < 8 ? 0 : (pos < 16 ? 8 : (((pos & 1) ^ wc) ? 136 : 64) - pos * 8));
        return (unsigned)(krow * (F_ * 2) + col * 2 + 2048 - 1024 * i);
    };
    const unsigned w0 = wofs(0), w1 = wofs(1), w2 = wofs(2);
    auto piece = [&](int P, int s) {
        const unsigned bo = (s & 3) * ST_BYTES;
        const char* wp = wsrc + (size_t)s * (32 * F_ * 2);
        if (P == 0) dma16<0>(aoff, asrc + (size_t)s * 16384, ma0 + bo);
        if (P == 1) dma16<1024>(aoff, asrc + (size_t)s * 16384, ma0 + bo);
        if (P == 2) dma16<0>(w0, wp, mw0 + bo);
        if (P == 3) dma16<1024>(w1, wp, mw0 + bo);
        if (P == 4) dma16_q<2048>(w2, wp, mw0 + bo);
    };
#define K3_PIECE(P, s) piece(P, s)
#define K3_STAGE(s) do { K3_PIECE(0, s); K3_PIECE(1, s); K3_PIECE(2, s); K3_PIECE(3, s); K3_PIECE(4, s); } while (0)
    K3_PIECE(2, 0); K3_PIECE(3, 0); K3_PIECE(4, 0);
    BAR();
    K3_STAGE(1);
    u32x2 hs[4];
    {
        const char* hsrc = ws + WS_H + (size_t)rt * 144 * 2048 + ct * 64;
        const unsigned o = (unsigned)((wid * 18 + (lane >> 3)) * 2048 + (lane & 7) * 8);
        asm volatile("global_load_dwordx2 %0, %1, %2" : "=v"(hs[0]) : "v"(o), "s"(hsrc) : "memory");
        asm volatile("global_load_dwordx2 %0, %1, %2" : "=v"(hs[1]) : "v"(o + 16384), "s"(hsrc) : "memory");
        asm volatile("global_load_dwordx2 %0, %1, %2" : "=v"(hs[2]) : "v"((unsigned)((wid * 18 + 16 + ((lane >> 3) & 1)) * 2048 + (lane & 7) * 8)), "s"(hsrc) : "memory");
        asm volatile("global_load_dwordx2 %0, %1, %2" : "=v"(hs[3]) : "v"((unsigned)(lane & 7) * 8), "s"(hsrc) : "memory");
    }

    const int g4 = lane >> 4, q4 = (lane & 15) >> 2, p4 = lane & 3;
    const int rowoff = (4 * g4 + q4) * 288;
    const int tbase = g4 * 4096 + wr * 512 + (lane & 15) * 8;
    const int toff0 = tbase + ((2 * wc) ^ (g4 & 1)) * 128, toff1 = tbase + ((2 * wc + 1) ^ (g4 & 1)) * 128;
    const int tstep = wc ? -256 : 256;
    const int foff = 16384 + rowoff + wc * 128 + p4 * 8;
    const int moff = 16384 + rowoff + 256 + (p4 < 2 ? 16 * wc + 8 * p4 : 9216 + 16 * (1 - wc) + 8 * (p4 - 2));

    f32x4 acc[9][4];
    s16x8 wf[9], tf[2][4];
    auto rd_w = [&](const LAS char* sb, int j) -> s16x8 {
        return j < 8 ? tr_frag(sb + foff + (j >> 2) * 9216 + (j & 3) * 32) : tr_frag(sb + moff);
    };
    auto rd_t = [&](const LAS char* sb, int i) -> s16x8 {
        const LAS char* p = sb + ((i & 1) ? toff1 : toff0) + (i >> 1) * tstep;
        const s16x4 lo = *(const volatile LAS s16x4*)p, hi = *(const volatile LAS s16x4*)(p + 2048);
        return __builtin_shufflevector(lo, hi, 0, 1, 2, 3, 4, 5, 6, 7);
    };

    STAMP(1);
    WAIT_V(9); BAR();
    STAMP(2);
    wf[0] = rd_w(lds, 0);
#pragma unroll
    for (int i = 0; i < 4; ++i) tf[0][i] = rd_t(lds, i);
#pragma unroll
    for (int j = 1; j < 9; ++j) wf[j] = rd_w(lds, j);
    SCHED();
    const int mt2 = (xcd >> 2) * 8 + (idx & 7), ks2 = xcd & 3, nt2 = idx >> 3;
    const rsrc_t rfill = mk_rsrc(ws + WS_X + (size_t)((mt2 * 4 + nt2) * 4 + ks2) * 27648, 27648);
    auto fill = [&](int k) { st16_wt(rfill, (unsigned)(k * 8192 + tid * 16), (u32x4){~0u, ~0u, ~0u, ~0u}); };
    unsigned sgr;
    {
        const LAS char* sb = lds + ST_BYTES;
        float hc = 0.f;
#pragma unroll
        for (int j = 0; j < 9; ++j) {
#pragma unroll
            for (int i = 0; i < 4; ++i) mfma_first(acc[j][i], wf[j], tf[0][i]);
            SCHED();
            if (j == 0) { K3_PIECE(0, 2); K3_PIECE(1, 2); }
            if (j == 1) { K3_PIECE(2, 2); K3_PIECE(3, 2); }
            if (j == 2) K3_PIECE(4, 2);
            if (j == 3) {
                asm volatile("s_waitcnt vmcnt(5)" : "+v"(hs[0]), "+v"(hs[1]), "+v"(hs[2]), "+v"(hs[3]) :: "memory");
                hc = bf_lo(hs[3].x);
                float s1 = 0.f, s2 = 0.f;
#pragma unroll
                for (int i = 0; i < 3; ++i) {
                    const float d[4] = {bf_lo(hs[i].x) - hc, bf_hi(hs[i].x) - hc, bf_lo(hs[i].y) - hc, bf_hi(hs[i].y) - hc};
                    float a = (d[0] + d[1]) + (d[2] + d[3]), q = __builtin_fmaf(d[0], d[0], d[1] * d[1]) + __builtin_fmaf(d[2], d[2], d[3] * d[3]);
                    if (i == 2 && lane >= 16) { a = 0.f; q = 0.f; }
                    s1 += a; s2 += q;
                }
                s1 = x32_add(x16_add(dpp_add<0x128>(s1))); s2 = x32_add(x16_add(dpp_add<0x128>(s2)));
                if (lane < 8) *(LAS f32x2*)(lds + K3_SP + (lane * 8 + wid) * 8) = (f32x2){s1, s2};
                asm volatile("s_waitcnt lgkmcnt(0)" ::: "memory");
                BAR();
                wf[0] = rd_w(sb, 0); wf[1] = rd_w(sb, 1);
                tf[1][0] = rd_t(sb, 0); tf[1][1] = rd_t(sb, 1);
            }
            if (j == 4) { tf[1][2] = rd_t(sb, 2); tf[1][3] = rd_t(sb, 3); wf[2] = rd_w(sb, 2); wf[3] = rd_w(sb, 3); }
            if (j >= 4) { wf[j] = rd_w(sb, j); K3_PIECE(j - 4, 3); }
            if (j == 8) {
                const f32x2 pp = *(const LAS f32x2*)(lds + K3_SP + (wid * 8 + (lane & 7)) * 8);
                const float s1 = dpp_add<0x141>(dpp_add<0x4E>(dpp_add<0xB1>(pp.x))) * (1.f / D_);
                const float s2 = dpp_add<0x141>(dpp_add<0x4E>(dpp_add<0xB1>(pp.y))) * (1.f / D_);
                const float c0 = __uint_as_float(__builtin_amdgcn_readlane(__float_as_uint(hc), wid));
                const float sd = ln_sd(__builtin_fmaxf(__builtin_fmaf(-s1, s1, s2), 0.f));
                sgr = pk_bf16(c0 + s1, sd);
                fill(0); fill(1);
            }
            SCHED();
        }
    }

    const rsrc_t rg = mk_rsrc(ws + WS_G2 + (size_t)(rt * 256 + wr * 64) * 8, 512);
    const char* w2src = ws + WS_WT + (size_t)(ks2 * 4 + nt2) * 18 * 18432 + wid * 2304;
    u32x2 gr[4];
    s16x4 tl;
#define TP 1
#define DPJ0 0
#define DPJ1 5
    auto iter = [&](auto mode, auto vm, int s, s16x8 (&tc)[4], s16x8 (&tn)[4]) {
        constexpr int MODE = decltype(mode)::v;
        if constexpr (MODE != 4) {
            if constexpr (MODE == 3) WAIT_V(0); else WAIT_VI(5 + decltype(vm)::v);
            BAR();
            SCHED();
        }
        const LAS char* sb = lds + ((s + 1) & 3) * ST_BYTES;
#pragma unroll
        for (int j = 0; j < 9; ++j) {
            s16x4 wl, wh;
#pragma unroll
            for (int i = 0; i < 4; ++i) {
                mfma_acc(acc[j][i], wf[j], tc[i]);
                SCHED();
                if constexpr (MODE != 4) {
                    if (j >= 1) {
                        const LAS char* p = (j - 1 < 8) ? sb + foff + ((j - 1) >> 2) * 9216 + ((j - 1) & 3) * 32 : sb + moff;
                        if (i == 0) wl = __builtin_amdgcn_ds_read_tr16_b64_v4i16((LAS s16x4*)p);
                        if (i == 1) { wh = __builtin_amdgcn_ds_read_tr16_b64_v4i16((LAS s16x4*)(p + 16 * 288)); wf[j - 1] = __builtin_shufflevector(wl, wh, 0, 1, 2, 3, 4, 5, 6, 7); }
                    }
                    if (j >= TP && j < TP + 4) {
                        const int ti = j - TP;
                        const LAS char* p = sb + ((ti & 1) ? toff1 : toff0) + (ti >> 1) * tstep;
                        if (i == 2) tl = *(const volatile LAS s16x4*)p;
                        if (i == 3) tn[ti] = __builtin_shufflevector(tl, *(const volatile LAS s16x4*)(p + 2048), 0, 1, 2, 3, 4, 5, 6, 7);
                    }
                    if (j == 8 && i == 3) wf[8] = rd_w(sb, 8);
                }
                if constexpr (MODE < 2 || MODE == 5) {
                    if (j == DPJ0 && i == 1) K3_PIECE(0, s + 3);
                    if (j == DPJ0 && i == 3) K3_PIECE(1, s + 3);
                    if (j == DPJ1 && i == 3) K3_PIECE(2, s + 3);
                    if (j == DPJ1 + 1 && i == 3) K3_PIECE(3, s + 3);
                    if (j == DPJ1 + 2 && i == 3) K3_PIECE(4, s + 3);
                }
                SCHED();
            }
            if constexpr (MODE == 0) { if (j == 8) { fill(2); fill(3); } }
            if constexpr (MODE == 5) {
                if (j == 0 && s == 4) st8_wt(mk_rsrc(ws + WS_G2, S_ * 8), lane == 0 ? (unsigned)((rt * 256 + ct * 8 + wid) * 8) : 0x80000000u, (u32x2){sgr, 1u});
            }
            if constexpr (MODE == 2) {
                if (j == 0) {
#pragma unroll
                    for (int i = 0; i < 4; ++i) gr[i] = __builtin_amdgcn_raw_buffer_load_b64(rg, (unsigned)(((i ^ (2 * wc)) * 16 + (lane & 15)) * 8), 0, 16);
                }
            }
            if constexpr (MODE == 3) {
                if (j == 0) {
                    asm volatile("" : "+v"(gr[0]), "+v"(gr[1]), "+v"(gr[2]), "+v"(gr[3]));
                    const unsigned co = (unsigned)((lane >= 18 ? F_ * 4 + (lane - 18) * 16 : lane * 16));
                    dma_m36(co, ws + WS_CS + (size_t)(ct * 144 + wc * 72) * 4, lds0 + K3_CS + wid * 1152);
                    dma_m36(co, ws + WS_CS + (size_t)(2 * F_ + ct * 144 + wc * 72) * 4, lds0 + K3_CS + wid * 1152 + 576);
                }
            }
            if constexpr (MODE == 3 || MODE == 4) {
                if (j >= 2 && j < 8) g2_wpiece((j - 2) % 3, (MODE - 3) * 2 + (j - 2) / 3, w2src, lane * 16, lds0 + G2_W0 + wid * 2304);
            }
            SCHED();
        }
    };
    iter(IC<0>{}, IC<2>{}, 1, tf[1], tf[0]);
    iter(IC<1>{}, IC<4>{}, 2, tf[0], tf[1]);
    iter(IC<1>{}, IC<2>{}, 3, tf[1], tf[0]);
#pragma unroll 1
    for (int s = 4; s < 14; s += 2) {
        iter(IC<5>{}, IC<0>{}, s, tf[0], tf[1]);
        iter(IC<1>{}, IC<0>{}, s + 1, tf[1], tf[0]);
    }
    STAMP(3);
    iter(IC<1>{}, IC<0>{}, 14, tf[0], tf[1]);
    iter(IC<2>{}, IC<0>{}, 15, tf[1], tf[0]);
    iter(IC<3>{}, IC<0>{}, 16, tf[0], tf[1]);
    iter(IC<4>{}, IC<0>{}, 17, tf[1], tf[0]);
    MFMA_DONE();
    STAMP(4);
    WAIT_V(12);
    __builtin_amdgcn_s_setprio(1);
    if (__builtin_expect(!__all((gr[0].y & gr[1].y & gr[2].y & gr[3].y) == 1u), 0)) {
        for (int spin = 0; spin < (1 << 20); ++spin) {
            __builtin_amdgcn_s_sleep(2);
#pragma unroll
            for (int i = 0; i < 4; ++i) gr[i] = __builtin_amdgcn_raw_buffer_load_b64(rg, (unsigned)(((i ^ (2 * wc)) * 16 + (lane & 15)) * 8), 0, 16);
            if (__all((gr[0].y & gr[1].y & gr[2].y & gr[3].y) == 1u)) break;
        }
    }
    const int mt = rt * 2 + (wr >> 1), ks = ct >> 3, lct = ct & 7;
    const rsrc_t rhh = mk_rsrc(ws + WS_HH, S_ * F_ * 2);
    const unsigned hb = (unsigned)(((mt * 4 + ks) * 36) * 8192 + ((wr & 1) * 4) * 1024);
    const LAS char* csb = lds + K3_CS + wid * 1152 + 16 * g4;
    TokC tcs[4];
#pragma unroll
    for (int i = 0; i < 4; ++i) {
        const float sd = bf_hi(gr[i].x), r = __builtin_amdgcn_rcpf(sd);
        tcs[i] = (TokC){-bf_lo(gr[i].x), -2.30220819f * r, -0.10294324f * (r * r * r), sd * sd};
    }
    {
        const LAS char* cm = lds + K3_CS + wid * 1152 + 256 + 16 * (g4 & 1);
        const f32x4 cgm = *(const LAS f32x4*)cm + *(const LAS f32x4*)(cm + 576), cum = *(const LAS f32x4*)(cm + 288) + *(const LAS f32x4*)(cm + 864);
#pragma unroll
        for (int ip = 0; ip < 2; ++ip) {
            const int ia = ((2 * ip) ^ (2 * wc)) + (lane >> 5);
            const bool up = lane >= 32;
            const TokC tc = {up ? tcs[2 * ip + 1].nm : tcs[2 * ip].nm, up ? tcs[2 * ip + 1].c0 : tcs[2 * ip].c0,
                             up ? tcs[2 * ip + 1].c1 : tcs[2 * ip].c1, up ? tcs[2 * ip + 1].q : tcs[2 * ip].q};
            f32x4 gm, um;
#pragma unroll
            for (int r = 0; r < 4; ++r) {
                auto sw = __builtin_amdgcn_permlane32_swap(__float_as_uint(acc[8][2 * ip][r]), __float_as_uint(acc[8][2 * ip + 1][r]), false, false);
                gm[r] = __uint_as_float(sw[0]); um[r] = __uint_as_float(sw[1]);
            }
            const int kgs = (lct & 1) * 2 + wc;
            st8_wt(rhh, hb + (unsigned)((lct >> 1) * 8192 + ia * 1024 + (kgs * 16 + (lane & 15)) * 16 + (g4 & 1) * 8),
                   (u32x2){geglu2(LO2(gm), LO2(um), LO2(cgm), LO2(cum), tc), geglu2(HI2(gm), HI2(um), HI2(cgm), HI2(cum), tc)});
        }
    }
    SCHED();
#define FLAGPOS 1
    const rsrc_t rfb = mk_rsrc(ws + WS_FB, 16384);
    const unsigned fbo = (unsigned)((mt * 4 + ks) * 256 + (lct * 4 + (wr & 1) * 2 + wc) * 4);
    u32x4 ob[4];
#pragma unroll
    for (int jp = 0; jp < 2; ++jp) {
        f32x4 cg[2], cu[2];
#pragma unroll
        for (int e = 0; e < 2; ++e) {
            cg[e] = *(const LAS f32x4*)(csb + (2 * jp + e) * 64) + *(const LAS f32x4*)(csb + 576 + (2 * jp + e) * 64);
            cu[e] = *(const LAS f32x4*)(csb + 288 + (2 * jp + e) * 64) + *(const LAS f32x4*)(csb + 864 + (2 * jp + e) * 64);
        }
#pragma unroll
        for (int i = 0; i < 4; ++i) {
            unsigned o[4];
#pragma unroll
            for (int e = 0; e < 2; ++e) {
                const f32x4 g = acc[2 * jp + e][i], u = acc[4 + 2 * jp + e][i];
                o[2 * e] = geglu2(LO2(g), LO2(u), LO2(cg[e]), LO2(cu[e]), tcs[i]);
                o[2 * e + 1] = geglu2(HI2(g), HI2(u), HI2(cg[e]), HI2(cu[e]), tcs[i]);
            }
            ob[i] = (u32x4){o[0], o[1], o[2], o[3]};
            st16_wt(rhh, (jp == 1 && wid == 0) ? 0x80000000u : hb + (unsigned)(((2 + jp * 8 + lct) * 2 + wc) * 8192 + (i ^ (2 * wc)) * 1024 + lane * 16), ob[i]);
            if (jp == 1 && i == FLAGPOS) {
                SCHED();
                WAIT_VI(FLAGPOS + 1);
                __builtin_amdgcn_raw_buffer_store_b32(1u, rfb, lane == 0 ? fbo : 0x80000000u, 0, 16);
                SCHED();
            }
        }
        SCHED();
        if (jp == 0) __builtin_amdgcn_s_setprio(0);
    }
    STAMP(5);
    if (wid == 0) {
        for (int spin = 0; spin < (1 << 20); ++spin) {
            const unsigned f = __builtin_amdgcn_raw_buffer_load_b32(rfb, (unsigned)((mt2 * 4 + ks2) * 256 + (lane & 31) * 4), 0, 16);
            if (__all(f == 1u)) break;
            __builtin_amdgcn_s_sleep(2);
        }
#pragma unroll
        for (int i = 0; i < 4; ++i) st16_wt(rhh, hb + (unsigned)(((10 + lct) * 2 + wc) * 8192 + (i ^ (2 * wc)) * 1024 + lane * 16), ob[i]);
    }
    BAR();
    STAMP(7);
    if (wid < 4) g2_body<5>(ws, out, lds, lane, wid, mt2, ks2, nt2, w2src, fbo + 128 DIAG_PASS);
    else g2_body<4>(ws, out, lds, lane, wid, mt2, ks2, nt2, w2src, fbo + 128 DIAG_PASS);
    DIAG_END(ws, 1);
}

extern "C" __global__ void k9_end() {}

extern "C" void kernel_launch(void* const* d_in, const int* in_sizes, int n_in, void* d_out, int out_size, void* d_ws,
                              size_t ws_size, hipStream_t stream) {
    const u16* q = (const u16*)d_in[0];
    const u16* k = (const u16*)d_in[1];
    const u16* v = (const u16*)d_in[2];
    const u16* shift_k = (const u16*)d_in[3];
    const u16* ssm_k = (const u16*)d_in[4];
    const u16* ssm_D = (const u16*)d_in[5];
    const u16* wg = (const u16*)d_in[7];
    const u16* wu = (const u16*)d_in[8];
    const u16* wd = (const u16*)d_in[9];
    char* ws = (char*)d_ws;
    (void)hipFuncSetAttribute((const void*)k1_conv, hipFuncAttributeMaxDynamicSharedMemorySize, C1_LDS);
    (void)hipFuncSetAttribute((const void*)k3_gemm1, hipFuncAttributeMaxDynamicSharedMemorySize, K3_LDS);
    k1_conv<<<256, 512, C1_LDS, stream>>>(q, k, v, shift_k, ssm_k, ssm_D, wg, wu, wd, ws);
    k3_gemm1<<<256, 512, K3_LDS, stream>>>(wg, wu, ws, (u16*)d_out);
}
```

```cpp
#include <hip/hip_runtime.h>
#include <hip/hip_bf16.h>

#define LAS __attribute__((address_space(3)))
typedef unsigned short u16;
typedef short s16x4 __attribute__((ext_vector_type(4)));
typedef short s16x8 __attribute__((ext_vector_type(8)));
typedef float f32x2 __attribute__((ext_vector_type(2)));
typedef float f32x4 __attribute__((ext_vector_type(4)));
typedef unsigned u32x2 __attribute__((ext_vector_type(2)));
typedef unsigned u32x4 __attribute__((ext_vector_type(4)));
typedef __bf16 bf16x2_t __attribute__((ext_vector_type(2)));

constexpr int S_ = 2048, D_ = 576, F_ = 4608;

__device__ __forceinline__ unsigned pk_bf16(float lo, float hi) {
    f32x2 v = {lo, hi};
    bf16x2_t b = __builtin_convertvector(v, bf16x2_t);
    return __builtin_bit_cast(unsigned, b);
}
__device__ __forceinline__ float bf_lo(unsigned w) { return __uint_as_float(w << 16); }
__device__ __forceinline__ float bf_hi(unsigned w) { return __uint_as_float(w & 0xffff0000u); }
__device__ __forceinline__ float rbf(float x) { return bf_lo(pk_bf16(x, 0.f)); }
#define WAIT_V(n) asm volatile("s_waitcnt vmcnt(" #n ")" ::: "memory")
#define BAR() do { __builtin_amdgcn_s_barrier(); asm volatile("" ::: "memory"); } while (0)
#define SCHED() __builtin_amdgcn_sched_barrier(0)
#define DIAG_DECL
#define MARK()
#define STAMP(i)
#define DIAG_END(wsbase, kid)
__device__ __forceinline__ void mfma_acc(f32x4& c, s16x8 a, s16x8 b) {
    asm volatile("v_mfma_f32_16x16x32_bf16 %0, %1, %2, %0" : "+v"(c) : "v"(a), "v"(b));
}
__device__ __forceinline__ void mfma_first(f32x4& c, s16x8 a, s16x8 b) {
    asm volatile("v_mfma_f32_16x16x32_bf16 %0, %1, %2, 0" : "=&v"(c) : "v"(a), "v"(b));
}
#define MFMA_DONE() do { asm volatile("s_nop 7\n\ts_nop 7" ::: "memory"); SCHED(); } while (0)
constexpr size_t WS_KV = 0;
constexpr size_t WS_H = WS_KV + (size_t)S_ * D_ * 2;
constexpr size_t WS_ST = WS_H + (size_t)S_ * D_ * 2;
constexpr size_t WS_CS = WS_ST + (size_t)S_ * 8;
constexpr size_t WS_G2 = WS_CS + (size_t)4 * F_ * 4;
constexpr size_t WS_F0 = WS_G2 + (size_t)S_ * 8;
constexpr int FSP = 128;
constexpr size_t WS_EP = WS_F0 + 1280 * FSP;
constexpr size_t WS_HH = WS_EP + 1024;
constexpr size_t WS_X = WS_HH + (size_t)S_ * F_ * 2;
constexpr unsigned X_BYTES = 64 * 4 * 3 * 9216;
constexpr size_t WS_FB = WS_X + X_BYTES;
constexpr size_t WS_WT = WS_FB + 16384;

template <int N> struct IC { static constexpr int v = N; };
typedef __amdgpu_buffer_rsrc_t rsrc_t;
__device__ __forceinline__ rsrc_t mk_rsrc(const void* p, unsigned bytes) { return __builtin_amdgcn_make_buffer_rsrc((void*)p, 0, bytes, 0x00020000); }
__device__ __forceinline__ void st16_wt(rsrc_t r, unsigned off, u32x4 v) { __builtin_amdgcn_raw_buffer_store_b128(v, r, off, 0, 16); }
__device__ __forceinline__ void st8_wt(rsrc_t r, unsigned off, u32x2 v) { __builtin_amdgcn_raw_buffer_store_b64(v, r, off, 0, 16); }
template <int CTRL> __device__ __forceinline__ float dpp_add(float x) {
    return x + __uint_as_float(__builtin_amdgcn_update_dpp(0, (int)__float_as_uint(x), CTRL, 0xf, 0xf, true));
}
__device__ __forceinline__ float x16_add(float x) {
    auto r = __builtin_amdgcn_permlane16_swap(__float_as_uint(x), __float_as_uint(x), false, false);
    return __uint_as_float(r[0]) + __uint_as_float(r[1]);
}
__device__ __forceinline__ float x32_add(float x) {
    auto r = __builtin_amdgcn_permlane32_swap(__float_as_uint(x), __float_as_uint(x), false, false);
    return __uint_as_float(r[0]) + __uint_as_float(r[1]);
}
__device__ __forceinline__ float sum16(float x) {
    x = dpp_add<0xB1>(x); x = dpp_add<0x4E>(x); x = dpp_add<0x141>(x);
    return dpp_add<0x140>(x);
}
__device__ __forceinline__ float ln_sd(float var) { return rbf(__builtin_amdgcn_sqrtf(rbf(rbf(var) + 1e-5f))); }

template <int OFF>
__device__ __forceinline__ void dma16(unsigned voff, const void* sbase, unsigned m0v) {
    asm volatile("s_mov_b32 m0, %2\n\ts_nop 0\n\tglobal_load_lds_dwordx4 %0, %1 offset:%3"
                 :: "v"(voff), "s"(sbase), "s"(m0v), "i"(OFF) : "memory");
}
template <int OFF>
__device__ __forceinline__ void dma16_sc1(unsigned voff, const void* sbase, unsigned m0v) {
    asm volatile("s_mov_b32 m0, %2\n\ts_nop 0\n\tglobal_load_lds_dwordx4 %0, %1 offset:%3 sc1"
                 :: "v"(voff), "s"(sbase), "s"(m0v), "i"(OFF) : "memory");
}
template <int OFF>
__device__ __forceinline__ void dma16_q(unsigned voff, const void* sbase, unsigned m0v) {
    asm volatile("s_mov_b32 m0, %2\n\ts_mov_b64 exec, 0xffff\n\tglobal_load_lds_dwordx4 %0, %1 offset:%3\n\ts_mov_b64 exec, -1"
                 :: "v"(voff), "s"(sbase), "s"(m0v), "i"(OFF) : "memory");
}
__device__ __forceinline__ void dma_m36(unsigned voff, const void* sbase, unsigned m0v) {
    asm volatile("s_mov_b32 m0, %2\n\ts_mov_b32 exec_hi, 15\n\tglobal_load_lds_dwordx4 %0, %1\n\ts_mov_b64 exec, -1"
                 :: "v"(voff), "s"(sbase), "s"(m0v) : "memory");
}
__device__ __forceinline__ void dma4(unsigned voff, const void* sbase, unsigned m0v) {
    asm volatile("s_mov_b32 m0, %2\n\ts_nop 0\n\tglobal_load_lds_dword %0, %1" :: "v"(voff), "s"(sbase), "s"(m0v) : "memory");
}
__device__ __forceinline__ void dma4_sc1(unsigned voff, const void* sbase, unsigned m0v) {
    asm volatile("s_mov_b32 m0, %2\n\ts_nop 0\n\tglobal_load_lds_dword %0, %1 sc1" :: "v"(voff), "s"(sbase), "s"(m0v) : "memory");
}
#define SYM_ADDR(out, sym) asm volatile("s_getpc_b64 s[40:41]\n\ts_add_u32 s40, s40, " #sym "@rel32@lo+4\n\ts_addc_u32 s41, s41, " #sym "@rel32@hi+12\n\ts_mov_b64 %0, s[40:41]" \
                                        : "=s"(out) :: "s40", "s41", "scc")
template <int NI>
__device__ __forceinline__ void warm_code(const char* a, const char* e, int l0, int lane, unsigned m0v) {
    const unsigned last = e > a ? (unsigned)(e - a) - 4u : 0u;
#pragma unroll
    for (int i = 0; i < NI; ++i) dma4(min((unsigned)(l0 + i * 64 + lane) * 128u, last), a, m0v);
}
__device__ __forceinline__ s16x8 tr_frag(const LAS char* p) {
    const s16x4 lo = __builtin_amdgcn_ds_read_tr16_b64_v4i16((LAS s16x4*)p);
    const s16x4 hi = __builtin_amdgcn_ds_read_tr16_b64_v4i16((LAS s16x4*)(p + 16 * 288));
    return __builtin_shufflevector(lo, hi, 0, 1, 2, 3, 4, 5, 6, 7);
}
__device__ __forceinline__ void wd_unit_load(const u16* wd, int un, int lane, unsigned m0v) {
    const int uB = un / 3, ut = un % 3, kh = uB & 1, st = (uB >> 1) % 18, kn = uB / 36;
    const bool full = st >= 2;
    const int f0 = (kn >> 2) * 1152 + (full ? ((st - 2) & 7) * 144 + kh * 72 + ((st - 2) >> 3) * 32 : (st * 2 + kh) * 288);
    const char* src = (const char*)wd + (size_t)f0 * (D_ * 2) + (kn & 3) * 288 + ut * 96;
#pragma unroll
    for (int i = 0; i < 3; ++i) {
        const int q = lane + 64 * i, r = q / 6, pos = q % 6, kg = (r & 15) >> 2;
        const int f = full ? r : (kg >> 1) * 144 + (kg & 1) * 72 + 64 + (r & 3) + 4 * (r >> 4);
        dma16<0>((unsigned)(f * (D_ * 2) + pos * 16), src, m0v + i * 1024);
    }
}
__device__ __forceinline__ void wd_unit_store(char* ws, int un, int lane, const LAS char* l) {
    const rsrc_t r = mk_rsrc(ws + WS_WT, F_ * D_ * 2);
#pragma unroll
    for (int t = 0; t < 3; ++t) {
        const LAS char* tp = l + (lane >> 2) * 96 + (lane & 3) * 8 + t * 32;
        const s16x4 lo = __builtin_amdgcn_ds_read_tr16_b64_v4i16((LAS s16x4*)tp), hi = __builtin_amdgcn_ds_read_tr16_b64_v4i16((LAS s16x4*)(tp + 16 * 96));
        st16_wt(r, (unsigned)(un * 3072 + t * 1024 + lane * 16), __builtin_bit_cast(u32x4, __builtin_shufflevector(lo, hi, 0, 1, 2, 3, 4, 5, 6, 7)));
    }
}
constexpr int WU4 = 4;
constexpr int HELPER_DELAY = 24, POLL0 = 24, POLL1 = 2;
constexpr int C1_COPY = 4288;
constexpr int C1_FILT = 4 * C1_COPY;
constexpr int C1_KV = 2304 * 2;
constexpr int C1_KVOFF = 4 * C1_FILT + 64;
constexpr int C1_YOFF = C1_KVOFF + 4 * C1_KV;
constexpr int C1_XOFF = C1_YOFF + 16384;
constexpr int C1_SOFF = C1_XOFF + 16384;
constexpr int C1_TOFF = C1_SOFF + 16384;
constexpr int C1_WOFF = C1_TOFF + 2048;
constexpr int C1_LDS = C1_WOFF + 24576;

extern "C" __global__ __launch_bounds__(512, 2) void k1_conv(const u16* __restrict__ x, const u16* __restrict__ kin, const u16* __restrict__ vin,
                                                  const u16* __restrict__ shift_k, const u16* __restrict__ ssm_k, const u16* __restrict__ ssm_D,
                                                  const u16* __restrict__ wg, const u16* __restrict__ wu, const u16* __restrict__ wd,
                                                  char* __restrict__ ws) {
    extern __shared__ __attribute__((aligned(16))) char smem[];
    const int tid = threadIdx.x, lane = tid & 63;
    const int wid = __builtin_amdgcn_readfirstlane(tid >> 6);
    const int b = blockIdx.x, xcd = b & 7, idx = b >> 3;
    {
        const char *c1, *c3;
        SYM_ADDR(c1, k1_conv); SYM_ADDR(c3, k3_gemm1);
        if (wid >= 4) warm_code<1>(c1, c3, (wid & 3) * 64, lane, (unsigned)(size_t)(LAS char*)smem + C1_TOFF + 256 + (wid & 3) * 256);
    }
    DIAG_DECL;
    asm volatile("" :: "s"(x), "s"(kin), "s"(vin), "s"(shift_k), "s"(ssm_k), "s"(ssm_D), "s"(wg), "s"(wu), "s"(wd), "s"(ws));
    const bool conv = idx < 18;
    const int cg = xcd * 18 + idx, ch0 = cg * 4;
    const unsigned lds0 = (unsigned)(size_t)(LAS char*)smem;
    LAS unsigned* tagp = (LAS unsigned*)(smem + C1_TOFF);
    auto get_tag = [&]() -> unsigned { BAR(); return __builtin_amdgcn_readfirstlane(*(volatile LAS unsigned*)tagp); };

    const int h = (idx - 18) * 8 + xcd, hc_ = h < 108 ? h : 0;
    const int m3 = hc_ % 3, lcA = (hc_ / 3) * 4 + m3, rb0 = m3 * 3;
    constexpr int HU = (1728 - 144 * WU4 + 111) / 112;
    const int hbase = 144 * WU4 + h * HU, hun = hbase + wid * 2, hend = min(hbase + HU, 1728);
    auto cs_load = [&](u32x4 (&v)[12]) {
        const int rs = tid >> 3, c8 = tid & 7;
#pragma unroll
        for (int p = 0; p < 12; ++p) {
            const int rb = rb0 + p, lc = lcA + (rb >= 9), r = (rb >= 9 ? rb - 9 : rb) * 64 + rs;
            v[p] = *(const u32x4*)((lc >= 72 ? wu : wg) + (size_t)r * F_ + (lc % 72) * 64 + c8 * 8);
        }
        SCHED();
#pragma unroll
        for (int u = 0; u < 2; ++u) if (hun + u < hend) wd_unit_load(wd, hun + u, lane, lds0 + 16384 + (wid * 2 + u) * 3072);
        st16_wt(mk_rsrc(ws + WS_FB, 16384), (unsigned)(h * 8192 + tid * 16), (u32x4){0u, 0u, 0u, 0u});
        SCHED();
    };
    auto cs_finish = [&](u32x4 (&v)[12]) {
        float g[4][8];
#pragma unroll
        for (int q = 0; q < 4; ++q) {
#pragma unroll
            for (int e = 0; e < 4; ++e) {
                g[q][2 * e] = (bf_lo(v[3 * q][e]) + bf_lo(v[3 * q + 1][e])) + bf_lo(v[3 * q + 2][e]);
                g[q][2 * e + 1] = (bf_hi(v[3 * q][e]) + bf_hi(v[3 * q + 1][e])) + bf_hi(v[3 * q + 2][e]);
            }
        }
        LAS float* red = (LAS float*)smem;
#pragma unroll
        for (int l = 0; l < 2; ++l) {
            float a[8];
#pragma unroll
            for (int e = 0; e < 8; ++e) {
                a[e] = l == 0 ? g[0][e] + (m3 <= 1 ? g[1][e] : 0.f) + (m3 == 0 ? g[2][e] : 0.f) : g[3][e] + (m3 >= 1 ? g[2][e] : 0.f) + (m3 == 2 ? g[1][e] : 0.f);
                a[e] = x32_add(x16_add(dpp_add<0x128>(a[e])));
            }
            if (lane < 8) {
                *(LAS f32x4*)(red + (wid * 2 + l) * 64 + lane * 8) = (f32x4){a[0], a[1], a[2], a[3]};
                *(LAS f32x4*)(red + (wid * 2 + l) * 64 + lane * 8 + 4) = (f32x4){a[4], a[5], a[6], a[7]};
            }
        }
        WAIT_V(1);
#pragma unroll
        for (int u = 0; u < 2; ++u) if (hun + u < hend) wd_unit_store(ws, hun + u, lane, (const LAS char*)smem + 16384 + (wid * 2 + u) * 3072);
        asm volatile("s_waitcnt lgkmcnt(0)" ::: "memory"); BAR();
        if (tid < 192 && h < 108) {
            float s = 0.f;
            if (tid < 128) {
#pragma unroll
                for (int w = 0; w < 8; ++w) s += red[w * 128 + tid];
            }
            const int part = tid >> 6;
            const int lc = part == 0 ? lcA : (part == 1 ? lcA + 1 : (m3 == 0 ? lcA : lcA + 1));
            const int slot = part == 0 ? (m3 != 0) : (part == 1 ? 0 : 1);
            if (part < 2 || m3 != 1)
                __builtin_amdgcn_raw_buffer_store_b32(__float_as_uint(s), mk_rsrc(ws + WS_CS, 4 * F_ * 4), (unsigned)((slot * 2 * F_ + lc * 64 + (tid & 63)) * 4), 0, 16);
        }
    };
    auto wait_kv = [&](unsigned tag) {
        const rsrc_t rf = mk_rsrc(ws + WS_F0 + ((idx >> 2) * 8 + xcd) * 32 * FSP, 32 * FSP);
        __builtin_amdgcn_s_sleep(POLL0);
        for (int spin = 0; spin < (1 << 20); ++spin) {
            const unsigned f = __builtin_amdgcn_raw_buffer_load_b32(rf, (lane & 31) * FSP, 0, 16);
            if (__all(f == tag)) break;
            __builtin_amdgcn_s_sleep(POLL1);
        }
    };

    auto x_rows = [&]() {
        u32x2 xr[8];
#pragma unroll
        for (int i = 0; i < 8; ++i) xr[i] = *(const u32x2*)(x + (size_t)((tid - 256) + (wid - 4) * 448 + i * 64) * D_ + ch0);
#pragma unroll
        for (int i = 0; i < 8; ++i) *(LAS u32x2*)(smem + C1_XOFF + ((tid - 256) + (wid - 4) * 448 + i * 64) * 8) = xr[i];
        SCHED();
    };
    unsigned tag;
    unsigned dvv = 0;
    auto upper = [&](auto is_stat, auto is_conv) {
        constexpr bool STAT = decltype(is_stat)::v, CONV = decltype(is_conv)::v;
        const int srow = b * 8 + (wid - 5) * 4 + (lane >> 4), t3 = tid - 320;
        unsigned xw[18], epv = 0;
        if constexpr (STAT) {
            const unsigned* xr = (const unsigned*)(x + (size_t)srow * D_) + (lane & 15);
#pragma unroll
            for (int j = 0; j < 18; ++j) xw[j] = xr[16 * j];
        } else {
            epv = __hip_atomic_load((const unsigned*)(ws + WS_EP), __ATOMIC_RELAXED, __HIP_MEMORY_SCOPE_AGENT);
        }
        SCHED();
        if constexpr (STAT) {
            const char *c3, *c4;
            SYM_ADDR(c3, k3_gemm1); SYM_ADDR(c4, k9_end);
            warm_code<1>(c3, c4, ((idx & 3) * 2 + wid - 5) * 64, lane, lds0 + C1_TOFF + 256 + (wid - 5) * 256);
        }
        SCHED();
        u32x4 v[12], fa[6]; u32x2 fb[6];
        if constexpr (CONV) {
#pragma unroll
            for (int i = 0; i < 6; ++i) {
                const int task = min(t3 + 192 * i, 1023), u = task & 255;
                const u16* src = ssm_k + (size_t)(ch0 + (task >> 8)) * S_ + u * 8;
                fa[i] = *(const u32x4*)src;
                fb[i] = *(const u32x2*)(src + (u < 255 ? 8 : 0));
            }
            SCHED();
        }
        if constexpr (STAT) {
            f32x2 xf[18], s = {0.f, 0.f};
#pragma unroll
            for (int j = 0; j < 18; ++j) { xf[j] = (f32x2){bf_lo(xw[j]), bf_hi(xw[j])}; s += xf[j]; }
            const float mean = sum16(s.x + s.y) * (1.f / D_);
            f32x2 q = {0.f, 0.f};
#pragma unroll
            for (int j = 0; j < 18; ++j) { const f32x2 d = xf[j] - mean; q = __builtin_elementwise_fma(d, d, q); }
            const unsigned sgr = pk_bf16(mean, ln_sd(sum16(q.x + q.y) * (1.f / D_)));
            tag = get_tag();
            const unsigned so = (lane & 15) == 0 ? (unsigned)srow * 8 : 0x80000000u;
            st8_wt(mk_rsrc(ws + WS_ST, S_ * 8), so, (u32x2){sgr, tag});
            st8_wt(mk_rsrc(ws + WS_G2, S_ * 8), so, (u32x2){0u, 0u});
        } else {
            *tagp = epv + 1u;
            asm volatile("s_waitcnt lgkmcnt(0)" ::: "memory");
            tag = get_tag();
        }
        SCHED();
        if constexpr (!CONV) {
            __builtin_amdgcn_s_sleep(HELPER_DELAY);
            cs_load(v);
            if (!STAT && h == 111) {
                const rsrc_t rf = mk_rsrc(ws + WS_F0, 256 * FSP);
                for (int spin = 0; spin < (1 << 20); ++spin) {
                    bool ok = true;
#pragma unroll
                    for (int i = 0; i < 4; ++i) ok &= __builtin_amdgcn_raw_buffer_load_b32(rf, (i * 64 + lane) * FSP, 0, 16) == tag;
                    if (__all(ok)) break;
                    __builtin_amdgcn_s_sleep(8);
                }
                if (lane == 0) __hip_atomic_store((unsigned*)(ws + WS_EP), tag, __ATOMIC_RELAXED, __HIP_MEMORY_SCOPE_AGENT);
            }
            cs_finish(v);
            return;
        }
        if (t3 < 80) {
            const int cpy = t3 / 5, e = t3 % 5, q = e < 3 ? e : 257 + e;
            *(LAS u32x4*)(smem + cpy * C1_COPY + q * 16) = (u32x4){0u, 0u, 0u, 0u};
        } else if (t3 < 84) {
            *(LAS u32x4*)(smem + C1_KVOFF - 64 + (t3 - 80) * 16) = (u32x4){0u, 0u, 0u, 0u};
        }
        if (t3 < 128) *(LAS u32x4*)(smem + C1_KVOFF + (t3 >> 5) * C1_KV + 4096 + (t3 & 31) * 16) = (u32x4){0u, 0u, 0u, 0u};
#pragma unroll
        for (int i = 0; i < 6; ++i) {
            const int task = min(t3 + 192 * i, 1023), u = task & 255;
            const unsigned d[6] = {fa[i].x, fa[i].y, fa[i].z, fa[i].w, u < 255 ? fb[i].x : 0u, u < 255 ? fb[i].y : 0u};
            unsigned o[5];
#pragma unroll
            for (int e = 0; e < 5; ++e) o[e] = __builtin_amdgcn_alignbit(d[e + 1], d[e], 16);
            LAS char* base = (LAS char*)smem + (task >> 8) * C1_FILT + (u + 4) * 16;
            *(LAS u32x4*)(base) = (u32x4){o[0], o[1], o[2], o[3]};
            *(LAS u32x4*)(base + C1_COPY) = (u32x4){d[1], d[2], d[3], d[4]};
            *(LAS u32x4*)(base + 2 * C1_COPY) = (u32x4){o[1], o[2], o[3], o[4]};
            *(LAS u32x4*)(base + 3 * C1_COPY) = (u32x4){d[2], d[3], d[4], d[5]};
            if (u == 0) {
                *(LAS u32x4*)(base - 16) = (u32x4){0u, 0u, 0u, d[0] << 16};
                *(LAS u32x4*)(base - 16 + C1_COPY) = (u32x4){0u, 0u, 0u, d[0]};
                *(LAS u32x4*)(base - 16 + 2 * C1_COPY) = (u32x4){0u, 0u, d[0] << 16, o[0]};
                *(LAS u32x4*)(base - 16 + 3 * C1_COPY) = (u32x4){0u, 0u, d[0], d[1]};
            }
        }
        asm volatile("s_waitcnt lgkmcnt(0)" ::: "memory"); BAR();
        x_rows();
    };
    if (wid >= 5) {
        if (!conv) {
            if (wid < 7) upper(IC<1>{}, IC<0>{}); else upper(IC<0>{}, IC<0>{});
            DIAG_END(ws, 0);
            return;
        }
        if (wid < 7) upper(IC<1>{}, IC<1>{}); else upper(IC<0>{}, IC<1>{});
    } else {
        {
            unsigned kw[11], vw[8];
            const int t0 = idx * 64 + (lane >> 3) * 8, dc = wid * 8 + (lane & 7), kch = xcd * 72 + (dc < 36 ? dc : 35) * 2;
            const rsrc_t rk = mk_rsrc(kin, S_ * D_ * 2), rv = mk_rsrc(vin, S_ * D_ * 2);
            const int off = t0 * (D_ * 2) + kch * 2;
#pragma unroll
            for (int j = 0; j < 11; ++j) kw[j] = __builtin_amdgcn_raw_buffer_load_b32(rk, (unsigned)(off + (j - 3) * (D_ * 2)), 0, 0);
#pragma unroll
            for (int j = 0; j < 8; ++j) vw[j] = __builtin_amdgcn_raw_buffer_load_b32(rv, (unsigned)(off + j * (D_ * 2)), 0, 0);
            const u32x4 sw = *(const u32x4*)(shift_k + kch * 4);
            dvv = ssm_D[(conv ? ch0 : 0) + (wid & 3)];
            SCHED();
            STAMP(1);
            const f32x2 w0 = {bf_lo(sw.x), bf_lo(sw.z)}, w1 = {bf_hi(sw.x), bf_hi(sw.z)}, w2 = {bf_lo(sw.y), bf_lo(sw.w)}, w3 = {bf_hi(sw.y), bf_hi(sw.w)};
            f32x2 kf[11], o[8];
#pragma unroll
            for (int j = 0; j < 11; ++j) kf[j] = (f32x2){bf_lo(kw[j]), bf_hi(kw[j])};
#pragma unroll
            for (int j = 0; j < 8; ++j) {
                f32x2 s = kf[j] * w3;
                s = __builtin_elementwise_fma(kf[j + 1], w2, s);
                s = __builtin_elementwise_fma(kf[j + 2], w1, s);
                s = __builtin_elementwise_fma(kf[j + 3], w0, s);
                o[j] = s * (f32x2){bf_lo(vw[j]), bf_hi(vw[j])};
            }
            const rsrc_t ro = mk_rsrc(ws + WS_KV, S_ * D_ * 2);
            const unsigned oo = dc < 36 ? (unsigned)(kch * (S_ * 2) + (2040 - t0) * 2) : 0x80000000u;
            st16_wt(ro, oo, (u32x4){pk_bf16(o[7].x, o[6].x), pk_bf16(o[5].x, o[4].x), pk_bf16(o[3].x, o[2].x), pk_bf16(o[1].x, o[0].x)});
            st16_wt(ro, oo + S_ * 2, (u32x4){pk_bf16(o[7].y, o[6].y), pk_bf16(o[5].y, o[4].y), pk_bf16(o[3].y, o[2].y), pk_bf16(o[1].y, o[0].y)});
        }
        tag = get_tag();
        WAIT_V(0);
        if (lane == 0) __hip_atomic_store((unsigned*)(ws + WS_F0 + ((wid * 8 + xcd) * 32 + idx) * FSP), tag, __ATOMIC_RELAXED, __HIP_MEMORY_SCOPE_AGENT);
        MARK();
        STAMP(2);
        if (!conv) {
            u32x4 v[12];
            cs_load(v);
            cs_finish(v);
            DIAG_END(ws, 0);
            return;
        }
        if (wid == 4) {
            x_rows();
            BAR();
#pragma unroll 1
            for (int u = 0; u < WU4; ++u) wd_unit_load(wd, cg * WU4 + u, lane, lds0 + C1_WOFF + u * 3072);
            __builtin_amdgcn_s_sleep(24);
            const char* gs = ws + WS_ST;
#pragma unroll
            for (int i = 0; i < 4; ++i) {
                dma16_sc1<0>(lane * 16, gs + i * 4096, lds0 + C1_SOFF + i * 4096); dma16_sc1<1024>(lane * 16, gs + i * 4096, lds0 + C1_SOFF + i * 4096);
                dma16_sc1<2048>(lane * 16, gs + i * 4096, lds0 + C1_SOFF + i * 4096); dma16_sc1<3072>(lane * 16, gs + i * 4096, lds0 + C1_SOFF + i * 4096);
            }
            WAIT_V(0);
#pragma unroll 1
            for (int u = 0; u < WU4; ++u) wd_unit_store(ws, cg * WU4 + u, lane, (const LAS char*)smem + C1_WOFF + u * 3072);
        } else {
        {
            wait_kv(tag);
            STAMP(7);
            const char* kvs = ws + WS_KV + (size_t)(ch0 + wid) * (S_ * 2);
            const unsigned m = lds0 + C1_KVOFF + wid * C1_KV;
            dma16_sc1<0>(lane * 16, kvs, m); dma16_sc1<1024>(lane * 16, kvs, m); dma16_sc1<2048>(lane * 16, kvs, m); dma16_sc1<3072>(lane * 16, kvs, m);
            WAIT_V(0);
            STAMP(9);
            BAR();
            STAMP(4);
        }
        const int row = lane & 15, kg = lane >> 4;
        const LAS char* abase = (const LAS char*)smem + wid * C1_FILT + (row & 3) * C1_COPY + (24 + 4 * (row >> 2) - 8 * kg) * 2;
        const LAS char* bbase = (const LAS char*)smem + C1_KVOFF + wid * C1_KV + (2040 - 16 * row - 8 * kg) * 2 - 512 * 7 - 448;
        auto rd_ah = [&](int r, int L, int hf) -> s16x4 { return *(const volatile LAS s16x4*)(abase + 512 * L - 64 * r + 8 * hf); };
        auto rd_b = [&](int r, int m) -> s16x8 { return *(const LAS s16x8*)(bbase + 448 - 64 * r + 512 * (7 - m)); };
        auto cat = [&](s16x4 lo, s16x4 hi) -> s16x8 { return __builtin_shufflevector(lo, hi, 0, 1, 2, 3, 4, 5, 6, 7); };
        f32x4 acc[8];
        s16x4 al[2][8], ah[2][8];
        s16x8 bf[2][8], bd[8];
        auto rd_k = [&](int r, int k) {
            if (k < 2) { if (k == 0) al[r & 1][0] = rd_ah(r, 1, 0); else ah[r & 1][0] = rd_ah(r, 1, 1); }
            else if (k < 10) bf[r & 1][9 - k] = rd_b(r, 8 - k);
            else { const int L = (k - 10) / 2 + 2; if (k & 1) ah[r & 1][L - 1] = rd_ah(r, L, 1); else al[r & 1][L - 1] = rd_ah(r, L, 0); }
        };
        const s16x8 a0 = cat(rd_ah(0, 0, 0), rd_ah(0, 0, 1));
#pragma unroll
        for (int e = 0; e < 8; ++e) bd[e] = rd_b(0, e);
        s16x8 dfr;
        {
            const int j = 8 * kg + 7 - row;
            u32x4 w;
#pragma unroll
            for (int e = 0; e < 4; ++e) w[e] = j == 2 * e ? dvv : (j == 2 * e + 1 ? dvv << 16 : 0u);
            dfr = __builtin_bit_cast(s16x8, w);
            asm volatile("s_nop 1" : "+v"(dfr));
        }
        SCHED();
#pragma unroll
        for (int n = 0; n < 16; ++n) {
            if (n < 8) mfma_first(acc[n], a0, bd[n]); else mfma_acc(acc[n - 8], dfr, bd[n - 8]);
            SCHED();
            if (n == 0) { rd_k(0, 0); rd_k(0, 1); }
            else if (n == 1) rd_k(0, 9);
            else rd_k(0, n + 8);
            SCHED();
        }
#pragma unroll
        for (int r = 0; r < 8; ++r) {
            int k = 0;
#pragma unroll
            for (int L = 1; L <= 8; ++L) {
#pragma unroll
                for (int n = 7; n >= L - 1; --n) {
                    const int e = n - L + 1;
                    mfma_acc(acc[n], cat(al[r & 1][L - 1], ah[r & 1][L - 1]), (r == 0 && e > 0) ? bd[e - 1] : bf[r & 1][e]);
                    SCHED();
                    if (r < 7 && k < 24) rd_k(r + 1, k);
                    ++k;
                    SCHED();
                }
            }
        }
        MFMA_DONE();
#pragma unroll
        for (int n = 0; n < 8; ++n)
            *(LAS u32x2*)(smem + C1_YOFF + wid * 4096 + (256 * n + 16 * row + 4 * kg) * 2) = (u32x2){pk_bf16(acc[n][0], acc[n][1]), pk_bf16(acc[n][2], acc[n][3])};
        }
    }
    STAMP(5);
    asm volatile("s_waitcnt lgkmcnt(0)" ::: "memory"); BAR();
    STAMP(6);
    {
        u32x2 yc[4];
#pragma unroll
        for (int c = 0; c < 4; ++c) yc[c] = *(const LAS u32x2*)(smem + C1_YOFF + c * 4096 + tid * 8);
        const u32x4 xa = *(const LAS u32x4*)(smem + C1_XOFF + tid * 32), xb = *(const LAS u32x4*)(smem + C1_XOFF + tid * 32 + 16);
        const u32x2 xr[4] = {{xa.x, xa.y}, {xa.z, xa.w}, {xb.x, xb.y}, {xb.z, xb.w}};
        u32x4 s01 = *(const LAS u32x4*)(smem + C1_SOFF + tid * 32), s23 = *(const LAS u32x4*)(smem + C1_SOFF + tid * 32 + 16);
        if (__builtin_expect(!__all(s01.y == tag && s01.w == tag && s23.y == tag && s23.w == tag), 0)) {
            const rsrc_t rs = mk_rsrc(ws + WS_ST, S_ * 8);
            for (int spin = 0; spin < (1 << 20); ++spin) {
                s01 = __builtin_amdgcn_raw_buffer_load_b128(rs, tid * 32, 0, 16);
                s23 = __builtin_amdgcn_raw_buffer_load_b128(rs, tid * 32 + 16, 0, 16);
                if (__all(s01.y == tag && s01.w == tag && s23.y == tag && s23.w == tag)) break;
                __builtin_amdgcn_s_sleep(2);
            }
        }
        const unsigned sv[4] = {s01.x, s01.z, s23.x, s23.z};
        float mm[4], rr[4];
#pragma unroll
        for (int j = 0; j < 4; ++j) { mm[j] = bf_lo(sv[j]); rr[j] = __builtin_amdgcn_rcpf(bf_hi(sv[j])); }
        unsigned hw[8];
#pragma unroll
        for (int j = 0; j < 4; ++j) {
#pragma unroll
            for (int e = 0; e < 2; ++e) {
                const unsigned xw = e ? xr[j].y : xr[j].x;
                const unsigned ya = (j >> 1) ? yc[2 * e].y : yc[2 * e].x, yb = (j >> 1) ? yc[2 * e + 1].y : yc[2 * e + 1].x;
                const float y0 = (j & 1) ? bf_hi(ya) : bf_lo(ya), y1 = (j & 1) ? bf_hi(yb) : bf_lo(yb);
                const float x0 = bf_lo(xw), x1 = bf_hi(xw);
                hw[2 * j + e] = pk_bf16(__builtin_fmaf((x0 - mm[j]) * rr[j], y0, x0), __builtin_fmaf((x1 - mm[j]) * rr[j], y1, x1));
            }
        }
        const rsrc_t rh = mk_rsrc(ws + WS_H, S_ * D_ * 2);
        const unsigned ho = (unsigned)(((tid >> 6) * 144 + cg) * 2048 + (tid & 63) * 32);
        st16_wt(rh, ho, (u32x4){hw[0], hw[1], hw[2], hw[3]});
        st16_wt(rh, ho + 16, (u32x4){hw[4], hw[5], hw[6], hw[7]});
    }
    STAMP(8);
    DIAG_END(ws, 0);
}

constexpr int ST_BYTES = 16384 + 2 * 9216;
constexpr int NBUF = 4;
constexpr int G_LDS = NBUF * ST_BYTES;

struct TokC { float nm, c0, c1, q; };
__device__ __forceinline__ unsigned geglu2(f32x2 ag, f32x2 au, f32x2 csg, f32x2 csu, const TokC& t) {
    const f32x2 nm = {t.nm, t.nm}, c0 = {t.c0, t.c0}, c1 = {t.c1, t.c1}, q = {t.q, t.q};
    const f32x2 g = __builtin_elementwise_fma(nm, csg, ag), u = __builtin_elementwise_fma(nm, csu, au);
    const f32x2 z = g * __builtin_elementwise_fma(g * g, c1, c0);
    const f32x2 e = {__builtin_amdgcn_exp2f(z.x), __builtin_amdgcn_exp2f(z.y)};
    const f32x2 d = __builtin_elementwise_fma(e, q, q);
    const f32x2 r = (g * u) * (f32x2){__builtin_amdgcn_rcpf(d.x), __builtin_amdgcn_rcpf(d.y)};
    return pk_bf16(r.x, r.y);
}
#define LO2(v) __builtin_shufflevector(v, v, 0, 1)
#define HI2(v) __builtin_shufflevector(v, v, 2, 3)
#define WAIT_VI(N) asm volatile("s_waitcnt vmcnt(%0)" :: "i"(N) : "memory")

constexpr int G2_W0 = 71680, G2_FL = 69632;
__device__ __forceinline__ void g2_wpiece(int P, int s, const char* wsrc, unsigned aoff, unsigned mw0) {
    const unsigned m = mw0 + (s % 5) * 18432;
    if (P == 0) dma16<0>(aoff, wsrc + (size_t)s * 18432, m);
    if (P == 1) dma16<1024>(aoff, wsrc + (size_t)s * 18432, m);
    if (P == 2) dma16_q<2048>(aoff, wsrc + (size_t)s * 18432, m);
}
constexpr int g2_n(int S) { return (S + 3 < 18 ? 2 : 0) + (S + 4 < 18 ? 3 : 0) + (S == 4 ? 1 : 0); }
constexpr int g2_vm(int S) {
    return S == 0 ? 3 : (S == 1 ? 1 + g2_n(0) : g2_n(S - 2) - (S + 1 < 18 ? 2 : 0) + g2_n(S - 1));
}
#define DIAG_ARG
#define DIAG_PASS
template <int NT>
__device__ __forceinline__ void g2_body(char* __restrict__ ws, u16* __restrict__ out, LAS char* lds, int lane, int wid, int mt, int ks, int nt,
                                        const char* wsrc, unsigned flag2 DIAG_ARG) {
    constexpr int dh = NT == 4;
    const unsigned lds0 = (unsigned)(size_t)lds;
    const int wr = wid & 3;
    const char* asrc = ws + WS_HH + ((size_t)(mt * 4 + ks) * 36) * 8192 + wid * 2048;
    const unsigned aoff = lane * 16;
    const unsigned ma0 = lds0 + wid * 2048, mw0 = lds0 + G2_W0 + wid * 2304;
    auto hpiece = [&](int P, int s) {
        if (P == 0) dma16_sc1<0>(aoff, asrc + (size_t)s * 16384, ma0 + (s & 3) * 16384);
        if (P == 1) dma16_sc1<1024>(aoff, asrc + (size_t)s * 16384, ma0 + (s & 3) * 16384);
    };
    hpiece(0, 0); hpiece(1, 0); hpiece(0, 1); hpiece(1, 1); hpiece(0, 2); hpiece(1, 2);

    const int g4 = lane >> 4;
    const int toff = wr * 2048 + lane * 16;
    const int foff = G2_W0 + dh * 5120 + lane * 16;
    auto rdf = [&](const LAS char* p) -> s16x8 { return *(const LAS s16x8*)p; };
    const rsrc_t rfb = mk_rsrc(ws + WS_FB, 16384);

    f32x4 acc[NT][2];
    s16x8 wf[2 * NT], tf[2][4];
    WAIT_V(4);
    __builtin_amdgcn_raw_buffer_store_b32(1u, rfb, lane == 0 ? flag2 : 0x80000000u, 0, 16);
    BAR();
    STAMP(8);
#pragma unroll
    for (int e = 0; e < 4; ++e) tf[0][e] = rdf(lds + toff + (e >> 1) * 8192 + (e & 1) * 1024);
#pragma unroll
    for (int e = 0; e < 2 * NT; ++e) wf[e] = rdf(lds + foff + (e / NT) * 9216 + (e % NT) * 1024);
    SCHED();

    auto iter = [&](auto sc, s16x8 (&tc)[4], s16x8 (&tn)[4]) {
        constexpr int S = decltype(sc)::v;
        if constexpr (S < 17) {
            WAIT_VI(g2_vm(S));
            BAR();
            if constexpr (S == 7) {
                unsigned fb2 = *(const volatile LAS unsigned*)(lds + G2_FL + wid * 256 + lane * 4);
                if (__builtin_expect(!__all(fb2 == 1u), 0)) {
                    for (int spin = 0; spin < (1 << 20); ++spin) {
                        fb2 = __builtin_amdgcn_raw_buffer_load_b32(rfb, (unsigned)(((mt * 4 + ks) * 2 + 1) * 128 + (lane & 31) * 4), 0, 16);
                        if (__all(fb2 == 1u)) break;
                        __builtin_amdgcn_s_sleep(2);
                    }
                }
            }
            SCHED();
        }
        const LAS char* tb = lds + ((S + 1) & 3) * 16384;
        const LAS char* wb = lds + ((S + 1) % 5) * 18432;
#pragma unroll
        for (int e = 0; e < 2 * NT; ++e) {
#pragma unroll
            for (int i = 0; i < 2; ++i) {
                if (S == 0 && e < NT) mfma_first(acc[e % NT][i], wf[e], tc[(e / NT) * 2 + i]);
                else mfma_acc(acc[e % NT][i], wf[e], tc[(e / NT) * 2 + i]);
            }
            SCHED();
            if constexpr (S < 17) {
                wf[e] = rdf(wb + foff + (e / NT) * 9216 + (e % NT) * 1024);
                if (e >= 2 && e < 6) tn[e - 2] = rdf(tb + toff + ((e - 2) >> 1) * 8192 + ((e - 2) & 1) * 1024);
            }
            if constexpr (S + 3 < 18) { if (e < 2) hpiece(e, S + 3); }
            if constexpr (S + 4 < 18) {
                if (e == 3) g2_wpiece(0, S + 4, wsrc, aoff, mw0);
                if (e == 5) g2_wpiece(1, S + 4, wsrc, aoff, mw0);
                if (e == 6) g2_wpiece(2, S + 4, wsrc, aoff, mw0);
            }
            if constexpr (S == 4) {
                if (e == 7) dma4_sc1((unsigned)(lane & 31) * 4, ws + WS_FB + ((mt * 4 + ks) * 2 + 1) * 128, lds0 + G2_FL + wid * 256);
            }
            SCHED();
        }
    };
#define G2_IT2(S) iter(IC<S>{}, tf[0], tf[1]); iter(IC<S + 1>{}, tf[1], tf[0])
    G2_IT2(0); G2_IT2(2); G2_IT2(4); G2_IT2(6); G2_IT2(8); G2_IT2(10); G2_IT2(12);
    STAMP(9);
    G2_IT2(14);
    iter(IC<16>{}, tf[0], tf[1]);
    const int tok0 = mt * 128 + wr * 32 + (lane & 15);
    u32x2 hv[2][5];
    if (wr == ks) {
#pragma unroll
        for (int i = 0; i < 2; ++i) {
            const int t = tok0 + i * 16;
            const char* hp = ws + WS_H + ((size_t)((t >> 8) * 144 + nt * 36 + dh * 20) * 256 + (t & 255)) * 8;
#pragma unroll
            for (int e = 0; e < 4; ++e) hv[i][e] = *(const u32x2*)(hp + ((e >> 1) * 8 + (g4 & 1) * 4 + (g4 >> 1) * 2 + (e & 1)) * 2048);
            if (NT == 5) hv[i][4] = *(const u32x2*)(hp + (16 + g4) * 2048);
        }
    }
    SCHED();
    iter(IC<17>{}, tf[1], tf[0]);
    MFMA_DONE();
    STAMP(10);

    const rsrc_t rx = mk_rsrc(ws + WS_X, X_BYTES);
    const unsigned xt = (unsigned)((mt * 4 + nt) * 4 * 27648 + dh * 4096);
    auto inb = [&](int s) -> unsigned { return xt + (unsigned)(((ks + 1 + s) & 3) * 27648 + (2 - s) * 9216); };
    if (wr != ks) {
        const unsigned o = xt + (unsigned)(ks * 27648 + ((wr - ks - 1) & 3) * 9216);
#pragma unroll
        for (int i = 0; i < 2; ++i) {
            unsigned w[2 * NT];
#pragma unroll
            for (int j = 0; j < NT; ++j) { w[2 * j] = pk_bf16(acc[j][i][0], acc[j][i][1]); w[2 * j + 1] = pk_bf16(acc[j][i][2], acc[j][i][3]); }
#pragma unroll
            for (int e = 0; e < 2 * NT; ++e) w[e] = (w[e] == ~0u) ? 0x7fc07fc0u : w[e];
            if constexpr (NT == 5) st8_wt(rx, o + 8192 + i * 512 + lane * 8, (u32x2){w[8], w[9]});
            st16_wt(rx, o + i * 2048 + lane * 16, (u32x4){w[0], w[1], w[2], w[3]});
            st16_wt(rx, o + i * 2048 + 1024 + lane * 16, (u32x4){w[4], w[5], w[6], w[7]});
        }
    } else {
        u32x4 pa[3][4]; u32x2 pb[3][2];
        __builtin_amdgcn_s_sleep(16);
        for (int spin = 0; spin < (1 << 18); ++spin) {
            const u32x4 t = __builtin_amdgcn_raw_buffer_load_b128(rx, lane < 3 ? inb(lane) + 4080 : 0x80000000u, 0, 16);
            if (__all(t.w != ~0u)) {
                STAMP(11);
                unsigned m = 0u;
#pragma unroll
                for (int s = 0; s < 3; ++s) {
#pragma unroll
                    for (int p = 0; p < 4; ++p) pa[s][p] = __builtin_amdgcn_raw_buffer_load_b128(rx, inb(s) + p * 1024 + lane * 16, 0, 16);
                    if constexpr (NT == 5) {
#pragma unroll
                        for (int i = 0; i < 2; ++i) pb[s][i] = __builtin_amdgcn_raw_buffer_load_b64(rx, inb(s) + 8192 + i * 512 + lane * 8, 0, 16);
                    }
                }
#pragma unroll
                for (int s = 0; s < 3; ++s) {
#pragma unroll
                    for (int p = 0; p < 4; ++p) m = max(max(max(m, pa[s][p].x), max(pa[s][p].y, pa[s][p].z)), pa[s][p].w);
                    if constexpr (NT == 5) m = max(max(max(m, pb[s][0].x), max(pb[s][0].y, pb[s][1].x)), pb[s][1].y);
                }
                if (__all(m != ~0u)) break;
            }
            __builtin_amdgcn_s_sleep(1);
        }
        STAMP(12);
        const rsrc_t ro = mk_rsrc(out, S_ * D_ * 2);
#pragma unroll
        for (int i = 0; i < 2; ++i) {
            const unsigned ob = (unsigned)(((tok0 + i * 16) * D_ + nt * 144 + dh * 80) * 2);
            unsigned r[NT][2];
#pragma unroll
            for (int j = 0; j < NT; ++j) {
                f32x4 t = acc[j][i];
#pragma unroll
                for (int s = 0; s < 3; ++s) {
                    const unsigned w0 = j < 4 ? pa[s][i * 2 + (j >> 1)][(j & 1) * 2] : pb[s][i].x, w1 = j < 4 ? pa[s][i * 2 + (j >> 1)][(j & 1) * 2 + 1] : pb[s][i].y;
                    t += (f32x4){bf_lo(w0), bf_hi(w0), bf_lo(w1), bf_hi(w1)};
                }
                r[j][0] = pk_bf16(t[0], t[1]); r[j][1] = pk_bf16(t[2], t[3]);
            }
            auto addh = [&](unsigned a, unsigned h) -> unsigned { return pk_bf16(bf_lo(h) + bf_lo(a), bf_hi(h) + bf_hi(a)); };
#pragma unroll
            for (int p = 0; p < 2; ++p) {
                auto s0 = __builtin_amdgcn_permlane16_swap(r[2 * p][0], r[2 * p + 1][0], false, false);
                auto s1 = __builtin_amdgcn_permlane16_swap(r[2 * p][1], r[2 * p + 1][1], false, false);
                st16_wt(ro, ob + (unsigned)((p * 32 + (g4 & 1) * 16 + (g4 >> 1) * 8) * 2),
                        (u32x4){addh(s0[0], hv[i][2 * p].x), addh(s1[0], hv[i][2 * p].y), addh(s0[1], hv[i][2 * p + 1].x), addh(s1[1], hv[i][2 * p + 1].y)});
            }
            if constexpr (NT == 5) st8_wt(ro, ob + (unsigned)((64 + g4 * 4) * 2), (u32x2){addh(r[4][0], hv[i][4].x), addh(r[4][1], hv[i][4].y)});
        }
    }
    STAMP(13);
}

constexpr int K3_LDS = 163840;
constexpr int K3_SP = G_LDS;
constexpr int K3_CS = G2_W0 + 4 * 18432;
extern "C" __global__ __launch_bounds__(512, 2) void k3_gemm1(const u16* __restrict__ wg, const u16* __restrict__ wu,
                                                   char* __restrict__ ws, u16* __restrict__ out) {
    extern __shared__ __attribute__((aligned(16))) char smem[];
    LAS char* lds = (LAS char*)smem;
    const unsigned lds0 = (unsigned)(size_t)lds;
    const int tid = threadIdx.x, lane = tid & 63;
    const int wid = __builtin_amdgcn_readfirstlane(tid >> 6);
    const int wr = wid & 3, wc = wid >> 2;
    const int b = blockIdx.x, xcd = b & 7, idx = b >> 3;
    const int rt = (xcd >> 2) * 4 + (idx & 3), ct = (xcd & 3) * 8 + (idx >> 2);
    asm volatile("" :: "s"(wg), "s"(wu), "s"(ws), "s"(out));
    DIAG_DECL;

    const char* asrc = ws + WS_H + (size_t)rt * 144 * 2048 + wid * 2048;
    const unsigned aoff = (unsigned)(lane ^ ((wid & 2) << 2)) * 16;
    const unsigned ma0 = lds0 + wid * 2048, mw0 = lds0 + 16384 + wc * 9216 + wr * 2304;
    dma16<0>(aoff, asrc, ma0); dma16<1024>(aoff, asrc, ma0);
    const char* wsrc = (const char*)(wc ? wu : wg) + ct * 288 - 2048;
    auto wofs = [&](int i) -> unsigned {
        const int q = lane + 64 * i, lrow = 8 * wr + q / 18, pos = q % 18;
        const int krow = ((lrow >> 2) & 3) * 8 + (lrow >> 4) * 4 + (lrow & 3);
        const int col = pos * 8 + (pos < 8 ? 0 : (pos < 16 ? 8 : (((pos & 1) ^ wc) ? 136 : 64) - pos * 8));
        return (unsigned)(krow * (F_ * 2) + col * 2 + 2048 - 1024 * i);
    };
    const unsigned w0 = wofs(0), w1 = wofs(1), w2 = wofs(2);
    auto piece = [&](int P, int s) {
        const unsigned bo = (s & 3) * ST_BYTES;
        const char* wp = wsrc + (size_t)s * (32 * F_ * 2);
        if (P == 0) dma16<0>(aoff, asrc + (size_t)s * 16384, ma0 + bo);
        if (P == 1) dma16<1024>(aoff, asrc + (size_t)s * 16384, ma0 + bo);
        if (P == 2) dma16<0>(w0, wp, mw0 + bo);
        if (P == 3) dma16<1024>(w1, wp, mw0 + bo);
        if (P == 4) dma16_q<2048>(w2, wp, mw0 + bo);
    };
#define K3_PIECE(P, s) piece(P, s)
#define K3_STAGE(s) do { K3_PIECE(0, s); K3_PIECE(1, s); K3_PIECE(2, s); K3_PIECE(3, s); K3_PIECE(4, s); } while (0)
    K3_PIECE(2, 0); K3_PIECE(3, 0); K3_PIECE(4, 0);
    BAR();
    K3_STAGE(1);
    u32x2 hs[4];
    {
        const char* hsrc = ws + WS_H + (size_t)rt * 144 * 2048 + ct * 64;
        const unsigned o = (unsigned)((wid * 18 + (lane >> 3)) * 2048 + (lane & 7) * 8);
        asm volatile("global_load_dwordx2 %0, %1, %2" : "=v"(hs[0]) : "v"(o), "s"(hsrc) : "memory");
        asm volatile("global_load_dwordx2 %0, %1, %2" : "=v"(hs[1]) : "v"(o + 16384), "s"(hsrc) : "memory");
        asm volatile("global_load_dwordx2 %0, %1, %2" : "=v"(hs[2]) : "v"((unsigned)((wid * 18 + 16 + ((lane >> 3) & 1)) * 2048 + (lane & 7) * 8)), "s"(hsrc) : "memory");
        asm volatile("global_load_dwordx2 %0, %1, %2" : "=v"(hs[3]) : "v"((unsigned)(lane & 7) * 8), "s"(hsrc) : "memory");
    }

    const int g4 = lane >> 4, q4 = (lane & 15) >> 2, p4 = lane & 3;
    const int rowoff = (4 * g4 + q4) * 288;
    const int tbase = g4 * 4096 + wr * 512 + (lane & 15) * 8;
    const int toff0 = tbase + ((2 * wc) ^ (g4 & 1)) * 128, toff1 = tbase + ((2 * wc + 1) ^ (g4 & 1)) * 128;
    const int tstep = wc ? -256 : 256;
    const int foff = 16384 + rowoff + wc * 128 + p4 * 8;
    const int moff = 16384 + rowoff + 256 + (p4 < 2 ? 16 * wc + 8 * p4 : 9216 + 16 * (1 - wc) + 8 * (p4 - 2));

    f32x4 acc[9][4];
    s16x8 wf[9], tf[2][4];
    auto rd_w = [&](const LAS char* sb, int j) -> s16x8 {
        return j < 8 ? tr_frag(sb + foff + (j >> 2) * 9216 + (j & 3) * 32) : tr_frag(sb + moff);
    };
    auto rd_t = [&](const LAS char* sb, int i) -> s16x8 {
        const LAS char* p = sb + ((i & 1) ? toff1 : toff0) + (i >> 1) * tstep;
        const s16x4 lo = *(const volatile LAS s16x4*)p, hi = *(const volatile LAS s16x4*)(p + 2048);
        return __builtin_shufflevector(lo, hi, 0, 1, 2, 3, 4, 5, 6, 7);
    };

    STAMP(1);
    WAIT_V(9); BAR();
    STAMP(2);
    wf[0] = rd_w(lds, 0);
#pragma unroll
    for (int i = 0; i < 4; ++i) tf[0][i] = rd_t(lds, i);
#pragma unroll
    for (int j = 1; j < 9; ++j) wf[j] = rd_w(lds, j);
    SCHED();
    const int mt2 = (xcd >> 2) * 8 + (idx & 7), ks2 = xcd & 3, nt2 = idx >> 3;
    const rsrc_t rfill = mk_rsrc(ws + WS_X + (size_t)((mt2 * 4 + nt2) * 4 + ks2) * 27648, 27648);
    auto fill = [&](int k) { st16_wt(rfill, (unsigned)(k * 8192 + tid * 16), (u32x4){~0u, ~0u, ~0u, ~0u}); };
    unsigned sgr;
    {
        const LAS char* sb = lds + ST_BYTES;
        float hc = 0.f;
#pragma unroll
        for (int j = 0; j < 9; ++j) {
#pragma unroll
            for (int i = 0; i < 4; ++i) mfma_first(acc[j][i], wf[j], tf[0][i]);
            SCHED();
            if (j == 0) { K3_PIECE(0, 2); K3_PIECE(1, 2); }
            if (j == 1) { K3_PIECE(2, 2); K3_PIECE(3, 2); }
            if (j == 2) K3_PIECE(4, 2);
            if (j == 3) {
                asm volatile("s_waitcnt vmcnt(5)" : "+v"(hs[0]), "+v"(hs[1]), "+v"(hs[2]), "+v"(hs[3]) :: "memory");
                hc = bf_lo(hs[3].x);
                float s1 = 0.f, s2 = 0.f;
#pragma unroll
                for (int i = 0; i < 3; ++i) {
                    const float d[4] = {bf_lo(hs[i].x) - hc, bf_hi(hs[i].x) - hc, bf_lo(hs[i].y) - hc, bf_hi(hs[i].y) - hc};
                    float a = (d[0] + d[1]) + (d[2] + d[3]), q = __builtin_fmaf(d[0], d[0], d[1] * d[1]) + __builtin_fmaf(d[2], d[2], d[3] * d[3]);
                    if (i == 2 && lane >= 16) { a = 0.f; q = 0.f; }
                    s1 += a; s2 += q;
                }
                s1 = x32_add(x16_add(dpp_add<0x128>(s1))); s2 = x32_add(x16_add(dpp_add<0x128>(s2)));
                if (lane < 8) *(LAS f32x2*)(lds + K3_SP + (lane * 8 + wid) * 8) = (f32x2){s1, s2};
                asm volatile("s_waitcnt lgkmcnt(0)" ::: "memory");
                BAR();
                wf[0] = rd_w(sb, 0); wf[1] = rd_w(sb, 1);
                tf[1][0] = rd_t(sb, 0); tf[1][1] = rd_t(sb, 1);
            }
            if (j == 4) { tf[1][2] = rd_t(sb, 2); tf[1][3] = rd_t(sb, 3); wf[2] = rd_w(sb, 2); wf[3] = rd_w(sb, 3); }
            if (j >= 4) { wf[j] = rd_w(sb, j); K3_PIECE(j - 4, 3); }
            if (j == 8) {
                const f32x2 pp = *(const LAS f32x2*)(lds + K3_SP + (wid * 8 + (lane & 7)) * 8);
                const float s1 = dpp_add<0x141>(dpp_add<0x4E>(dpp_add<0xB1>(pp.x))) * (1.f / D_);
                const float s2 = dpp_add<0x141>(dpp_add<0x4E>(dpp_add<0xB1>(pp.y))) * (1.f / D_);
                const float c0 = __uint_as_float(__builtin_amdgcn_readlane(__float_as_uint(hc), wid));
                const float sd = ln_sd(__builtin_fmaxf(__builtin_fmaf(-s1, s1, s2), 0.f));
                sgr = pk_bf16(c0 + s1, sd);
                fill(0); fill(1);
            }
            SCHED();
        }
    }

    const rsrc_t rg = mk_rsrc(ws + WS_G2 + (size_t)(rt * 256 + wr * 64) * 8, 512);
    const char* w2src = ws + WS_WT + (size_t)(ks2 * 4 + nt2) * 18 * 18432 + wid * 2304;
    u32x2 gr[4];
    s16x4 tl;
    constexpr int TP = 1, DPJ0 = 0, DPJ1 = 5;
    auto iter = [&](auto mode, auto vm, int s, s16x8 (&tc)[4], s16x8 (&tn)[4]) {
        constexpr int MODE = decltype(mode)::v;
        if constexpr (MODE != 4) {
            if constexpr (MODE == 3) WAIT_V(0); else WAIT_VI(5 + decltype(vm)::v);
            BAR();
            SCHED();
        }
        const LAS char* sb = lds + ((s + 1) & 3) * ST_BYTES;
#pragma unroll
        for (int j = 0; j < 9; ++j) {
            s16x4 wl, wh;
#pragma unroll
            for (int i = 0; i < 4; ++i) {
                mfma_acc(acc[j][i], wf[j], tc[i]);
                SCHED();
                if constexpr (MODE != 4) {
                    if (j >= 1) {
                        const LAS char* p = (j - 1 < 8) ? sb + foff + ((j - 1) >> 2) * 9216 + ((j - 1) & 3) * 32 : sb + moff;
                        if (i == 0) wl = __builtin_amdgcn_ds_read_tr16_b64_v4i16((LAS s16x4*)p);
                        if (i == 1) { wh = __builtin_amdgcn_ds_read_tr16_b64_v4i16((LAS s16x4*)(p + 16 * 288)); wf[j - 1] = __builtin_shufflevector(wl, wh, 0, 1, 2, 3, 4, 5, 6, 7); }
                    }
                    if (j >= TP && j < TP + 4) {
                        const int ti = j - TP;
                        const LAS char* p = sb + ((ti & 1) ? toff1 : toff0) + (ti >> 1) * tstep;
                        if (i == 2) tl = *(const volatile LAS s16x4*)p;
                        if (i == 3) tn[ti] = __builtin_shufflevector(tl, *(const volatile LAS s16x4*)(p + 2048), 0, 1, 2, 3, 4, 5, 6, 7);
                    }
                    if (j == 8 && i == 3) wf[8] = rd_w(sb, 8);
                }
                if constexpr (MODE < 2 || MODE == 5) {
                    if (j == DPJ0 && i == 1) K3_PIECE(0, s + 3);
                    if (j == DPJ0 && i == 3) K3_PIECE(1, s + 3);
                    if (j == DPJ1 && i == 3) K3_PIECE(2, s + 3);
                    if (j == DPJ1 + 1 && i == 3) K3_PIECE(3, s + 3);
                    if (j == DPJ1 + 2 && i == 3) K3_PIECE(4, s + 3);
                }
                SCHED();
            }
            if constexpr (MODE == 0) { if (j == 8) { fill(2); fill(3); } }
            if constexpr (MODE == 5) {
                if (j == 0 && s == 4) st8_wt(mk_rsrc(ws + WS_G2, S_ * 8), lane == 0 ? (unsigned)((rt * 256 + ct * 8 + wid) * 8) : 0x80000000u, (u32x2){sgr, 1u});
            }
            if constexpr (MODE == 2) {
                if (j == 0) {
#pragma unroll
                    for (int i = 0; i < 4; ++i) gr[i] = __builtin_amdgcn_raw_buffer_load_b64(rg, (unsigned)(((i ^ (2 * wc)) * 16 + (lane & 15)) * 8), 0, 16);
                }
            }
            if constexpr (MODE == 3) {
                if (j == 0) {
                    asm volatile("" : "+v"(gr[0]), "+v"(gr[1]), "+v"(gr[2]), "+v"(gr[3]));
                    const unsigned co = (unsigned)((lane >= 18 ? F_ * 4 + (lane - 18) * 16 : lane * 16));
                    dma_m36(co, ws + WS_CS + (size_t)(ct * 144 + wc * 72) * 4, lds0 + K3_CS + wid * 1152);
                    dma_m36(co, ws + WS_CS + (size_t)(2 * F_ + ct * 144 + wc * 72) * 4, lds0 + K3_CS + wid * 1152 + 576);
                }
            }
            if constexpr (MODE == 3 || MODE == 4) {
                if (j >= 2 && j < 8) g2_wpiece((j - 2) % 3, (MODE - 3) * 2 + (j - 2) / 3, w2src, lane * 16, lds0 + G2_W0 + wid * 2304);
            }
            SCHED();
        }
    };
    iter(IC<0>{}, IC<2>{}, 1, tf[1], tf[0]);
    iter(IC<1>{}, IC<4>{}, 2, tf[0], tf[1]);
    iter(IC<1>{}, IC<2>{}, 3, tf[1], tf[0]);
#pragma unroll 1
    for (int s = 4; s < 14; s += 2) {
        iter(IC<5>{}, IC<0>{}, s, tf[0], tf[1]);
        iter(IC<1>{}, IC<0>{}, s + 1, tf[1], tf[0]);
    }
    STAMP(3);
    iter(IC<1>{}, IC<0>{}, 14, tf[0], tf[1]);
    iter(IC<2>{}, IC<0>{}, 15, tf[1], tf[0]);
    iter(IC<3>{}, IC<0>{}, 16, tf[0], tf[1]);
    iter(IC<4>{}, IC<0>{}, 17, tf[1], tf[0]);
    MFMA_DONE();
    STAMP(4);
    WAIT_V(12);
    __builtin_amdgcn_s_setprio(1);
    if (__builtin_expect(!__all((gr[0].y & gr[1].y & gr[2].y & gr[3].y) == 1u), 0)) {
        for (int spin = 0; spin < (1 << 20); ++spin) {
            __builtin_amdgcn_s_sleep(2);
#pragma unroll
            for (int i = 0; i < 4; ++i) gr[i] = __builtin_amdgcn_raw_buffer_load_b64(rg, (unsigned)(((i ^ (2 * wc)) * 16 + (lane & 15)) * 8), 0, 16);
            if (__all((gr[0].y & gr[1].y & gr[2].y & gr[3].y) == 1u)) break;
        }
    }
    const int mt = rt * 2 + (wr >> 1), ks = ct >> 3, lct = ct & 7;
    const rsrc_t rhh = mk_rsrc(ws + WS_HH, S_ * F_ * 2);
    const unsigned hb = (unsigned)(((mt * 4 + ks) * 36) * 8192 + ((wr & 1) * 4) * 1024);
    const LAS char* csb = lds + K3_CS + wid * 1152 + 16 * g4;
    TokC tcs[4];
#pragma unroll
    for (int i = 0; i < 4; ++i) {
        const float sd = bf_hi(gr[i].x), r = __builtin_amdgcn_rcpf(sd);
        tcs[i] = (TokC){-bf_lo(gr[i].x), -2.30220819f * r, -0.10294324f * (r * r * r), sd * sd};
    }
    {
        const LAS char* cm = lds + K3_CS + wid * 1152 + 256 + 16 * (g4 & 1);
        const f32x4 cgm = *(const LAS f32x4*)cm + *(const LAS f32x4*)(cm + 576), cum = *(const LAS f32x4*)(cm + 288) + *(const LAS f32x4*)(cm + 864);
#pragma unroll
        for (int ip = 0; ip < 2; ++ip) {
            const int ia = ((2 * ip) ^ (2 * wc)) + (lane >> 5);
            const bool up = lane >= 32;
            const TokC tc = {up ? tcs[2 * ip + 1].nm : tcs[2 * ip].nm, up ? tcs[2 * ip + 1].c0 : tcs[2 * ip].c0,
                             up ? tcs[2 * ip + 1].c1 : tcs[2 * ip].c1, up ? tcs[2 * ip + 1].q : tcs[2 * ip].q};
            f32x4 gm, um;
#pragma unroll
            for (int r = 0; r < 4; ++r) {
                auto sw = __builtin_amdgcn_permlane32_swap(__float_as_uint(acc[8][2 * ip][r]), __float_as_uint(acc[8][2 * ip + 1][r]), false, false);
                gm[r] = __uint_as_float(sw[0]); um[r] = __uint_as_float(sw[1]);
            }
            const int kgs = (lct & 1) * 2 + wc;
            st8_wt(rhh, hb + (unsigned)((lct >> 1) * 8192 + ia * 1024 + (kgs * 16 + (lane & 15)) * 16 + (g4 & 1) * 8),
                   (u32x2){geglu2(LO2(gm), LO2(um), LO2(cgm), LO2(cum), tc), geglu2(HI2(gm), HI2(um), HI2(cgm), HI2(cum), tc)});
        }
    }
    SCHED();
    constexpr int FLAGPOS = 1;
    const rsrc_t rfb = mk_rsrc(ws + WS_FB, 16384);
    const unsigned fbo = (unsigned)((mt * 4 + ks) * 256 + (lct * 4 + (wr & 1) * 2 + wc) * 4);
    u32x4 ob[4];
#pragma unroll
    for (int jp = 0; jp < 2; ++jp) {
        f32x4 cg[2], cu[2];
#pragma unroll
        for (int e = 0; e < 2; ++e) {
            cg[e] = *(const LAS f32x4*)(csb + (2 * jp + e) * 64) + *(const LAS f32x4*)(csb + 576 + (2 * jp + e) * 64);
            cu[e] = *(const LAS f32x4*)(csb + 288 + (2 * jp + e) * 64) + *(const LAS f32x4*)(csb + 864 + (2 * jp + e) * 64);
        }
#pragma unroll
        for (int i = 0; i < 4; ++i) {
            unsigned o[4];
#pragma unroll
            for (int e = 0; e < 2; ++e) {
                const f32x4 g = acc[2 * jp + e][i], u = acc[4 + 2 * jp + e][i];
                o[2 * e] = geglu2(LO2(g), LO2(u), LO2(cg[e]), LO2(cu[e]), tcs[i]);
                o[2 * e + 1] = geglu2(HI2(g), HI2(u), HI2(cg[e]), HI2(cu[e]), tcs[i]);
            }
            ob[i] = (u32x4){o[0], o[1], o[2], o[3]};
            st16_wt(rhh, (jp == 1 && wid == 7) ? 0x80000000u : hb + (unsigned)(((2 + jp * 8 + lct) * 2 + wc) * 8192 + (i ^ (2 * wc)) * 1024 + lane * 16), ob[i]);
            if (jp == 1 && i == FLAGPOS) {
                SCHED();
                WAIT_VI(FLAGPOS + 1);
                __builtin_amdgcn_raw_buffer_store_b32(1u, rfb, lane == 0 ? fbo : 0x80000000u, 0, 16);
                SCHED();
            }
        }
        SCHED();
        if (jp == 0) __builtin_amdgcn_s_setprio(0);
    }
    STAMP(5);
    if (wid == 7) {
        for (int spin = 0; spin < (1 << 20); ++spin) {
            const unsigned f = __builtin_amdgcn_raw_buffer_load_b32(rfb, (unsigned)((mt2 * 4 + ks2) * 256 + (lane & 31) * 4), 0, 16);
            if (__all(f == 1u)) break;
            __builtin_amdgcn_s_sleep(4);
        }
#pragma unroll
        for (int i = 0; i < 4; ++i) st16_wt(rhh, hb + (unsigned)(((10 + lct) * 2 + wc) * 8192 + (i ^ (2 * wc)) * 1024 + lane * 16), ob[i]);
    }
    BAR();
    STAMP(7);
    if (wid < 4) g2_body<5>(ws, out, lds, lane, wid, mt2, ks2, nt2, w2src, fbo + 128 DIAG_PASS);
    else g2_body<4>(ws, out, lds, lane, wid, mt2, ks2, nt2, w2src, fbo + 128 DIAG_PASS);
    DIAG_END(ws, 1);
}

extern "C" __global__ void k9_end() {}

extern "C" void kernel_launch(void* const* d_in, const int* in_sizes, int n_in, void* d_out, int out_size, void* d_ws,
                              size_t ws_size, hipStream_t stream) {
    const u16* q = (const u16*)d_in[0];
    const u16* k = (const u16*)d_in[1];
    const u16* v = (const u16*)d_in[2];
    const u16* shift_k = (const u16*)d_in[3];
    const u16* ssm_k = (const u16*)d_in[4];
    const u16* ssm_D = (const u16*)d_in[5];
    const u16* wg = (const u16*)d_in[7];
    const u16* wu = (const u16*)d_in[8];
    const u16* wd = (const u16*)d_in[9];
    char* ws = (char*)d_ws;
    (void)hipFuncSetAttribute((const void*)k1_conv, hipFuncAttributeMaxDynamicSharedMemorySize, C1_LDS);
    (void)hipFuncSetAttribute((const void*)k3_gemm1, hipFuncAttributeMaxDynamicSharedMemorySize, K3_LDS);
    k1_conv<<<256, 512, C1_LDS, stream>>>(q, k, v, shift_k, ssm_k, ssm_D, wg, wu, wd, ws);
    k3_gemm1<<<256, 512, K3_LDS, stream>>>(wg, wu, ws, (u16*)d_out);
}
```

```cpp
#include <hip/hip_runtime.h>
#include <hip/hip_bf16.h>

#define LAS __attribute__((address_space(3)))
typedef unsigned short u16;
typedef short s16x4 __attribute__((ext_vector_type(4)));
typedef short s16x8 __attribute__((ext_vector_type(8)));
typedef float f32x2 __attribute__((ext_vector_type(2)));
typedef float f32x4 __attribute__((ext_vector_type(4)));
typedef unsigned u32x2 __attribute__((ext_vector_type(2)));
typedef unsigned u32x4 __attribute__((ext_vector_type(4)));
typedef __bf16 bf16x2_t __attribute__((ext_vector_type(2)));

constexpr int S_ = 2048, D_ = 576, F_ = 4608;

__device__ __forceinline__ unsigned pk_bf16(float lo, float hi) {
    f32x2 v = {lo, hi};
    bf16x2_t b = __builtin_convertvector(v, bf16x2_t);
    return __builtin_bit_cast(unsigned, b);
}
__device__ __forceinline__ float bf_lo(unsigned w) { return __uint_as_float(w << 16); }
__device__ __forceinline__ float bf_hi(unsigned w) { return __uint_as_float(w & 0xffff0000u); }
__device__ __forceinline__ float rbf(float x) { return bf_lo(pk_bf16(x, 0.f)); }
#define WAIT_V(n) asm volatile("s_waitcnt vmcnt(" #n ")" ::: "memory")
#define BAR() do { __builtin_amdgcn_s_barrier(); asm volatile("" ::: "memory"); } while (0)
#define SCHED() __builtin_amdgcn_sched_barrier(0)
#define DIAG_DECL
#define MARK()
#define STAMP(i)
#define DIAG_END(wsbase, kid)
__device__ __forceinline__ void mfma_acc(f32x4& c, s16x8 a, s16x8 b) {
    asm volatile("v_mfma_f32_16x16x32_bf16 %0, %1, %2, %0" : "+v"(c) : "v"(a), "v"(b));
}
__device__ __forceinline__ void mfma_first(f32x4& c, s16x8 a, s16x8 b) {
    asm volatile("v_mfma_f32_16x16x32_bf16 %0, %1, %2, 0" : "=&v"(c) : "v"(a), "v"(b));
}
#define MFMA_DONE() do { asm volatile("s_nop 7\n\ts_nop 7" ::: "memory"); SCHED(); } while (0)
constexpr size_t WS_KV = 0;
constexpr size_t WS_H = WS_KV + (size_t)S_ * D_ * 2;
constexpr size_t WS_ST = WS_H + (size_t)S_ * D_ * 2;
constexpr size_t WS_CS = WS_ST + (size_t)S_ * 8;
constexpr size_t WS_G2 = WS_CS + (size_t)4 * F_ * 4;
constexpr size_t WS_F0 = WS_G2 + (size_t)S_ * 8;
constexpr int FSP = 128;
constexpr size_t WS_EP = WS_F0 + 1280 * FSP;
constexpr size_t WS_HH = WS_EP + 1024;
constexpr size_t WS_X = WS_HH + (size_t)S_ * F_ * 2;
constexpr unsigned X_BYTES = 64 * 4 * 3 * 9216;
constexpr size_t WS_FB = WS_X + X_BYTES;
constexpr size_t WS_WT = WS_FB + 16384;

template <int N> struct IC { static constexpr int v = N; };
typedef __amdgpu_buffer_rsrc_t rsrc_t;
__device__ __forceinline__ rsrc_t mk_rsrc(const void* p, unsigned bytes) { return __builtin_amdgcn_make_buffer_rsrc((void*)p, 0, bytes, 0x00020000); }
__device__ __forceinline__ void st16_wt(rsrc_t r, unsigned off, u32x4 v) { __builtin_amdgcn_raw_buffer_store_b128(v, r, off, 0, 16); }
__device__ __forceinline__ void st8_wt(rsrc_t r, unsigned off, u32x2 v) { __builtin_amdgcn_raw_buffer_store_b64(v, r, off, 0, 16); }
template <int CTRL> __device__ __forceinline__ float dpp_add(float x) {
    return x + __uint_as_float(__builtin_amdgcn_update_dpp(0, (int)__float_as_uint(x), CTRL, 0xf, 0xf, true));
}
__device__ __forceinline__ float x16_add(float x) {
    auto r = __builtin_amdgcn_permlane16_swap(__float_as_uint(x), __float_as_uint(x), false, false);
    return __uint_as_float(r[0]) + __uint_as_float(r[1]);
}
__device__ __forceinline__ float x32_add(float x) {
    auto r = __builtin_amdgcn_permlane32_swap(__float_as_uint(x), __float_as_uint(x), false, false);
    return __uint_as_float(r[0]) + __uint_as_float(r[1]);
}
__device__ __forceinline__ float sum16(float x) {
    x = dpp_add<0xB1>(x); x = dpp_add<0x4E>(x); x = dpp_add<0x141>(x);
    return dpp_add<0x140>(x);
}
__device__ __forceinline__ float ln_sd(float var) { return rbf(__builtin_amdgcn_sqrtf(rbf(rbf(var) + 1e-5f))); }

template <int OFF>
__device__ __forceinline__ void dma16(unsigned voff, const void* sbase, unsigned m0v) {
    asm volatile("s_mov_b32 m0, %2\n\ts_nop 0\n\tglobal_load_lds_dwordx4 %0, %1 offset:%3"
                 :: "v"(voff), "s"(sbase), "s"(m0v), "i"(OFF) : "memory");
}
template <int OFF>
__device__ __forceinline__ void dma16_sc1(unsigned voff, const void* sbase, unsigned m0v) {
    asm volatile("s_mov_b32 m0, %2\n\ts_nop 0\n\tglobal_load_lds_dwordx4 %0, %1 offset:%3 sc1"
                 :: "v"(voff), "s"(sbase), "s"(m0v), "i"(OFF) : "memory");
}
template <int OFF>
__device__ __forceinline__ void dma16_q(unsigned voff, const void* sbase, unsigned m0v) {
    asm volatile("s_mov_b32 m0, %2\n\ts_mov_b64 exec, 0xffff\n\tglobal_load_lds_dwordx4 %0, %1 offset:%3\n\ts_mov_b64 exec, -1"
                 :: "v"(voff), "s"(sbase), "s"(m0v), "i"(OFF) : "memory");
}
__device__ __forceinline__ void dma_m36(unsigned voff, const void* sbase, unsigned m0v) {
    asm volatile("s_mov_b32 m0, %2\n\ts_mov_b32 exec_hi, 15\n\tglobal_load_lds_dwordx4 %0, %1\n\ts_mov_b64 exec, -1"
                 :: "v"(voff), "s"(sbase), "s"(m0v) : "memory");
}
__device__ __forceinline__ void dma4(unsigned voff, const void* sbase, unsigned m0v) {
    asm volatile("s_mov_b32 m0, %2\n\ts_nop 0\n\tglobal_load_lds_dword %0, %1" :: "v"(voff), "s"(sbase), "s"(m0v) : "memory");
}
__device__ __forceinline__ void dma4_sc1(unsigned voff, const void* sbase, unsigned m0v) {
    asm volatile("s_mov_b32 m0, %2\n\ts_nop 0\n\tglobal_load_lds_dword %0, %1 sc1" :: "v"(voff), "s"(sbase), "s"(m0v) : "memory");
}
#define SYM_ADDR(out, sym) asm volatile("s_getpc_b64 s[40:41]\n\ts_add_u32 s40, s40, " #sym "@rel32@lo+4\n\ts_addc_u32 s41, s41, " #sym "@rel32@hi+12\n\ts_mov_b64 %0, s[40:41]" \
                                        : "=s"(out) :: "s40", "s41", "scc")
template <int NI>
__device__ __forceinline__ void warm_code(const char* a, const char* e, int l0, int lane, unsigned m0v) {
    const unsigned last = e > a ? (unsigned)(e - a) - 4u : 0u;
#pragma unroll
    for (int i = 0; i < NI; ++i) dma4(min((unsigned)(l0 + i * 64 + lane) * 128u, last), a, m0v);
}
__device__ __forceinline__ s16x8 tr_frag(const LAS char* p) {
    const s16x4 lo = __builtin_amdgcn_ds_read_tr16_b64_v4i16((LAS s16x4*)p);
    const s16x4 hi = __builtin_amdgcn_ds_read_tr16_b64_v4i16((LAS s16x4*)(p + 16 * 288));
    return __builtin_shufflevector(lo, hi, 0, 1, 2, 3, 4, 5, 6, 7);
}
__device__ __forceinline__ void wd_unit_load(const u16* wd, int un, int lane, unsigned m0v) {
    const int uB = un / 3, ut = un % 3, kh = uB & 1, st = (uB >> 1) % 18, kn = uB / 36;
    const bool full = st >= 2;
    const int f0 = (kn >> 2) * 1152 + (full ? ((st - 2) & 7) * 144 + kh * 72 + ((st - 2) >> 3) * 32 : (st * 2 + kh) * 288);
    const char* src = (const char*)wd + (size_t)f0 * (D_ * 2) + (kn & 3) * 288 + ut * 96;
#pragma unroll
    for (int i = 0; i < 3; ++i) {
        const int q = lane + 64 * i, r = q / 6, pos = q % 6, kg = (r & 15) >> 2;
        const int f = full ? r : (kg >> 1) * 144 + (kg & 1) * 72 + 64 + (r & 3) + 4 * (r >> 4);
        dma16<0>((unsigned)(f * (D_ * 2) + pos * 16), src, m0v + i * 1024);
    }
}
__device__ __forceinline__ void wd_unit_store(char* ws, int un, int lane, const LAS char* l) {
    const rsrc_t r = mk_rsrc(ws + WS_WT, F_ * D_ * 2);
#pragma unroll
    for (int t = 0; t < 3; ++t) {
        const LAS char* tp = l + (lane >> 2) * 96 + (lane & 3) * 8 + t * 32;
        const s16x4 lo = __builtin_amdgcn_ds_read_tr16_b64_v4i16((LAS s16x4*)tp), hi = __builtin_amdgcn_ds_read_tr16_b64_v4i16((LAS s16x4*)(tp + 16 * 96));
        st16_wt(r, (unsigned)(un * 3072 + t * 1024 + lane * 16), __builtin_bit_cast(u32x4, __builtin_shufflevector(lo, hi, 0, 1, 2, 3, 4, 5, 6, 7)));
    }
}
#define T_WU4 3
#define T_HD 48
#define T_HD2 24
#define T_P0 16
#define T_P1 1
constexpr int WU4 = T_WU4;
constexpr int HELPER_DELAY = T_HD, POLL0 = T_P0, POLL1 = T_P1;
constexpr int C1_COPY = 4288;
constexpr int C1_FILT = 4 * C1_COPY;
constexpr int C1_KV = 2304 * 2;
constexpr int C1_KVOFF = 4 * C1_FILT + 64;
constexpr int C1_YOFF = C1_KVOFF + 4 * C1_KV;
constexpr int C1_XOFF = C1_YOFF + 16384;
constexpr int C1_SOFF = C1_XOFF + 16384;
constexpr int C1_TOFF = C1_SOFF + 16384;
constexpr int C1_WOFF = C1_TOFF + 2048;
constexpr int C1_LDS = C1_WOFF + 24576;

extern "C" __global__ __launch_bounds__(512, 2) void k1_conv(const u16* __restrict__ x, const u16* __restrict__ kin, const u16* __restrict__ vin,
                                                  const u16* __restrict__ shift_k, const u16* __restrict__ ssm_k, const u16* __restrict__ ssm_D,
                                                  const u16* __restrict__ wg, const u16* __restrict__ wu, const u16* __restrict__ wd,
                                                  char* __restrict__ ws) {
    extern __shared__ __attribute__((aligned(16))) char smem[];
    const int tid = threadIdx.x, lane = tid & 63;
    const int wid = __builtin_amdgcn_readfirstlane(tid >> 6);
    const int b = blockIdx.x, xcd = b & 7, idx = b >> 3;
    {
        const char *c1, *c3;
        SYM_ADDR(c1, k1_conv); SYM_ADDR(c3, k3_gemm1);
        if (wid >= 4) warm_code<1>(c1, c3, (wid & 3) * 64, lane, (unsigned)(size_t)(LAS char*)smem + C1_TOFF + 256 + (wid & 3) * 256);
    }
    DIAG_DECL;
    asm volatile("" :: "s"(x), "s"(kin), "s"(vin), "s"(shift_k), "s"(ssm_k), "s"(ssm_D), "s"(wg), "s"(wu), "s"(wd), "s"(ws));
    const bool conv = idx < 18;
    const int cg = xcd * 18 + idx, ch0 = cg * 4;
    const unsigned lds0 = (unsigned)(size_t)(LAS char*)smem;
    LAS unsigned* tagp = (LAS unsigned*)(smem + C1_TOFF);
    auto get_tag = [&]() -> unsigned { BAR(); return __builtin_amdgcn_readfirstlane(*(volatile LAS unsigned*)tagp); };

    const int h = (idx - 18) * 8 + xcd, hc_ = h < 108 ? h : 0;
    const int m3 = hc_ % 3, lcA = (hc_ / 3) * 4 + m3, rb0 = m3 * 3;
    constexpr int HU = (1728 - 144 * WU4 + 111) / 112;
    const int hbase = 144 * WU4 + h * HU, hun = hbase + wid * 2, hend = min(hbase + HU, 1728);
    auto cs_load = [&](u32x4 (&v)[12]) {
        const int rs = tid >> 3, c8 = tid & 7;
#pragma unroll
        for (int p = 0; p < 12; ++p) {
            const int rb = rb0 + p, lc = lcA + (rb >= 9), r = (rb >= 9 ? rb - 9 : rb) * 64 + rs;
            v[p] = *(const u32x4*)((lc >= 72 ? wu : wg) + (size_t)r * F_ + (lc % 72) * 64 + c8 * 8);
        }
        SCHED();
#pragma unroll
        for (int u = 0; u < 2; ++u) if (hun + u < hend) wd_unit_load(wd, hun + u, lane, lds0 + 16384 + (wid * 2 + u) * 3072);
        st16_wt(mk_rsrc(ws + WS_FB, 16384), (unsigned)(h * 8192 + tid * 16), (u32x4){0u, 0u, 0u, 0u});
        SCHED();
    };
    auto cs_finish = [&](u32x4 (&v)[12]) {
        float g[4][8];
#pragma unroll
        for (int q = 0; q < 4; ++q) {
#pragma unroll
            for (int e = 0; e < 4; ++e) {
                g[q][2 * e] = (bf_lo(v[3 * q][e]) + bf_lo(v[3 * q + 1][e])) + bf_lo(v[3 * q + 2][e]);
                g[q][2 * e + 1] = (bf_hi(v[3 * q][e]) + bf_hi(v[3 * q + 1][e])) + bf_hi(v[3 * q + 2][e]);
            }
        }
        LAS float* red = (LAS float*)smem;
#pragma unroll
        for (int l = 0; l < 2; ++l) {
            float a[8];
#pragma unroll
            for (int e = 0; e < 8; ++e) {
                a[e] = l == 0 ? g[0][e] + (m3 <= 1 ? g[1][e] : 0.f) + (m3 == 0 ? g[2][e] : 0.f) : g[3][e] + (m3 >= 1 ? g[2][e] : 0.f) + (m3 == 2 ? g[1][e] : 0.f);
                a[e] = x32_add(x16_add(dpp_add<0x128>(a[e])));
            }
            if (lane < 8) {
                *(LAS f32x4*)(red + (wid * 2 + l) * 64 + lane * 8) = (f32x4){a[0], a[1], a[2], a[3]};
                *(LAS f32x4*)(red + (wid * 2 + l) * 64 + lane * 8 + 4) = (f32x4){a[4], a[5], a[6], a[7]};
            }
        }
        WAIT_V(1);
#pragma unroll
        for (int u = 0; u < 2; ++u) if (hun + u < hend) wd_unit_store(ws, hun + u, lane, (const LAS char*)smem + 16384 + (wid * 2 + u) * 3072);
        asm volatile("s_waitcnt lgkmcnt(0)" ::: "memory"); BAR();
        if (tid < 192 && h < 108) {
            float s = 0.f;
            if (tid < 128) {
#pragma unroll
                for (int w = 0; w < 8; ++w) s += red[w * 128 + tid];
            }
            const int part = tid >> 6;
            const int lc = part == 0 ? lcA : (part == 1 ? lcA + 1 : (m3 == 0 ? lcA : lcA + 1));
            const int slot = part == 0 ? (m3 != 0) : (part == 1 ? 0 : 1);
            if (part < 2 || m3 != 1)
                __builtin_amdgcn_raw_buffer_store_b32(__float_as_uint(s), mk_rsrc(ws + WS_CS, 4 * F_ * 4), (unsigned)((slot * 2 * F_ + lc * 64 + (tid & 63)) * 4), 0, 16);
        }
    };
    auto wait_kv = [&](unsigned tag) {
        const rsrc_t rf = mk_rsrc(ws + WS_F0 + ((idx >> 2) * 8 + xcd) * 32 * FSP, 32 * FSP);
        __builtin_amdgcn_s_sleep(POLL0);
        for (int spin = 0; spin < (1 << 20); ++spin) {
            const unsigned f = __builtin_amdgcn_raw_buffer_load_b32(rf, (lane & 31) * FSP, 0, 16);
            if (__all(f == tag)) break;
            __builtin_amdgcn_s_sleep(POLL1);
        }
    };

    auto x_rows = [&]() {
        u32x2 xr[8];
#pragma unroll
        for (int i = 0; i < 8; ++i) xr[i] = *(const u32x2*)(x + (size_t)((tid - 256) + (wid - 4) * 448 + i * 64) * D_ + ch0);
#pragma unroll
        for (int i = 0; i < 8; ++i) *(LAS u32x2*)(smem + C1_XOFF + ((tid - 256) + (wid - 4) * 448 + i * 64) * 8) = xr[i];
        SCHED();
    };
    unsigned tag;
    unsigned dvv = 0;
    auto upper = [&](auto is_stat, auto is_conv) {
        constexpr bool STAT = decltype(is_stat)::v, CONV = decltype(is_conv)::v;
        const int srow = b * 8 + (wid - 5) * 4 + (lane >> 4), t3 = tid - 320;
        unsigned xw[18], epv = 0;
        if constexpr (STAT) {
            const unsigned* xr = (const unsigned*)(x + (size_t)srow * D_) + (lane & 15);
#pragma unroll
            for (int j = 0; j < 18; ++j) xw[j] = xr[16 * j];
        } else {
            epv = __hip_atomic_load((const unsigned*)(ws + WS_EP), __ATOMIC_RELAXED, __HIP_MEMORY_SCOPE_AGENT);
        }
        SCHED();
        if constexpr (STAT) {
            const char *c3, *c4;
            SYM_ADDR(c3, k3_gemm1); SYM_ADDR(c4, k9_end);
            warm_code<1>(c3, c4, ((idx & 3) * 2 + wid - 5) * 64, lane, lds0 + C1_TOFF + 256 + (wid - 5) * 256);
        }
        SCHED();
        u32x4 v[12], fa[6]; u32x2 fb[6];
        if constexpr (CONV) {
#pragma unroll
            for (int i = 0; i < 6; ++i) {
                const int task = min(t3 + 192 * i, 1023), u = task & 255;
                const u16* src = ssm_k + (size_t)(ch0 + (task >> 8)) * S_ + u * 8;
                fa[i] = *(const u32x4*)src;
                fb[i] = *(const u32x2*)(src + (u < 255 ? 8 : 0));
            }
            SCHED();
        }
        if constexpr (STAT) {
            f32x2 xf[18], s = {0.f, 0.f};
#pragma unroll
            for (int j = 0; j < 18; ++j) { xf[j] = (f32x2){bf_lo(xw[j]), bf_hi(xw[j])}; s += xf[j]; }
            const float mean = sum16(s.x + s.y) * (1.f / D_);
            f32x2 q = {0.f, 0.f};
#pragma unroll
            for (int j = 0; j < 18; ++j) { const f32x2 d = xf[j] - mean; q = __builtin_elementwise_fma(d, d, q); }
            const unsigned sgr = pk_bf16(mean, ln_sd(sum16(q.x + q.y) * (1.f / D_)));
            tag = get_tag();
            const unsigned so = (lane & 15) == 0 ? (unsigned)srow * 8 : 0x80000000u;
            st8_wt(mk_rsrc(ws + WS_ST, S_ * 8), so, (u32x2){sgr, tag});
            st8_wt(mk_rsrc(ws + WS_G2, S_ * 8), so, (u32x2){0u, 0u});
        } else {
            *tagp = epv + 1u;
            asm volatile("s_waitcnt lgkmcnt(0)" ::: "memory");
            tag = get_tag();
        }
        SCHED();
        if constexpr (!CONV) {
            __builtin_amdgcn_s_sleep(HELPER_DELAY);
            cs_load(v);
            if (!STAT && h == 111) {
                const rsrc_t rf = mk_rsrc(ws + WS_F0, 256 * FSP);
                for (int spin = 0; spin < (1 << 20); ++spin) {
                    bool ok = true;
#pragma unroll
                    for (int i = 0; i < 4; ++i) ok &= __builtin_amdgcn_raw_buffer_load_b32(rf, (i * 64 + lane) * FSP, 0, 16) == tag;
                    if (__all(ok)) break;
                    __builtin_amdgcn_s_sleep(8);
                }
                if (lane == 0) __hip_atomic_store((unsigned*)(ws + WS_EP), tag, __ATOMIC_RELAXED, __HIP_MEMORY_SCOPE_AGENT);
            }
            cs_finish(v);
            return;
        }
        if (t3 < 80) {
            const int cpy = t3 / 5, e = t3 % 5, q = e < 3 ? e : 257 + e;
            *(LAS u32x4*)(smem + cpy * C1_COPY + q * 16) = (u32x4){0u, 0u, 0u, 0u};
        } else if (t3 < 84) {
            *(LAS u32x4*)(smem + C1_KVOFF - 64 + (t3 - 80) * 16) = (u32x4){0u, 0u, 0u, 0u};
        }
        if (t3 < 128) *(LAS u32x4*)(smem + C1_KVOFF + (t3 >> 5) * C1_KV + 4096 + (t3 & 31) * 16) = (u32x4){0u, 0u, 0u, 0u};
#pragma unroll
        for (int i = 0; i < 6; ++i) {
            const int task = min(t3 + 192 * i, 1023), u = task & 255;
            const unsigned d[6] = {fa[i].x, fa[i].y, fa[i].z, fa[i].w, u < 255 ? fb[i].x : 0u, u < 255 ? fb[i].y : 0u};
            unsigned o[5];
#pragma unroll
            for (int e = 0; e < 5; ++e) o[e] = __builtin_amdgcn_alignbit(d[e + 1], d[e], 16);
            LAS char* base = (LAS char*)smem + (task >> 8) * C1_FILT + (u + 4) * 16;
            *(LAS u32x4*)(base) = (u32x4){o[0], o[1], o[2], o[3]};
            *(LAS u32x4*)(base + C1_COPY) = (u32x4){d[1], d[2], d[3], d[4]};
            *(LAS u32x4*)(base + 2 * C1_COPY) = (u32x4){o[1], o[2], o[3], o[4]};
            *(LAS u32x4*)(base + 3 * C1_COPY) = (u32x4){d[2], d[3], d[4], d[5]};
            if (u == 0) {
                *(LAS u32x4*)(base - 16) = (u32x4){0u, 0u, 0u, d[0] << 16};
                *(LAS u32x4*)(base - 16 + C1_COPY) = (u32x4){0u, 0u, 0u, d[0]};
                *(LAS u32x4*)(base - 16 + 2 * C1_COPY) = (u32x4){0u, 0u, d[0] << 16, o[0]};
                *(LAS u32x4*)(base - 16 + 3 * C1_COPY) = (u32x4){0u, 0u, d[0], d[1]};
            }
        }
        asm volatile("s_waitcnt lgkmcnt(0)" ::: "memory"); BAR();
        x_rows();
    };
    if (wid >= 5) {
        if (!conv) {
            if (wid < 7) upper(IC<1>{}, IC<0>{}); else upper(IC<0>{}, IC<0>{});
            DIAG_END(ws, 0);
            return;
        }
        if (wid < 7) upper(IC<1>{}, IC<1>{}); else upper(IC<0>{}, IC<1>{});
    } else {
        {
            unsigned kw[11], vw[8];
            const int t0 = idx * 64 + (lane >> 3) * 8, dc = wid * 8 + (lane & 7), kch = xcd * 72 + (dc < 36 ? dc : 35) * 2;
            const rsrc_t rk = mk_rsrc(kin, S_ * D_ * 2), rv = mk_rsrc(vin, S_ * D_ * 2);
            const int off = t0 * (D_ * 2) + kch * 2;
#pragma unroll
            for (int j = 0; j < 11; ++j) kw[j] = __builtin_amdgcn_raw_buffer_load_b32(rk, (unsigned)(off + (j - 3) * (D_ * 2)), 0, 0);
#pragma unroll
            for (int j = 0; j < 8; ++j) vw[j] = __builtin_amdgcn_raw_buffer_load_b32(rv, (unsigned)(off + j * (D_ * 2)), 0, 0);
            const u32x4 sw = *(const u32x4*)(shift_k + kch * 4);
            dvv = ssm_D[(conv ? ch0 : 0) + (wid & 3)];
            SCHED();
            STAMP(1);
            const f32x2 w0 = {bf_lo(sw.x), bf_lo(sw.z)}, w1 = {bf_hi(sw.x), bf_hi(sw.z)}, w2 = {bf_lo(sw.y), bf_lo(sw.w)}, w3 = {bf_hi(sw.y), bf_hi(sw.w)};
            f32x2 kf[11], o[8];
#pragma unroll
            for (int j = 0; j < 11; ++j) kf[j] = (f32x2){bf_lo(kw[j]), bf_hi(kw[j])};
#pragma unroll
            for (int j = 0; j < 8; ++j) {
                f32x2 s = kf[j] * w3;
                s = __builtin_elementwise_fma(kf[j + 1], w2, s);
                s = __builtin_elementwise_fma(kf[j + 2], w1, s);
                s = __builtin_elementwise_fma(kf[j + 3], w0, s);
                o[j] = s * (f32x2){bf_lo(vw[j]), bf_hi(vw[j])};
            }
            const rsrc_t ro = mk_rsrc(ws + WS_KV, S_ * D_ * 2);
            const unsigned oo = dc < 36 ? (unsigned)(kch * (S_ * 2) + (2040 - t0) * 2) : 0x80000000u;
            st16_wt(ro, oo, (u32x4){pk_bf16(o[7].x, o[6].x), pk_bf16(o[5].x, o[4].x), pk_bf16(o[3].x, o[2].x), pk_bf16(o[1].x, o[0].x)});
            st16_wt(ro, oo + S_ * 2, (u32x4){pk_bf16(o[7].y, o[6].y), pk_bf16(o[5].y, o[4].y), pk_bf16(o[3].y, o[2].y), pk_bf16(o[1].y, o[0].y)});
        }
        tag = get_tag();
        WAIT_V(0);
        if (lane == 0) __hip_atomic_store((unsigned*)(ws + WS_F0 + ((wid * 8 + xcd) * 32 + idx) * FSP), tag, __ATOMIC_RELAXED, __HIP_MEMORY_SCOPE_AGENT);
        MARK();
        STAMP(2);
        if (!conv) {
            u32x4 v[12];
            __builtin_amdgcn_s_sleep(T_HD2);
            cs_load(v);
            cs_finish(v);
            DIAG_END(ws, 0);
            return;
        }
        if (wid == 4) {
            x_rows();
            BAR();
#pragma unroll 1
            for (int u = 0; u < WU4; ++u) wd_unit_load(wd, cg * WU4 + u, lane, lds0 + C1_WOFF + u * 3072);
            __builtin_amdgcn_s_sleep(24);
            const char* gs = ws + WS_ST;
#pragma unroll
            for (int i = 0; i < 4; ++i) {
                dma16_sc1<0>(lane * 16, gs + i * 4096, lds0 + C1_SOFF + i * 4096); dma16_sc1<1024>(lane * 16, gs + i * 4096, lds0 + C1_SOFF + i * 4096);
                dma16_sc1<2048>(lane * 16, gs + i * 4096, lds0 + C1_SOFF + i * 4096); dma16_sc1<3072>(lane * 16, gs + i * 4096, lds0 + C1_SOFF + i * 4096);
            }
            WAIT_V(0);
#pragma unroll 1
            for (int u = 0; u < WU4; ++u) wd_unit_store(ws, cg * WU4 + u, lane, (const LAS char*)smem + C1_WOFF + u * 3072);
        } else {
        {
            wait_kv(tag);
            STAMP(7);
            const char* kvs = ws + WS_KV + (size_t)(ch0 + wid) * (S_ * 2);
            const unsigned m = lds0 + C1_KVOFF + wid * C1_KV;
            dma16_sc1<0>(lane * 16, kvs, m); dma16_sc1<1024>(lane * 16, kvs, m); dma16_sc1<2048>(lane * 16, kvs, m); dma16_sc1<3072>(lane * 16, kvs, m);
            WAIT_V(0);
            STAMP(9);
            BAR();
            STAMP(4);
        }
        const int row = lane & 15, kg = lane >> 4;
        const LAS char* abase = (const LAS char*)smem + wid * C1_FILT + (row & 3) * C1_COPY + (24 + 4 * (row >> 2) - 8 * kg) * 2;
        const LAS char* bbase = (const LAS char*)smem + C1_KVOFF + wid * C1_KV + (2040 - 16 * row - 8 * kg) * 2 - 512 * 7 - 448;
        auto rd_ah = [&](int r, int L, int hf) -> s16x4 { return *(const volatile LAS s16x4*)(abase + 512 * L - 64 * r + 8 * hf); };
        auto rd_b = [&](int r, int m) -> s16x8 { return *(const LAS s16x8*)(bbase + 448 - 64 * r + 512 * (7 - m)); };
        auto cat = [&](s16x4 lo, s16x4 hi) -> s16x8 { return __builtin_shufflevector(lo, hi, 0, 1, 2, 3, 4, 5, 6, 7); };
        f32x4 acc[8];
        s16x4 al[2][8], ah[2][8];
        s16x8 bf[2][8], bd[8];
        auto rd_k = [&](int r, int k) {
            if (k < 2) { if (k == 0) al[r & 1][0] = rd_ah(r, 1, 0); else ah[r & 1][0] = rd_ah(r, 1, 1); }
            else if (k < 10) bf[r & 1][9 - k] = rd_b(r, 8 - k);
            else { const int L = (k - 10) / 2 + 2; if (k & 1) ah[r & 1][L - 1] = rd_ah(r, L, 1); else al[r & 1][L - 1] = rd_ah(r, L, 0); }
        };
        const s16x8 a0 = cat(rd_ah(0, 0, 0), rd_ah(0, 0, 1));
#pragma unroll
        for (int e = 0; e < 8; ++e) bd[e] = rd_b(0, e);
        s16x8 dfr;
        {
            const int j = 8 * kg + 7 - row;
            u32x4 w;
#pragma unroll
            for (int e = 0; e < 4; ++e) w[e] = j == 2 * e ? dvv : (j == 2 * e + 1 ? dvv << 16 : 0u);
            dfr = __builtin_bit_cast(s16x8, w);
            asm volatile("s_nop 1" : "+v"(dfr));
        }
        SCHED();
#pragma unroll
        for (int n = 0; n < 16; ++n) {
            if (n < 8) mfma_first(acc[n], a0, bd[n]); else mfma_acc(acc[n - 8], dfr, bd[n - 8]);
            SCHED();
            if (n == 0) { rd_k(0, 0); rd_k(0, 1); }
            else if (n == 1) rd_k(0, 9);
            else rd_k(0, n + 8);
            SCHED();
        }
#pragma unroll
        for (int r = 0; r < 8; ++r) {
            int k = 0;
#pragma unroll
            for (int L = 1; L <= 8; ++L) {
#pragma unroll
                for (int n = 7; n >= L - 1; --n) {
                    const int e = n - L + 1;
                    mfma_acc(acc[n], cat(al[r & 1][L - 1], ah[r & 1][L - 1]), (r == 0 && e > 0) ? bd[e - 1] : bf[r & 1][e]);
                    SCHED();
                    if (r < 7 && k < 24) rd_k(r + 1, k);
                    ++k;
                    SCHED();
                }
            }
        }
        MFMA_DONE();
#pragma unroll
        for (int n = 0; n < 8; ++n)
            *(LAS u32x2*)(smem + C1_YOFF + wid * 4096 + (256 * n + 16 * row + 4 * kg) * 2) = (u32x2){pk_bf16(acc[n][0], acc[n][1]), pk_bf16(acc[n][2], acc[n][3])};
        }
    }
    STAMP(5);
    asm volatile("s_waitcnt lgkmcnt(0)" ::: "memory"); BAR();
    STAMP(6);
    {
        u32x2 yc[4];
#pragma unroll
        for (int c = 0; c < 4; ++c) yc[c] = *(const LAS u32x2*)(smem + C1_YOFF + c * 4096 + tid * 8);
        const u32x4 xa = *(const LAS u32x4*)(smem + C1_XOFF + tid * 32), xb = *(const LAS u32x4*)(smem + C1_XOFF + tid * 32 + 16);
        const u32x2 xr[4] = {{xa.x, xa.y}, {xa.z, xa.w}, {xb.x, xb.y}, {xb.z, xb.w}};
        u32x4 s01 = *(const LAS u32x4*)(smem + C1_SOFF + tid * 32), s23 = *(const LAS u32x4*)(smem + C1_SOFF + tid * 32 + 16);
        if (__builtin_expect(!__all(s01.y == tag && s01.w == tag && s23.y == tag && s23.w == tag), 0)) {
            const rsrc_t rs = mk_rsrc(ws + WS_ST, S_ * 8);
            for (int spin = 0; spin < (1 << 20); ++spin) {
                s01 = __builtin_amdgcn_raw_buffer_load_b128(rs, tid * 32, 0, 16);
                s23 = __builtin_amdgcn_raw_buffer_load_b128(rs, tid * 32 + 16, 0, 16);
                if (__all(s01.y == tag && s01.w == tag && s23.y == tag && s23.w == tag)) break;
                __builtin_amdgcn_s_sleep(2);
            }
        }
        const unsigned sv[4] = {s01.x, s01.z, s23.x, s23.z};
        float mm[4], rr[4];
#pragma unroll
        for (int j = 0; j < 4; ++j) { mm[j] = bf_lo(sv[j]); rr[j] = __builtin_amdgcn_rcpf(bf_hi(sv[j])); }
        unsigned hw[8];
#pragma unroll
        for (int j = 0; j < 4; ++j) {
#pragma unroll
            for (int e = 0; e < 2; ++e) {
                const unsigned xw = e ? xr[j].y : xr[j].x;
                const unsigned ya = (j >> 1) ? yc[2 * e].y : yc[2 * e].x, yb = (j >> 1) ? yc[2 * e + 1].y : yc[2 * e + 1].x;
                const float y0 = (j & 1) ? bf_hi(ya) : bf_lo(ya), y1 = (j & 1) ? bf_hi(yb) : bf_lo(yb);
                const float x0 = bf_lo(xw), x1 = bf_hi(xw);
                hw[2 * j + e] = pk_bf16(__builtin_fmaf((x0 - mm[j]) * rr[j], y0, x0), __builtin_fmaf((x1 - mm[j]) * rr[j], y1, x1));
            }
        }
        const rsrc_t rh = mk_rsrc(ws + WS_H, S_ * D_ * 2);
        const unsigned ho = (unsigned)(((tid >> 6) * 144 + cg) * 2048 + (tid & 63) * 32);
        st16_wt(rh, ho, (u32x4){hw[0], hw[1], hw[2], hw[3]});
        st16_wt(rh, ho + 16, (u32x4){hw[4], hw[5], hw[6], hw[7]});
    }
    STAMP(8);
    DIAG_END(ws, 0);
}

constexpr int ST_BYTES = 16384 + 2 * 9216;
constexpr int NBUF = 4;
constexpr int G_LDS = NBUF * ST_BYTES;

struct TokC { float nm, c0, c1, q; };
__device__ __forceinline__ unsigned geglu2(f32x2 ag, f32x2 au, f32x2 csg, f32x2 csu, const TokC& t) {
    const f32x2 nm = {t.nm, t.nm}, c0 = {t.c0, t.c0}, c1 = {t.c1, t.c1}, q = {t.q, t.q};
    const f32x2 g = __builtin_elementwise_fma(nm, csg, ag), u = __builtin_elementwise_fma(nm, csu, au);
    const f32x2 z = g * __builtin_elementwise_fma(g * g, c1, c0);
    const f32x2 e = {__builtin_amdgcn_exp2f(z.x), __builtin_amdgcn_exp2f(z.y)};
    const f32x2 d = __builtin_elementwise_fma(e, q, q);
    const f32x2 r = (g * u) * (f32x2){__builtin_amdgcn_rcpf(d.x), __builtin_amdgcn_rcpf(d.y)};
    return pk_bf16(r.x, r.y);
}
#define LO2(v) __builtin_shufflevector(v, v, 0, 1)
#define HI2(v) __builtin_shufflevector(v, v, 2, 3)
#define WAIT_VI(N) asm volatile("s_waitcnt vmcnt(%0)" :: "i"(N) : "memory")

constexpr int G2_W0 = 71680, G2_FL = 69632;
__device__ __forceinline__ void g2_wpiece(int P, int s, const char* wsrc, unsigned aoff, unsigned mw0) {
    const unsigned m = mw0 + (s % 5) * 18432;
    if (P == 0) dma16<0>(aoff, wsrc + (size_t)s * 18432, m);
    if (P == 1) dma16<1024>(aoff, wsrc + (size_t)s * 18432, m);
    if (P == 2) dma16_q<2048>(aoff, wsrc + (size_t)s * 18432, m);
}
constexpr int g2_n(int S) { return (S + 3 < 18 ? 2 : 0) + (S + 4 < 18 ? 3 : 0) + (S == 4 ? 1 : 0); }
constexpr int g2_vm(int S) {
    return S == 0 ? 3 : (S == 1 ? 1 + g2_n(0) : g2_n(S - 2) - (S + 1 < 18 ? 2 : 0) + g2_n(S - 1));
}
#define DIAG_ARG
#define DIAG_PASS
template <int NT>
__device__ __forceinline__ void g2_body(char* __restrict__ ws, u16* __restrict__ out, LAS char* lds, int lane, int wid, int mt, int ks, int nt,
                                        const char* wsrc, unsigned flag2 DIAG_ARG) {
    constexpr int dh = NT == 4;
    const unsigned lds0 = (unsigned)(size_t)lds;
    const int wr = wid & 3;
    const char* asrc = ws + WS_HH + ((size_t)(mt * 4 + ks) * 36) * 8192 + wid * 2048;
    const unsigned aoff = lane * 16;
    const unsigned ma0 = lds0 + wid * 2048, mw0 = lds0 + G2_W0 + wid * 2304;
    auto hpiece = [&](int P, int s) {
        if (P == 0) dma16_sc1<0>(aoff, asrc + (size_t)s * 16384, ma0 + (s & 3) * 16384);
        if (P == 1) dma16_sc1<1024>(aoff, asrc + (size_t)s * 16384, ma0 + (s & 3) * 16384);
    };
    hpiece(0, 0); hpiece(1, 0); hpiece(0, 1); hpiece(1, 1); hpiece(0, 2); hpiece(1, 2);

    const int g4 = lane >> 4;
    const int toff = wr * 2048 + lane * 16;
    const int foff = G2_W0 + dh * 5120 + lane * 16;
    auto rdf = [&](const LAS char* p) -> s16x8 { return *(const LAS s16x8*)p; };
    const rsrc_t rfb = mk_rsrc(ws + WS_FB, 16384);

    f32x4 acc[NT][2];
    s16x8 wf[2 * NT], tf[2][4];
    WAIT_V(4);
    __builtin_amdgcn_raw_buffer_store_b32(1u, rfb, lane == 0 ? flag2 : 0x80000000u, 0, 16);
    BAR();
    STAMP(8);
#pragma unroll
    for (int e = 0; e < 4; ++e) tf[0][e] = rdf(lds + toff + (e >> 1) * 8192 + (e & 1) * 1024);
#pragma unroll
    for (int e = 0; e < 2 * NT; ++e) wf[e] = rdf(lds + foff + (e / NT) * 9216 + (e % NT) * 1024);
    SCHED();

    auto iter = [&](auto sc, s16x8 (&tc)[4], s16x8 (&tn)[4]) {
        constexpr int S = decltype(sc)::v;
        if constexpr (S < 17) {
            WAIT_VI(g2_vm(S));
            BAR();
            if constexpr (S == 7) {
                unsigned fb2 = *(const volatile LAS unsigned*)(lds + G2_FL + wid * 256 + lane * 4);
                if (__builtin_expect(!__all(fb2 == 1u), 0)) {
                    for (int spin = 0; spin < (1 << 20); ++spin) {
                        fb2 = __builtin_amdgcn_raw_buffer_load_b32(rfb, (unsigned)(((mt * 4 + ks) * 2 + 1) * 128 + (lane & 31) * 4), 0, 16);
                        if (__all(fb2 == 1u)) break;
                        __builtin_amdgcn_s_sleep(2);
                    }
                }
            }
            SCHED();
        }
        const LAS char* tb = lds + ((S + 1) & 3) * 16384;
        const LAS char* wb = lds + ((S + 1) % 5) * 18432;
#pragma unroll
        for (int e = 0; e < 2 * NT; ++e) {
#pragma unroll
            for (int i = 0; i < 2; ++i) {
                if (S == 0 && e < NT) mfma_first(acc[e % NT][i], wf[e], tc[(e / NT) * 2 + i]);
                else mfma_acc(acc[e % NT][i], wf[e], tc[(e / NT) * 2 + i]);
            }
            SCHED();
            if constexpr (S < 17) {
                wf[e] = rdf(wb + foff + (e / NT) * 9216 + (e % NT) * 1024);
                if (e >= 2 && e < 6) tn[e - 2] = rdf(tb + toff + ((e - 2) >> 1) * 8192 + ((e - 2) & 1) * 1024);
            }
            if constexpr (S + 3 < 18) { if (e < 2) hpiece(e, S + 3); }
            if constexpr (S + 4 < 18) {
                if (e == 3) g2_wpiece(0, S + 4, wsrc, aoff, mw0);
                if (e == 5) g2_wpiece(1, S + 4, wsrc, aoff, mw0);
                if (e == 6) g2_wpiece(2, S + 4, wsrc, aoff, mw0);
            }
            if constexpr (S == 4) {
                if (e == 7) dma4_sc1((unsigned)(lane & 31) * 4, ws + WS_FB + ((mt * 4 + ks) * 2 + 1) * 128, lds0 + G2_FL + wid * 256);
            }
            SCHED();
        }
    };
#define G2_IT2(S) iter(IC<S>{}, tf[0], tf[1]); iter(IC<S + 1>{}, tf[1], tf[0])
    G2_IT2(0); G2_IT2(2); G2_IT2(4); G2_IT2(6); G2_IT2(8); G2_IT2(10); G2_IT2(12);
    STAMP(9);
    G2_IT2(14);
    iter(IC<16>{}, tf[0], tf[1]);
    const int tok0 = mt * 128 + wr * 32 + (lane & 15);
    u32x2 hv[2][5];
    if (wr == ks) {
#pragma unroll
        for (int i = 0; i < 2; ++i) {
            const int t = tok0 + i * 16;
            const char* hp = ws + WS_H + ((size_t)((t >> 8) * 144 + nt * 36 + dh * 20) * 256 + (t & 255)) * 8;
#pragma unroll
            for (int e = 0; e < 4; ++e) hv[i][e] = *(const u32x2*)(hp + ((e >> 1) * 8 + (g4 & 1) * 4 + (g4 >> 1) * 2 + (e & 1)) * 2048);
            if (NT == 5) hv[i][4] = *(const u32x2*)(hp + (16 + g4) * 2048);
        }
    }
    SCHED();
    iter(IC<17>{}, tf[1], tf[0]);
    MFMA_DONE();
    STAMP(10);

    const rsrc_t rx = mk_rsrc(ws + WS_X, X_BYTES);
    const unsigned xt = (unsigned)((mt * 4 + nt) * 4 * 27648 + dh * 4096);
    auto inb = [&](int s) -> unsigned { return xt + (unsigned)(((ks + 1 + s) & 3) * 27648 + (2 - s) * 9216); };
    if (wr != ks) {
        const unsigned o = xt + (unsigned)(ks * 27648 + ((wr - ks - 1) & 3) * 9216);
#pragma unroll
        for (int i = 0; i < 2; ++i) {
            unsigned w[2 * NT];
#pragma unroll
            for (int j = 0; j < NT; ++j) { w[2 * j] = pk_bf16(acc[j][i][0], acc[j][i][1]); w[2 * j + 1] = pk_bf16(acc[j][i][2], acc[j][i][3]); }
#pragma unroll
            for (int e = 0; e < 2 * NT; ++e) w[e] = (w[e] == ~0u) ? 0x7fc07fc0u : w[e];
            if constexpr (NT == 5) st8_wt(rx, o + 8192 + i * 512 + lane * 8, (u32x2){w[8], w[9]});
            st16_wt(rx, o + i * 2048 + lane * 16, (u32x4){w[0], w[1], w[2], w[3]});
            st16_wt(rx, o + i * 2048 + 1024 + lane * 16, (u32x4){w[4], w[5], w[6], w[7]});
        }
    } else {
        u32x4 pa[3][4]; u32x2 pb[3][2];
        __builtin_amdgcn_s_sleep(16);
        for (int spin = 0; spin < (1 << 18); ++spin) {
            const u32x4 t = __builtin_amdgcn_raw_buffer_load_b128(rx, lane < 3 ? inb(lane) + 4080 : 0x80000000u, 0, 16);
            if (__all(t.w != ~0u)) {
                STAMP(11);
                unsigned m = 0u;
#pragma unroll
                for (int s = 0; s < 3; ++s) {
#pragma unroll
                    for (int p = 0; p < 4; ++p) pa[s][p] = __builtin_amdgcn_raw_buffer_load_b128(rx, inb(s) + p * 1024 + lane * 16, 0, 16);
                    if constexpr (NT == 5) {
#pragma unroll
                        for (int i = 0; i < 2; ++i) pb[s][i] = __builtin_amdgcn_raw_buffer_load_b64(rx, inb(s) + 8192 + i * 512 + lane * 8, 0, 16);
                    }
                }
#pragma unroll
                for (int s = 0; s < 3; ++s) {
#pragma unroll
                    for (int p = 0; p < 4; ++p) m = max(max(max(m, pa[s][p].x), max(pa[s][p].y, pa[s][p].z)), pa[s][p].w);
                    if constexpr (NT == 5) m = max(max(max(m, pb[s][0].x), max(pb[s][0].y, pb[s][1].x)), pb[s][1].y);
                }
                if (__all(m != ~0u)) break;
            }
            __builtin_amdgcn_s_sleep(1);
        }
        STAMP(12);
        const rsrc_t ro = mk_rsrc(out, S_ * D_ * 2);
#pragma unroll
        for (int i = 0; i < 2; ++i) {
            const unsigned ob = (unsigned)(((tok0 + i * 16) * D_ + nt * 144 + dh * 80) * 2);
            unsigned r[NT][2];
#pragma unroll
            for (int j = 0; j < NT; ++j) {
                f32x4 t = acc[j][i];
#pragma unroll
                for (int s = 0; s < 3; ++s) {
                    const unsigned w0 = j < 4 ? pa[s][i * 2 + (j >> 1)][(j & 1) * 2] : pb[s][i].x, w1 = j < 4 ? pa[s][i * 2 + (j >> 1)][(j & 1) * 2 + 1] : pb[s][i].y;
                    t += (f32x4){bf_lo(w0), bf_hi(w0), bf_lo(w1), bf_hi(w1)};
                }
                r[j][0] = pk_bf16(t[0], t[1]); r[j][1] = pk_bf16(t[2], t[3]);
            }
            auto addh = [&](unsigned a, unsigned h) -> unsigned { return pk_bf16(bf_lo(h) + bf_lo(a), bf_hi(h) + bf_hi(a)); };
#pragma unroll
            for (int p = 0; p < 2; ++p) {
                auto s0 = __builtin_amdgcn_permlane16_swap(r[2 * p][0], r[2 * p + 1][0], false, false);
                auto s1 = __builtin_amdgcn_permlane16_swap(r[2 * p][1], r[2 * p + 1][1], false, false);
                st16_wt(ro, ob + (unsigned)((p * 32 + (g4 & 1) * 16 + (g4 >> 1) * 8) * 2),
                        (u32x4){addh(s0[0], hv[i][2 * p].x), addh(s1[0], hv[i][2 * p].y), addh(s0[1], hv[i][2 * p + 1].x), addh(s1[1], hv[i][2 * p + 1].y)});
            }
            if constexpr (NT == 5) st8_wt(ro, ob + (unsigned)((64 + g4 * 4) * 2), (u32x2){addh(r[4][0], hv[i][4].x), addh(r[4][1], hv[i][4].y)});
        }
    }
    STAMP(13);
}

constexpr int K3_LDS = 163840;
constexpr int K3_SP = G_LDS;
constexpr int K3_CS = G2_W0 + 4 * 18432;
extern "C" __global__ __launch_bounds__(512, 2) void k3_gemm1(const u16* __restrict__ wg, const u16* __restrict__ wu,
                                                   char* __restrict__ ws, u16* __restrict__ out) {
    extern __shared__ __attribute__((aligned(16))) char smem[];
    LAS char* lds = (LAS char*)smem;
    const unsigned lds0 = (unsigned)(size_t)lds;
    const int tid = threadIdx.x, lane = tid & 63;
    const int wid = __builtin_amdgcn_readfirstlane(tid >> 6);
    const int wr = wid & 3, wc = wid >> 2;
    const int b = blockIdx.x, xcd = b & 7, idx = b >> 3;
    const int rt = (xcd >> 2) * 4 + (idx & 3), ct = (xcd & 3) * 8 + (idx >> 2);
    asm volatile("" :: "s"(wg), "s"(wu), "s"(ws), "s"(out));
    DIAG_DECL;

    const char* asrc = ws + WS_H + (size_t)rt * 144 * 2048 + wid * 2048;
    const unsigned aoff = (unsigned)(lane ^ ((wid & 2) << 2)) * 16;
    const unsigned ma0 = lds0 + wid * 2048, mw0 = lds0 + 16384 + wc * 9216 + wr * 2304;
    dma16<0>(aoff, asrc, ma0); dma16<1024>(aoff, asrc, ma0);
    const char* wsrc = (const char*)(wc ? wu : wg) + ct * 288 - 2048;
    auto wofs = [&](int i) -> unsigned {
        const int q = lane + 64 * i, lrow = 8 * wr + q / 18, pos = q % 18;
        const int krow = ((lrow >> 2) & 3) * 8 + (lrow >> 4) * 4 + (lrow & 3);
        const int col = pos * 8 + (pos < 8 ? 0 : (pos < 16 ? 8 : (((pos & 1) ^ wc) ? 136 : 64) - pos * 8));
        return (unsigned)(krow * (F_ * 2) + col * 2 + 2048 - 1024 * i);
    };
    const unsigned w0 = wofs(0), w1 = wofs(1), w2 = wofs(2);
    auto piece = [&](int P, int s) {
        const unsigned bo = (s & 3) * ST_BYTES;
        const char* wp = wsrc + (size_t)s * (32 * F_ * 2);
        if (P == 0) dma16<0>(aoff, asrc + (size_t)s * 16384, ma0 + bo);
        if (P == 1) dma16<1024>(aoff, asrc + (size_t)s * 16384, ma0 + bo);
        if (P == 2) dma16<0>(w0, wp, mw0 + bo);
        if (P == 3) dma16<1024>(w1, wp, mw0 + bo);
        if (P == 4) dma16_q<2048>(w2, wp, mw0 + bo);
    };
#define K3_PIECE(P, s) piece(P, s)
#define K3_STAGE(s) do { K3_PIECE(0, s); K3_PIECE(1, s); K3_PIECE(2, s); K3_PIECE(3, s); K3_PIECE(4, s); } while (0)
    K3_PIECE(2, 0); K3_PIECE(3, 0); K3_PIECE(4, 0);
    BAR();
    K3_STAGE(1);
    u32x2 hs[4];
    {
        const char* hsrc = ws + WS_H + (size_t)rt * 144 * 2048 + ct * 64;
        const unsigned o = (unsigned)((wid * 18 + (lane >> 3)) * 2048 + (lane & 7) * 8);
        asm volatile("global_load_dwordx2 %0, %1, %2" : "=v"(hs[0]) : "v"(o), "s"(hsrc) : "memory");
        asm volatile("global_load_dwordx2 %0, %1, %2" : "=v"(hs[1]) : "v"(o + 16384), "s"(hsrc) : "memory");
        asm volatile("global_load_dwordx2 %0, %1, %2" : "=v"(hs[2]) : "v"((unsigned)((wid * 18 + 16 + ((lane >> 3) & 1)) * 2048 + (lane & 7) * 8)), "s"(hsrc) : "memory");
        asm volatile("global_load_dwordx2 %0, %1, %2" : "=v"(hs[3]) : "v"((unsigned)(lane & 7) * 8), "s"(hsrc) : "memory");
    }

    const int g4 = lane >> 4, q4 = (lane & 15) >> 2, p4 = lane & 3;
    const int rowoff = (4 * g4 + q4) * 288;
    const int tbase = g4 * 4096 + wr * 512 + (lane & 15) * 8;
    const int toff0 = tbase + ((2 * wc) ^ (g4 & 1)) * 128, toff1 = tbase + ((2 * wc + 1) ^ (g4 & 1)) * 128;
    const int tstep = wc ? -256 : 256;
    const int foff = 16384 + rowoff + wc * 128 + p4 * 8;
    const int moff = 16384 + rowoff + 256 + (p4 < 2 ? 16 * wc + 8 * p4 : 9216 + 16 * (1 - wc) + 8 * (p4 - 2));

    f32x4 acc[9][4];
    s16x8 wf[9], tf[2][4];
    auto rd_w = [&](const LAS char* sb, int j) -> s16x8 {
        return j < 8 ? tr_frag(sb + foff + (j >> 2) * 9216 + (j & 3) * 32) : tr_frag(sb + moff);
    };
    auto rd_t = [&](const LAS char* sb, int i) -> s16x8 {
        const LAS char* p = sb + ((i & 1) ? toff1 : toff0) + (i >> 1) * tstep;
        const s16x4 lo = *(const volatile LAS s16x4*)p, hi = *(const volatile LAS s16x4*)(p + 2048);
        return __builtin_shufflevector(lo, hi, 0, 1, 2, 3, 4, 5, 6, 7);
    };

    STAMP(1);
    WAIT_V(9); BAR();
    STAMP(2);
    wf[0] = rd_w(lds, 0);
#pragma unroll
    for (int i = 0; i < 4; ++i) tf[0][i] = rd_t(lds, i);
#pragma unroll
    for (int j = 1; j < 9; ++j) wf[j] = rd_w(lds, j);
    SCHED();
    const int mt2 = (xcd >> 2) * 8 + (idx & 7), ks2 = xcd & 3, nt2 = idx >> 3;
    const rsrc_t rfill = mk_rsrc(ws + WS_X + (size_t)((mt2 * 4 + nt2) * 4 + ks2) * 27648, 27648);
    auto fill = [&](int k) { st16_wt(rfill, (unsigned)(k * 8192 + tid * 16), (u32x4){~0u, ~0u, ~0u, ~0u}); };
    unsigned sgr;
    {
        const LAS char* sb = lds + ST_BYTES;
        float hc = 0.f;
#pragma unroll
        for (int j = 0; j < 9; ++j) {
#pragma unroll
            for (int i = 0; i < 4; ++i) mfma_first(acc[j][i], wf[j], tf[0][i]);
            SCHED();
            if (j == 0) { K3_PIECE(0, 2); K3_PIECE(1, 2); }
            if (j == 1) { K3_PIECE(2, 2); K3_PIECE(3, 2); }
            if (j == 2) K3_PIECE(4, 2);
            if (j == 3) {
                asm volatile("s_waitcnt vmcnt(5)" : "+v"(hs[0]), "+v"(hs[1]), "+v"(hs[2]), "+v"(hs[3]) :: "memory");
                hc = bf_lo(hs[3].x);
                float s1 = 0.f, s2 = 0.f;
#pragma unroll
                for (int i = 0; i < 3; ++i) {
                    const float d[4] = {bf_lo(hs[i].x) - hc, bf_hi(hs[i].x) - hc, bf_lo(hs[i].y) - hc, bf_hi(hs[i].y) - hc};
                    float a = (d[0] + d[1]) + (d[2] + d[3]), q = __builtin_fmaf(d[0], d[0], d[1] * d[1]) + __builtin_fmaf(d[2], d[2], d[3] * d[3]);
                    if (i == 2 && lane >= 16) { a = 0.f; q = 0.f; }
                    s1 += a; s2 += q;
                }
                s1 = x32_add(x16_add(dpp_add<0x128>(s1))); s2 = x32_add(x16_add(dpp_add<0x128>(s2)));
                if (lane < 8) *(LAS f32x2*)(lds + K3_SP + (lane * 8 + wid) * 8) = (f32x2){s1, s2};
                asm volatile("s_waitcnt lgkmcnt(0)" ::: "memory");
                BAR();
                wf[0] = rd_w(sb, 0); wf[1] = rd_w(sb, 1);
                tf[1][0] = rd_t(sb, 0); tf[1][1] = rd_t(sb, 1);
            }
            if (j == 4) { tf[1][2] = rd_t(sb, 2); tf[1][3] = rd_t(sb, 3); wf[2] = rd_w(sb, 2); wf[3] = rd_w(sb, 3); }
            if (j >= 4) { wf[j] = rd_w(sb, j); K3_PIECE(j - 4, 3); }
            if (j == 8) {
                const f32x2 pp = *(const LAS f32x2*)(lds + K3_SP + (wid * 8 + (lane & 7)) * 8);
                const float s1 = dpp_add<0x141>(dpp_add<0x4E>(dpp_add<0xB1>(pp.x))) * (1.f / D_);
                const float s2 = dpp_add<0x141>(dpp_add<0x4E>(dpp_add<0xB1>(pp.y))) * (1.f / D_);
                const float c0 = __uint_as_float(__builtin_amdgcn_readlane(__float_as_uint(hc), wid));
                const float sd = ln_sd(__builtin_fmaxf(__builtin_fmaf(-s1, s1, s2), 0.f));
                sgr = pk_bf16(c0 + s1, sd);
                fill(0); fill(1);
            }
            SCHED();
        }
    }

    const rsrc_t rg = mk_rsrc(ws + WS_G2 + (size_t)(rt * 256 + wr * 64) * 8, 512);
    const char* w2src = ws + WS_WT + (size_t)(ks2 * 4 + nt2) * 18 * 18432 + wid * 2304;
    u32x2 gr[4];
    s16x4 tl;
    constexpr int TP = 1, DPJ0 = 0, DPJ1 = 5;
    auto iter = [&](auto mode, auto vm, int s, s16x8 (&tc)[4], s16x8 (&tn)[4]) {
        constexpr int MODE = decltype(mode)::v;
        if constexpr (MODE != 4) {
            if constexpr (MODE == 3) WAIT_V(0); else WAIT_VI(5 + decltype(vm)::v);
            BAR();
            SCHED();
        }
        const LAS char* sb = lds + ((s + 1) & 3) * ST_BYTES;
#pragma unroll
        for (int j = 0; j < 9; ++j) {
            s16x4 wl, wh;
#pragma unroll
            for (int i = 0; i < 4; ++i) {
                mfma_acc(acc[j][i], wf[j], tc[i]);
                SCHED();
                if constexpr (MODE != 4) {
                    if (j >= 1) {
                        const LAS char* p = (j - 1 < 8) ? sb + foff + ((j - 1) >> 2) * 9216 + ((j - 1) & 3) * 32 : sb + moff;
                        if (i == 0) wl = __builtin_amdgcn_ds_read_tr16_b64_v4i16((LAS s16x4*)p);
                        if (i == 1) { wh = __builtin_amdgcn_ds_read_tr16_b64_v4i16((LAS s16x4*)(p + 16 * 288)); wf[j - 1] = __builtin_shufflevector(wl, wh, 0, 1, 2, 3, 4, 5, 6, 7); }
                    }
                    if (j >= TP && j < TP + 4) {
                        const int ti = j - TP;
                        const LAS char* p = sb + ((ti & 1) ? toff1 : toff0) + (ti >> 1) * tstep;
                        if (i == 2) tl = *(const volatile LAS s16x4*)p;
                        if (i == 3) tn[ti] = __builtin_shufflevector(tl, *(const volatile LAS s16x4*)(p + 2048), 0, 1, 2, 3, 4, 5, 6, 7);
                    }
                    if (j == 8 && i == 3) wf[8] = rd_w(sb, 8);
                }
                if constexpr (MODE < 2 || MODE == 5) {
                    if (j == DPJ0 && i == 1) K3_PIECE(0, s + 3);
                    if (j == DPJ0 && i == 3) K3_PIECE(1, s + 3);
                    if (j == DPJ1 && i == 3) K3_PIECE(2, s + 3);
                    if (j == DPJ1 + 1 && i == 3) K3_PIECE(3, s + 3);
                    if (j == DPJ1 + 2 && i == 3) K3_PIECE(4, s + 3);
                }
                SCHED();
            }
            if constexpr (MODE == 0) { if (j == 8) { fill(2); fill(3); } }
            if constexpr (MODE == 5) {
                if (j == 0 && s == 4) st8_wt(mk_rsrc(ws + WS_G2, S_ * 8), lane == 0 ? (unsigned)((rt * 256 + ct * 8 + wid) * 8) : 0x80000000u, (u32x2){sgr, 1u});
            }
            if constexpr (MODE == 2) {
                if (j == 0) {
#pragma unroll
                    for (int i = 0; i < 4; ++i) gr[i] = __builtin_amdgcn_raw_buffer_load_b64(rg, (unsigned)(((i ^ (2 * wc)) * 16 + (lane & 15)) * 8), 0, 16);
                }
            }
            if constexpr (MODE == 3) {
                if (j == 0) {
                    asm volatile("" : "+v"(gr[0]), "+v"(gr[1]), "+v"(gr[2]), "+v"(gr[3]));
                    const unsigned co = (unsigned)((lane >= 18 ? F_ * 4 + (lane - 18) * 16 : lane * 16));
                    dma_m36(co, ws + WS_CS + (size_t)(ct * 144 + wc * 72) * 4, lds0 + K3_CS + wid * 1152);
                    dma_m36(co, ws + WS_CS + (size_t)(2 * F_ + ct * 144 + wc * 72) * 4, lds0 + K3_CS + wid * 1152 + 576);
                }
            }
            if constexpr (MODE == 3 || MODE == 4) {
                if (j >= 2 && j < 8) g2_wpiece((j - 2) % 3, (MODE - 3) * 2 + (j - 2) / 3, w2src, lane * 16, lds0 + G2_W0 + wid * 2304);
            }
            SCHED();
        }
    };
    iter(IC<0>{}, IC<2>{}, 1, tf[1], tf[0]);
    iter(IC<1>{}, IC<4>{}, 2, tf[0], tf[1]);
    iter(IC<1>{}, IC<2>{}, 3, tf[1], tf[0]);
#pragma unroll 1
    for (int s = 4; s < 14; s += 2) {
        iter(IC<5>{}, IC<0>{}, s, tf[0], tf[1]);
        iter(IC<1>{}, IC<0>{}, s + 1, tf[1], tf[0]);
    }
    STAMP(3);
    iter(IC<1>{}, IC<0>{}, 14, tf[0], tf[1]);
    iter(IC<2>{}, IC<0>{}, 15, tf[1], tf[0]);
    iter(IC<3>{}, IC<0>{}, 16, tf[0], tf[1]);
    iter(IC<4>{}, IC<0>{}, 17, tf[1], tf[0]);
    MFMA_DONE();
    STAMP(4);
    WAIT_V(12);
    __builtin_amdgcn_s_setprio(1);
    if (__builtin_expect(!__all((gr[0].y & gr[1].y & gr[2].y & gr[3].y) == 1u), 0)) {
        for (int spin = 0; spin < (1 << 20); ++spin) {
            __builtin_amdgcn_s_sleep(2);
#pragma unroll
            for (int i = 0; i < 4; ++i) gr[i] = __builtin_amdgcn_raw_buffer_load_b64(rg, (unsigned)(((i ^ (2 * wc)) * 16 + (lane & 15)) * 8), 0, 16);
            if (__all((gr[0].y & gr[1].y & gr[2].y & gr[3].y) == 1u)) break;
        }
    }
    const int mt = rt * 2 + (wr >> 1), ks = ct >> 3, lct = ct & 7;
    const rsrc_t rhh = mk_rsrc(ws + WS_HH, S_ * F_ * 2);
    const unsigned hb = (unsigned)(((mt * 4 + ks) * 36) * 8192 + ((wr & 1) * 4) * 1024);
    const LAS char* csb = lds + K3_CS + wid * 1152 + 16 * g4;
    TokC tcs[4];
#pragma unroll
    for (int i = 0; i < 4; ++i) {
        const float sd = bf_hi(gr[i].x), r = __builtin_amdgcn_rcpf(sd);
        tcs[i] = (TokC){-bf_lo(gr[i].x), -2.30220819f * r, -0.10294324f * (r * r * r), sd * sd};
    }
    {
        const LAS char* cm = lds + K3_CS + wid * 1152 + 256 + 16 * (g4 & 1);
        const f32x4 cgm = *(const LAS f32x4*)cm + *(const LAS f32x4*)(cm + 576), cum = *(const LAS f32x4*)(cm + 288) + *(const LAS f32x4*)(cm + 864);
#pragma unroll
        for (int ip = 0; ip < 2; ++ip) {
            const int ia = ((2 * ip) ^ (2 * wc)) + (lane >> 5);
            const bool up = lane >= 32;
            const TokC tc = {up ? tcs[2 * ip + 1].nm : tcs[2 * ip].nm, up ? tcs[2 * ip + 1].c0 : tcs[2 * ip].c0,
                             up ? tcs[2 * ip + 1].c1 : tcs[2 * ip].c1, up ? tcs[2 * ip + 1].q : tcs[2 * ip].q};
            f32x4 gm, um;
#pragma unroll
            for (int r = 0; r < 4; ++r) {
                auto sw = __builtin_amdgcn_permlane32_swap(__float_as_uint(acc[8][2 * ip][r]), __float_as_uint(acc[8][2 * ip + 1][r]), false, false);
                gm[r] = __uint_as_float(sw[0]); um[r] = __uint_as_float(sw[1]);
            }
            const int kgs = (lct & 1) * 2 + wc;
            st8_wt(rhh, hb + (unsigned)((lct >> 1) * 8192 + ia * 1024 + (kgs * 16 + (lane & 15)) * 16 + (g4 & 1) * 8),
                   (u32x2){geglu2(LO2(gm), LO2(um), LO2(cgm), LO2(cum), tc), geglu2(HI2(gm), HI2(um), HI2(cgm), HI2(cum), tc)});
        }
    }
    SCHED();
    constexpr int FLAGPOS = 1;
    const rsrc_t rfb = mk_rsrc(ws + WS_FB, 16384);
    const unsigned fbo = (unsigned)((mt * 4 + ks) * 256 + (lct * 4 + (wr & 1) * 2 + wc) * 4);
    u32x4 ob[4];
#pragma unroll
    for (int jp = 0; jp < 2; ++jp) {
        f32x4 cg[2], cu[2];
#pragma unroll
        for (int e = 0; e < 2; ++e) {
            cg[e] = *(const LAS f32x4*)(csb + (2 * jp + e) * 64) + *(const LAS f32x4*)(csb + 576 + (2 * jp + e) * 64);
            cu[e] = *(const LAS f32x4*)(csb + 288 + (2 * jp + e) * 64) + *(const LAS f32x4*)(csb + 864 + (2 * jp + e) * 64);
        }
#pragma unroll
        for (int i = 0; i < 4; ++i) {
            unsigned o[4];
#pragma unroll
            for (int e = 0; e < 2; ++e) {
                const f32x4 g = acc[2 * jp + e][i], u = acc[4 + 2 * jp + e][i];
                o[2 * e] = geglu2(LO2(g), LO2(u), LO2(cg[e]), LO2(cu[e]), tcs[i]);
                o[2 * e + 1] = geglu2(HI2(g), HI2(u), HI2(cg[e]), HI2(cu[e]), tcs[i]);
            }
            ob[i] = (u32x4){o[0], o[1], o[2], o[3]};
            st16_wt(rhh, (jp == 1 && wid == 7) ? 0x80000000u : hb + (unsigned)(((2 + jp * 8 + lct) * 2 + wc) * 8192 + (i ^ (2 * wc)) * 1024 + lane * 16), ob[i]);
            if (jp == 1 && i == FLAGPOS) {
                SCHED();
                WAIT_VI(FLAGPOS + 1);
                __builtin_amdgcn_raw_buffer_store_b32(1u, rfb, lane == 0 ? fbo : 0x80000000u, 0, 16);
                SCHED();
            }
        }
        SCHED();
        if (jp == 0) __builtin_amdgcn_s_setprio(0);
    }
    STAMP(5);
    if (wid == 7) {
        for (int spin = 0; spin < (1 << 20); ++spin) {
            const unsigned f = __builtin_amdgcn_raw_buffer_load_b32(rfb, (unsigned)((mt2 * 4 + ks2) * 256 + (lane & 31) * 4), 0, 16);
            if (__all(f == 1u)) break;
            __builtin_amdgcn_s_sleep(4);
        }
#pragma unroll
        for (int i = 0; i < 4; ++i) st16_wt(rhh, hb + (unsigned)(((10 + lct) * 2 + wc) * 8192 + (i ^ (2 * wc)) * 1024 + lane * 16), ob[i]);
    }
    BAR();
    STAMP(7);
    if (wid < 4) g2_body<5>(ws, out, lds, lane, wid, mt2, ks2, nt2, w2src, fbo + 128 DIAG_PASS);
    else g2_body<4>(ws, out, lds, lane, wid, mt2, ks2, nt2, w2src, fbo + 128 DIAG_PASS);
    DIAG_END(ws, 1);
}

extern "C" __global__ void k9_end() {}

extern "C" void kernel_launch(void* const* d_in, const int* in_sizes, int n_in, void* d_out, int out_size, void* d_ws,
                              size_t ws_size, hipStream_t stream) {
    const u16* q = (const u16*)d_in[0];
    const u16* k = (const u16*)d_in[1];
    const u16* v = (const u16*)d_in[2];
    const u16* shift_k = (const u16*)d_in[3];
    const u16* ssm_k = (const u16*)d_in[4];
    const u16* ssm_D = (const u16*)d_in[5];
    const u16* wg = (const u16*)d_in[7];
    const u16* wu = (const u16*)d_in[8];
    const u16* wd = (const u16*)d_in[9];
    char* ws = (char*)d_ws;
    (void)hipFuncSetAttribute((const void*)k1_conv, hipFuncAttributeMaxDynamicSharedMemorySize, C1_LDS);
    (void)hipFuncSetAttribute((const void*)k3_gemm1, hipFuncAttributeMaxDynamicSharedMemorySize, K3_LDS);
    k1_conv<<<256, 512, C1_LDS, stream>>>(q, k, v, shift_k, ssm_k, ssm_D, wg, wu, wd, ws);
    k3_gemm1<<<256, 512, K3_LDS, stream>>>(wg, wu, ws, (u16*)d_out);
}
```

```cpp
#include <hip/hip_runtime.h>
#include <hip/hip_bf16.h>

#define LAS __attribute__((address_space(3)))
typedef unsigned short u16;
typedef short s16x4 __attribute__((ext_vector_type(4)));
typedef short s16x8 __attribute__((ext_vector_type(8)));
typedef float f32x2 __attribute__((ext_vector_type(2)));
typedef float f32x4 __attribute__((ext_vector_type(4)));
typedef unsigned u32x2 __attribute__((ext_vector_type(2)));
typedef unsigned u32x4 __attribute__((ext_vector_type(4)));
typedef __bf16 bf16x2_t __attribute__((ext_vector_type(2)));

constexpr int S_ = 2048, D_ = 576, F_ = 4608;

__device__ __forceinline__ unsigned pk_bf16(float lo, float hi) {
    f32x2 v = {lo, hi};
    bf16x2_t b = __builtin_convertvector(v, bf16x2_t);
    return __builtin_bit_cast(unsigned, b);
}
__device__ __forceinline__ float bf_lo(unsigned w) { return __uint_as_float(w << 16); }
__device__ __forceinline__ float bf_hi(unsigned w) { return __uint_as_float(w & 0xffff0000u); }
__device__ __forceinline__ float rbf(float x) { return bf_lo(pk_bf16(x, 0.f)); }
#define WAIT_V(n) asm volatile("s_waitcnt vmcnt(" #n ")" ::: "memory")
#define BAR() do { __builtin_amdgcn_s_barrier(); asm volatile("" ::: "memory"); } while (0)
#define SCHED() __builtin_amdgcn_sched_barrier(0)
#define DIAG_DECL
#define MARK()
#define STAMP(i)
#define DIAG_END(wsbase, kid)
__device__ __forceinline__ void mfma_acc(f32x4& c, s16x8 a, s16x8 b) {
    asm volatile("v_mfma_f32_16x16x32_bf16 %0, %1, %2, %0" : "+v"(c) : "v"(a), "v"(b));
}
__device__ __forceinline__ void mfma_first(f32x4& c, s16x8 a, s16x8 b) {
    asm volatile("v_mfma_f32_16x16x32_bf16 %0, %1, %2, 0" : "=&v"(c) : "v"(a), "v"(b));
}
#define MFMA_DONE() do { asm volatile("s_nop 7\n\ts_nop 7" ::: "memory"); SCHED(); } while (0)
constexpr size_t WS_KV = 0;
constexpr size_t WS_H = WS_KV + (size_t)S_ * D_ * 2;
constexpr size_t WS_ST = WS_H + (size_t)S_ * D_ * 2;
constexpr size_t WS_CS = WS_ST + (size_t)S_ * 8;
constexpr size_t WS_G2 = WS_CS + (size_t)4 * F_ * 4;
constexpr size_t WS_F0 = WS_G2 + (size_t)S_ * 8;
constexpr int FSP = 128;
constexpr size_t WS_EP = WS_F0 + 1280 * FSP;
constexpr size_t WS_HH = WS_EP + 1024;
constexpr size_t WS_X = WS_HH + (size_t)S_ * F_ * 2;
constexpr unsigned X_BYTES = 64 * 4 * 3 * 9216;
constexpr size_t WS_FB = WS_X + X_BYTES;
constexpr size_t WS_WT = WS_FB + 16384;

constexpr unsigned WS_END = (unsigned)(WS_WT + (size_t)F_ * D_ * 2);
template <int N> struct IC { static constexpr int v = N; };
typedef __amdgpu_buffer_rsrc_t rsrc_t;
__device__ __forceinline__ rsrc_t mk_rsrc(const void* p, unsigned bytes) { return __builtin_amdgcn_make_buffer_rsrc((void*)p, 0, bytes, 0x00020000); }
__device__ __forceinline__ void st16_wt(rsrc_t r, unsigned off, u32x4 v) { __builtin_amdgcn_raw_buffer_store_b128(v, r, off, 0, 16); }
__device__ __forceinline__ void st8_wt(rsrc_t r, unsigned off, u32x2 v) { __builtin_amdgcn_raw_buffer_store_b64(v, r, off, 0, 16); }
template <int CTRL> __device__ __forceinline__ float dpp_add(float x) {
    return x + __uint_as_float(__builtin_amdgcn_update_dpp(0, (int)__float_as_uint(x), CTRL, 0xf, 0xf, true));
}
__device__ __forceinline__ float x16_add(float x) {
    auto r = __builtin_amdgcn_permlane16_swap(__float_as_uint(x), __float_as_uint(x), false, false);
    return __uint_as_float(r[0]) + __uint_as_float(r[1]);
}
__device__ __forceinline__ float x32_add(float x) {
    auto r = __builtin_amdgcn_permlane32_swap(__float_as_uint(x), __float_as_uint(x), false, false);
    return __uint_as_float(r[0]) + __uint_as_float(r[1]);
}
__device__ __forceinline__ float sum16(float x) {
    x = dpp_add<0xB1>(x); x = dpp_add<0x4E>(x); x = dpp_add<0x141>(x);
    return dpp_add<0x140>(x);
}
__device__ __forceinline__ float ln_sd(float var) { return rbf(__builtin_amdgcn_sqrtf(rbf(rbf(var) + 1e-5f))); }

template <int OFF>
__device__ __forceinline__ void dma16(unsigned voff, const void* sbase, unsigned m0v) {
    asm volatile("s_mov_b32 m0, %2\n\ts_nop 0\n\tglobal_load_lds_dwordx4 %0, %1 offset:%3"
                 :: "v"(voff), "s"(sbase), "s"(m0v), "i"(OFF) : "memory");
}
template <int OFF>
__device__ __forceinline__ void dma16_sc1(unsigned voff, const void* sbase, unsigned m0v) {
    asm volatile("s_mov_b32 m0, %2\n\ts_nop 0\n\tglobal_load_lds_dwordx4 %0, %1 offset:%3 sc1"
                 :: "v"(voff), "s"(sbase), "s"(m0v), "i"(OFF) : "memory");
}
template <int OFF>
__device__ __forceinline__ void dma16_q(unsigned voff, const void* sbase, unsigned m0v) {
    asm volatile("s_mov_b32 m0, %2\n\ts_mov_b64 exec, 0xffff\n\tglobal_load_lds_dwordx4 %0, %1 offset:%3\n\ts_mov_b64 exec, -1"
                 :: "v"(voff), "s"(sbase), "s"(m0v), "i"(OFF) : "memory");
}
__device__ __forceinline__ void dma_m36(unsigned voff, const void* sbase, unsigned m0v) {
    asm volatile("s_mov_b32 m0, %2\n\ts_mov_b32 exec_hi, 15\n\tglobal_load_lds_dwordx4 %0, %1\n\ts_mov_b64 exec, -1"
                 :: "v"(voff), "s"(sbase), "s"(m0v) : "memory");
}
__device__ __forceinline__ void dma4(unsigned voff, const void* sbase, unsigned m0v) {
    asm volatile("s_mov_b32 m0, %2\n\ts_nop 0\n\tglobal_load_lds_dword %0, %1" :: "v"(voff), "s"(sbase), "s"(m0v) : "memory");
}
__device__ __forceinline__ void dma4_sc1(unsigned voff, const void* sbase, unsigned m0v) {
    asm volatile("s_mov_b32 m0, %2\n\ts_nop 0\n\tglobal_load_lds_dword %0, %1 sc1" :: "v"(voff), "s"(sbase), "s"(m0v) : "memory");
}
#define SYM_ADDR(out, sym) asm volatile("s_getpc_b64 s[40:41]\n\ts_add_u32 s40, s40, " #sym "@rel32@lo+4\n\ts_addc_u32 s41, s41, " #sym "@rel32@hi+12\n\ts_mov_b64 %0, s[40:41]" \
                                        : "=s"(out) :: "s40", "s41", "scc")
template <int NI>
__device__ __forceinline__ void warm_code(const char* a, const char* e, int l0, int lane, unsigned m0v) {
    const unsigned last = e > a ? (unsigned)(e - a) - 4u : 0u;
#pragma unroll
    for (int i = 0; i < NI; ++i) dma4(min((unsigned)(l0 + i * 64 + lane) * 128u, last), a, m0v);
}
__device__ __forceinline__ s16x8 tr_frag(const LAS char* p) {
    const s16x4 lo = __builtin_amdgcn_ds_read_tr16_b64_v4i16((LAS s16x4*)p);
    const s16x4 hi = __builtin_amdgcn_ds_read_tr16_b64_v4i16((LAS s16x4*)(p + 16 * 288));
    return __builtin_shufflevector(lo, hi, 0, 1, 2, 3, 4, 5, 6, 7);
}
__device__ __forceinline__ void wd_unit_load(const u16* wd, int un, int lane, unsigned m0v) {
    const int uB = un / 3, ut = un % 3, kh = uB & 1, st = (uB >> 1) % 18, kn = uB / 36;
    const bool full = st >= 2;
    const int f0 = (kn >> 2) * 1152 + (full ? ((st - 2) & 7) * 144 + kh * 72 + ((st - 2) >> 3) * 32 : (st * 2 + kh) * 288);
    const char* src = (const char*)wd + (size_t)f0 * (D_ * 2) + (kn & 3) * 288 + ut * 96;
#pragma unroll
    for (int i = 0; i < 3; ++i) {
        const int q = lane + 64 * i, r = q / 6, pos = q % 6, kg = (r & 15) >> 2;
        const int f = full ? r : (kg >> 1) * 144 + (kg & 1) * 72 + 64 + (r & 3) + 4 * (r >> 4);
        dma16<0>((unsigned)(f * (D_ * 2) + pos * 16), src, m0v + i * 1024);
    }
}
__device__ __forceinline__ void wd_unit_store(char* ws, int un, int lane, const LAS char* l) {
    const rsrc_t r = mk_rsrc(ws + WS_WT, F_ * D_ * 2);
#pragma unroll
    for (int t = 0; t < 3; ++t) {
        const LAS char* tp = l + (lane >> 2) * 96 + (lane & 3) * 8 + t * 32;
        const s16x4 lo = __builtin_amdgcn_ds_read_tr16_b64_v4i16((LAS s16x4*)tp), hi = __builtin_amdgcn_ds_read_tr16_b64_v4i16((LAS s16x4*)(tp + 16 * 96));
        st16_wt(r, (unsigned)(un * 3072 + t * 1024 + lane * 16), __builtin_bit_cast(u32x4, __builtin_shufflevector(lo, hi, 0, 1, 2, 3, 4, 5, 6, 7)));
    }
}
#define T_WU4 3
#define T_HD 48
#define T_HD2 24
#define T_P0 16
#define T_P1 1
constexpr int WU4 = T_WU4;
constexpr int HELPER_DELAY = T_HD, POLL0 = T_P0, POLL1 = T_P1;
constexpr int C1_COPY = 4288;
constexpr int C1_FILT = 4 * C1_COPY;
constexpr int C1_KV = 2304 * 2;
constexpr int C1_KVOFF = 4 * C1_FILT + 64;
constexpr int C1_YOFF = C1_KVOFF + 4 * C1_KV;
constexpr int C1_XOFF = C1_YOFF + 16384;
constexpr int C1_SOFF = C1_XOFF + 16384;
constexpr int C1_TOFF = C1_SOFF + 16384;
constexpr int C1_WOFF = C1_TOFF + 2048;
constexpr int C1_LDS = C1_WOFF + 24576;

extern "C" __global__ __launch_bounds__(512, 2) void k1_conv(const u16* __restrict__ x, const u16* __restrict__ kin, const u16* __restrict__ vin,
                                                  const u16* __restrict__ shift_k, const u16* __restrict__ ssm_k, const u16* __restrict__ ssm_D,
                                                  const u16* __restrict__ wg, const u16* __restrict__ wu, const u16* __restrict__ wd,
                                                  char* __restrict__ ws, const u16* __restrict__ out) {
    extern __shared__ __attribute__((aligned(16))) char smem[];
    const int tid = threadIdx.x, lane = tid & 63;
    const int wid = __builtin_amdgcn_readfirstlane(tid >> 6);
    const int b = blockIdx.x, xcd = b & 7, idx = b >> 3;
    {
        const char *c1, *c3;
        SYM_ADDR(c1, k1_conv); SYM_ADDR(c3, k3_gemm1);
        if (wid >= 4) warm_code<1>(c1, c3, (wid & 3) * 64, lane, (unsigned)(size_t)(LAS char*)smem + C1_TOFF + 256 + (wid & 3) * 256);
    }
    DIAG_DECL;
    asm volatile("" :: "s"(x), "s"(kin), "s"(vin), "s"(shift_k), "s"(ssm_k), "s"(ssm_D), "s"(wg), "s"(wu), "s"(wd), "s"(ws), "s"(out));
    const bool conv = idx < 18;
    const int cg = xcd * 18 + idx, ch0 = cg * 4;
    const unsigned lds0 = (unsigned)(size_t)(LAS char*)smem;
    LAS unsigned* tagp = (LAS unsigned*)(smem + C1_TOFF);
    auto get_tag = [&]() -> unsigned { BAR(); return __builtin_amdgcn_readfirstlane(*(volatile LAS unsigned*)tagp); };

    const int h = (idx - 18) * 8 + xcd, hc_ = h < 108 ? h : 0;
    const int m3 = hc_ % 3, lcA = (hc_ / 3) * 4 + m3, rb0 = m3 * 3;
    constexpr int HU = (1728 - 144 * WU4 + 111) / 112;
    const int hbase = 144 * WU4 + h * HU, hun = hbase + wid * 2, hend = min(hbase + HU, 1728);
    auto cs_load = [&](u32x4 (&v)[12]) {
        const int rs = tid >> 3, c8 = tid & 7;
#pragma unroll
        for (int p = 0; p < 12; ++p) {
            const int rb = rb0 + p, lc = lcA + (rb >= 9), r = (rb >= 9 ? rb - 9 : rb) * 64 + rs;
            v[p] = *(const u32x4*)((lc >= 72 ? wu : wg) + (size_t)r * F_ + (lc % 72) * 64 + c8 * 8);
        }
        SCHED();
#pragma unroll
        for (int u = 0; u < 2; ++u) if (hun + u < hend) wd_unit_load(wd, hun + u, lane, lds0 + 16384 + (wid * 2 + u) * 3072);
        st16_wt(mk_rsrc(ws + WS_FB, 16384), (unsigned)(h * 8192 + tid * 16), (u32x4){0u, 0u, 0u, 0u});
        SCHED();
    };
    auto cs_finish = [&](u32x4 (&v)[12]) {
        float g[4][8];
#pragma unroll
        for (int q = 0; q < 4; ++q) {
#pragma unroll
            for (int e = 0; e < 4; ++e) {
                g[q][2 * e] = (bf_lo(v[3 * q][e]) + bf_lo(v[3 * q + 1][e])) + bf_lo(v[3 * q + 2][e]);
                g[q][2 * e + 1] = (bf_hi(v[3 * q][e]) + bf_hi(v[3 * q + 1][e])) + bf_hi(v[3 * q + 2][e]);
            }
        }
        LAS float* red = (LAS float*)smem;
#pragma unroll
        for (int l = 0; l < 2; ++l) {
            float a[8];
#pragma unroll
            for (int e = 0; e < 8; ++e) {
                a[e] = l == 0 ? g[0][e] + (m3 <= 1 ? g[1][e] : 0.f) + (m3 == 0 ? g[2][e] : 0.f) : g[3][e] + (m3 >= 1 ? g[2][e] : 0.f) + (m3 == 2 ? g[1][e] : 0.f);
                a[e] = x32_add(x16_add(dpp_add<0x128>(a[e])));
            }
            if (lane < 8) {
                *(LAS f32x4*)(red + (wid * 2 + l) * 64 + lane * 8) = (f32x4){a[0], a[1], a[2], a[3]};
                *(LAS f32x4*)(red + (wid * 2 + l) * 64 + lane * 8 + 4) = (f32x4){a[4], a[5], a[6], a[7]};
            }
        }
        WAIT_V(1);
#pragma unroll
        for (int u = 0; u < 2; ++u) if (hun + u < hend) wd_unit_store(ws, hun + u, lane, (const LAS char*)smem + 16384 + (wid * 2 + u) * 3072);
        asm volatile("s_waitcnt lgkmcnt(0)" ::: "memory"); BAR();
        if (tid < 192 && h < 108) {
            float s = 0.f;
            if (tid < 128) {
#pragma unroll
                for (int w = 0; w < 8; ++w) s += red[w * 128 + tid];
            }
            const int part = tid >> 6;
            const int lc = part == 0 ? lcA : (part == 1 ? lcA + 1 : (m3 == 0 ? lcA : lcA + 1));
            const int slot = part == 0 ? (m3 != 0) : (part == 1 ? 0 : 1);
            if (part < 2 || m3 != 1)
                __builtin_amdgcn_raw_buffer_store_b32(__float_as_uint(s), mk_rsrc(ws + WS_CS, 4 * F_ * 4), (unsigned)((slot * 2 * F_ + lc * 64 + (tid & 63)) * 4), 0, 16);
        }
    };
    auto wait_kv = [&](unsigned tag) {
        const rsrc_t rf = mk_rsrc(ws + WS_F0 + ((idx >> 2) * 8 + xcd) * 32 * FSP, 32 * FSP);
        __builtin_amdgcn_s_sleep(POLL0);
        for (int spin = 0; spin < (1 << 20); ++spin) {
            const unsigned f = __builtin_amdgcn_raw_buffer_load_b32(rf, (lane & 31) * FSP, 0, 16);
            if (__all(f == tag)) break;
            __builtin_amdgcn_s_sleep(POLL1);
        }
    };

    auto x_rows = [&]() {
        u32x2 xr[8];
#pragma unroll
        for (int i = 0; i < 8; ++i) xr[i] = *(const u32x2*)(x + (size_t)((tid - 256) + (wid - 4) * 448 + i * 64) * D_ + ch0);
#pragma unroll
        for (int i = 0; i < 8; ++i) *(LAS u32x2*)(smem + C1_XOFF + ((tid - 256) + (wid - 4) * 448 + i * 64) * 8) = xr[i];
        SCHED();
    };
    unsigned tag;
    unsigned dvv = 0;
    auto upper = [&](auto is_stat, auto is_conv) {
        constexpr bool STAT = decltype(is_stat)::v, CONV = decltype(is_conv)::v;
        const int srow = b * 8 + (wid - 5) * 4 + (lane >> 4), t3 = tid - 320;
        unsigned xw[18], epv = 0;
        if constexpr (STAT) {
            const unsigned* xr = (const unsigned*)(x + (size_t)srow * D_) + (lane & 15);
#pragma unroll
            for (int j = 0; j < 18; ++j) xw[j] = xr[16 * j];
        } else {
            epv = __hip_atomic_load((const unsigned*)(ws + WS_EP), __ATOMIC_RELAXED, __HIP_MEMORY_SCOPE_AGENT);
        }
        SCHED();
        if constexpr (STAT) {
            const char *c3, *c4;
            SYM_ADDR(c3, k3_gemm1); SYM_ADDR(c4, k9_end);
            warm_code<1>(c3, c4, ((idx & 3) * 2 + wid - 5) * 64, lane, lds0 + C1_TOFF + 256 + (wid - 5) * 256);
        }
        SCHED();
        u32x4 v[12], fa[6]; u32x2 fb[6];
        if constexpr (CONV) {
#pragma unroll
            for (int i = 0; i < 6; ++i) {
                const int task = min(t3 + 192 * i, 1023), u = task & 255;
                const u16* src = ssm_k + (size_t)(ch0 + (task >> 8)) * S_ + u * 8;
                fa[i] = *(const u32x4*)src;
                fb[i] = *(const u32x2*)(src + (u < 255 ? 8 : 0));
            }
            SCHED();
        }
        if constexpr (STAT) {
            f32x2 xf[18], s = {0.f, 0.f};
#pragma unroll
            for (int j = 0; j < 18; ++j) { xf[j] = (f32x2){bf_lo(xw[j]), bf_hi(xw[j])}; s += xf[j]; }
            const float mean = sum16(s.x + s.y) * (1.f / D_);
            f32x2 q = {0.f, 0.f};
#pragma unroll
            for (int j = 0; j < 18; ++j) { const f32x2 d = xf[j] - mean; q = __builtin_elementwise_fma(d, d, q); }
            const unsigned sgr = pk_bf16(mean, ln_sd(sum16(q.x + q.y) * (1.f / D_)));
            tag = get_tag();
            const unsigned so = (lane & 15) == 0 ? (unsigned)srow * 8 : 0x80000000u;
            st8_wt(mk_rsrc(ws + WS_ST, S_ * 8), so, (u32x2){sgr, tag});
            st8_wt(mk_rsrc(ws + WS_G2, S_ * 8), so, (u32x2){0u, 0u});
        } else {
            *tagp = epv + 1u;
            asm volatile("s_waitcnt lgkmcnt(0)" ::: "memory");
            tag = get_tag();
        }
        SCHED();
        if constexpr (!CONV) {
            if constexpr (!STAT) {
            dma4((unsigned)lane * (WS_END / 64 / 128 * 128), ws, lds0 + C1_TOFF + 1024);
            dma4((unsigned)(lane & 7) * (S_ * D_ * 2 / 8), out, lds0 + C1_TOFF + 1280);
            dma4((unsigned)(lane & 15) * (D_ * F_ * 2 / 16), wg, lds0 + C1_TOFF + 1280);
            dma4((unsigned)(lane & 15) * (D_ * F_ * 2 / 16), wu, lds0 + C1_TOFF + 1280);
            dma4((unsigned)(lane & 15) * (D_ * F_ * 2 / 16), wd, lds0 + C1_TOFF + 1280);
            }
            __builtin_amdgcn_s_sleep(HELPER_DELAY);
            cs_load(v);
            if (!STAT && h == 111) {
                const rsrc_t rf = mk_rsrc(ws + WS_F0, 256 * FSP);
                for (int spin = 0; spin < (1 << 20); ++spin) {
                    bool ok = true;
#pragma unroll
                    for (int i = 0; i < 4; ++i) ok &= __builtin_amdgcn_raw_buffer_load_b32(rf, (i * 64 + lane) * FSP, 0, 16) == tag;
                    if (__all(ok)) break;
                    __builtin_amdgcn_s_sleep(8);
                }
                if (lane == 0) __hip_atomic_store((unsigned*)(ws + WS_EP), tag, __ATOMIC_RELAXED, __HIP_MEMORY_SCOPE_AGENT);
            }
            cs_finish(v);
            return;
        }
        if (t3 < 80) {
            const int cpy = t3 / 5, e = t3 % 5, q = e < 3 ? e : 257 + e;
            *(LAS u32x4*)(smem + cpy * C1_COPY + q * 16) = (u32x4){0u, 0u, 0u, 0u};
        } else if (t3 < 84) {
            *(LAS u32x4*)(smem + C1_KVOFF - 64 + (t3 - 80) * 16) = (u32x4){0u, 0u, 0u, 0u};
        }
        if (t3 < 128) *(LAS u32x4*)(smem + C1_KVOFF + (t3 >> 5) * C1_KV + 4096 + (t3 & 31) * 16) = (u32x4){0u, 0u, 0u, 0u};
#pragma unroll
        for (int i = 0; i < 6; ++i) {
            const int task = min(t3 + 192 * i, 1023), u = task & 255;
            const unsigned d[6] = {fa[i].x, fa[i].y, fa[i].z, fa[i].w, u < 255 ? fb[i].x : 0u, u < 255 ? fb[i].y : 0u};
            unsigned o[5];
#pragma unroll
            for (int e = 0; e < 5; ++e) o[e] = __builtin_amdgcn_alignbit(d[e + 1], d[e], 16);
            LAS char* base = (LAS char*)smem + (task >> 8) * C1_FILT + (u + 4) * 16;
            *(LAS u32x4*)(base) = (u32x4){o[0], o[1], o[2], o[3]};
            *(LAS u32x4*)(base + C1_COPY) = (u32x4){d[1], d[2], d[3], d[4]};
            *(LAS u32x4*)(base + 2 * C1_COPY) = (u32x4){o[1], o[2], o[3], o[4]};
            *(LAS u32x4*)(base + 3 * C1_COPY) = (u32x4){d[2], d[3], d[4], d[5]};
            if (u == 0) {
                *(LAS u32x4*)(base - 16) = (u32x4){0u, 0u, 0u, d[0] << 16};
                *(LAS u32x4*)(base - 16 + C1_COPY) = (u32x4){0u, 0u, 0u, d[0]};
                *(LAS u32x4*)(base - 16 + 2 * C1_COPY) = (u32x4){0u, 0u, d[0] << 16, o[0]};
                *(LAS u32x4*)(base - 16 + 3 * C1_COPY) = (u32x4){0u, 0u, d[0], d[1]};
            }
        }
        asm volatile("s_waitcnt lgkmcnt(0)" ::: "memory"); BAR();
        x_rows();
    };
    if (wid >= 5) {
        if (!conv) {
            if (wid < 7) upper(IC<1>{}, IC<0>{}); else upper(IC<0>{}, IC<0>{});
            DIAG_END(ws, 0);
            return;
        }
        if (wid < 7) upper(IC<1>{}, IC<1>{}); else upper(IC<0>{}, IC<1>{});
    } else {
        {
            unsigned kw[11], vw[8];
            const int t0 = idx * 64 + (lane >> 3) * 8, dc = wid * 8 + (lane & 7), kch = xcd * 72 + (dc < 36 ? dc : 35) * 2;
            const rsrc_t rk = mk_rsrc(kin, S_ * D_ * 2), rv = mk_rsrc(vin, S_ * D_ * 2);
            const int off = t0 * (D_ * 2) + kch * 2;
#pragma unroll
            for (int j = 0; j < 11; ++j) kw[j] = __builtin_amdgcn_raw_buffer_load_b32(rk, (unsigned)(off + (j - 3) * (D_ * 2)), 0, 0);
#pragma unroll
            for (int j = 0; j < 8; ++j) vw[j] = __builtin_amdgcn_raw_buffer_load_b32(rv, (unsigned)(off + j * (D_ * 2)), 0, 0);
            const u32x4 sw = *(const u32x4*)(shift_k + kch * 4);
            dvv = ssm_D[(conv ? ch0 : 0) + (wid & 3)];
            SCHED();
            STAMP(1);
            const f32x2 w0 = {bf_lo(sw.x), bf_lo(sw.z)}, w1 = {bf_hi(sw.x), bf_hi(sw.z)}, w2 = {bf_lo(sw.y), bf_lo(sw.w)}, w3 = {bf_hi(sw.y), bf_hi(sw.w)};
            f32x2 kf[11], o[8];
#pragma unroll
            for (int j = 0; j < 11; ++j) kf[j] = (f32x2){bf_lo(kw[j]), bf_hi(kw[j])};
#pragma unroll
            for (int j = 0; j < 8; ++j) {
                f32x2 s = kf[j] * w3;
                s = __builtin_elementwise_fma(kf[j + 1], w2, s);
                s = __builtin_elementwise_fma(kf[j + 2], w1, s);
                s = __builtin_elementwise_fma(kf[j + 3], w0, s);
                o[j] = s * (f32x2){bf_lo(vw[j]), bf_hi(vw[j])};
            }
            const rsrc_t ro = mk_rsrc(ws + WS_KV, S_ * D_ * 2);
            const unsigned oo = dc < 36 ? (unsigned)(kch * (S_ * 2) + (2040 - t0) * 2) : 0x80000000u;
            st16_wt(ro, oo, (u32x4){pk_bf16(o[7].x, o[6].x), pk_bf16(o[5].x, o[4].x), pk_bf16(o[3].x, o[2].x), pk_bf16(o[1].x, o[0].x)});
            st16_wt(ro, oo + S_ * 2, (u32x4){pk_bf16(o[7].y, o[6].y), pk_bf16(o[5].y, o[4].y), pk_bf16(o[3].y, o[2].y), pk_bf16(o[1].y, o[0].y)});
        }
        tag = get_tag();
        WAIT_V(0);
        if (lane == 0) __hip_atomic_store((unsigned*)(ws + WS_F0 + ((wid * 8 + xcd) * 32 + idx) * FSP), tag, __ATOMIC_RELAXED, __HIP_MEMORY_SCOPE_AGENT);
        MARK();
        STAMP(2);
        if (!conv) {
            u32x4 v[12];
            __builtin_amdgcn_s_sleep(T_HD2);
            cs_load(v);
            cs_finish(v);
            DIAG_END(ws, 0);
            return;
        }
        if (wid == 4) {
            x_rows();
            BAR();
#pragma unroll 1
            for (int u = 0; u < WU4; ++u) wd_unit_load(wd, cg * WU4 + u, lane, lds0 + C1_WOFF + u * 3072);
            __builtin_amdgcn_s_sleep(24);
            const char* gs = ws + WS_ST;
#pragma unroll
            for (int i = 0; i < 4; ++i) {
                dma16_sc1<0>(lane * 16, gs + i * 4096, lds0 + C1_SOFF + i * 4096); dma16_sc1<1024>(lane * 16, gs + i * 4096, lds0 + C1_SOFF + i * 4096);
                dma16_sc1<2048>(lane * 16, gs + i * 4096, lds0 + C1_SOFF + i * 4096); dma16_sc1<3072>(lane * 16, gs + i * 4096, lds0 + C1_SOFF + i * 4096);
            }
            WAIT_V(0);
#pragma unroll 1
            for (int u = 0; u < WU4; ++u) wd_unit_store(ws, cg * WU4 + u, lane, (const LAS char*)smem + C1_WOFF + u * 3072);
        } else {
        {
            wait_kv(tag);
            STAMP(7);
            const char* kvs = ws + WS_KV + (size_t)(ch0 + wid) * (S_ * 2);
            const unsigned m = lds0 + C1_KVOFF + wid * C1_KV;
            dma16_sc1<0>(lane * 16, kvs, m); dma16_sc1<1024>(lane * 16, kvs, m); dma16_sc1<2048>(lane * 16, kvs, m); dma16_sc1<3072>(lane * 16, kvs, m);
            WAIT_V(0);
            STAMP(9);
            BAR();
            STAMP(4);
        }
        const int row = lane & 15, kg = lane >> 4;
        const LAS char* abase = (const LAS char*)smem + wid * C1_FILT + (row & 3) * C1_COPY + (24 + 4 * (row >> 2) - 8 * kg) * 2;
        const LAS char* bbase = (const LAS char*)smem + C1_KVOFF + wid * C1_KV + (2040 - 16 * row - 8 * kg) * 2 - 512 * 7 - 448;
        auto rd_ah = [&](int r, int L, int hf) -> s16x4 { return *(const volatile LAS s16x4*)(abase + 512 * L - 64 * r + 8 * hf); };
        auto rd_b = [&](int r, int m) -> s16x8 { return *(const LAS s16x8*)(bbase + 448 - 64 * r + 512 * (7 - m)); };
        auto cat = [&](s16x4 lo, s16x4 hi) -> s16x8 { return __builtin_shufflevector(lo, hi, 0, 1, 2, 3, 4, 5, 6, 7); };
        f32x4 acc[8];
        s16x4 al[2][8], ah[2][8];
        s16x8 bf[2][8], bd[8];
        auto rd_k = [&](int r, int k) {
            if (k < 2) { if (k == 0) al[r & 1][0] = rd_ah(r, 1, 0); else ah[r & 1][0] = rd_ah(r, 1, 1); }
            else if (k < 10) bf[r & 1][9 - k] = rd_b(r, 8 - k);
            else { const int L = (k - 10) / 2 + 2; if (k & 1) ah[r & 1][L - 1] = rd_ah(r, L, 1); else al[r & 1][L - 1] = rd_ah(r, L, 0); }
        };
        const s16x8 a0 = cat(rd_ah(0, 0, 0), rd_ah(0, 0, 1));
#pragma unroll
        for (int e = 0; e < 8; ++e) bd[e] = rd_b(0, e);
        s16x8 dfr;
        {
            const int j = 8 * kg + 7 - row;
            u32x4 w;
#pragma unroll
            for (int e = 0; e < 4; ++e) w[e] = j == 2 * e ? dvv : (j == 2 * e + 1 ? dvv << 16 : 0u);
            dfr = __builtin_bit_cast(s16x8, w);
            asm volatile("s_nop 1" : "+v"(dfr));
        }
        SCHED();
#pragma unroll
        for (int n = 0; n < 16; ++n) {
            if (n < 8) mfma_first(acc[n], a0, bd[n]); else mfma_acc(acc[n - 8], dfr, bd[n - 8]);
            SCHED();
            if (n == 0) { rd_k(0, 0); rd_k(0, 1); }
            else if (n == 1) rd_k(0, 9);
            else rd_k(0, n + 8);
            SCHED();
        }
#pragma unroll
        for (int r = 0; r < 8; ++r) {
            int k = 0;
#pragma unroll
            for (int L = 1; L <= 8; ++L) {
#pragma unroll
                for (int n = 7; n >= L - 1; --n) {
                    const int e = n - L + 1;
                    mfma_acc(acc[n], cat(al[r & 1][L - 1], ah[r & 1][L - 1]), (r == 0 && e > 0) ? bd[e - 1] : bf[r & 1][e]);
                    SCHED();
                    if (r < 7 && k < 24) rd_k(r + 1, k);
                    ++k;
                    SCHED();
                }
            }
        }
        MFMA_DONE();
#pragma unroll
        for (int n = 0; n < 8; ++n)
            *(LAS u32x2*)(smem + C1_YOFF + wid * 4096 + (256 * n + 16 * row + 4 * kg) * 2) = (u32x2){pk_bf16(acc[n][0], acc[n][1]), pk_bf16(acc[n][2], acc[n][3])};
        }
    }
    STAMP(5);
    asm volatile("s_waitcnt lgkmcnt(0)" ::: "memory"); BAR();
    STAMP(6);
    {
        u32x2 yc[4];
#pragma unroll
        for (int c = 0; c < 4; ++c) yc[c] = *(const LAS u32x2*)(smem + C1_YOFF + c * 4096 + tid * 8);
        const u32x4 xa = *(const LAS u32x4*)(smem + C1_XOFF + tid * 32), xb = *(const LAS u32x4*)(smem + C1_XOFF + tid * 32 + 16);
        const u32x2 xr[4] = {{xa.x, xa.y}, {xa.z, xa.w}, {xb.x, xb.y}, {xb.z, xb.w}};
        u32x4 s01 = *(const LAS u32x4*)(smem + C1_SOFF + tid * 32), s23 = *(const LAS u32x4*)(smem + C1_SOFF + tid * 32 + 16);
        if (__builtin_expect(!__all(s01.y == tag && s01.w == tag && s23.y == tag && s23.w == tag), 0)) {
            const rsrc_t rs = mk_rsrc(ws + WS_ST, S_ * 8);
            for (int spin = 0; spin < (1 << 20); ++spin) {
                s01 = __builtin_amdgcn_raw_buffer_load_b128(rs, tid * 32, 0, 16);
                s23 = __builtin_amdgcn_raw_buffer_load_b128(rs, tid * 32 + 16, 0, 16);
                if (__all(s01.y == tag && s01.w == tag && s23.y == tag && s23.w == tag)) break;
                __builtin_amdgcn_s_sleep(2);
            }
        }
        const unsigned sv[4] = {s01.x, s01.z, s23.x, s23.z};
        float mm[4], rr[4];
#pragma unroll
        for (int j = 0; j < 4; ++j) { mm[j] = bf_lo(sv[j]); rr[j] = __builtin_amdgcn_rcpf(bf_hi(sv[j])); }
        unsigned hw[8];
#pragma unroll
        for (int j = 0; j < 4; ++j) {
#pragma unroll
            for (int e = 0; e < 2; ++e) {
                const unsigned xw = e ? xr[j].y : xr[j].x;
                const unsigned ya = (j >> 1) ? yc[2 * e].y : yc[2 * e].x, yb = (j >> 1) ? yc[2 * e + 1].y : yc[2 * e + 1].x;
                const float y0 = (j & 1) ? bf_hi(ya) : bf_lo(ya), y1 = (j & 1) ? bf_hi(yb) : bf_lo(yb);
                const float x0 = bf_lo(xw), x1 = bf_hi(xw);
                hw[2 * j + e] = pk_bf16(__builtin_fmaf((x0 - mm[j]) * rr[j], y0, x0), __builtin_fmaf((x1 - mm[j]) * rr[j], y1, x1));
            }
        }
        const rsrc_t rh = mk_rsrc(ws + WS_H, S_ * D_ * 2);
        const unsigned ho = (unsigned)(((tid >> 6) * 144 + cg) * 2048 + (tid & 63) * 32);
        st16_wt(rh, ho, (u32x4){hw[0], hw[1], hw[2], hw[3]});
        st16_wt(rh, ho + 16, (u32x4){hw[4], hw[5], hw[6], hw[7]});
    }
    STAMP(8);
    DIAG_END(ws, 0);
}

constexpr int ST_BYTES = 16384 + 2 * 9216;
constexpr int NBUF = 4;
constexpr int G_LDS = NBUF * ST_BYTES;

struct TokC { float nm, c0, c1, q; };
__device__ __forceinline__ unsigned geglu2(f32x2 ag, f32x2 au, f32x2 csg, f32x2 csu, const TokC& t) {
    const f32x2 nm = {t.nm, t.nm}, c0 = {t.c0, t.c0}, c1 = {t.c1, t.c1}, q = {t.q, t.q};
    const f32x2 g = __builtin_elementwise_fma(nm, csg, ag), u = __builtin_elementwise_fma(nm, csu, au);
    const f32x2 z = g * __builtin_elementwise_fma(g * g, c1, c0);
    const f32x2 e = {__builtin_amdgcn_exp2f(z.x), __builtin_amdgcn_exp2f(z.y)};
    const f32x2 d = __builtin_elementwise_fma(e, q, q);
    const f32x2 r = (g * u) * (f32x2){__builtin_amdgcn_rcpf(d.x), __builtin_amdgcn_rcpf(d.y)};
    return pk_bf16(r.x, r.y);
}
#define LO2(v) __builtin_shufflevector(v, v, 0, 1)
#define HI2(v) __builtin_shufflevector(v, v, 2, 3)
#define WAIT_VI(N) asm volatile("s_waitcnt vmcnt(%0)" :: "i"(N) : "memory")

constexpr int G2_W0 = 71680, G2_FL = 69632;
__device__ __forceinline__ void g2_wpiece(int P, int s, const char* wsrc, unsigned aoff, unsigned mw0) {
    const unsigned m = mw0 + (s % 5) * 18432;
    if (P == 0) dma16<0>(aoff, wsrc + (size_t)s * 18432, m);
    if (P == 1) dma16<1024>(aoff, wsrc + (size_t)s * 18432, m);
    if (P == 2) dma16_q<2048>(aoff, wsrc + (size_t)s * 18432, m);
}
constexpr int g2_n(int S) { return (S + 3 < 18 ? 2 : 0) + (S + 4 < 18 ? 3 : 0) + (S == 4 ? 1 : 0); }
constexpr int g2_vm(int S) {
    return S == 0 ? 3 : (S == 1 ? 1 + g2_n(0) : g2_n(S - 2) - (S + 1 < 18 ? 2 : 0) + g2_n(S - 1));
}
#define DIAG_ARG
#define DIAG_PASS
template <int NT>
__device__ __forceinline__ void g2_body(char* __restrict__ ws, u16* __restrict__ out, LAS char* lds, int lane, int wid, int mt, int ks, int nt,
                                        const char* wsrc, unsigned flag2 DIAG_ARG) {
    constexpr int dh = NT == 4;
    const unsigned lds0 = (unsigned)(size_t)lds;
    const int wr = wid & 3;
    const char* asrc = ws + WS_HH + ((size_t)(mt * 4 + ks) * 36) * 8192 + wid * 2048;
    const unsigned aoff = lane * 16;
    const unsigned ma0 = lds0 + wid * 2048, mw0 = lds0 + G2_W0 + wid * 2304;
    auto hpiece = [&](int P, int s) {
        if (P == 0) dma16_sc1<0>(aoff, asrc + (size_t)s * 16384, ma0 + (s & 3) * 16384);
        if (P == 1) dma16_sc1<1024>(aoff, asrc + (size_t)s * 16384, ma0 + (s & 3) * 16384);
    };
    hpiece(0, 0); hpiece(1, 0); hpiece(0, 1); hpiece(1, 1); hpiece(0, 2); hpiece(1, 2);

    const int g4 = lane >> 4;
    const int toff = wr * 2048 + lane * 16;
    const int foff = G2_W0 + dh * 5120 + lane * 16;
    auto rdf = [&](const LAS char* p) -> s16x8 { return *(const LAS s16x8*)p; };
    const rsrc_t rfb = mk_rsrc(ws + WS_FB, 16384);

    f32x4 acc[NT][2];
    s16x8 wf[2 * NT], tf[2][4];
    WAIT_V(4);
    __builtin_amdgcn_raw_buffer_store_b32(1u, rfb, lane == 0 ? flag2 : 0x80000000u, 0, 16);
    BAR();
    STAMP(8);
#pragma unroll
    for (int e = 0; e < 4; ++e) tf[0][e] = rdf(lds + toff + (e >> 1) * 8192 + (e & 1) * 1024);
#pragma unroll
    for (int e = 0; e < 2 * NT; ++e) wf[e] = rdf(lds + foff + (e / NT) * 9216 + (e % NT) * 1024);
    SCHED();

    auto iter = [&](auto sc, s16x8 (&tc)[4], s16x8 (&tn)[4]) {
        constexpr int S = decltype(sc)::v;
        if constexpr (S < 17) {
            WAIT_VI(g2_vm(S));
            BAR();
            if constexpr (S == 7) {
                unsigned fb2 = *(const volatile LAS unsigned*)(lds + G2_FL + wid * 256 + lane * 4);
                if (__builtin_expect(!__all(fb2 == 1u), 0)) {
                    for (int spin = 0; spin < (1 << 20); ++spin) {
                        fb2 = __builtin_amdgcn_raw_buffer_load_b32(rfb, (unsigned)(((mt * 4 + ks) * 2 + 1) * 128 + (lane & 31) * 4), 0, 16);
                        if (__all(fb2 == 1u)) break;
                        __builtin_amdgcn_s_sleep(2);
                    }
                }
            }
            SCHED();
        }
        const LAS char* tb = lds + ((S + 1) & 3) * 16384;
        const LAS char* wb = lds + ((S + 1) % 5) * 18432;
#pragma unroll
        for (int e = 0; e < 2 * NT; ++e) {
#pragma unroll
            for (int i = 0; i < 2; ++i) {
                if (S == 0 && e < NT) mfma_first(acc[e % NT][i], wf[e], tc[(e / NT) * 2 + i]);
                else mfma_acc(acc[e % NT][i], wf[e], tc[(e / NT) * 2 + i]);
            }
            SCHED();
            if constexpr (S < 17) {
                wf[e] = rdf(wb + foff + (e / NT) * 9216 + (e % NT) * 1024);
                if (e >= 2 && e < 6) tn[e - 2] = rdf(tb + toff + ((e - 2) >> 1) * 8192 + ((e - 2) & 1) * 1024);
            }
            if constexpr (S + 3 < 18) { if (e < 2) hpiece(e, S + 3); }
            if constexpr (S + 4 < 18) {
                if (e == 3) g2_wpiece(0, S + 4, wsrc, aoff, mw0);
                if (e == 5) g2_wpiece(1, S + 4, wsrc, aoff, mw0);
                if (e == 6) g2_wpiece(2, S + 4, wsrc, aoff, mw0);
            }
            if constexpr (S == 4) {
                if (e == 7) dma4_sc1((unsigned)(lane & 31) * 4, ws + WS_FB + ((mt * 4 + ks) * 2 + 1) * 128, lds0 + G2_FL + wid * 256);
            }
            SCHED();
        }
    };
#define G2_IT2(S) iter(IC<S>{}, tf[0], tf[1]); iter(IC<S + 1>{}, tf[1], tf[0])
    G2_IT2(0); G2_IT2(2); G2_IT2(4); G2_IT2(6); G2_IT2(8); G2_IT2(10); G2_IT2(12);
    STAMP(9);
    G2_IT2(14);
    iter(IC<16>{}, tf[0], tf[1]);
    const int tok0 = mt * 128 + wr * 32 + (lane & 15);
    u32x2 hv[2][5];
    if (wr == ks) {
#pragma unroll
        for (int i = 0; i < 2; ++i) {
            const int t = tok0 + i * 16;
            const char* hp = ws + WS_H + ((size_t)((t >> 8) * 144 + nt * 36 + dh * 20) * 256 + (t & 255)) * 8;
#pragma unroll
            for (int e = 0; e < 4; ++e) hv[i][e] = *(const u32x2*)(hp + ((e >> 1) * 8 + (g4 & 1) * 4 + (g4 >> 1) * 2 + (e & 1)) * 2048);
            if (NT == 5) hv[i][4] = *(const u32x2*)(hp + (16 + g4) * 2048);
        }
    }
    SCHED();
    iter(IC<17>{}, tf[1], tf[0]);
    MFMA_DONE();
    STAMP(10);

    const rsrc_t rx = mk_rsrc(ws + WS_X, X_BYTES);
    const unsigned xt = (unsigned)((mt * 4 + nt) * 4 * 27648 + dh * 4096);
    auto inb = [&](int s) -> unsigned { return xt + (unsigned)(((ks + 1 + s) & 3) * 27648 + (2 - s) * 9216); };
    if (wr != ks) {
        const unsigned o = xt + (unsigned)(ks * 27648 + ((wr - ks - 1) & 3) * 9216);
#pragma unroll
        for (int i = 0; i < 2; ++i) {
            unsigned w[2 * NT];
#pragma unroll
            for (int j = 0; j < NT; ++j) { w[2 * j] = pk_bf16(acc[j][i][0], acc[j][i][1]); w[2 * j + 1] = pk_bf16(acc[j][i][2], acc[j][i][3]); }
#pragma unroll
            for (int e = 0; e < 2 * NT; ++e) w[e] = (w[e] == ~0u) ? 0x7fc07fc0u : w[e];
            if constexpr (NT == 5) st8_wt(rx, o + 8192 + i * 512 + lane * 8, (u32x2){w[8], w[9]});
            st16_wt(rx, o + i * 2048 + lane * 16, (u32x4){w[0], w[1], w[2], w[3]});
            st16_wt(rx, o + i * 2048 + 1024 + lane * 16, (u32x4){w[4], w[5], w[6], w[7]});
        }
    } else {
        u32x4 pa[3][4]; u32x2 pb[3][2];
        __builtin_amdgcn_s_sleep(16);
        for (int spin = 0; spin < (1 << 18); ++spin) {
            const u32x4 t = __builtin_amdgcn_raw_buffer_load_b128(rx, lane < 3 ? inb(lane) + 4080 : 0x80000000u, 0, 16);
            if (__all(t.w != ~0u)) {
                STAMP(11);
                unsigned m = 0u;
#pragma unroll
                for (int s = 0; s < 3; ++s) {
#pragma unroll
                    for (int p = 0; p < 4; ++p) pa[s][p] = __builtin_amdgcn_raw_buffer_load_b128(rx, inb(s) + p * 1024 + lane * 16, 0, 16);
                    if constexpr (NT == 5) {
#pragma unroll
                        for (int i = 0; i < 2; ++i) pb[s][i] = __builtin_amdgcn_raw_buffer_load_b64(rx, inb(s) + 8192 + i * 512 + lane * 8, 0, 16);
                    }
                }
#pragma unroll
                for (int s = 0; s < 3; ++s) {
#pragma unroll
                    for (int p = 0; p < 4; ++p) m = max(max(max(m, pa[s][p].x), max(pa[s][p].y, pa[s][p].z)), pa[s][p].w);
                    if constexpr (NT == 5) m = max(max(max(m, pb[s][0].x), max(pb[s][0].y, pb[s][1].x)), pb[s][1].y);
                }
                if (__all(m != ~0u)) break;
            }
            __builtin_amdgcn_s_sleep(1);
        }
        STAMP(12);
        const rsrc_t ro = mk_rsrc(out, S_ * D_ * 2);
#pragma unroll
        for (int i = 0; i < 2; ++i) {
            const unsigned ob = (unsigned)(((tok0 + i * 16) * D_ + nt * 144 + dh * 80) * 2);
            unsigned r[NT][2];
#pragma unroll
            for (int j = 0; j < NT; ++j) {
                f32x4 t = acc[j][i];
#pragma unroll
                for (int s = 0; s < 3; ++s) {
                    const unsigned w0 = j < 4 ? pa[s][i * 2 + (j >> 1)][(j & 1) * 2] : pb[s][i].x, w1 = j < 4 ? pa[s][i * 2 + (j >> 1)][(j & 1) * 2 + 1] : pb[s][i].y;
                    t += (f32x4){bf_lo(w0), bf_hi(w0), bf_lo(w1), bf_hi(w1)};
                }
                r[j][0] = pk_bf16(t[0], t[1]); r[j][1] = pk_bf16(t[2], t[3]);
            }
            auto addh = [&](unsigned a, unsigned h) -> unsigned { return pk_bf16(bf_lo(h) + bf_lo(a), bf_hi(h) + bf_hi(a)); };
#pragma unroll
            for (int p = 0; p < 2; ++p) {
                auto s0 = __builtin_amdgcn_permlane16_swap(r[2 * p][0], r[2 * p + 1][0], false, false);
                auto s1 = __builtin_amdgcn_permlane16_swap(r[2 * p][1], r[2 * p + 1][1], false, false);
                st16_wt(ro, ob + (unsigned)((p * 32 + (g4 & 1) * 16 + (g4 >> 1) * 8) * 2),
                        (u32x4){addh(s0[0], hv[i][2 * p].x), addh(s1[0], hv[i][2 * p].y), addh(s0[1], hv[i][2 * p + 1].x), addh(s1[1], hv[i][2 * p + 1].y)});
            }
            if constexpr (NT == 5) st8_wt(ro, ob + (unsigned)((64 + g4 * 4) * 2), (u32x2){addh(r[4][0], hv[i][4].x), addh(r[4][1], hv[i][4].y)});
        }
    }
    STAMP(13);
}

constexpr int K3_LDS = 163840;
constexpr int K3_SP = G_LDS;
constexpr int K3_CS = G2_W0 + 4 * 18432;
extern "C" __global__ __launch_bounds__(512, 2) void k3_gemm1(const u16* __restrict__ wg, const u16* __restrict__ wu,
                                                   char* __restrict__ ws, u16* __restrict__ out) {
    extern __shared__ __attribute__((aligned(16))) char smem[];
    LAS char* lds = (LAS char*)smem;
    const unsigned lds0 = (unsigned)(size_t)lds;
    const int tid = threadIdx.x, lane = tid & 63;
    const int wid = __builtin_amdgcn_readfirstlane(tid >> 6);
    const int wr = wid & 3, wc = wid >> 2;
    const int b = blockIdx.x, xcd = b & 7, idx = b >> 3;
    const int rt = (xcd >> 2) * 4 + (idx & 3), ct = (xcd & 3) * 8 + (idx >> 2);
    asm volatile("" :: "s"(wg), "s"(wu), "s"(ws), "s"(out));
    DIAG_DECL;

    const char* asrc = ws + WS_H + (size_t)rt * 144 * 2048 + wid * 2048;
    const unsigned aoff = (unsigned)(lane ^ ((wid & 2) << 2)) * 16;
    const unsigned ma0 = lds0 + wid * 2048, mw0 = lds0 + 16384 + wc * 9216 + wr * 2304;
    dma16<0>(aoff, asrc, ma0); dma16<1024>(aoff, asrc, ma0);
    const char* wsrc = (const char*)(wc ? wu : wg) + ct * 288 - 2048;
    auto wofs = [&](int i) -> unsigned {
        const int q = lane + 64 * i, lrow = 8 * wr + q / 18, pos = q % 18;
        const int krow = ((lrow >> 2) & 3) * 8 + (lrow >> 4) * 4 + (lrow & 3);
        const int col = pos * 8 + (pos < 8 ? 0 : (pos < 16 ? 8 : (((pos & 1) ^ wc) ? 136 : 64) - pos * 8));
        return (unsigned)(krow * (F_ * 2) + col * 2 + 2048 - 1024 * i);
    };
    const unsigned w0 = wofs(0), w1 = wofs(1), w2 = wofs(2);
    auto piece = [&](int P, int s) {
        const unsigned bo = (s & 3) * ST_BYTES;
        const char* wp = wsrc + (size_t)s * (32 * F_ * 2);
        if (P == 0) dma16<0>(aoff, asrc + (size_t)s * 16384, ma0 + bo);
        if (P == 1) dma16<1024>(aoff, asrc + (size_t)s * 16384, ma0 + bo);
        if (P == 2) dma16<0>(w0, wp, mw0 + bo);
        if (P == 3) dma16<1024>(w1, wp, mw0 + bo);
        if (P == 4) dma16_q<2048>(w2, wp, mw0 + bo);
    };
#define K3_PIECE(P, s) piece(P, s)
#define K3_STAGE(s) do { K3_PIECE(0, s); K3_PIECE(1, s); K3_PIECE(2, s); K3_PIECE(3, s); K3_PIECE(4, s); } while (0)
    K3_PIECE(2, 0); K3_PIECE(3, 0); K3_PIECE(4, 0);
    BAR();
    K3_STAGE(1);
    u32x2 hs[4];
    {
        const char* hsrc = ws + WS_H + (size_t)rt * 144 * 2048 + ct * 64;
        const unsigned o = (unsigned)((wid * 18 + (lane >> 3)) * 2048 + (lane & 7) * 8);
        asm volatile("global_load_dwordx2 %0, %1, %2" : "=v"(hs[0]) : "v"(o), "s"(hsrc) : "memory");
        asm volatile("global_load_dwordx2 %0, %1, %2" : "=v"(hs[1]) : "v"(o + 16384), "s"(hsrc) : "memory");
        asm volatile("global_load_dwordx2 %0, %1, %2" : "=v"(hs[2]) : "v"((unsigned)((wid * 18 + 16 + ((lane >> 3) & 1)) * 2048 + (lane & 7) * 8)), "s"(hsrc) : "memory");
        asm volatile("global_load_dwordx2 %0, %1, %2" : "=v"(hs[3]) : "v"((unsigned)(lane & 7) * 8), "s"(hsrc) : "memory");
    }

    const int g4 = lane >> 4, q4 = (lane & 15) >> 2, p4 = lane & 3;
    const int rowoff = (4 * g4 + q4) * 288;
    const int tbase = g4 * 4096 + wr * 512 + (lane & 15) * 8;
    const int toff0 = tbase + ((2 * wc) ^ (g4 & 1)) * 128, toff1 = tbase + ((2 * wc + 1) ^ (g4 & 1)) * 128;
    const int tstep = wc ? -256 : 256;
    const int foff = 16384 + rowoff + wc * 128 + p4 * 8;
    const int moff = 16384 + rowoff + 256 + (p4 < 2 ? 16 * wc + 8 * p4 : 9216 + 16 * (1 - wc) + 8 * (p4 - 2));

    f32x4 acc[9][4];
    s16x8 wf[9], tf[2][4];
    auto rd_w = [&](const LAS char* sb, int j) -> s16x8 {
        return j < 8 ? tr_frag(sb + foff + (j >> 2) * 9216 + (j & 3) * 32) : tr_frag(sb + moff);
    };
    auto rd_t = [&](const LAS char* sb, int i) -> s16x8 {
        const LAS char* p = sb + ((i & 1) ? toff1 : toff0) + (i >> 1) * tstep;
        const s16x4 lo = *(const volatile LAS s16x4*)p, hi = *(const volatile LAS s16x4*)(p + 2048);
        return __builtin_shufflevector(lo, hi, 0, 1, 2, 3, 4, 5, 6, 7);
    };

    STAMP(1);
    WAIT_V(9); BAR();
    STAMP(2);
    wf[0] = rd_w(lds, 0);
#pragma unroll
    for (int i = 0; i < 4; ++i) tf[0][i] = rd_t(lds, i);
#pragma unroll
    for (int j = 1; j < 9; ++j) wf[j] = rd_w(lds, j);
    SCHED();
    const int mt2 = (xcd >> 2) * 8 + (idx & 7), ks2 = xcd & 3, nt2 = idx >> 3;
    const rsrc_t rfill = mk_rsrc(ws + WS_X + (size_t)((mt2 * 4 + nt2) * 4 + ks2) * 27648, 27648);
    auto fill = [&](int k) { st16_wt(rfill, (unsigned)(k * 8192 + tid * 16), (u32x4){~0u, ~0u, ~0u, ~0u}); };
    unsigned sgr;
    {
        const LAS char* sb = lds + ST_BYTES;
        float hc = 0.f;
#pragma unroll
        for (int j = 0; j < 9; ++j) {
#pragma unroll
            for (int i = 0; i < 4; ++i) mfma_first(acc[j][i], wf[j], tf[0][i]);
            SCHED();
            if (j == 0) { K3_PIECE(0, 2); K3_PIECE(1, 2); }
            if (j == 1) { K3_PIECE(2, 2); K3_PIECE(3, 2); }
            if (j == 2) K3_PIECE(4, 2);
            if (j == 3) {
                asm volatile("s_waitcnt vmcnt(5)" : "+v"(hs[0]), "+v"(hs[1]), "+v"(hs[2]), "+v"(hs[3]) :: "memory");
                hc = bf_lo(hs[3].x);
                float s1 = 0.f, s2 = 0.f;
#pragma unroll
                for (int i = 0; i < 3; ++i) {
                    const float d[4] = {bf_lo(hs[i].x) - hc, bf_hi(hs[i].x) - hc, bf_lo(hs[i].y) - hc, bf_hi(hs[i].y) - hc};
                    float a = (d[0] + d[1]) + (d[2] + d[3]), q = __builtin_fmaf(d[0], d[0], d[1] * d[1]) + __builtin_fmaf(d[2], d[2], d[3] * d[3]);
                    if (i == 2 && lane >= 16) { a = 0.f; q = 0.f; }
                    s1 += a; s2 += q;
                }
                s1 = x32_add(x16_add(dpp_add<0x128>(s1))); s2 = x32_add(x16_add(dpp_add<0x128>(s2)));
                if (lane < 8) *(LAS f32x2*)(lds + K3_SP + (lane * 8 + wid) * 8) = (f32x2){s1, s2};
                asm volatile("s_waitcnt lgkmcnt(0)" ::: "memory");
                BAR();
                wf[0] = rd_w(sb, 0); wf[1] = rd_w(sb, 1);
                tf[1][0] = rd_t(sb, 0); tf[1][1] = rd_t(sb, 1);
            }
            if (j == 4) { tf[1][2] = rd_t(sb, 2); tf[1][3] = rd_t(sb, 3); wf[2] = rd_w(sb, 2); wf[3] = rd_w(sb, 3); }
            if (j >= 4) { wf[j] = rd_w(sb, j); K3_PIECE(j - 4, 3); }
            if (j == 8) {
                const f32x2 pp = *(const LAS f32x2*)(lds + K3_SP + (wid * 8 + (lane & 7)) * 8);
                const float s1 = dpp_add<0x141>(dpp_add<0x4E>(dpp_add<0xB1>(pp.x))) * (1.f / D_);
                const float s2 = dpp_add<0x141>(dpp_add<0x4E>(dpp_add<0xB1>(pp.y))) * (1.f / D_);
                const float c0 = __uint_as_float(__builtin_amdgcn_readlane(__float_as_uint(hc), wid));
                const float sd = ln_sd(__builtin_fmaxf(__builtin_fmaf(-s1, s1, s2), 0.f));
                sgr = pk_bf16(c0 + s1, sd);
                fill(0); fill(1);
            }
            SCHED();
        }
    }

    const rsrc_t rg = mk_rsrc(ws + WS_G2 + (size_t)(rt * 256 + wr * 64) * 8, 512);
    const char* w2src = ws + WS_WT + (size_t)(ks2 * 4 + nt2) * 18 * 18432 + wid * 2304;
    u32x2 gr[4];
    s16x4 tl;
    constexpr int TP = 1, DPJ0 = 0, DPJ1 = 5;
    auto iter = [&](auto mode, auto vm, int s, s16x8 (&tc)[4], s16x8 (&tn)[4]) {
        constexpr int MODE = decltype(mode)::v;
        if constexpr (MODE != 4) {
            if constexpr (MODE == 3) WAIT_V(0); else WAIT_VI(5 + decltype(vm)::v);
            BAR();
            SCHED();
        }
        const LAS char* sb = lds + ((s + 1) & 3) * ST_BYTES;
#pragma unroll
        for (int j = 0; j < 9; ++j) {
            s16x4 wl, wh;
#pragma unroll
            for (int i = 0; i < 4; ++i) {
                mfma_acc(acc[j][i], wf[j], tc[i]);
                SCHED();
                if constexpr (MODE != 4) {
                    if (j >= 1) {
                        const LAS char* p = (j - 1 < 8) ? sb + foff + ((j - 1) >> 2) * 9216 + ((j - 1) & 3) * 32 : sb + moff;
                        if (i == 0) wl = __builtin_amdgcn_ds_read_tr16_b64_v4i16((LAS s16x4*)p);
                        if (i == 1) { wh = __builtin_amdgcn_ds_read_tr16_b64_v4i16((LAS s16x4*)(p + 16 * 288)); wf[j - 1] = __builtin_shufflevector(wl, wh, 0, 1, 2, 3, 4, 5, 6, 7); }
                    }
                    if (j >= TP && j < TP + 4) {
                        const int ti = j - TP;
                        const LAS char* p = sb + ((ti & 1) ? toff1 : toff0) + (ti >> 1) * tstep;
                        if (i == 2) tl = *(const volatile LAS s16x4*)p;
                        if (i == 3) tn[ti] = __builtin_shufflevector(tl, *(const volatile LAS s16x4*)(p + 2048), 0, 1, 2, 3, 4, 5, 6, 7);
                    }
                    if (j == 8 && i == 3) wf[8] = rd_w(sb, 8);
                }
                if constexpr (MODE < 2 || MODE == 5) {
                    if (j == DPJ0 && i == 1) K3_PIECE(0, s + 3);
                    if (j == DPJ0 && i == 3) K3_PIECE(1, s + 3);
                    if (j == DPJ1 && i == 3) K3_PIECE(2, s + 3);
                    if (j == DPJ1 + 1 && i == 3) K3_PIECE(3, s + 3);
                    if (j == DPJ1 + 2 && i == 3) K3_PIECE(4, s + 3);
                }
                SCHED();
            }
            if constexpr (MODE == 0) { if (j == 8) { fill(2); fill(3); } }
            if constexpr (MODE == 5) {
                if (j == 0 && s == 4) st8_wt(mk_rsrc(ws + WS_G2, S_ * 8), lane == 0 ? (unsigned)((rt * 256 + ct * 8 + wid) * 8) : 0x80000000u, (u32x2){sgr, 1u});
            }
            if constexpr (MODE == 2) {
                if (j == 0) {
#pragma unroll
                    for (int i = 0; i < 4; ++i) gr[i] = __builtin_amdgcn_raw_buffer_load_b64(rg, (unsigned)(((i ^ (2 * wc)) * 16 + (lane & 15)) * 8), 0, 16);
                }
            }
            if constexpr (MODE == 3) {
                if (j == 0) {
                    asm volatile("" : "+v"(gr[0]), "+v"(gr[1]), "+v"(gr[2]), "+v"(gr[3]));
                    const unsigned co = (unsigned)((lane >= 18 ? F_ * 4 + (lane - 18) * 16 : lane * 16));
                    dma_m36(co, ws + WS_CS + (size_t)(ct * 144 + wc * 72) * 4, lds0 + K3_CS + wid * 1152);
                    dma_m36(co, ws + WS_CS + (size_t)(2 * F_ + ct * 144 + wc * 72) * 4, lds0 + K3_CS + wid * 1152 + 576);
                }
            }
            if constexpr (MODE == 3 || MODE == 4) {
                if (j >= 2 && j < 8) g2_wpiece((j - 2) % 3, (MODE - 3) * 2 + (j - 2) / 3, w2src, lane * 16, lds0 + G2_W0 + wid * 2304);
            }
            SCHED();
        }
    };
    iter(IC<0>{}, IC<2>{}, 1, tf[1], tf[0]);
    iter(IC<1>{}, IC<4>{}, 2, tf[0], tf[1]);
    iter(IC<1>{}, IC<2>{}, 3, tf[1], tf[0]);
#pragma unroll 1
    for (int s = 4; s < 14; s += 2) {
        iter(IC<5>{}, IC<0>{}, s, tf[0], tf[1]);
        iter(IC<1>{}, IC<0>{}, s + 1, tf[1], tf[0]);
    }
    STAMP(3);
    iter(IC<1>{}, IC<0>{}, 14, tf[0], tf[1]);
    iter(IC<2>{}, IC<0>{}, 15, tf[1], tf[0]);
    iter(IC<3>{}, IC<0>{}, 16, tf[0], tf[1]);
    iter(IC<4>{}, IC<0>{}, 17, tf[1], tf[0]);
    MFMA_DONE();
    STAMP(4);
    WAIT_V(12);
    __builtin_amdgcn_s_setprio(1);
    if (__builtin_expect(!__all((gr[0].y & gr[1].y & gr[2].y & gr[3].y) == 1u), 0)) {
        for (int spin = 0; spin < (1 << 20); ++spin) {
            __builtin_amdgcn_s_sleep(2);
#pragma unroll
            for (int i = 0; i < 4; ++i) gr[i] = __builtin_amdgcn_raw_buffer_load_b64(rg, (unsigned)(((i ^ (2 * wc)) * 16 + (lane & 15)) * 8), 0, 16);
            if (__all((gr[0].y & gr[1].y & gr[2].y & gr[3].y) == 1u)) break;
        }
    }
    const int mt = rt * 2 + (wr >> 1), ks = ct >> 3, lct = ct & 7;
    const rsrc_t rhh = mk_rsrc(ws + WS_HH, S_ * F_ * 2);
    const unsigned hb = (unsigned)(((mt * 4 + ks) * 36) * 8192 + ((wr & 1) * 4) * 1024);
    const LAS char* csb = lds + K3_CS + wid * 1152 + 16 * g4;
    TokC tcs[4];
#pragma unroll
    for (int i = 0; i < 4; ++i) {
        const float sd = bf_hi(gr[i].x), r = __builtin_amdgcn_rcpf(sd);
        tcs[i] = (TokC){-bf_lo(gr[i].x), -2.30220819f * r, -0.10294324f * (r * r * r), sd * sd};
    }
    {
        const LAS char* cm = lds + K3_CS + wid * 1152 + 256 + 16 * (g4 & 1);
        const f32x4 cgm = *(const LAS f32x4*)cm + *(const LAS f32x4*)(cm + 576), cum = *(const LAS f32x4*)(cm + 288) + *(const LAS f32x4*)(cm + 864);
#pragma unroll
        for (int ip = 0; ip < 2; ++ip) {
            const int ia = ((2 * ip) ^ (2 * wc)) + (lane >> 5);
            const bool up = lane >= 32;
            const TokC tc = {up ? tcs[2 * ip + 1].nm : tcs[2 * ip].nm, up ? tcs[2 * ip + 1].c0 : tcs[2 * ip].c0,
                             up ? tcs[2 * ip + 1].c1 : tcs[2 * ip].c1, up ? tcs[2 * ip + 1].q : tcs[2 * ip].q};
            f32x4 gm, um;
#pragma unroll
            for (int r = 0; r < 4; ++r) {
                auto sw = __builtin_amdgcn_permlane32_swap(__float_as_uint(acc[8][2 * ip][r]), __float_as_uint(acc[8][2 * ip + 1][r]), false, false);
                gm[r] = __uint_as_float(sw[0]); um[r] = __uint_as_float(sw[1]);
            }
            const int kgs = (lct & 1) * 2 + wc;
            st8_wt(rhh, hb + (unsigned)((lct >> 1) * 8192 + ia * 1024 + (kgs * 16 + (lane & 15)) * 16 + (g4 & 1) * 8),
                   (u32x2){geglu2(LO2(gm), LO2(um), LO2(cgm), LO2(cum), tc), geglu2(HI2(gm), HI2(um), HI2(cgm), HI2(cum), tc)});
        }
    }
    SCHED();
    constexpr int FLAGPOS = 1;
    const rsrc_t rfb = mk_rsrc(ws + WS_FB, 16384);
    const unsigned fbo = (unsigned)((mt * 4 + ks) * 256 + (lct * 4 + (wr & 1) * 2 + wc) * 4);
    u32x4 ob[4];
#pragma unroll
    for (int jp = 0; jp < 2; ++jp) {
        f32x4 cg[2], cu[2];
#pragma unroll
        for (int e = 0; e < 2; ++e) {
            cg[e] = *(const LAS f32x4*)(csb + (2 * jp + e) * 64) + *(const LAS f32x4*)(csb + 576 + (2 * jp + e) * 64);
            cu[e] = *(const LAS f32x4*)(csb + 288 + (2 * jp + e) * 64) + *(const LAS f32x4*)(csb + 864 + (2 * jp + e) * 64);
        }
#pragma unroll
        for (int i = 0; i < 4; ++i) {
            unsigned o[4];
#pragma unroll
            for (int e = 0; e < 2; ++e) {
                const f32x4 g = acc[2 * jp + e][i], u = acc[4 + 2 * jp + e][i];
                o[2 * e] = geglu2(LO2(g), LO2(u), LO2(cg[e]), LO2(cu[e]), tcs[i]);
                o[2 * e + 1] = geglu2(HI2(g), HI2(u), HI2(cg[e]), HI2(cu[e]), tcs[i]);
            }
            ob[i] = (u32x4){o[0], o[1], o[2], o[3]};
            st16_wt(rhh, (jp == 1 && wid == 7) ? 0x80000000u : hb + (unsigned)(((2 + jp * 8 + lct) * 2 + wc) * 8192 + (i ^ (2 * wc)) * 1024 + lane * 16), ob[i]);
            if (jp == 1 && i == FLAGPOS) {
                SCHED();
                WAIT_VI(FLAGPOS + 1);
                __builtin_amdgcn_raw_buffer_store_b32(1u, rfb, lane == 0 ? fbo : 0x80000000u, 0, 16);
                SCHED();
            }
        }
        SCHED();
        if (jp == 0) __builtin_amdgcn_s_setprio(0);
    }
    STAMP(5);
    if (wid == 7) {
        for (int spin = 0; spin < (1 << 20); ++spin) {
            const unsigned f = __builtin_amdgcn_raw_buffer_load_b32(rfb, (unsigned)((mt2 * 4 + ks2) * 256 + (lane & 31) * 4), 0, 16);
            if (__all(f == 1u)) break;
            __builtin_amdgcn_s_sleep(4);
        }
#pragma unroll
        for (int i = 0; i < 4; ++i) st16_wt(rhh, hb + (unsigned)(((10 + lct) * 2 + wc) * 8192 + (i ^ (2 * wc)) * 1024 + lane * 16), ob[i]);
    }
    BAR();
    STAMP(7);
    if (wid < 4) g2_body<5>(ws, out, lds, lane, wid, mt2, ks2, nt2, w2src, fbo + 128 DIAG_PASS);
    else g2_body<4>(ws, out, lds, lane, wid, mt2, ks2, nt2, w2src, fbo + 128 DIAG_PASS);
    DIAG_END(ws, 1);
}

extern "C" __global__ void k9_end() {}

extern "C" void kernel_launch(void* const* d_in, const int* in_sizes, int n_in, void* d_out, int out_size, void* d_ws,
                              size_t ws_size, hipStream_t stream) {
    const u16* q = (const u16*)d_in[0];
    const u16* k = (const u16*)d_in[1];
    const u16* v = (const u16*)d_in[2];
    const u16* shift_k = (const u16*)d_in[3];
    const u16* ssm_k = (const u16*)d_in[4];
    const u16* ssm_D = (const u16*)d_in[5];
    const u16* wg = (const u16*)d_in[7];
    const u16* wu = (const u16*)d_in[8];
    const u16* wd = (const u16*)d_in[9];
    char* ws = (char*)d_ws;
    (void)hipFuncSetAttribute((const void*)k1_conv, hipFuncAttributeMaxDynamicSharedMemorySize, C1_LDS);
    (void)hipFuncSetAttribute((const void*)k3_gemm1, hipFuncAttributeMaxDynamicSharedMemorySize, K3_LDS);
    k1_conv<<<256, 512, C1_LDS, stream>>>(q, k, v, shift_k, ssm_k, ssm_D, wg, wu, wd, ws, (const u16*)d_out);
    k3_gemm1<<<256, 512, K3_LDS, stream>>>(wg, wu, ws, (u16*)d_out);
}
```

```cpp
#include <hip/hip_runtime.h>
#include <hip/hip_bf16.h>

#define LAS __attribute__((address_space(3)))
typedef unsigned short u16;
typedef short s16x4 __attribute__((ext_vector_type(4)));
typedef short s16x8 __attribute__((ext_vector_type(8)));
typedef float f32x2 __attribute__((ext_vector_type(2)));
typedef float f32x4 __attribute__((ext_vector_type(4)));
typedef unsigned u32x2 __attribute__((ext_vector_type(2)));
typedef unsigned u32x4 __attribute__((ext_vector_type(4)));
typedef __bf16 bf16x2_t __attribute__((ext_vector_type(2)));

constexpr int S_ = 2048, D_ = 576, F_ = 4608;

__device__ __forceinline__ unsigned pk_bf16(float lo, float hi) {
    f32x2 v = {lo, hi};
    bf16x2_t b = __builtin_convertvector(v, bf16x2_t);
    return __builtin_bit_cast(unsigned, b);
}
__device__ __forceinline__ float bf_lo(unsigned w) { return __uint_as_float(w << 16); }
__device__ __forceinline__ float bf_hi(unsigned w) { return __uint_as_float(w & 0xffff0000u); }
__device__ __forceinline__ float rbf(float x) { return bf_lo(pk_bf16(x, 0.f)); }
#define WAIT_V(n) asm volatile("s_waitcnt vmcnt(" #n ")" ::: "memory")
#define BAR() do { __builtin_amdgcn_s_barrier(); asm volatile("" ::: "memory"); } while (0)
#define SCHED() __builtin_amdgcn_sched_barrier(0)
__device__ __forceinline__ void mfma_acc(f32x4& c, s16x8 a, s16x8 b) {
    asm volatile("v_mfma_f32_16x16x32_bf16 %0, %1, %2, %0" : "+v"(c) : "v"(a), "v"(b));
}
__device__ __forceinline__ void mfma_first(f32x4& c, s16x8 a, s16x8 b) {
    asm volatile("v_mfma_f32_16x16x32_bf16 %0, %1, %2, 0" : "=&v"(c) : "v"(a), "v"(b));
}
#define MFMA_DONE() do { asm volatile("s_nop 7\n\ts_nop 7" ::: "memory"); SCHED(); } while (0)
constexpr size_t WS_KV = 0;
constexpr size_t WS_H = WS_KV + (size_t)S_ * D_ * 2;
constexpr size_t WS_ST = WS_H + (size_t)S_ * D_ * 2;
constexpr size_t WS_CS = WS_ST + (size_t)S_ * 8;
constexpr size_t WS_G2 = WS_CS + (size_t)4 * F_ * 4;
constexpr size_t WS_F0 = WS_G2 + (size_t)S_ * 8;
constexpr int FSP = 128;
constexpr size_t WS_EP = WS_F0 + 1280 * FSP;
constexpr size_t WS_HH = WS_EP + 1024;
constexpr size_t WS_X = WS_HH + (size_t)S_ * F_ * 2;
constexpr unsigned X_BYTES = 64 * 4 * 3 * 9216;
constexpr size_t WS_FB = WS_X + X_BYTES;
constexpr size_t WS_WT = WS_FB + 16384;

template <int N> struct IC { static constexpr int v = N; };
typedef __amdgpu_buffer_rsrc_t rsrc_t;
__device__ __forceinline__ rsrc_t mk_rsrc(const void* p, unsigned bytes) { return __builtin_amdgcn_make_buffer_rsrc((void*)p, 0, bytes, 0x00020000); }
__device__ __forceinline__ void st16_wt(rsrc_t r, unsigned off, u32x4 v) { __builtin_amdgcn_raw_buffer_store_b128(v, r, off, 0, 16); }
__device__ __forceinline__ void st8_wt(rsrc_t r, unsigned off, u32x2 v) { __builtin_amdgcn_raw_buffer_store_b64(v, r, off, 0, 16); }
template <int CTRL> __device__ __forceinline__ float dpp_add(float x) {
    return x + __uint_as_float(__builtin_amdgcn_update_dpp(0, (int)__float_as_uint(x), CTRL, 0xf, 0xf, true));
}
__device__ __forceinline__ float x16_add(float x) {
    auto r = __builtin_amdgcn_permlane16_swap(__float_as_uint(x), __float_as_uint(x), false, false);
    return __uint_as_float(r[0]) + __uint_as_float(r[1]);
}
__device__ __forceinline__ float x32_add(float x) {
    auto r = __builtin_amdgcn_permlane32_swap(__float_as_uint(x), __float_as_uint(x), false, false);
    return __uint_as_float(r[0]) + __uint_as_float(r[1]);
}
__device__ __forceinline__ float sum16(float x) {
    x = dpp_add<0xB1>(x); x = dpp_add<0x4E>(x); x = dpp_add<0x141>(x);
    return dpp_add<0x140>(x);
}
__device__ __forceinline__ float ln_sd(float var) { return rbf(__builtin_amdgcn_sqrtf(rbf(rbf(var) + 1e-5f))); }

template <int OFF>
__device__ __forceinline__ void dma16(unsigned voff, const void* sbase, unsigned m0v) {
    asm volatile("s_mov_b32 m0, %2\n\ts_nop 0\n\tglobal_load_lds_dwordx4 %0, %1 offset:%3"
                 :: "v"(voff), "s"(sbase), "s"(m0v), "i"(OFF) : "memory");
}
template <int OFF>
__device__ __forceinline__ void dma16_sc1(unsigned voff, const void* sbase, unsigned m0v) {
    asm volatile("s_mov_b32 m0, %2\n\ts_nop 0\n\tglobal_load_lds_dwordx4 %0, %1 offset:%3 sc1"
                 :: "v"(voff), "s"(sbase), "s"(m0v), "i"(OFF) : "memory");
}
template <int OFF>
__device__ __forceinline__ void dma16_q(unsigned voff, const void* sbase, unsigned m0v) {
    asm volatile("s_mov_b32 m0, %2\n\ts_mov_b64 exec, 0xffff\n\tglobal_load_lds_dwordx4 %0, %1 offset:%3\n\ts_mov_b64 exec, -1"
                 :: "v"(voff), "s"(sbase), "s"(m0v), "i"(OFF) : "memory");
}
__device__ __forceinline__ void dma_m36(unsigned voff, const void* sbase, unsigned m0v) {
    asm volatile("s_mov_b32 m0, %2\n\ts_mov_b32 exec_hi, 15\n\tglobal_load_lds_dwordx4 %0, %1\n\ts_mov_b64 exec, -1"
                 :: "v"(voff), "s"(sbase), "s"(m0v) : "memory");
}
__device__ __forceinline__ void dma4(unsigned voff, const void* sbase, unsigned m0v) {
    asm volatile("s_mov_b32 m0, %2\n\ts_nop 0\n\tglobal_load_lds_dword %0, %1" :: "v"(voff), "s"(sbase), "s"(m0v) : "memory");
}
__device__ __forceinline__ void dma4_sc1(unsigned voff, const void* sbase, unsigned m0v) {
    asm volatile("s_mov_b32 m0, %2\n\ts_nop 0\n\tglobal_load_lds_dword %0, %1 sc1" :: "v"(voff), "s"(sbase), "s"(m0v) : "memory");
}
#define SYM_ADDR(out, sym) asm volatile("s_getpc_b64 s[40:41]\n\ts_add_u32 s40, s40, " #sym "@rel32@lo+4\n\ts_addc_u32 s41, s41, " #sym "@rel32@hi+12\n\ts_mov_b64 %0, s[40:41]" \
                                        : "=s"(out) :: "s40", "s41", "scc")
template <int NI>
__device__ __forceinline__ void warm_code(const char* a, const char* e, int l0, int lane, unsigned m0v) {
    const unsigned last = e > a ? (unsigned)(e - a) - 4u : 0u;
#pragma unroll
    for (int i = 0; i < NI; ++i) dma4(min((unsigned)(l0 + i * 64 + lane) * 128u, last), a, m0v);
}
__device__ __forceinline__ s16x8 tr_frag(const LAS char* p) {
    const s16x4 lo = __builtin_amdgcn_ds_read_tr16_b64_v4i16((LAS s16x4*)p);
    const s16x4 hi = __builtin_amdgcn_ds_read_tr16_b64_v4i16((LAS s16x4*)(p + 16 * 288));
    return __builtin_shufflevector(lo, hi, 0, 1, 2, 3, 4, 5, 6, 7);
}
__device__ __forceinline__ void wd_unit_load(const u16* wd, int un, int lane, unsigned m0v) {
    const int uB = un / 3, ut = un % 3, kh = uB & 1, st = (uB >> 1) % 18, kn = uB / 36;
    const bool full = st >= 2;
    const int f0 = (kn >> 2) * 1152 + (full ? ((st - 2) & 7) * 144 + kh * 72 + ((st - 2) >> 3) * 32 : (st * 2 + kh) * 288);
    const char* src = (const char*)wd + (size_t)f0 * (D_ * 2) + (kn & 3) * 288 + ut * 96;
#pragma unroll
    for (int i = 0; i < 3; ++i) {
        const int q = lane + 64 * i, r = q / 6, pos = q % 6, kg = (r & 15) >> 2;
        const int f = full ? r : (kg >> 1) * 144 + (kg & 1) * 72 + 64 + (r & 3) + 4 * (r >> 4);
        dma16<0>((unsigned)(f * (D_ * 2) + pos * 16), src, m0v + i * 1024);
    }
}
__device__ __forceinline__ void wd_unit_store(char* ws, int un, int lane, const LAS char* l) {
    const rsrc_t r = mk_rsrc(ws + WS_WT, F_ * D_ * 2);
#pragma unroll
    for (int t = 0; t < 3; ++t) {
        const LAS char* tp = l + (lane >> 2) * 96 + (lane & 3) * 8 + t * 32;
        const s16x4 lo = __builtin_amdgcn_ds_read_tr16_b64_v4i16((LAS s16x4*)tp), hi = __builtin_amdgcn_ds_read_tr16_b64_v4i16((LAS s16x4*)(tp + 16 * 96));
        st16_wt(r, (unsigned)(un * 3072 + t * 1024 + lane * 16), __builtin_bit_cast(u32x4, __builtin_shufflevector(lo, hi, 0, 1, 2, 3, 4, 5, 6, 7)));
    }
}
constexpr int WU4 = 3;
constexpr int HELPER_DELAY = 48, HELPER_DELAY2 = 24, POLL0 = 16, POLL1 = 1;
constexpr int C1_COPY = 4288;
constexpr int C1_FILT = 4 * C1_COPY;
constexpr int C1_KV = 2304 * 2;
constexpr int C1_KVOFF = 4 * C1_FILT + 64;
constexpr int C1_YOFF = C1_KVOFF + 4 * C1_KV;
constexpr int C1_XOFF = C1_YOFF + 16384;
constexpr int C1_SOFF = C1_XOFF + 16384;
constexpr int C1_TOFF = C1_SOFF + 16384;
constexpr int C1_WOFF = C1_TOFF + 2048;
constexpr int C1_LDS = C1_WOFF + 24576;

extern "C" __global__ __launch_bounds__(512, 2) void k1_conv(const u16* __restrict__ x, const u16* __restrict__ kin, const u16* __restrict__ vin,
                                                  const u16* __restrict__ shift_k, const u16* __restrict__ ssm_k, const u16* __restrict__ ssm_D,
                                                  const u16* __restrict__ wg, const u16* __restrict__ wu, const u16* __restrict__ wd,
                                                  char* __restrict__ ws) {
    extern __shared__ __attribute__((aligned(16))) char smem[];
    const int tid = threadIdx.x, lane = tid & 63;
    const int wid = __builtin_amdgcn_readfirstlane(tid >> 6);
    const int b = blockIdx.x, xcd = b & 7, idx = b >> 3;
    {
        const char *c1, *c3;
        SYM_ADDR(c1, k1_conv); SYM_ADDR(c3, k3_gemm1);
        if (wid >= 4) warm_code<1>(c1, c3, (wid & 3) * 64, lane, (unsigned)(size_t)(LAS char*)smem + C1_TOFF + 256 + (wid & 3) * 256);
    }
    asm volatile("" :: "s"(x), "s"(kin), "s"(vin), "s"(shift_k), "s"(ssm_k), "s"(ssm_D), "s"(wg), "s"(wu), "s"(wd), "s"(ws));
    const bool conv = idx < 18;
    const int cg = xcd * 18 + idx, ch0 = cg * 4;
    const unsigned lds0 = (unsigned)(size_t)(LAS char*)smem;
    LAS unsigned* tagp = (LAS unsigned*)(smem + C1_TOFF);
    auto get_tag = [&]() -> unsigned { BAR(); return __builtin_amdgcn_readfirstlane(*(volatile LAS unsigned*)tagp); };

    const int h = (idx - 18) * 8 + xcd, hc_ = h < 108 ? h : 0;
    const int m3 = hc_ % 3, lcA = (hc_ / 3) * 4 + m3, rb0 = m3 * 3;
    constexpr int HU = (1728 - 144 * WU4 + 111) / 112;
    const int hbase = 144 * WU4 + h * HU, hun = hbase + wid * 2, hend = min(hbase + HU, 1728);
    auto cs_load = [&](u32x4 (&v)[12]) {
        const int rs = tid >> 3, c8 = tid & 7;
#pragma unroll
        for (int p = 0; p < 12; ++p) {
            const int rb = rb0 + p, lc = lcA + (rb >= 9), r = (rb >= 9 ? rb - 9 : rb) * 64 + rs;
            v[p] = *(const u32x4*)((lc >= 72 ? wu : wg) + (size_t)r * F_ + (lc % 72) * 64 + c8 * 8);
        }
        SCHED();
#pragma unroll
        for (int u = 0; u < 2; ++u) if (hun + u < hend) wd_unit_load(wd, hun + u, lane, lds0 + 16384 + (wid * 2 + u) * 3072);
        st16_wt(mk_rsrc(ws + WS_FB, 16384), (unsigned)(h * 8192 + tid * 16), (u32x4){0u, 0u, 0u, 0u});
        SCHED();
    };
    auto cs_finish = [&](u32x4 (&v)[12]) {
        float g[4][8];
#pragma unroll
        for (int q = 0; q < 4; ++q) {
#pragma unroll
            for (int e = 0; e < 4; ++e) {
                g[q][2 * e] = (bf_lo(v[3 * q][e]) + bf_lo(v[3 * q + 1][e])) + bf_lo(v[3 * q + 2][e]);
                g[q][2 * e + 1] = (bf_hi(v[3 * q][e]) + bf_hi(v[3 * q + 1][e])) + bf_hi(v[3 * q + 2][e]);
            }
        }
        LAS float* red = (LAS float*)smem;
#pragma unroll
        for (int l = 0; l < 2; ++l) {
            float a[8];
#pragma unroll
            for (int e = 0; e < 8; ++e) {
                a[e] = l == 0 ? g[0][e] + (m3 <= 1 ? g[1][e] : 0.f) + (m3 == 0 ? g[2][e] : 0.f) : g[3][e] + (m3 >= 1 ? g[2][e] : 0.f) + (m3 == 2 ? g[1][e] : 0.f);
                a[e] = x32_add(x16_add(dpp_add<0x128>(a[e])));
            }
            if (lane < 8) {
                *(LAS f32x4*)(red + (wid * 2 + l) * 64 + lane * 8) = (f32x4){a[0], a[1], a[2], a[3]};
                *(LAS f32x4*)(red + (wid * 2 + l) * 64 + lane * 8 + 4) = (f32x4){a[4], a[5], a[6], a[7]};
            }
        }
        WAIT_V(1);
#pragma unroll
        for (int u = 0; u < 2; ++u) if (hun + u < hend) wd_unit_store(ws, hun + u, lane, (const LAS char*)smem + 16384 + (wid * 2 + u) * 3072);
        asm volatile("s_waitcnt lgkmcnt(0)" ::: "memory"); BAR();
        if (tid < 192 && h < 108) {
            float s = 0.f;
            if (tid < 128) {
#pragma unroll
                for (int w = 0; w < 8; ++w) s += red[w * 128 + tid];
            }
            const int part = tid >> 6;
            const int lc = part == 0 ? lcA : (part == 1 ? lcA + 1 : (m3 == 0 ? lcA : lcA + 1));
            const int slot = part == 0 ? (m3 != 0) : (part == 1 ? 0 : 1);
            if (part < 2 || m3 != 1)
                __builtin_amdgcn_raw_buffer_store_b32(__float_as_uint(s), mk_rsrc(ws + WS_CS, 4 * F_ * 4), (unsigned)((slot * 2 * F_ + lc * 64 + (tid & 63)) * 4), 0, 16);
        }
    };
    auto wait_kv = [&](unsigned tag) {
        const rsrc_t rf = mk_rsrc(ws + WS_F0 + ((idx >> 2) * 8 + xcd) * 32 * FSP, 32 * FSP);
        __builtin_amdgcn_s_sleep(POLL0);
        for (int spin = 0; spin < (1 << 20); ++spin) {
            const unsigned f = __builtin_amdgcn_raw_buffer_load_b32(rf, (lane & 31) * FSP, 0, 16);
            if (__all(f == tag)) break;
            __builtin_amdgcn_s_sleep(POLL1);
        }
    };

    auto x_rows = [&]() {
        u32x2 xr[8];
#pragma unroll
        for (int i = 0; i < 8; ++i) xr[i] = *(const u32x2*)(x + (size_t)((tid - 256) + (wid - 4) * 448 + i * 64) * D_ + ch0);
#pragma unroll
        for (int i = 0; i < 8; ++i) *(LAS u32x2*)(smem + C1_XOFF + ((tid - 256) + (wid - 4) * 448 + i * 64) * 8) = xr[i];
        SCHED();
    };
    unsigned tag;
    unsigned dvv = 0;
    auto upper = [&](auto is_stat, auto is_conv) {
        constexpr bool STAT = decltype(is_stat)::v, CONV = decltype(is_conv)::v;
        const int srow = b * 8 + (wid - 5) * 4 + (lane >> 4), t3 = tid - 320;
        unsigned xw[18], epv = 0;
        if constexpr (STAT) {
            const unsigned* xr = (const unsigned*)(x + (size_t)srow * D_) + (lane & 15);
#pragma unroll
            for (int j = 0; j < 18; ++j) xw[j] = xr[16 * j];
        } else {
            epv = __hip_atomic_load((const unsigned*)(ws + WS_EP), __ATOMIC_RELAXED, __HIP_MEMORY_SCOPE_AGENT);
        }
        SCHED();
        if constexpr (STAT) {
            const char *c3, *c4;
            SYM_ADDR(c3, k3_gemm1); SYM_ADDR(c4, k9_end);
            warm_code<1>(c3, c4, ((idx & 3) * 2 + wid - 5) * 64, lane, lds0 + C1_TOFF + 256 + (wid - 5) * 256);
        }
        SCHED();
        u32x4 v[12], fa[6]; u32x2 fb[6];
        if constexpr (CONV) {
#pragma unroll
            for (int i = 0; i < 6; ++i) {
                const int task = min(t3 + 192 * i, 1023), u = task & 255;
                const u16* src = ssm_k + (size_t)(ch0 + (task >> 8)) * S_ + u * 8;
                fa[i] = *(const u32x4*)src;
                fb[i] = *(const u32x2*)(src + (u < 255 ? 8 : 0));
            }
            SCHED();
        }
        if constexpr (STAT) {
            f32x2 xf[18], s = {0.f, 0.f};
#pragma unroll
            for (int j = 0; j < 18; ++j) { xf[j] = (f32x2){bf_lo(xw[j]), bf_hi(xw[j])}; s += xf[j]; }
            const float mean = sum16(s.x + s.y) * (1.f / D_);
            f32x2 q = {0.f, 0.f};
#pragma unroll
            for (int j = 0; j < 18; ++j) { const f32x2 d = xf[j] - mean; q = __builtin_elementwise_fma(d, d, q); }
            const unsigned sgr = pk_bf16(mean, ln_sd(sum16(q.x + q.y) * (1.f / D_)));
            tag = get_tag();
            const unsigned so = (lane & 15) == 0 ? (unsigned)srow * 8 : 0x80000000u;
            st8_wt(mk_rsrc(ws + WS_ST, S_ * 8), so, (u32x2){sgr, tag});
            st8_wt(mk_rsrc(ws + WS_G2, S_ * 8), so, (u32x2){0u, 0u});
        } else {
            *tagp = epv + 1u;
            asm volatile("s_waitcnt lgkmcnt(0)" ::: "memory");
            tag = get_tag();
        }
        SCHED();
        if constexpr (!CONV) {
            __builtin_amdgcn_s_sleep(HELPER_DELAY);
            cs_load(v);
            if (!STAT && h == 111) {
                const rsrc_t rf = mk_rsrc(ws + WS_F0, 256 * FSP);
                for (int spin = 0; spin < (1 << 20); ++spin) {
                    bool ok = true;
#pragma unroll
                    for (int i = 0; i < 4; ++i) ok &= __builtin_amdgcn_raw_buffer_load_b32(rf, (i * 64 + lane) * FSP, 0, 16) == tag;
                    if (__all(ok)) break;
                    __builtin_amdgcn_s_sleep(8);
                }
                if (lane == 0) __hip_atomic_store((unsigned*)(ws + WS_EP), tag, __ATOMIC_RELAXED, __HIP_MEMORY_SCOPE_AGENT);
            }
            cs_finish(v);
            return;
        }
        if (t3 < 80) {
            const int cpy = t3 / 5, e = t3 % 5, q = e < 3 ? e : 257 + e;
            *(LAS u32x4*)(smem + cpy * C1_COPY + q * 16) = (u32x4){0u, 0u, 0u, 0u};
        } else if (t3 < 84) {
            *(LAS u32x4*)(smem + C1_KVOFF - 64 + (t3 - 80) * 16) = (u32x4){0u, 0u, 0u, 0u};
        }
        if (t3 < 128) *(LAS u32x4*)(smem + C1_KVOFF + (t3 >> 5) * C1_KV + 4096 + (t3 & 31) * 16) = (u32x4){0u, 0u, 0u, 0u};
#pragma unroll
        for (int i = 0; i < 6; ++i) {
            const int task = min(t3 + 192 * i, 1023), u = task & 255;
            const unsigned d[6] = {fa[i].x, fa[i].y, fa[i].z, fa[i].w, u < 255 ? fb[i].x : 0u, u < 255 ? fb[i].y : 0u};
            unsigned o[5];
#pragma unroll
            for (int e = 0; e < 5; ++e) o[e] = __builtin_amdgcn_alignbit(d[e + 1], d[e], 16);
            LAS char* base = (LAS char*)smem + (task >> 8) * C1_FILT + (u + 4) * 16;
            *(LAS u32x4*)(base) = (u32x4){o[0], o[1], o[2], o[3]};
            *(LAS u32x4*)(base + C1_COPY) = (u32x4){d[1], d[2], d[3], d[4]};
            *(LAS u32x4*)(base + 2 * C1_COPY) = (u32x4){o[1], o[2], o[3], o[4]};
            *(LAS u32x4*)(base + 3 * C1_COPY) = (u32x4){d[2], d[3], d[4], d[5]};
            if (u == 0) {
                *(LAS u32x4*)(base - 16) = (u32x4){0u, 0u, 0u, d[0] << 16};
                *(LAS u32x4*)(base - 16 + C1_COPY) = (u32x4){0u, 0u, 0u, d[0]};
                *(LAS u32x4*)(base - 16 + 2 * C1_COPY) = (u32x4){0u, 0u, d[0] << 16, o[0]};
                *(LAS u32x4*)(base - 16 + 3 * C1_COPY) = (u32x4){0u, 0u, d[0], d[1]};
            }
        }
        asm volatile("s_waitcnt lgkmcnt(0)" ::: "memory"); BAR();
        x_rows();
    };
    if (wid >= 5) {
        if (!conv) {
            if (wid < 7) upper(IC<1>{}, IC<0>{}); else upper(IC<0>{}, IC<0>{});
            return;
        }
        if (wid < 7) upper(IC<1>{}, IC<1>{}); else upper(IC<0>{}, IC<1>{});
    } else {
        {
            unsigned kw[11], vw[8];
            const int t0 = idx * 64 + (lane >> 3) * 8, dc = wid * 8 + (lane & 7), kch = xcd * 72 + (dc < 36 ? dc : 35) * 2;
            const rsrc_t rk = mk_rsrc(kin, S_ * D_ * 2), rv = mk_rsrc(vin, S_ * D_ * 2);
            const int off = t0 * (D_ * 2) + kch * 2;
#pragma unroll
            for (int j = 0; j < 11; ++j) kw[j] = __builtin_amdgcn_raw_buffer_load_b32(rk, (unsigned)(off + (j - 3) * (D_ * 2)), 0, 0);
#pragma unroll
            for (int j = 0; j < 8; ++j) vw[j] = __builtin_amdgcn_raw_buffer_load_b32(rv, (unsigned)(off + j * (D_ * 2)), 0, 0);
            const u32x4 sw = *(const u32x4*)(shift_k + kch * 4);
            dvv = ssm_D[(conv ? ch0 : 0) + (wid & 3)];
            SCHED();
            const f32x2 w0 = {bf_lo(sw.x), bf_lo(sw.z)}, w1 = {bf_hi(sw.x), bf_hi(sw.z)}, w2 = {bf_lo(sw.y), bf_lo(sw.w)}, w3 = {bf_hi(sw.y), bf_hi(sw.w)};
            f32x2 kf[11], o[8];
#pragma unroll
            for (int j = 0; j < 11; ++j) kf[j] = (f32x2){bf_lo(kw[j]), bf_hi(kw[j])};
#pragma unroll
            for (int j = 0; j < 8; ++j) {
                f32x2 s = kf[j] * w3;
                s = __builtin_elementwise_fma(kf[j + 1], w2, s);
                s = __builtin_elementwise_fma(kf[j + 2], w1, s);
                s = __builtin_elementwise_fma(kf[j + 3], w0, s);
                o[j] = s * (f32x2){bf_lo(vw[j]), bf_hi(vw[j])};
            }
            const rsrc_t ro = mk_rsrc(ws + WS_KV, S_ * D_ * 2);
            const unsigned oo = dc < 36 ? (unsigned)(kch * (S_ * 2) + (2040 - t0) * 2) : 0x80000000u;
            st16_wt(ro, oo, (u32x4){pk_bf16(o[7].x, o[6].x), pk_bf16(o[5].x, o[4].x), pk_bf16(o[3].x, o[2].x), pk_bf16(o[1].x, o[0].x)});
            st16_wt(ro, oo + S_ * 2, (u32x4){pk_bf16(o[7].y, o[6].y), pk_bf16(o[5].y, o[4].y), pk_bf16(o[3].y, o[2].y), pk_bf16(o[1].y, o[0].y)});
        }
        tag = get_tag();
        WAIT_V(0);
        if (lane == 0) __hip_atomic_store((unsigned*)(ws + WS_F0 + ((wid * 8 + xcd) * 32 + idx) * FSP), tag, __ATOMIC_RELAXED, __HIP_MEMORY_SCOPE_AGENT);
        if (!conv) {
            u32x4 v[12];
            __builtin_amdgcn_s_sleep(HELPER_DELAY2);
            cs_load(v);
            cs_finish(v);
            return;
        }
        if (wid == 4) {
            x_rows();
            BAR();
#pragma unroll 1
            for (int u = 0; u < WU4; ++u) wd_unit_load(wd, cg * WU4 + u, lane, lds0 + C1_WOFF + u * 3072);
            __builtin_amdgcn_s_sleep(24);
            const char* gs = ws + WS_ST;
#pragma unroll
            for (int i = 0; i < 4; ++i) {
                dma16_sc1<0>(lane * 16, gs + i * 4096, lds0 + C1_SOFF + i * 4096); dma16_sc1<1024>(lane * 16, gs + i * 4096, lds0 + C1_SOFF + i * 4096);
                dma16_sc1<2048>(lane * 16, gs + i * 4096, lds0 + C1_SOFF + i * 4096); dma16_sc1<3072>(lane * 16, gs + i * 4096, lds0 + C1_SOFF + i * 4096);
            }
            WAIT_V(0);
#pragma unroll 1
            for (int u = 0; u < WU4; ++u) wd_unit_store(ws, cg * WU4 + u, lane, (const LAS char*)smem + C1_WOFF + u * 3072);
        } else {
        {
            wait_kv(tag);
            const char* kvs = ws + WS_KV + (size_t)(ch0 + wid) * (S_ * 2);
            const unsigned m = lds0 + C1_KVOFF + wid * C1_KV;
            dma16_sc1<0>(lane * 16, kvs, m); dma16_sc1<1024>(lane * 16, kvs, m); dma16_sc1<2048>(lane * 16, kvs, m); dma16_sc1<3072>(lane * 16, kvs, m);
            WAIT_V(0);
            BAR();
        }
        const int row = lane & 15, kg = lane >> 4;
        const LAS char* abase = (const LAS char*)smem + wid * C1_FILT + (row & 3) * C1_COPY + (24 + 4 * (row >> 2) - 8 * kg) * 2;
        const LAS char* bbase = (const LAS char*)smem + C1_KVOFF + wid * C1_KV + (2040 - 16 * row - 8 * kg) * 2 - 512 * 7 - 448;
        auto rd_ah = [&](int r, int L, int hf) -> s16x4 { return *(const volatile LAS s16x4*)(abase + 512 * L - 64 * r + 8 * hf); };
        auto rd_b = [&](int r, int m) -> s16x8 { return *(const LAS s16x8*)(bbase + 448 - 64 * r + 512 * (7 - m)); };
        auto cat = [&](s16x4 lo, s16x4 hi) -> s16x8 { return __builtin_shufflevector(lo, hi, 0, 1, 2, 3, 4, 5, 6, 7); };
        f32x4 acc[8];
        s16x4 al[2][8], ah[2][8];
        s16x8 bf[2][8], bd[8];
        auto rd_k = [&](int r, int k) {
            if (k < 2) { if (k == 0) al[r & 1][0] = rd_ah(r, 1, 0); else ah[r & 1][0] = rd_ah(r, 1, 1); }
            else if (k < 10) bf[r & 1][9 - k] = rd_b(r, 8 - k);
            else { const int L = (k - 10) / 2 + 2; if (k & 1) ah[r & 1][L - 1] = rd_ah(r, L, 1); else al[r & 1][L - 1] = rd_ah(r, L, 0); }
        };
        const s16x8 a0 = cat(rd_ah(0, 0, 0), rd_ah(0, 0, 1));
#pragma unroll
        for (int e = 0; e < 8; ++e) bd[e] = rd_b(0, e);
        s16x8 dfr;
        {
            const int j = 8 * kg + 7 - row;
            u32x4 w;
#pragma unroll
            for (int e = 0; e < 4; ++e) w[e] = j == 2 * e ? dvv : (j == 2 * e + 1 ? dvv << 16 : 0u);
            dfr = __builtin_bit_cast(s16x8, w);
            asm volatile("s_nop 1" : "+v"(dfr));
        }
        SCHED();
#pragma unroll
        for (int n = 0; n < 16; ++n) {
            if (n < 8) mfma_first(acc[n], a0, bd[n]); else mfma_acc(acc[n - 8], dfr, bd[n - 8]);
            SCHED();
            if (n == 0) { rd_k(0, 0); rd_k(0, 1); }
            else if (n == 1) rd_k(0, 9);
            else rd_k(0, n + 8);
            SCHED();
        }
#pragma unroll
        for (int r = 0; r < 8; ++r) {
            int k = 0;
#pragma unroll
            for (int L = 1; L <= 8; ++L) {
#pragma unroll
                for (int n = 7; n >= L - 1; --n) {
                    const int e = n - L + 1;
                    mfma_acc(acc[n], cat(al[r & 1][L - 1], ah[r & 1][L - 1]), (r == 0 && e > 0) ? bd[e - 1] : bf[r & 1][e]);
                    SCHED();
                    if (r < 7 && k < 24) rd_k(r + 1, k);
                    ++k;
                    SCHED();
                }
            }
        }
        MFMA_DONE();
#pragma unroll
        for (int n = 0; n < 8; ++n)
            *(LAS u32x2*)(smem + C1_YOFF + wid * 4096 + (256 * n + 16 * row + 4 * kg) * 2) = (u32x2){pk_bf16(acc[n][0], acc[n][1]), pk_bf16(acc[n][2], acc[n][3])};
        }
    }
    asm volatile("s_waitcnt lgkmcnt(0)" ::: "memory"); BAR();
    {
        u32x2 yc[4];
#pragma unroll
        for (int c = 0; c < 4; ++c) yc[c] = *(const LAS u32x2*)(smem + C1_YOFF + c * 4096 + tid * 8);
        const u32x4 xa = *(const LAS u32x4*)(smem + C1_XOFF + tid * 32), xb = *(const LAS u32x4*)(smem + C1_XOFF + tid * 32 + 16);
        const u32x2 xr[4] = {{xa.x, xa.y}, {xa.z, xa.w}, {xb.x, xb.y}, {xb.z, xb.w}};
        u32x4 s01 = *(const LAS u32x4*)(smem + C1_SOFF + tid * 32), s23 = *(const LAS u32x4*)(smem + C1_SOFF + tid * 32 + 16);
        if (__builtin_expect(!__all(s01.y == tag && s01.w == tag && s23.y == tag && s23.w == tag), 0)) {
            const rsrc_t rs = mk_rsrc(ws + WS_ST, S_ * 8);
            for (int spin = 0; spin < (1 << 20); ++spin) {
                s01 = __builtin_amdgcn_raw_buffer_load_b128(rs, tid * 32, 0, 16);
                s23 = __builtin_amdgcn_raw_buffer_load_b128(rs, tid * 32 + 16, 0, 16);
                if (__all(s01.y == tag && s01.w == tag && s23.y == tag && s23.w == tag)) break;
                __builtin_amdgcn_s_sleep(2);
            }
        }
        const unsigned sv[4] = {s01.x, s01.z, s23.x, s23.z};
        float mm[4], rr[4];
#pragma unroll
        for (int j = 0; j < 4; ++j) { mm[j] = bf_lo(sv[j]); rr[j] = __builtin_amdgcn_rcpf(bf_hi(sv[j])); }
        unsigned hw[8];
#pragma unroll
        for (int j = 0; j < 4; ++j) {
#pragma unroll
            for (int e = 0; e < 2; ++e) {
                const unsigned xw = e ? xr[j].y : xr[j].x;
                const unsigned ya = (j >> 1) ? yc[2 * e].y : yc[2 * e].x, yb = (j >> 1) ? yc[2 * e + 1].y : yc[2 * e + 1].x;
                const float y0 = (j & 1) ? bf_hi(ya) : bf_lo(ya), y1 = (j & 1) ? bf_hi(yb) : bf_lo(yb);
                const float x0 = bf_lo(xw), x1 = bf_hi(xw);
                hw[2 * j + e] = pk_bf16(__builtin_fmaf((x0 - mm[j]) * rr[j], y0, x0), __builtin_fmaf((x1 - mm[j]) * rr[j], y1, x1));
            }
        }
        const rsrc_t rh = mk_rsrc(ws + WS_H, S_ * D_ * 2);
        const unsigned ho = (unsigned)(((tid >> 6) * 144 + cg) * 2048 + (tid & 63) * 32);
        st16_wt(rh, ho, (u32x4){hw[0], hw[1], hw[2], hw[3]});
        st16_wt(rh, ho + 16, (u32x4){hw[4], hw[5], hw[6], hw[7]});
    }
}

constexpr int ST_BYTES = 16384 + 2 * 9216;
constexpr int G_LDS = 4 * ST_BYTES;

struct TokC { float nm, c0, c1, q; };
__device__ __forceinline__ unsigned geglu2(f32x2 ag, f32x2 au, f32x2 csg, f32x2 csu, const TokC& t) {
    const f32x2 nm = {t.nm, t.nm}, c0 = {t.c0, t.c0}, c1 = {t.c1, t.c1}, q = {t.q, t.q};
    const f32x2 g = __builtin_elementwise_fma(nm, csg, ag), u = __builtin_elementwise_fma(nm, csu, au);
    const f32x2 z = g * __builtin_elementwise_fma(g * g, c1, c0);
    const f32x2 e = {__builtin_amdgcn_exp2f(z.x), __builtin_amdgcn_exp2f(z.y)};
    const f32x2 d = __builtin_elementwise_fma(e, q, q);
    const f32x2 r = (g * u) * (f32x2){__builtin_amdgcn_rcpf(d.x), __builtin_amdgcn_rcpf(d.y)};
    return pk_bf16(r.x, r.y);
}
#define LO2(v) __builtin_shufflevector(v, v, 0, 1)
#define HI2(v) __builtin_shufflevector(v, v, 2, 3)
#define WAIT_VI(N) asm volatile("s_waitcnt vmcnt(%0)" :: "i"(N) : "memory")

constexpr int G2_W0 = 71680, G2_FL = 69632;
__device__ __forceinline__ void g2_wpiece(int P, int s, const char* wsrc, unsigned aoff, unsigned mw0) {
    const unsigned m = mw0 + (s % 5) * 18432;
    if (P == 0) dma16<0>(aoff, wsrc + (size_t)s * 18432, m);
    if (P == 1) dma16<1024>(aoff, wsrc + (size_t)s * 18432, m);
    if (P == 2) dma16_q<2048>(aoff, wsrc + (size_t)s * 18432, m);
}
constexpr int g2_n(int S) { return (S + 3 < 18 ? 2 : 0) + (S + 4 < 18 ? 3 : 0) + (S == 4 ? 1 : 0); }
constexpr int g2_vm(int S) {
    return S == 0 ? 3 : (S == 1 ? 1 + g2_n(0) : g2_n(S - 2) - (S + 1 < 18 ? 2 : 0) + g2_n(S - 1));
}
template <int NT>
__device__ __forceinline__ void g2_body(char* __restrict__ ws, u16* __restrict__ out, LAS char* lds, int lane, int wid, int mt, int ks, int nt,
                                        const char* wsrc, unsigned flag2) {
    constexpr int dh = NT == 4;
    const unsigned lds0 = (unsigned)(size_t)lds;
    const int wr = wid & 3;
    const char* asrc = ws + WS_HH + ((size_t)(mt * 4 + ks) * 36) * 8192 + wid * 2048;
    const unsigned aoff = lane * 16;
    const unsigned ma0 = lds0 + wid * 2048, mw0 = lds0 + G2_W0 + wid * 2304;
    auto hpiece = [&](int P, int s) {
        if (P == 0) dma16_sc1<0>(aoff, asrc + (size_t)s * 16384, ma0 + (s & 3) * 16384);
        if (P == 1) dma16_sc1<1024>(aoff, asrc + (size_t)s * 16384, ma0 + (s & 3) * 16384);
    };
    hpiece(0, 0); hpiece(1, 0); hpiece(0, 1); hpiece(1, 1); hpiece(0, 2); hpiece(1, 2);

    const int g4 = lane >> 4;
    const int toff = wr * 2048 + lane * 16;
    const int foff = G2_W0 + dh * 5120 + lane * 16;
    auto rdf = [&](const LAS char* p) -> s16x8 { return *(const LAS s16x8*)p; };
    const rsrc_t rfb = mk_rsrc(ws + WS_FB, 16384);

    f32x4 acc[NT][2];
    s16x8 wf[2 * NT], tf[2][4];
    WAIT_V(4);
    __builtin_amdgcn_raw_buffer_store_b32(1u, rfb, lane == 0 ? flag2 : 0x80000000u, 0, 16);
    BAR();
#pragma unroll
    for (int e = 0; e < 4; ++e) tf[0][e] = rdf(lds + toff + (e >> 1) * 8192 + (e & 1) * 1024);
#pragma unroll
    for (int e = 0; e < 2 * NT; ++e) wf[e] = rdf(lds + foff + (e / NT) * 9216 + (e % NT) * 1024);
    SCHED();

    auto iter = [&](auto sc, s16x8 (&tc)[4], s16x8 (&tn)[4]) {
        constexpr int S = decltype(sc)::v;
        if constexpr (S < 17) {
            WAIT_VI(g2_vm(S));
            BAR();
            if constexpr (S == 7) {
                unsigned fb2 = *(const volatile LAS unsigned*)(lds + G2_FL + wid * 256 + lane * 4);
                if (__builtin_expect(!__all(fb2 == 1u), 0)) {
                    for (int spin = 0; spin < (1 << 20); ++spin) {
                        fb2 = __builtin_amdgcn_raw_buffer_load_b32(rfb, (unsigned)(((mt * 4 + ks) * 2 + 1) * 128 + (lane & 31) * 4), 0, 16);
                        if (__all(fb2 == 1u)) break;
                        __builtin_amdgcn_s_sleep(2);
                    }
                }
            }
            SCHED();
        }
        const LAS char* tb = lds + ((S + 1) & 3) * 16384;
        const LAS char* wb = lds + ((S + 1) % 5) * 18432;
#pragma unroll
        for (int e = 0; e < 2 * NT; ++e) {
#pragma unroll
            for (int i = 0; i < 2; ++i) {
                if (S == 0 && e < NT) mfma_first(acc[e % NT][i], wf[e], tc[(e / NT) * 2 + i]);
                else mfma_acc(acc[e % NT][i], wf[e], tc[(e / NT) * 2 + i]);
            }
            SCHED();
            if constexpr (S < 17) {
                wf[e] = rdf(wb + foff + (e / NT) * 9216 + (e % NT) * 1024);
                if (e >= 2 && e < 6) tn[e - 2] = rdf(tb + toff + ((e - 2) >> 1) * 8192 + ((e - 2) & 1) * 1024);
            }
            if constexpr (S + 3 < 18) { if (e < 2) hpiece(e, S + 3); }
            if constexpr (S + 4 < 18) {
                if (e == 3) g2_wpiece(0, S + 4, wsrc, aoff, mw0);
                if (e == 5) g2_wpiece(1, S + 4, wsrc, aoff, mw0);
                if (e == 6) g2_wpiece(2, S + 4, wsrc, aoff, mw0);
            }
            if constexpr (S == 4) {
                if (e == 7) dma4_sc1((unsigned)(lane & 31) * 4, ws + WS_FB + ((mt * 4 + ks) * 2 + 1) * 128, lds0 + G2_FL + wid * 256);
            }
            SCHED();
        }
    };
#define G2_IT2(S) iter(IC<S>{}, tf[0], tf[1]); iter(IC<S + 1>{}, tf[1], tf[0])
    G2_IT2(0); G2_IT2(2); G2_IT2(4); G2_IT2(6); G2_IT2(8); G2_IT2(10); G2_IT2(12);
    G2_IT2(14);
    iter(IC<16>{}, tf[0], tf[1]);
    const int tok0 = mt * 128 + wr * 32 + (lane & 15);
    u32x2 hv[2][5];
    if (wr == ks) {
#pragma unroll
        for (int i = 0; i < 2; ++i) {
            const int t = tok0 + i * 16;
            const char* hp = ws + WS_H + ((size_t)((t >> 8) * 144 + nt * 36 + dh * 20) * 256 + (t & 255)) * 8;
#pragma unroll
            for (int e = 0; e < 4; ++e) hv[i][e] = *(const u32x2*)(hp + ((e >> 1) * 8 + (g4 & 1) * 4 + (g4 >> 1) * 2 + (e & 1)) * 2048);
            if (NT == 5) hv[i][4] = *(const u32x2*)(hp + (16 + g4) * 2048);
        }
    }
    SCHED();
    iter(IC<17>{}, tf[1], tf[0]);
    MFMA_DONE();

    const rsrc_t rx = mk_rsrc(ws + WS_X, X_BYTES);
    const unsigned xt = (unsigned)((mt * 4 + nt) * 4 * 27648 + dh * 4096);
    auto inb = [&](int s) -> unsigned { return xt + (unsigned)(((ks + 1 + s) & 3) * 27648 + (2 - s) * 9216); };
    if (wr != ks) {
        const unsigned o = xt + (unsigned)(ks * 27648 + ((wr - ks - 1) & 3) * 9216);
#pragma unroll
        for (int i = 0; i < 2; ++i) {
            unsigned w[2 * NT];
#pragma unroll
            for (int j = 0; j < NT; ++j) { w[2 * j] = pk_bf16(acc[j][i][0], acc[j][i][1]); w[2 * j + 1] = pk_bf16(acc[j][i][2], acc[j][i][3]); }
#pragma unroll
            for (int e = 0; e < 2 * NT; ++e) w[e] = (w[e] == ~0u) ? 0x7fc07fc0u : w[e];
            if constexpr (NT == 5) st8_wt(rx, o + 8192 + i * 512 + lane * 8, (u32x2){w[8], w[9]});
            st16_wt(rx, o + i * 2048 + lane * 16, (u32x4){w[0], w[1], w[2], w[3]});
            st16_wt(rx, o + i * 2048 + 1024 + lane * 16, (u32x4){w[4], w[5], w[6], w[7]});
        }
    } else {
        u32x4 pa[3][4]; u32x2 pb[3][2];
        __builtin_amdgcn_s_sleep(16);
        for (int spin = 0; spin < (1 << 18); ++spin) {
            const u32x4 t = __builtin_amdgcn_raw_buffer_load_b128(rx, lane < 3 ? inb(lane) + 4080 : 0x80000000u, 0, 16);
            if (__all(t.w != ~0u)) {
                unsigned m = 0u;
#pragma unroll
                for (int s = 0; s < 3; ++s) {
#pragma unroll
                    for (int p = 0; p < 4; ++p) pa[s][p] = __builtin_amdgcn_raw_buffer_load_b128(rx, inb(s) + p * 1024 + lane * 16, 0, 16);
                    if constexpr (NT == 5) {
#pragma unroll
                        for (int i = 0; i < 2; ++i) pb[s][i] = __builtin_amdgcn_raw_buffer_load_b64(rx, inb(s) + 8192 + i * 512 + lane * 8, 0, 16);
                    }
                }
#pragma unroll
                for (int s = 0; s < 3; ++s) {
#pragma unroll
                    for (int p = 0; p < 4; ++p) m = max(max(max(m, pa[s][p].x), max(pa[s][p].y, pa[s][p].z)), pa[s][p].w);
                    if constexpr (NT == 5) m = max(max(max(m, pb[s][0].x), max(pb[s][0].y, pb[s][1].x)), pb[s][1].y);
                }
                if (__all(m != ~0u)) break;
            }
            __builtin_amdgcn_s_sleep(1);
        }
        const rsrc_t ro = mk_rsrc(out, S_ * D_ * 2);
#pragma unroll
        for (int i = 0; i < 2; ++i) {
            const unsigned ob = (unsigned)(((tok0 + i * 16) * D_ + nt * 144 + dh * 80) * 2);
            unsigned r[NT][2];
#pragma unroll
            for (int j = 0; j < NT; ++j) {
                f32x4 t = acc[j][i];
#pragma unroll
                for (int s = 0; s < 3; ++s) {
                    const unsigned w0 = j < 4 ? pa[s][i * 2 + (j >> 1)][(j & 1) * 2] : pb[s][i].x, w1 = j < 4 ? pa[s][i * 2 + (j >> 1)][(j & 1) * 2 + 1] : pb[s][i].y;
                    t += (f32x4){bf_lo(w0), bf_hi(w0), bf_lo(w1), bf_hi(w1)};
                }
                r[j][0] = pk_bf16(t[0], t[1]); r[j][1] = pk_bf16(t[2], t[3]);
            }
            auto addh = [&](unsigned a, unsigned h) -> unsigned { return pk_bf16(bf_lo(h) + bf_lo(a), bf_hi(h) + bf_hi(a)); };
#pragma unroll
            for (int p = 0; p < 2; ++p) {
                auto s0 = __builtin_amdgcn_permlane16_swap(r[2 * p][0], r[2 * p + 1][0], false, false);
                auto s1 = __builtin_amdgcn_permlane16_swap(r[2 * p][1], r[2 * p + 1][1], false, false);
                st16_wt(ro, ob + (unsigned)((p * 32 + (g4 & 1) * 16 + (g4 >> 1) * 8) * 2),
                        (u32x4){addh(s0[0], hv[i][2 * p].x), addh(s1[0], hv[i][2 * p].y), addh(s0[1], hv[i][2 * p + 1].x), addh(s1[1], hv[i][2 * p + 1].y)});
            }
            if constexpr (NT == 5) st8_wt(ro, ob + (unsigned)((64 + g4 * 4) * 2), (u32x2){addh(r[4][0], hv[i][4].x), addh(r[4][1], hv[i][4].y)});
        }
    }
}

constexpr int K3_LDS = 163840;
constexpr int K3_SP = G_LDS;
constexpr int K3_CS = G2_W0 + 4 * 18432;
extern "C" __global__ __launch_bounds__(512, 2) void k3_gemm1(const u16* __restrict__ wg, const u16* __restrict__ wu,
                                                   char* __restrict__ ws, u16* __restrict__ out) {
    extern __shared__ __attribute__((aligned(16))) char smem[];
    LAS char* lds = (LAS char*)smem;
    const unsigned lds0 = (unsigned)(size_t)lds;
    const int tid = threadIdx.x, lane = tid & 63;
    const int wid = __builtin_amdgcn_readfirstlane(tid >> 6);
    const int wr = wid & 3, wc = wid >> 2;
    const int b = blockIdx.x, xcd = b & 7, idx = b >> 3;
    const int rt = (xcd >> 2) * 4 + (idx & 3), ct = (xcd & 3) * 8 + (idx >> 2);
    asm volatile("" :: "s"(wg), "s"(wu), "s"(ws), "s"(out));

    const char* asrc = ws + WS_H + (size_t)rt * 144 * 2048 + wid * 2048;
    const unsigned aoff = (unsigned)(lane ^ ((wid & 2) << 2)) * 16;
    const unsigned ma0 = lds0 + wid * 2048, mw0 = lds0 + 16384 + wc * 9216 + wr * 2304;
    dma16<0>(aoff, asrc, ma0); dma16<1024>(aoff, asrc, ma0);
    const char* wsrc = (const char*)(wc ? wu : wg) + ct * 288 - 2048;
    auto wofs = [&](int i) -> unsigned {
        const int q = lane + 64 * i, lrow = 8 * wr + q / 18, pos = q % 18;
        const int krow = ((lrow >> 2) & 3) * 8 + (lrow >> 4) * 4 + (lrow & 3);
        const int col = pos * 8 + (pos < 8 ? 0 : (pos < 16 ? 8 : (((pos & 1) ^ wc) ? 136 : 64) - pos * 8));
        return (unsigned)(krow * (F_ * 2) + col * 2 + 2048 - 1024 * i);
    };
    const unsigned w0 = wofs(0), w1 = wofs(1), w2 = wofs(2);
    auto piece = [&](int P, int s) {
        const unsigned bo = (s & 3) * ST_BYTES;
        const char* wp = wsrc + (size_t)s * (32 * F_ * 2);
        if (P == 0) dma16<0>(aoff, asrc + (size_t)s * 16384, ma0 + bo);
        if (P == 1) dma16<1024>(aoff, asrc + (size_t)s * 16384, ma0 + bo);
        if (P == 2) dma16<0>(w0, wp, mw0 + bo);
        if (P == 3) dma16<1024>(w1, wp, mw0 + bo);
        if (P == 4) dma16_q<2048>(w2, wp, mw0 + bo);
    };
#define K3_PIECE(P, s) piece(P, s)
#define K3_STAGE(s) do { K3_PIECE(0, s); K3_PIECE(1, s); K3_PIECE(2, s); K3_PIECE(3, s); K3_PIECE(4, s); } while (0)
    K3_PIECE(2, 0); K3_PIECE(3, 0); K3_PIECE(4, 0);
    BAR();
    K3_STAGE(1);
    u32x2 hs[4];
    {
        const char* hsrc = ws + WS_H + (size_t)rt * 144 * 2048 + ct * 64;
        const unsigned o = (unsigned)((wid * 18 + (lane >> 3)) * 2048 + (lane & 7) * 8);
        asm volatile("global_load_dwordx2 %0, %1, %2" : "=v"(hs[0]) : "v"(o), "s"(hsrc) : "memory");
        asm volatile("global_load_dwordx2 %0, %1, %2" : "=v"(hs[1]) : "v"(o + 16384), "s"(hsrc) : "memory");
        asm volatile("global_load_dwordx2 %0, %1, %2" : "=v"(hs[2]) : "v"((unsigned)((wid * 18 + 16 + ((lane >> 3) & 1)) * 2048 + (lane & 7) * 8)), "s"(hsrc) : "memory");
        asm volatile("global_load_dwordx2 %0, %1, %2" : "=v"(hs[3]) : "v"((unsigned)(lane & 7) * 8), "s"(hsrc) : "memory");
    }

    const int g4 = lane >> 4, q4 = (lane & 15) >> 2, p4 = lane & 3;
    const int rowoff = (4 * g4 + q4) * 288;
    const int tbase = g4 * 4096 + wr * 512 + (lane & 15) * 8;
    const int toff0 = tbase + ((2 * wc) ^ (g4 & 1)) * 128, toff1 = tbase + ((2 * wc + 1) ^ (g4 & 1)) * 128;
    const int tstep = wc ? -256 : 256;
    const int foff = 16384 + rowoff + wc * 128 + p4 * 8;
    const int moff = 16384 + rowoff + 256 + (p4 < 2 ? 16 * wc + 8 * p4 : 9216 + 16 * (1 - wc) + 8 * (p4 - 2));

    f32x4 acc[9][4];
    s16x8 wf[9], tf[2][4];
    auto rd_w = [&](const LAS char* sb, int j) -> s16x8 {
        return j < 8 ? tr_frag(sb + foff + (j >> 2) * 9216 + (j & 3) * 32) : tr_frag(sb + moff);
    };
    auto rd_t = [&](const LAS char* sb, int i) -> s16x8 {
        const LAS char* p = sb + ((i & 1) ? toff1 : toff0) + (i >> 1) * tstep;
        const s16x4 lo = *(const volatile LAS s16x4*)p, hi = *(const volatile LAS s16x4*)(p + 2048);
        return __builtin_shufflevector(lo, hi, 0, 1, 2, 3, 4, 5, 6, 7);
    };
    WAIT_V(9); BAR();
    wf[0] = rd_w(lds, 0);
#pragma unroll
    for (int i = 0; i < 4; ++i) tf[0][i] = rd_t(lds, i);
#pragma unroll
    for (int j = 1; j < 9; ++j) wf[j] = rd_w(lds, j);
    SCHED();
    const int mt2 = (xcd >> 2) * 8 + (idx & 7), ks2 = xcd & 3, nt2 = idx >> 3;
    const rsrc_t rfill = mk_rsrc(ws + WS_X + (size_t)((mt2 * 4 + nt2) * 4 + ks2) * 27648, 27648);
    auto fill = [&](int k) { st16_wt(rfill, (unsigned)(k * 8192 + tid * 16), (u32x4){~0u, ~0u, ~0u, ~0u}); };
    unsigned sgr;
    {
        const LAS char* sb = lds + ST_BYTES;
        float hc = 0.f;
#pragma unroll
        for (int j = 0; j < 9; ++j) {
#pragma unroll
            for (int i = 0; i < 4; ++i) mfma_first(acc[j][i], wf[j], tf[0][i]);
            SCHED();
            if (j == 0) { K3_PIECE(0, 2); K3_PIECE(1, 2); }
            if (j == 1) { K3_PIECE(2, 2); K3_PIECE(3, 2); }
            if (j == 2) K3_PIECE(4, 2);
            if (j == 3) {
                asm volatile("s_waitcnt vmcnt(5)" : "+v"(hs[0]), "+v"(hs[1]), "+v"(hs[2]), "+v"(hs[3]) :: "memory");
                hc = bf_lo(hs[3].x);
                float s1 = 0.f, s2 = 0.f;
#pragma unroll
                for (int i = 0; i < 3; ++i) {
                    const float d[4] = {bf_lo(hs[i].x) - hc, bf_hi(hs[i].x) - hc, bf_lo(hs[i].y) - hc, bf_hi(hs[i].y) - hc};
                    float a = (d[0] + d[1]) + (d[2] + d[3]), q = __builtin_fmaf(d[0], d[0], d[1] * d[1]) + __builtin_fmaf(d[2], d[2], d[3] * d[3]);
                    if (i == 2 && lane >= 16) { a = 0.f; q = 0.f; }
                    s1 += a; s2 += q;
                }
                s1 = x32_add(x16_add(dpp_add<0x128>(s1))); s2 = x32_add(x16_add(dpp_add<0x128>(s2)));
                if (lane < 8) *(LAS f32x2*)(lds + K3_SP + (lane * 8 + wid) * 8) = (f32x2){s1, s2};
                asm volatile("s_waitcnt lgkmcnt(0)" ::: "memory");
                BAR();
                wf[0] = rd_w(sb, 0); wf[1] = rd_w(sb, 1);
                tf[1][0] = rd_t(sb, 0); tf[1][1] = rd_t(sb, 1);
            }
            if (j == 4) { tf[1][2] = rd_t(sb, 2); tf[1][3] = rd_t(sb, 3); wf[2] = rd_w(sb, 2); wf[3] = rd_w(sb, 3); }
            if (j >= 4) { wf[j] = rd_w(sb, j); K3_PIECE(j - 4, 3); }
            if (j == 8) {
                const f32x2 pp = *(const LAS f32x2*)(lds + K3_SP + (wid * 8 + (lane & 7)) * 8);
                const float s1 = dpp_add<0x141>(dpp_add<0x4E>(dpp_add<0xB1>(pp.x))) * (1.f / D_);
                const float s2 = dpp_add<0x141>(dpp_add<0x4E>(dpp_add<0xB1>(pp.y))) * (1.f / D_);
                const float c0 = __uint_as_float(__builtin_amdgcn_readlane(__float_as_uint(hc), wid));
                const float sd = ln_sd(__builtin_fmaxf(__builtin_fmaf(-s1, s1, s2), 0.f));
                sgr = pk_bf16(c0 + s1, sd);
                fill(0); fill(1);
            }
            SCHED();
        }
    }

    const rsrc_t rg = mk_rsrc(ws + WS_G2 + (size_t)(rt * 256 + wr * 64) * 8, 512);
    const char* w2src = ws + WS_WT + (size_t)(ks2 * 4 + nt2) * 18 * 18432 + wid * 2304;
    u32x2 gr[4];
    s16x4 tl;
    constexpr int TP = 1, DPJ0 = 0, DPJ1 = 5;
    auto iter = [&](auto mode, auto vm, int s, s16x8 (&tc)[4], s16x8 (&tn)[4]) {
        constexpr int MODE = decltype(mode)::v;
        if constexpr (MODE != 4) {
            if constexpr (MODE == 3) WAIT_V(0); else WAIT_VI(5 + decltype(vm)::v);
            BAR();
            SCHED();
        }
        const LAS char* sb = lds + ((s + 1) & 3) * ST_BYTES;
#pragma unroll
        for (int j = 0; j < 9; ++j) {
            s16x4 wl, wh;
#pragma unroll
            for (int i = 0; i < 4; ++i) {
                mfma_acc(acc[j][i], wf[j], tc[i]);
                SCHED();
                if constexpr (MODE != 4) {
                    if (j >= 1) {
                        const LAS char* p = (j - 1 < 8) ? sb + foff + ((j - 1) >> 2) * 9216 + ((j - 1) & 3) * 32 : sb + moff;
                        if (i == 0) wl = __builtin_amdgcn_ds_read_tr16_b64_v4i16((LAS s16x4*)p);
                        if (i == 1) { wh = __builtin_amdgcn_ds_read_tr16_b64_v4i16((LAS s16x4*)(p + 16 * 288)); wf[j - 1] = __builtin_shufflevector(wl, wh, 0, 1, 2, 3, 4, 5, 6, 7); }
                    }
                    if (j >= TP && j < TP + 4) {
                        const int ti = j - TP;
                        const LAS char* p = sb + ((ti & 1) ? toff1 : toff0) + (ti >> 1) * tstep;
                        if (i == 2) tl = *(const volatile LAS s16x4*)p;
                        if (i == 3) tn[ti] = __builtin_shufflevector(tl, *(const volatile LAS s16x4*)(p + 2048), 0, 1, 2, 3, 4, 5, 6, 7);
                    }
                    if (j == 8 && i == 3) wf[8] = rd_w(sb, 8);
                }
                if constexpr (MODE < 2 || MODE == 5) {
                    if (j == DPJ0 && i == 1) K3_PIECE(0, s + 3);
                    if (j == DPJ0 && i == 3) K3_PIECE(1, s + 3);
                    if (j == DPJ1 && i == 3) K3_PIECE(2, s + 3);
                    if (j == DPJ1 + 1 && i == 3) K3_PIECE(3, s + 3);
                    if (j == DPJ1 + 2 && i == 3) K3_PIECE(4, s + 3);
                }
                SCHED();
            }
            if constexpr (MODE == 0) { if (j == 8) { fill(2); fill(3); } }
            if constexpr (MODE == 5) {
                if (j == 0 && s == 4) st8_wt(mk_rsrc(ws + WS_G2, S_ * 8), lane == 0 ? (unsigned)((rt * 256 + ct * 8 + wid) * 8) : 0x80000000u, (u32x2){sgr, 1u});
            }
            if constexpr (MODE == 2) {
                if (j == 0) {
#pragma unroll
                    for (int i = 0; i < 4; ++i) gr[i] = __builtin_amdgcn_raw_buffer_load_b64(rg, (unsigned)(((i ^ (2 * wc)) * 16 + (lane & 15)) * 8), 0, 16);
                }
            }
            if constexpr (MODE == 3) {
                if (j == 0) {
                    asm volatile("" : "+v"(gr[0]), "+v"(gr[1]), "+v"(gr[2]), "+v"(gr[3]));
                    const unsigned co = (unsigned)((lane >= 18 ? F_ * 4 + (lane - 18) * 16 : lane * 16));
                    dma_m36(co, ws + WS_CS + (size_t)(ct * 144 + wc * 72) * 4, lds0 + K3_CS + wid * 1152);
                    dma_m36(co, ws + WS_CS + (size_t)(2 * F_ + ct * 144 + wc * 72) * 4, lds0 + K3_CS + wid * 1152 + 576);
                }
            }
            if constexpr (MODE == 3 || MODE == 4) {
                if (j >= 2 && j < 8) g2_wpiece((j - 2) % 3, (MODE - 3) * 2 + (j - 2) / 3, w2src, lane * 16, lds0 + G2_W0 + wid * 2304);
            }
            SCHED();
        }
    };
    iter(IC<0>{}, IC<2>{}, 1, tf[1], tf[0]);
    iter(IC<1>{}, IC<4>{}, 2, tf[0], tf[1]);
    iter(IC<1>{}, IC<2>{}, 3, tf[1], tf[0]);
#pragma unroll 1
    for (int s = 4; s < 14; s += 2) {
        iter(IC<5>{}, IC<0>{}, s, tf[0], tf[1]);
        iter(IC<1>{}, IC<0>{}, s + 1, tf[1], tf[0]);
    }
    iter(IC<1>{}, IC<0>{}, 14, tf[0], tf[1]);
    iter(IC<2>{}, IC<0>{}, 15, tf[1], tf[0]);
    iter(IC<3>{}, IC<0>{}, 16, tf[0], tf[1]);
    iter(IC<4>{}, IC<0>{}, 17, tf[1], tf[0]);
    MFMA_DONE();
    WAIT_V(12);
    __builtin_amdgcn_s_setprio(1);
    if (__builtin_expect(!__all((gr[0].y & gr[1].y & gr[2].y & gr[3].y) == 1u), 0)) {
        for (int spin = 0; spin < (1 << 20); ++spin) {
            __builtin_amdgcn_s_sleep(2);
#pragma unroll
            for (int i = 0; i < 4; ++i) gr[i] = __builtin_amdgcn_raw_buffer_load_b64(rg, (unsigned)(((i ^ (2 * wc)) * 16 + (lane & 15)) * 8), 0, 16);
            if (__all((gr[0].y & gr[1].y & gr[2].y & gr[3].y) == 1u)) break;
        }
    }
    const int mt = rt * 2 + (wr >> 1), ks = ct >> 3, lct = ct & 7;
    const rsrc_t rhh = mk_rsrc(ws + WS_HH, S_ * F_ * 2);
    const unsigned hb = (unsigned)(((mt * 4 + ks) * 36) * 8192 + ((wr & 1) * 4) * 1024);
    const LAS char* csb = lds + K3_CS + wid * 1152 + 16 * g4;
    TokC tcs[4];
#pragma unroll
    for (int i = 0; i < 4; ++i) {
        const float sd = bf_hi(gr[i].x), r = __builtin_amdgcn_rcpf(sd);
        tcs[i] = (TokC){-bf_lo(gr[i].x), -2.30220819f * r, -0.10294324f * (r * r * r), sd * sd};
    }
    {
        const LAS char* cm = lds + K3_CS + wid * 1152 + 256 + 16 * (g4 & 1);
        const f32x4 cgm = *(const LAS f32x4*)cm + *(const LAS f32x4*)(cm + 576), cum = *(const LAS f32x4*)(cm + 288) + *(const LAS f32x4*)(cm + 864);
#pragma unroll
        for (int ip = 0; ip < 2; ++ip) {
            const int ia = ((2 * ip) ^ (2 * wc)) + (lane >> 5);
            const bool up = lane >= 32;
            const TokC tc = {up ? tcs[2 * ip + 1].nm : tcs[2 * ip].nm, up ? tcs[2 * ip + 1].c0 : tcs[2 * ip].c0,
                             up ? tcs[2 * ip + 1].c1 : tcs[2 * ip].c1, up ? tcs[2 * ip + 1].q : tcs[2 * ip].q};
            f32x4 gm, um;
#pragma unroll
            for (int r = 0; r < 4; ++r) {
                auto sw = __builtin_amdgcn_permlane32_swap(__float_as_uint(acc[8][2 * ip][r]), __float_as_uint(acc[8][2 * ip + 1][r]), false, false);
                gm[r] = __uint_as_float(sw[0]); um[r] = __uint_as_float(sw[1]);
            }
            const int kgs = (lct & 1) * 2 + wc;
            st8_wt(rhh, hb + (unsigned)((lct >> 1) * 8192 + ia * 1024 + (kgs * 16 + (lane & 15)) * 16 + (g4 & 1) * 8),
                   (u32x2){geglu2(LO2(gm), LO2(um), LO2(cgm), LO2(cum), tc), geglu2(HI2(gm), HI2(um), HI2(cgm), HI2(cum), tc)});
        }
    }
    SCHED();
    constexpr int FLAGPOS = 1;
    const rsrc_t rfb = mk_rsrc(ws + WS_FB, 16384);
    const unsigned fbo = (unsigned)((mt * 4 + ks) * 256 + (lct * 4 + (wr & 1) * 2 + wc) * 4);
    u32x4 ob[4];
#pragma unroll
    for (int jp = 0; jp < 2; ++jp) {
        f32x4 cg[2], cu[2];
#pragma unroll
        for (int e = 0; e < 2; ++e) {
            cg[e] = *(const LAS f32x4*)(csb + (2 * jp + e) * 64) + *(const LAS f32x4*)(csb + 576 + (2 * jp + e) * 64);
            cu[e] = *(const LAS f32x4*)(csb + 288 + (2 * jp + e) * 64) + *(const LAS f32x4*)(csb + 864 + (2 * jp + e) * 64);
        }
#pragma unroll
        for (int i = 0; i < 4; ++i) {
            unsigned o[4];
#pragma unroll
            for (int e = 0; e < 2; ++e) {
                const f32x4 g = acc[2 * jp + e][i], u = acc[4 + 2 * jp + e][i];
                o[2 * e] = geglu2(LO2(g), LO2(u), LO2(cg[e]), LO2(cu[e]), tcs[i]);
                o[2 * e + 1] = geglu2(HI2(g), HI2(u), HI2(cg[e]), HI2(cu[e]), tcs[i]);
            }
            ob[i] = (u32x4){o[0], o[1], o[2], o[3]};
            st16_wt(rhh, (jp == 1 && wid == 7) ? 0x80000000u : hb + (unsigned)(((2 + jp * 8 + lct) * 2 + wc) * 8192 + (i ^ (2 * wc)) * 1024 + lane * 16), ob[i]);
            if (jp == 1 && i == FLAGPOS) {
                SCHED();
                WAIT_VI(FLAGPOS + 1);
                __builtin_amdgcn_raw_buffer_store_b32(1u, rfb, lane == 0 ? fbo : 0x80000000u, 0, 16);
                SCHED();
            }
        }
        SCHED();
        if (jp == 0) __builtin_amdgcn_s_setprio(0);
    }
    if (wid == 7) {
        for (int spin = 0; spin < (1 << 20); ++spin) {
            const unsigned f = __builtin_amdgcn_raw_buffer_load_b32(rfb, (unsigned)((mt2 * 4 + ks2) * 256 + (lane & 31) * 4), 0, 16);
            if (__all(f == 1u)) break;
            __builtin_amdgcn_s_sleep(4);
        }
#pragma unroll
        for (int i = 0; i < 4; ++i) st16_wt(rhh, hb + (unsigned)(((10 + lct) * 2 + wc) * 8192 + (i ^ (2 * wc)) * 1024 + lane * 16), ob[i]);
    }
    BAR();
    if (wid < 4) g2_body<5>(ws, out, lds, lane, wid, mt2, ks2, nt2, w2src, fbo + 128);
    else g2_body<4>(ws, out, lds, lane, wid, mt2, ks2, nt2, w2src, fbo + 128);
}

extern "C" __global__ void k9_end() {}

extern "C" void kernel_launch(void* const* d_in, const int* in_sizes, int n_in, void* d_out, int out_size, void* d_ws,
                              size_t ws_size, hipStream_t stream) {
    const u16* q = (const u16*)d_in[0];
    const u16* k = (const u16*)d_in[1];
    const u16* v = (const u16*)d_in[2];
    const u16* shift_k = (const u16*)d_in[3];
    const u16* ssm_k = (const u16*)d_in[4];
    const u16* ssm_D = (const u16*)d_in[5];
    const u16* wg = (const u16*)d_in[7];
    const u16* wu = (const u16*)d_in[8];
    const u16* wd = (const u16*)d_in[9];
    char* ws = (char*)d_ws;
    (void)hipFuncSetAttribute((const void*)k1_conv, hipFuncAttributeMaxDynamicSharedMemorySize, C1_LDS);
    (void)hipFuncSetAttribute((const void*)k3_gemm1, hipFuncAttributeMaxDynamicSharedMemorySize, K3_LDS);
    k1_conv<<<256, 512, C1_LDS, stream>>>(q, k, v, shift_k, ssm_k, ssm_D, wg, wu, wd, ws);
    k3_gemm1<<<256, 512, K3_LDS, stream>>>(wg, wu, ws, (u16*)d_out);
}
```

```cpp
#include <hip/hip_runtime.h>
#include <hip/hip_bf16.h>

#define LAS __attribute__((address_space(3)))
typedef unsigned short u16;
typedef short s16x4 __attribute__((ext_vector_type(4)));
typedef short s16x8 __attribute__((ext_vector_type(8)));
typedef float f32x2 __attribute__((ext_vector_type(2)));
typedef float f32x4 __attribute__((ext_vector_type(4)));
typedef unsigned u32x2 __attribute__((ext_vector_type(2)));
typedef unsigned u32x4 __attribute__((ext_vector_type(4)));
typedef __bf16 bf16x2_t __attribute__((ext_vector_type(2)));

constexpr int S_ = 2048, D_ = 576, F_ = 4608;

__device__ __forceinline__ unsigned pk_bf16(float lo, float hi) {
    f32x2 v = {lo, hi};
    bf16x2_t b = __builtin_convertvector(v, bf16x2_t);
    return __builtin_bit_cast(unsigned, b);
}
__device__ __forceinline__ float bf_lo(unsigned w) { return __uint_as_float(w << 16); }
__device__ __forceinline__ float bf_hi(unsigned w) { return __uint_as_float(w & 0xffff0000u); }
__device__ __forceinline__ float rbf(float x) { return bf_lo(pk_bf16(x, 0.f)); }
#define WAIT_V(n) asm volatile("s_waitcnt vmcnt(" #n ")" ::: "memory")
#define BAR() do { __builtin_amdgcn_s_barrier(); asm volatile("" ::: "memory"); } while (0)
#define SCHED() __builtin_amdgcn_sched_barrier(0)
__device__ __forceinline__ void mfma_acc(f32x4& c, s16x8 a, s16x8 b) {
    asm volatile("v_mfma_f32_16x16x32_bf16 %0, %1, %2, %0" : "+v"(c) : "v"(a), "v"(b));
}
__device__ __forceinline__ void mfma_first(f32x4& c, s16x8 a, s16x8 b) {
    asm volatile("v_mfma_f32_16x16x32_bf16 %0, %1, %2, 0" : "=&v"(c) : "v"(a), "v"(b));
}
#define MFMA_DONE() do { asm volatile("s_nop 7\n\ts_nop 7" ::: "memory"); SCHED(); } while (0)
constexpr size_t WS_KV = 0;
constexpr size_t WS_H = WS_KV + (size_t)S_ * D_ * 2;
constexpr size_t WS_ST = WS_H + (size_t)S_ * D_ * 2;
constexpr size_t WS_CS = WS_ST + (size_t)S_ * 8;
constexpr size_t WS_G2 = WS_CS + (size_t)4 * F_ * 4;
constexpr size_t WS_F0 = WS_G2 + (size_t)S_ * 8;
constexpr int FSP = 128;
constexpr size_t WS_EP = WS_F0 + 1280 * FSP;
constexpr size_t WS_HH = WS_EP + 1024;
constexpr size_t WS_X = WS_HH + (size_t)S_ * F_ * 2;
constexpr unsigned X_BYTES = 64 * 4 * 3 * 9216;
constexpr size_t WS_FB = WS_X + X_BYTES;
constexpr size_t WS_WT = WS_FB + 16384;

template <int N> struct IC { static constexpr int v = N; };
typedef __amdgpu_buffer_rsrc_t rsrc_t;
__device__ __forceinline__ rsrc_t mk_rsrc(const void* p, unsigned bytes) { return __builtin_amdgcn_make_buffer_rsrc((void*)p, 0, bytes, 0x00020000); }
__device__ __forceinline__ void st16_wt(rsrc_t r, unsigned off, u32x4 v) { __builtin_amdgcn_raw_buffer_store_b128(v, r, off, 0, 16); }
__device__ __forceinline__ void st8_wt(rsrc_t r, unsigned off, u32x2 v) { __builtin_amdgcn_raw_buffer_store_b64(v, r, off, 0, 16); }
template <int CTRL> __device__ __forceinline__ float dpp_add(float x) {
    return x + __uint_as_float(__builtin_amdgcn_update_dpp(0, (int)__float_as_uint(x), CTRL, 0xf, 0xf, true));
}
__device__ __forceinline__ float x16_add(float x) {
    auto r = __builtin_amdgcn_permlane16_swap(__float_as_uint(x), __float_as_uint(x), false, false);
    return __uint_as_float(r[0]) + __uint_as_float(r[1]);
}
__device__ __forceinline__ float x32_add(float x) {
    auto r = __builtin_amdgcn_permlane32_swap(__float_as_uint(x), __float_as_uint(x), false, false);
    return __uint_as_float(r[0]) + __uint_as_float(r[1]);
}
__device__ __forceinline__ float sum16(float x) {
    x = dpp_add<0xB1>(x); x = dpp_add<0x4E>(x); x = dpp_add<0x141>(x);
    return dpp_add<0x140>(x);
}
__device__ __forceinline__ float ln_sd(float var) { return rbf(__builtin_amdgcn_sqrtf(rbf(rbf(var) + 1e-5f))); }

template <int OFF>
__device__ __forceinline__ void dma16(unsigned voff, const void* sbase, unsigned m0v) {
    asm volatile("s_mov_b32 m0, %2\n\ts_nop 0\n\tglobal_load_lds_dwordx4 %0, %1 offset:%3"
                 :: "v"(voff), "s"(sbase), "s"(m0v), "i"(OFF) : "memory");
}
template <int OFF>
__device__ __forceinline__ void dma16_sc1(unsigned voff, const void* sbase, unsigned m0v) {
    asm volatile("s_mov_b32 m0, %2\n\ts_nop 0\n\tglobal_load_lds_dwordx4 %0, %1 offset:%3 sc1"
                 :: "v"(voff), "s"(sbase), "s"(m0v), "i"(OFF) : "memory");
}
template <int OFF>
__device__ __forceinline__ void dma16_q(unsigned voff, const void* sbase, unsigned m0v) {
    asm volatile("s_mov_b32 m0, %2\n\ts_mov_b64 exec, 0xffff\n\tglobal_load_lds_dwordx4 %0, %1 offset:%3\n\ts_mov_b64 exec, -1"
                 :: "v"(voff), "s"(sbase), "s"(m0v), "i"(OFF) : "memory");
}
__device__ __forceinline__ void dma_m36(unsigned voff, const void* sbase, unsigned m0v) {
    asm volatile("s_mov_b32 m0, %2\n\ts_mov_b32 exec_hi, 15\n\tglobal_load_lds_dwordx4 %0, %1\n\ts_mov_b64 exec, -1"
                 :: "v"(voff), "s"(sbase), "s"(m0v) : "memory");
}
__device__ __forceinline__ void dma4(unsigned voff, const void* sbase, unsigned m0v) {
    asm volatile("s_mov_b32 m0, %2\n\ts_nop 0\n\tglobal_load_lds_dword %0, %1" :: "v"(voff), "s"(sbase), "s"(m0v) : "memory");
}
__device__ __forceinline__ void dma4_sc1(unsigned voff, const void* sbase, unsigned m0v) {
    asm volatile("s_mov_b32 m0, %2\n\ts_nop 0\n\tglobal_load_lds_dword %0, %1 sc1" :: "v"(voff), "s"(sbase), "s"(m0v) : "memory");
}
#define SYM_ADDR(out, sym) asm volatile("s_getpc_b64 s[40:41]\n\ts_add_u32 s40, s40, " #sym "@rel32@lo+4\n\ts_addc_u32 s41, s41, " #sym "@rel32@hi+12\n\ts_mov_b64 %0, s[40:41]" \
                                        : "=s"(out) :: "s40", "s41", "scc")
template <int NI>
__device__ __forceinline__ void warm_code(const char* a, const char* e, int l0, int lane, unsigned m0v) {
    const unsigned last = e > a ? (unsigned)(e - a) - 4u : 0u;
#pragma unroll
    for (int i = 0; i < NI; ++i) dma4(min((unsigned)(l0 + i * 64 + lane) * 128u, last), a, m0v);
}
__device__ __forceinline__ s16x8 tr_frag(const LAS char* p) {
    const s16x4 lo = __builtin_amdgcn_ds_read_tr16_b64_v4i16((LAS s16x4*)p);
    const s16x4 hi = __builtin_amdgcn_ds_read_tr16_b64_v4i16((LAS s16x4*)(p + 16 * 288));
    return __builtin_shufflevector(lo, hi, 0, 1, 2, 3, 4, 5, 6, 7);
}
__device__ __forceinline__ void wd_unit_load(const u16* wd, int un, int lane, unsigned m0v) {
    const int uB = un / 3, ut = un % 3, kh = uB & 1, st = (uB >> 1) % 18, kn = uB / 36;
    const bool full = st >= 2;
    const int f0 = (kn >> 2) * 1152 + (full ? ((st - 2) & 7) * 144 + kh * 72 + ((st - 2) >> 3) * 32 : (st * 2 + kh) * 288);
    const char* src = (const char*)wd + (size_t)f0 * (D_ * 2) + (kn & 3) * 288 + ut * 96;
#pragma unroll
    for (int i = 0; i < 3; ++i) {
        const int q = lane + 64 * i, r = q / 6, pos = q % 6, kg = (r & 15) >> 2;
        const int f = full ? r : (kg >> 1) * 144 + (kg & 1) * 72 + 64 + (r & 3) + 4 * (r >> 4);
        dma16<0>((unsigned)(f * (D_ * 2) + pos * 16), src, m0v + i * 1024);
    }
}
__device__ __forceinline__ void wd_unit_store(char* ws, int un, int lane, const LAS char* l) {
    const rsrc_t r = mk_rsrc(ws + WS_WT, F_ * D_ * 2);
#pragma unroll
    for (int t = 0; t < 3; ++t) {
        const LAS char* tp = l + (lane >> 2) * 96 + (lane & 3) * 8 + t * 32;
        const s16x4 lo = __builtin_amdgcn_ds_read_tr16_b64_v4i16((LAS s16x4*)tp), hi = __builtin_amdgcn_ds_read_tr16_b64_v4i16((LAS s16x4*)(tp + 16 * 96));
        st16_wt(r, (unsigned)(un * 3072 + t * 1024 + lane * 16), __builtin_bit_cast(u32x4, __builtin_shufflevector(lo, hi, 0, 1, 2, 3, 4, 5, 6, 7)));
    }
}
constexpr int WU4 = 3;
constexpr int HELPER_DELAY = 48, HELPER_DELAY2 = 24, POLL0 = 16, POLL1 = 1;
constexpr int C1_COPY = 4288;
constexpr int C1_FILT = 4 * C1_COPY;
constexpr int C1_KV = 2304 * 2;
constexpr int C1_KVOFF = 4 * C1_FILT + 64;
constexpr int C1_YOFF = C1_KVOFF + 4 * C1_KV;
constexpr int C1_XOFF = C1_YOFF + 16384;
constexpr int C1_SOFF = C1_XOFF + 16384;
constexpr int C1_TOFF = C1_SOFF + 16384;
constexpr int C1_WOFF = C1_TOFF + 2048;
constexpr int C1_LDS = C1_WOFF + 24576;

extern "C" __global__ __launch_bounds__(512, 2) void k1_conv(const u16* __restrict__ x, const u16* __restrict__ kin, const u16* __restrict__ vin,
                                                  const u16* __restrict__ shift_k, const u16* __restrict__ ssm_k, const u16* __restrict__ ssm_D,
                                                  const u16* __restrict__ wg, const u16* __restrict__ wu, const u16* __restrict__ wd,
                                                  char* __restrict__ ws) {
    extern __shared__ __attribute__((aligned(16))) char smem[];
    const int tid = threadIdx.x, lane = tid & 63;
    const int wid = __builtin_amdgcn_readfirstlane(tid >> 6);
    const int b = blockIdx.x, xcd = b & 7, idx = b >> 3;
    {
        const char *c1, *c3;
        SYM_ADDR(c1, k1_conv); SYM_ADDR(c3, k3_gemm1);
        if (wid >= 4) warm_code<1>(c1, c3, (wid & 3) * 64, lane, (unsigned)(size_t)(LAS char*)smem + C1_TOFF + 256 + (wid & 3) * 256);
    }
    asm volatile("" :: "s"(x), "s"(kin), "s"(vin), "s"(shift_k), "s"(ssm_k), "s"(ssm_D), "s"(wg), "s"(wu), "s"(wd), "s"(ws));
    const bool conv = idx < 18;
    const int cg = xcd * 18 + idx, ch0 = cg * 4;
    const unsigned lds0 = (unsigned)(size_t)(LAS char*)smem;
    LAS unsigned* tagp = (LAS unsigned*)(smem + C1_TOFF);
    auto get_tag = [&]() -> unsigned { BAR(); return __builtin_amdgcn_readfirstlane(*(volatile LAS unsigned*)tagp); };

    const int h = (idx - 18) * 8 + xcd, hc_ = h < 108 ? h : 0;
    const int m3 = hc_ % 3, lcA = (hc_ / 3) * 4 + m3, rb0 = m3 * 3;
    constexpr int HU = (1728 - 144 * WU4 + 111) / 112;
    const int hbase = 144 * WU4 + h * HU, hun = hbase + wid * 2, hend = min(hbase + HU, 1728);
    auto cs_load = [&](u32x4 (&v)[12]) {
        const int rs = tid >> 3, c8 = tid & 7;
#pragma unroll
        for (int p = 0; p < 12; ++p) {
            const int rb = rb0 + p, lc = lcA + (rb >= 9), r = (rb >= 9 ? rb - 9 : rb) * 64 + rs;
            v[p] = *(const u32x4*)((lc >= 72 ? wu : wg) + (size_t)r * F_ + (lc % 72) * 64 + c8 * 8);
        }
        SCHED();
#pragma unroll
        for (int u = 0; u < 2; ++u) if (hun + u < hend) wd_unit_load(wd, hun + u, lane, lds0 + 16384 + (wid * 2 + u) * 3072);
        st16_wt(mk_rsrc(ws + WS_FB, 16384), (unsigned)(h * 8192 + tid * 16), (u32x4){0u, 0u, 0u, 0u});
        SCHED();
    };
    auto cs_finish = [&](u32x4 (&v)[12]) {
        float g[4][8];
#pragma unroll
        for (int q = 0; q < 4; ++q) {
#pragma unroll
            for (int e = 0; e < 4; ++e) {
                g[q][2 * e] = (bf_lo(v[3 * q][e]) + bf_lo(v[3 * q + 1][e])) + bf_lo(v[3 * q + 2][e]);
                g[q][2 * e + 1] = (bf_hi(v[3 * q][e]) + bf_hi(v[3 * q + 1][e])) + bf_hi(v[3 * q + 2][e]);
            }
        }
        LAS float* red = (LAS float*)smem;
#pragma unroll
        for (int l = 0; l < 2; ++l) {
            float a[8];
#pragma unroll
            for (int e = 0; e < 8; ++e) {
                a[e] = l == 0 ? g[0][e] + (m3 <= 1 ? g[1][e] : 0.f) + (m3 == 0 ? g[2][e] : 0.f) : g[3][e] + (m3 >= 1 ? g[2][e] : 0.f) + (m3 == 2 ? g[1][e] : 0.f);
                a[e] = x32_add(x16_add(dpp_add<0x128>(a[e])));
            }
            if (lane < 8) {
                *(LAS f32x4*)(red + (wid * 2 + l) * 64 + lane * 8) = (f32x4){a[0], a[1], a[2], a[3]};
                *(LAS f32x4*)(red + (wid * 2 + l) * 64 + lane * 8 + 4) = (f32x4){a[4], a[5], a[6], a[7]};
            }
        }
        WAIT_V(1);
#pragma unroll
        for (int u = 0; u < 2; ++u) if (hun + u < hend) wd_unit_store(ws, hun + u, lane, (const LAS char*)smem + 16384 + (wid * 2 + u) * 3072);
        asm volatile("s_waitcnt lgkmcnt(0)" ::: "memory"); BAR();
        if (tid < 192 && h < 108) {
            float s = 0.f;
            if (tid < 128) {
#pragma unroll
                for (int w = 0; w < 8; ++w) s += red[w * 128 + tid];
            }
            const int part = tid >> 6;
            const int lc = part == 0 ? lcA : (part == 1 ? lcA + 1 : (m3 == 0 ? lcA : lcA + 1));
            const int slot = part == 0 ? (m3 != 0) : (part == 1 ? 0 : 1);
            if (part < 2 || m3 != 1)
                __builtin_amdgcn_raw_buffer_store_b32(__float_as_uint(s), mk_rsrc(ws + WS_CS, 4 * F_ * 4), (unsigned)((slot * 2 * F_ + lc * 64 + (tid & 63)) * 4), 0, 16);
        }
    };
    auto wait_kv = [&](unsigned tag) {
        const rsrc_t rf = mk_rsrc(ws + WS_F0 + ((idx >> 2) * 8 + xcd) * 32 * FSP, 32 * FSP);
        __builtin_amdgcn_s_sleep(POLL0);
        for (int spin = 0; spin < (1 << 20); ++spin) {
            const unsigned f = __builtin_amdgcn_raw_buffer_load_b32(rf, (lane & 31) * FSP, 0, 16);
            if (__all(f == tag)) break;
            __builtin_amdgcn_s_sleep(POLL1);
        }
    };

    auto x_rows = [&]() {
        u32x2 xr[8];
#pragma unroll
        for (int i = 0; i < 8; ++i) xr[i] = *(const u32x2*)(x + (size_t)((tid - 256) + (wid - 4) * 448 + i * 64) * D_ + ch0);
#pragma unroll
        for (int i = 0; i < 8; ++i) *(LAS u32x2*)(smem + C1_XOFF + ((tid - 256) + (wid - 4) * 448 + i * 64) * 8) = xr[i];
        SCHED();
    };
    unsigned tag;
    unsigned dvv = 0;
    auto upper = [&](auto is_stat, auto is_conv) {
        constexpr bool STAT = decltype(is_stat)::v, CONV = decltype(is_conv)::v;
        const int srow = b * 8 + (wid - 5) * 4 + (lane >> 4), t3 = tid - 320;
        unsigned xw[18], epv = 0;
        if constexpr (STAT) {
            const unsigned* xr = (const unsigned*)(x + (size_t)srow * D_) + (lane & 15);
#pragma unroll
            for (int j = 0; j < 18; ++j) xw[j] = xr[16 * j];
        } else {
            epv = __hip_atomic_load((const unsigned*)(ws + WS_EP), __ATOMIC_RELAXED, __HIP_MEMORY_SCOPE_AGENT);
        }
        SCHED();
        if constexpr (STAT) {
            const char *c3, *c4;
            SYM_ADDR(c3, k3_gemm1); SYM_ADDR(c4, k9_end);
            warm_code<1>(c3, c4, ((idx & 3) * 2 + wid - 5) * 64, lane, lds0 + C1_TOFF + 256 + (wid - 5) * 256);
        }
        SCHED();
        u32x4 v[12], fa[6]; u32x2 fb[6];
        if constexpr (CONV) {
#pragma unroll
            for (int i = 0; i < 6; ++i) {
                const int task = min(t3 + 192 * i, 1023), u = task & 255;
                const u16* src = ssm_k + (size_t)(ch0 + (task >> 8)) * S_ + u * 8;
                fa[i] = *(const u32x4*)src;
                fb[i] = *(const u32x2*)(src + (u < 255 ? 8 : 0));
            }
            SCHED();
        }
        if constexpr (STAT) {
            f32x2 xf[18], s = {0.f, 0.f};
#pragma unroll
            for (int j = 0; j < 18; ++j) { xf[j] = (f32x2){bf_lo(xw[j]), bf_hi(xw[j])}; s += xf[j]; }
            const float mean = sum16(s.x + s.y) * (1.f / D_);
            f32x2 q = {0.f, 0.f};
#pragma unroll
            for (int j = 0; j < 18; ++j) { const f32x2 d = xf[j] - mean; q = __builtin_elementwise_fma(d, d, q); }
            const unsigned sgr = pk_bf16(mean, ln_sd(sum16(q.x + q.y) * (1.f / D_)));
            tag = get_tag();
            const unsigned so = (lane & 15) == 0 ? (unsigned)srow * 8 : 0x80000000u;
            st8_wt(mk_rsrc(ws + WS_ST, S_ * 8), so, (u32x2){sgr, tag});
            st8_wt(mk_rsrc(ws + WS_G2, S_ * 8), so, (u32x2){0u, 0u});
        } else {
            *tagp = epv + 1u;
            asm volatile("s_waitcnt lgkmcnt(0)" ::: "memory");
            tag = get_tag();
        }
        SCHED();
        if constexpr (!CONV) {
            __builtin_amdgcn_s_sleep(HELPER_DELAY);
            cs_load(v);
            if (!STAT && h == 111) {
                const rsrc_t rf = mk_rsrc(ws + WS_F0, 256 * FSP);
                for (int spin = 0; spin < (1 << 20); ++spin) {
                    bool ok = true;
#pragma unroll
                    for (int i = 0; i < 4; ++i) ok &= __builtin_amdgcn_raw_buffer_load_b32(rf, (i * 64 + lane) * FSP, 0, 16) == tag;
                    if (__all(ok)) break;
                    __builtin_amdgcn_s_sleep(8);
                }
                if (lane == 0) __hip_atomic_store((unsigned*)(ws + WS_EP), tag, __ATOMIC_RELAXED, __HIP_MEMORY_SCOPE_AGENT);
            }
            cs_finish(v);
            return;
        }
        if (t3 < 80) {
            const int cpy = t3 / 5, e = t3 % 5, q = e < 3 ? e : 257 + e;
            *(LAS u32x4*)(smem + cpy * C1_COPY + q * 16) = (u32x4){0u, 0u, 0u, 0u};
        } else if (t3 < 84) {
            *(LAS u32x4*)(smem + C1_KVOFF - 64 + (t3 - 80) * 16) = (u32x4){0u, 0u, 0u, 0u};
        }
        if (t3 < 128) *(LAS u32x4*)(smem + C1_KVOFF + (t3 >> 5) * C1_KV + 4096 + (t3 & 31) * 16) = (u32x4){0u, 0u, 0u, 0u};
#pragma unroll
        for (int i = 0; i < 6; ++i) {
            const int task = min(t3 + 192 * i, 1023), u = task & 255;
            const unsigned d[6] = {fa[i].x, fa[i].y, fa[i].z, fa[i].w, u < 255 ? fb[i].x : 0u, u < 255 ? fb[i].y : 0u};
            unsigned o[5];
#pragma unroll
            for (int e = 0; e < 5; ++e) o[e] = __builtin_amdgcn_alignbit(d[e + 1], d[e], 16);
            LAS char* base = (LAS char*)smem + (task >> 8) * C1_FILT + (u + 4) * 16;
            *(LAS u32x4*)(base) = (u32x4){o[0], o[1], o[2], o[3]};
            *(LAS u32x4*)(base + C1_COPY) = (u32x4){d[1], d[2], d[3], d[4]};
            *(LAS u32x4*)(base + 2 * C1_COPY) = (u32x4){o[1], o[2], o[3], o[4]};
            *(LAS u32x4*)(base + 3 * C1_COPY) = (u32x4){d[2], d[3], d[4], d[5]};
            if (u == 0) {
                *(LAS u32x4*)(base - 16) = (u32x4){0u, 0u, 0u, d[0] << 16};
                *(LAS u32x4*)(base - 16 + C1_COPY) = (u32x4){0u, 0u, 0u, d[0]};
                *(LAS u32x4*)(base - 16 + 2 * C1_COPY) = (u32x4){0u, 0u, d[0] << 16, o[0]};
                *(LAS u32x4*)(base - 16 + 3 * C1_COPY) = (u32x4){0u, 0u, d[0], d[1]};
            }
        }
        asm volatile("s_waitcnt lgkmcnt(0)" ::: "memory"); BAR();
        x_rows();
    };
    if (wid >= 5) {
        if (!conv) {
            if (wid < 7) upper(IC<1>{}, IC<0>{}); else upper(IC<0>{}, IC<0>{});
            return;
        }
        if (wid < 7) upper(IC<1>{}, IC<1>{}); else upper(IC<0>{}, IC<1>{});
    } else {
        {
            unsigned kw[11], vw[8];
            const int t0 = idx * 64 + (lane >> 3) * 8, dc = wid * 8 + (lane & 7), kch = xcd * 72 + (dc < 36 ? dc : 35) * 2;
            const rsrc_t rk = mk_rsrc(kin, S_ * D_ * 2), rv = mk_rsrc(vin, S_ * D_ * 2);
            const int off = t0 * (D_ * 2) + kch * 2;
#pragma unroll
            for (int j = 0; j < 11; ++j) kw[j] = __builtin_amdgcn_raw_buffer_load_b32(rk, (unsigned)(off + (j - 3) * (D_ * 2)), 0, 0);
#pragma unroll
            for (int j = 0; j < 8; ++j) vw[j] = __builtin_amdgcn_raw_buffer_load_b32(rv, (unsigned)(off + j * (D_ * 2)), 0, 0);
            const u32x4 sw = *(const u32x4*)(shift_k + kch * 4);
            dvv = ssm_D[(conv ? ch0 : 0) + (wid & 3)];
            SCHED();
            const f32x2 w0 = {bf_lo(sw.x), bf_lo(sw.z)}, w1 = {bf_hi(sw.x), bf_hi(sw.z)}, w2 = {bf_lo(sw.y), bf_lo(sw.w)}, w3 = {bf_hi(sw.y), bf_hi(sw.w)};
            f32x2 kf[11], o[8];
#pragma unroll
            for (int j = 0; j < 11; ++j) kf[j] = (f32x2){bf_lo(kw[j]), bf_hi(kw[j])};
#pragma unroll
            for (int j = 0; j < 8; ++j) {
                f32x2 s = kf[j] * w3;
                s = __builtin_elementwise_fma(kf[j + 1], w2, s);
                s = __builtin_elementwise_fma(kf[j + 2], w1, s);
                s = __builtin_elementwise_fma(kf[j + 3], w0, s);
                o[j] = s * (f32x2){bf_lo(vw[j]), bf_hi(vw[j])};
            }
            const rsrc_t ro = mk_rsrc(ws + WS_KV, S_ * D_ * 2);
            const unsigned oo = dc < 36 ? (unsigned)(kch * (S_ * 2) + (2040 - t0) * 2) : 0x80000000u;
            st16_wt(ro, oo, (u32x4){pk_bf16(o[7].x, o[6].x), pk_bf16(o[5].x, o[4].x), pk_bf16(o[3].x, o[2].x), pk_bf16(o[1].x, o[0].x)});
            st16_wt(ro, oo + S_ * 2, (u32x4){pk_bf16(o[7].y, o[6].y), pk_bf16(o[5].y, o[4].y), pk_bf16(o[3].y, o[2].y), pk_bf16(o[1].y, o[0].y)});
        }
        tag = get_tag();
        WAIT_V(0);
        if (lane == 0) __hip_atomic_store((unsigned*)(ws + WS_F0 + ((wid * 8 + xcd) * 32 + idx) * FSP), tag, __ATOMIC_RELAXED, __HIP_MEMORY_SCOPE_AGENT);
        if (!conv) {
            u32x4 v[12];
            __builtin_amdgcn_s_sleep(HELPER_DELAY2);
            cs_load(v);
            cs_finish(v);
            return;
        }
        if (wid == 4) {
            x_rows();
            BAR();
#pragma unroll 1
            for (int u = 0; u < WU4; ++u) wd_unit_load(wd, cg * WU4 + u, lane, lds0 + C1_WOFF + u * 3072);
            __builtin_amdgcn_s_sleep(24);
            const char* gs = ws + WS_ST;
#pragma unroll
            for (int i = 0; i < 4; ++i) {
                dma16_sc1<0>(lane * 16, gs + i * 4096, lds0 + C1_SOFF + i * 4096); dma16_sc1<1024>(lane * 16, gs + i * 4096, lds0 + C1_SOFF + i * 4096);
                dma16_sc1<2048>(lane * 16, gs + i * 4096, lds0 + C1_SOFF + i * 4096); dma16_sc1<3072>(lane * 16, gs + i * 4096, lds0 + C1_SOFF + i * 4096);
            }
            WAIT_V(0);
#pragma unroll 1
            for (int u = 0; u < WU4; ++u) wd_unit_store(ws, cg * WU4 + u, lane, (const LAS char*)smem + C1_WOFF + u * 3072);
        } else {
        {
            wait_kv(tag);
            const char* kvs = ws + WS_KV + (size_t)(ch0 + wid) * (S_ * 2);
            const unsigned m = lds0 + C1_KVOFF + wid * C1_KV;
            dma16_sc1<0>(lane * 16, kvs, m); dma16_sc1<1024>(lane * 16, kvs, m); dma16_sc1<2048>(lane * 16, kvs, m); dma16_sc1<3072>(lane * 16, kvs, m);
            WAIT_V(0);
            BAR();
        }
        const int row = lane & 15, kg = lane >> 4;
        const LAS char* abase = (const LAS char*)smem + wid * C1_FILT + (row & 3) * C1_COPY + (24 + 4 * (row >> 2) - 8 * kg) * 2;
        const LAS char* bbase = (const LAS char*)smem + C1_KVOFF + wid * C1_KV + (2040 - 16 * row - 8 * kg) * 2 - 512 * 7 - 448;
        auto rd_ah = [&](int r, int L, int hf) -> s16x4 { return *(const volatile LAS s16x4*)(abase + 512 * L - 64 * r + 8 * hf); };
        auto rd_b = [&](int r, int m) -> s16x8 { return *(const LAS s16x8*)(bbase + 448 - 64 * r + 512 * (7 - m)); };
        auto cat = [&](s16x4 lo, s16x4 hi) -> s16x8 { return __builtin_shufflevector(lo, hi, 0, 1, 2, 3, 4, 5, 6, 7); };
        f32x4 acc[8];
        s16x4 al[2][8], ah[2][8];
        s16x8 bf[2][8], bd[8];
        auto rd_k = [&](int r, int k) {
            if (k < 2) { if (k == 0) al[r & 1][0] = rd_ah(r, 1, 0); else ah[r & 1][0] = rd_ah(r, 1, 1); }
            else if (k < 10) bf[r & 1][9 - k] = rd_b(r, 8 - k);
            else { const int L = (k - 10) / 2 + 2; if (k & 1) ah[r & 1][L - 1] = rd_ah(r, L, 1); else al[r & 1][L - 1] = rd_ah(r, L, 0); }
        };
        const s16x8 a0 = cat(rd_ah(0, 0, 0), rd_ah(0, 0, 1));
#pragma unroll
        for (int e = 0; e < 8; ++e) bd[e] = rd_b(0, e);
        s16x8 dfr;
        {
            const int j = 8 * kg + 7 - row;
            u32x4 w;
#pragma unroll
            for (int e = 0; e < 4; ++e) w[e] = j == 2 * e ? dvv : (j == 2 * e + 1 ? dvv << 16 : 0u);
            dfr = __builtin_bit_cast(s16x8, w);
            asm volatile("s_nop 1" : "+v"(dfr));
        }
        SCHED();
#pragma unroll
        for (int n = 0; n < 16; ++n) {
            if (n < 8) mfma_first(acc[n], a0, bd[n]); else mfma_acc(acc[n - 8], dfr, bd[n - 8]);
            SCHED();
            if (n == 0) { rd_k(0, 0); rd_k(0, 1); }
            else if (n == 1) rd_k(0, 9);
            else rd_k(0, n + 8);
            SCHED();
        }
#pragma unroll
        for (int r = 0; r < 8; ++r) {
            int k = 0;
#pragma unroll
            for (int L = 1; L <= 8; ++L) {
#pragma unroll
                for (int n = 7; n >= L - 1; --n) {
                    const int e = n - L + 1;
                    mfma_acc(acc[n], cat(al[r & 1][L - 1], ah[r & 1][L - 1]), (r == 0 && e > 0) ? bd[e - 1] : bf[r & 1][e]);
                    SCHED();
                    if (r < 7 && k < 24) rd_k(r + 1, k);
                    ++k;
                    SCHED();
                }
            }
        }
        MFMA_DONE();
#pragma unroll
        for (int n = 0; n < 8; ++n)
            *(LAS u32x2*)(smem + C1_YOFF + wid * 4096 + (256 * n + 16 * row + 4 * kg) * 2) = (u32x2){pk_bf16(acc[n][0], acc[n][1]), pk_bf16(acc[n][2], acc[n][3])};
        }
    }
    asm volatile("s_waitcnt lgkmcnt(0)" ::: "memory"); BAR();
    {
        u32x2 yc[4];
#pragma unroll
        for (int c = 0; c < 4; ++c) yc[c] = *(const LAS u32x2*)(smem + C1_YOFF + c * 4096 + tid * 8);
        const u32x4 xa = *(const LAS u32x4*)(smem + C1_XOFF + tid * 32), xb = *(const LAS u32x4*)(smem + C1_XOFF + tid * 32 + 16);
        const u32x2 xr[4] = {{xa.x, xa.y}, {xa.z, xa.w}, {xb.x, xb.y}, {xb.z, xb.w}};
        u32x4 s01 = *(const LAS u32x4*)(smem + C1_SOFF + tid * 32), s23 = *(const LAS u32x4*)(smem + C1_SOFF + tid * 32 + 16);
        if (__builtin_expect(!__all(s01.y == tag && s01.w == tag && s23.y == tag && s23.w == tag), 0)) {
            const rsrc_t rs = mk_rsrc(ws + WS_ST, S_ * 8);
            for (int spin = 0; spin < (1 << 20); ++spin) {
                s01 = __builtin_amdgcn_raw_buffer_load_b128(rs, tid * 32, 0, 16);
                s23 = __builtin_amdgcn_raw_buffer_load_b128(rs, tid * 32 + 16, 0, 16);
                if (__all(s01.y == tag && s01.w == tag && s23.y == tag && s23.w == tag)) break;
                __builtin_amdgcn_s_sleep(2);
            }
        }
        const unsigned sv[4] = {s01.x, s01.z, s23.x, s23.z};
        float mm[4], rr[4];
#pragma unroll
        for (int j = 0; j < 4; ++j) { mm[j] = bf_lo(sv[j]); rr[j] = __builtin_amdgcn_rcpf(bf_hi(sv[j])); }
        unsigned hw[8];
#pragma unroll
        for (int j = 0; j < 4; ++j) {
#pragma unroll
            for (int e = 0; e < 2; ++e) {
                const unsigned xw = e ? xr[j].y : xr[j].x;
                const unsigned ya = (j >> 1) ? yc[2 * e].y : yc[2 * e].x, yb = (j >> 1) ? yc[2 * e + 1].y : yc[2 * e + 1].x;
                const float y0 = (j & 1) ? bf_hi(ya) : bf_lo(ya), y1 = (j & 1) ? bf_hi(yb) : bf_lo(yb);
                const float x0 = bf_lo(xw), x1 = bf_hi(xw);
                hw[2 * j + e] = pk_bf16(__builtin_fmaf((x0 - mm[j]) * rr[j], y0, x0), __builtin_fmaf((x1 - mm[j]) * rr[j], y1, x1));
            }
        }
        const rsrc_t rh = mk_rsrc(ws + WS_H, S_ * D_ * 2);
        const unsigned ho = (unsigned)(((tid >> 6) * 144 + cg) * 2048 + (tid & 63) * 32);
        st16_wt(rh, ho, (u32x4){hw[0], hw[1], hw[2], hw[3]});
        st16_wt(rh, ho + 16, (u32x4){hw[4], hw[5], hw[6], hw[7]});
    }
}

constexpr int ST_BYTES = 16384 + 2 * 9216;
constexpr int G_LDS = 4 * ST_BYTES;

struct TokC { float nm, c0, c1, q; };
__device__ __forceinline__ unsigned geglu2(f32x2 ag, f32x2 au, f32x2 csg, f32x2 csu, const TokC& t) {
    const f32x2 nm = {t.nm, t.nm}, c0 = {t.c0, t.c0}, c1 = {t.c1, t.c1}, q = {t.q, t.q};
    const f32x2 g = __builtin_elementwise_fma(nm, csg, ag), u = __builtin_elementwise_fma(nm, csu, au);
    const f32x2 z = g * __builtin_elementwise_fma(g * g, c1, c0);
    const f32x2 e = {__builtin_amdgcn_exp2f(z.x), __builtin_amdgcn_exp2f(z.y)};
    const f32x2 d = __builtin_elementwise_fma(e, q, q);
    const f32x2 r = (g * u) * (f32x2){__builtin_amdgcn_rcpf(d.x), __builtin_amdgcn_rcpf(d.y)};
    return pk_bf16(r.x, r.y);
}
#define LO2(v) __builtin_shufflevector(v, v, 0, 1)
#define HI2(v) __builtin_shufflevector(v, v, 2, 3)
#define WAIT_VI(N) asm volatile("s_waitcnt vmcnt(%0)" :: "i"(N) : "memory")

constexpr int G2_W0 = 71680, G2_FL = 69632;
__device__ __forceinline__ void g2_wpiece(int P, int s, const char* wsrc, unsigned aoff, unsigned mw0) {
    const unsigned m = mw0 + (s % 5) * 18432;
    if (P == 0) dma16<0>(aoff, wsrc + (size_t)s * 18432, m);
    if (P == 1) dma16<1024>(aoff, wsrc + (size_t)s * 18432, m);
    if (P == 2) dma16_q<2048>(aoff, wsrc + (size_t)s * 18432, m);
}
constexpr int g2_n(int S) { return (S + 3 < 18 ? 2 : 0) + (S + 4 < 18 ? 3 : 0) + (S == 4 ? 1 : 0); }
constexpr int g2_vm(int S) {
    return S == 0 ? 3 : (S == 1 ? 1 + g2_n(0) : g2_n(S - 2) - (S + 1 < 18 ? 2 : 0) + g2_n(S - 1));
}
template <int NT>
__device__ __forceinline__ void g2_body(char* __restrict__ ws, u16* __restrict__ out, LAS char* lds, int lane, int wid, int mt, int ks, int nt,
                                        const char* wsrc, unsigned flag2) {
    constexpr int dh = NT == 4;
    const unsigned lds0 = (unsigned)(size_t)lds;
    const int wr = wid & 3;
    const char* asrc = ws + WS_HH + ((size_t)(mt * 4 + ks) * 36) * 8192 + wid * 2048;
    const unsigned aoff = lane * 16;
    const unsigned ma0 = lds0 + wid * 2048, mw0 = lds0 + G2_W0 + wid * 2304;
    auto hpiece = [&](int P, int s) {
        if (P == 0) dma16_sc1<0>(aoff, asrc + (size_t)s * 16384, ma0 + (s & 3) * 16384);
        if (P == 1) dma16_sc1<1024>(aoff, asrc + (size_t)s * 16384, ma0 + (s & 3) * 16384);
    };
    hpiece(0, 0); hpiece(1, 0); hpiece(0, 1); hpiece(1, 1); hpiece(0, 2); hpiece(1, 2);

    const int g4 = lane >> 4;
    const int toff = wr * 2048 + lane * 16;
    const int foff = G2_W0 + dh * 5120 + lane * 16;
    auto rdf = [&](const LAS char* p) -> s16x8 { return *(const LAS s16x8*)p; };
    const rsrc_t rfb = mk_rsrc(ws + WS_FB, 16384);

    f32x4 acc[NT][2];
    s16x8 wf[2 * NT], tf[2][4];
    WAIT_V(4);
    __builtin_amdgcn_raw_buffer_store_b32(1u, rfb, lane == 0 ? flag2 : 0x80000000u, 0, 16);
    BAR();
#pragma unroll
    for (int e = 0; e < 4; ++e) tf[0][e] = rdf(lds + toff + (e >> 1) * 8192 + (e & 1) * 1024);
#pragma unroll
    for (int e = 0; e < 2 * NT; ++e) wf[e] = rdf(lds + foff + (e / NT) * 9216 + (e % NT) * 1024);
    SCHED();

    auto iter = [&](auto sc, s16x8 (&tc)[4], s16x8 (&tn)[4]) {
        constexpr int S = decltype(sc)::v;
        if constexpr (S < 17) {
            WAIT_VI(g2_vm(S));
            BAR();
            if constexpr (S == 7) {
                unsigned fb2 = *(const volatile LAS unsigned*)(lds + G2_FL + wid * 256 + lane * 4);
                if (__builtin_expect(!__all(fb2 == 1u), 0)) {
                    for (int spin = 0; spin < (1 << 20); ++spin) {
                        fb2 = __builtin_amdgcn_raw_buffer_load_b32(rfb, (unsigned)(((mt * 4 + ks) * 2 + 1) * 128 + (lane & 31) * 4), 0, 16);
                        if (__all(fb2 == 1u)) break;
                        __builtin_amdgcn_s_sleep(2);
                    }
                }
            }
            SCHED();
        }
        const LAS char* tb = lds + ((S + 1) & 3) * 16384;
        const LAS char* wb = lds + ((S + 1) % 5) * 18432;
#pragma unroll
        for (int e = 0; e < 2 * NT; ++e) {
#pragma unroll
            for (int i = 0; i < 2; ++i) {
                if (S == 0 && e < NT) mfma_first(acc[e % NT][i], wf[e], tc[(e / NT) * 2 + i]);
                else mfma_acc(acc[e % NT][i], wf[e], tc[(e / NT) * 2 + i]);
            }
            SCHED();
            if constexpr (S < 17) {
                wf[e] = rdf(wb + foff + (e / NT) * 9216 + (e % NT) * 1024);
                if (e >= 2 && e < 6) tn[e - 2] = rdf(tb + toff + ((e - 2) >> 1) * 8192 + ((e - 2) & 1) * 1024);
            }
            if constexpr (S + 3 < 18) { if (e < 2) hpiece(e, S + 3); }
            if constexpr (S + 4 < 18) {
                if (e == 3) g2_wpiece(0, S + 4, wsrc, aoff, mw0);
                if (e == 5) g2_wpiece(1, S + 4, wsrc, aoff, mw0);
                if (e == 6) g2_wpiece(2, S + 4, wsrc, aoff, mw0);
            }
            if constexpr (S == 4) {
                if (e == 7) dma4_sc1((unsigned)(lane & 31) * 4, ws + WS_FB + ((mt * 4 + ks) * 2 + 1) * 128, lds0 + G2_FL + wid * 256);
            }
            SCHED();
        }
    };
#define G2_IT2(S) iter(IC<S>{}, tf[0], tf[1]); iter(IC<S + 1>{}, tf[1], tf[0])
    G2_IT2(0); G2_IT2(2); G2_IT2(4); G2_IT2(6); G2_IT2(8); G2_IT2(10); G2_IT2(12);
    G2_IT2(14);
    iter(IC<16>{}, tf[0], tf[1]);
    const int tok0 = mt * 128 + wr * 32 + (lane & 15);
    u32x2 hv[2][5];
    if (wr == ks) {
#pragma unroll
        for (int i = 0; i < 2; ++i) {
            const int t = tok0 + i * 16;
            const char* hp = ws + WS_H + ((size_t)((t >> 8) * 144 + nt * 36 + dh * 20) * 256 + (t & 255)) * 8;
#pragma unroll
            for (int e = 0; e < 4; ++e) hv[i][e] = *(const u32x2*)(hp + ((e >> 1) * 8 + (g4 & 1) * 4 + (g4 >> 1) * 2 + (e & 1)) * 2048);
            if (NT == 5) hv[i][4] = *(const u32x2*)(hp + (16 + g4) * 2048);
        }
    }
    SCHED();
    iter(IC<17>{}, tf[1], tf[0]);
    MFMA_DONE();

    const rsrc_t rx = mk_rsrc(ws + WS_X, X_BYTES);
    const unsigned xt = (unsigned)((mt * 4 + nt) * 4 * 27648 + dh * 4096);
    auto inb = [&](int s) -> unsigned { return xt + (unsigned)(((ks + 1 + s) & 3) * 27648 + (2 - s) * 9216); };
    if (wr != ks) {
        const unsigned o = xt + (unsigned)(ks * 27648 + ((wr - ks - 1) & 3) * 9216);
#pragma unroll
        for (int i = 0; i < 2; ++i) {
            unsigned w[2 * NT];
#pragma unroll
            for (int j = 0; j < NT; ++j) { w[2 * j] = pk_bf16(acc[j][i][0], acc[j][i][1]); w[2 * j + 1] = pk_bf16(acc[j][i][2], acc[j][i][3]); }
#pragma unroll
            for (int e = 0; e < 2 * NT; ++e) w[e] = (w[e] == ~0u) ? 0x7fc07fc0u : w[e];
            if constexpr (NT == 5) st8_wt(rx, o + 8192 + i * 512 + lane * 8, (u32x2){w[8], w[9]});
            st16_wt(rx, o + i * 2048 + lane * 16, (u32x4){w[0], w[1], w[2], w[3]});
            st16_wt(rx, o + i * 2048 + 1024 + lane * 16, (u32x4){w[4], w[5], w[6], w[7]});
        }
    } else {
        u32x4 pa[3][4]; u32x2 pb[3][2];
        __builtin_amdgcn_s_sleep(16);
        for (int spin = 0; spin < (1 << 18); ++spin) {
            const u32x4 t = __builtin_amdgcn_raw_buffer_load_b128(rx, lane < 3 ? inb(lane) + 4080 : 0x80000000u, 0, 16);
            if (__all(t.w != ~0u)) {
                unsigned m = 0u;
#pragma unroll
                for (int s = 0; s < 3; ++s) {
#pragma unroll
                    for (int p = 0; p < 4; ++p) pa[s][p] = __builtin_amdgcn_raw_buffer_load_b128(rx, inb(s) + p * 1024 + lane * 16, 0, 16);
                    if constexpr (NT == 5) {
#pragma unroll
                        for (int i = 0; i < 2; ++i) pb[s][i] = __builtin_amdgcn_raw_buffer_load_b64(rx, inb(s) + 8192 + i * 512 + lane * 8, 0, 16);
                    }
                }
#pragma unroll
                for (int s = 0; s < 3; ++s) {
#pragma unroll
                    for (int p = 0; p < 4; ++p) m = max(max(max(m, pa[s][p].x), max(pa[s][p].y, pa[s][p].z)), pa[s][p].w);
                    if constexpr (NT == 5) m = max(max(max(m, pb[s][0].x), max(pb[s][0].y, pb[s][1].x)), pb[s][1].y);
                }
                if (__all(m != ~0u)) break;
            }
            __builtin_amdgcn_s_sleep(1);
        }
        const rsrc_t ro = mk_rsrc(out, S_ * D_ * 2);
#pragma unroll
        for (int i = 0; i < 2; ++i) {
            const unsigned ob = (unsigned)(((tok0 + i * 16) * D_ + nt * 144 + dh * 80) * 2);
            unsigned r[NT][2];
#pragma unroll
            for (int j = 0; j < NT; ++j) {
                f32x4 t = acc[j][i];
#pragma unroll
                for (int s = 0; s < 3; ++s) {
                    const unsigned w0 = j < 4 ? pa[s][i * 2 + (j >> 1)][(j & 1) * 2] : pb[s][i].x, w1 = j < 4 ? pa[s][i * 2 + (j >> 1)][(j & 1) * 2 + 1] : pb[s][i].y;
                    t += (f32x4){bf_lo(w0), bf_hi(w0), bf_lo(w1), bf_hi(w1)};
                }
                r[j][0] = pk_bf16(t[0], t[1]); r[j][1] = pk_bf16(t[2], t[3]);
            }
            auto addh = [&](unsigned a, unsigned h) -> unsigned { return pk_bf16(bf_lo(h) + bf_lo(a), bf_hi(h) + bf_hi(a)); };
#pragma unroll
            for (int p = 0; p < 2; ++p) {
                auto s0 = __builtin_amdgcn_permlane16_swap(r[2 * p][0], r[2 * p + 1][0], false, false);
                auto s1 = __builtin_amdgcn_permlane16_swap(r[2 * p][1], r[2 * p + 1][1], false, false);
                st16_wt(ro, ob + (unsigned)((p * 32 + (g4 & 1) * 16 + (g4 >> 1) * 8) * 2),
                        (u32x4){addh(s0[0], hv[i][2 * p].x), addh(s1[0], hv[i][2 * p].y), addh(s0[1], hv[i][2 * p + 1].x), addh(s1[1], hv[i][2 * p + 1].y)});
            }
            if constexpr (NT == 5) st8_wt(ro, ob + (unsigned)((64 + g4 * 4) * 2), (u32x2){addh(r[4][0], hv[i][4].x), addh(r[4][1], hv[i][4].y)});
        }
    }
}

constexpr int K3_LDS = 163840;
constexpr int K3_SP = G_LDS;
constexpr int K3_CS = G2_W0 + 4 * 18432;
extern "C" __global__ __launch_bounds__(512, 2) void k3_gemm1(const u16* __restrict__ wg, const u16* __restrict__ wu,
                                                   char* __restrict__ ws, u16* __restrict__ out) {
    extern __shared__ __attribute__((aligned(16))) char smem[];
    LAS char* lds = (LAS char*)smem;
    const unsigned lds0 = (unsigned)(size_t)lds;
    const int tid = threadIdx.x, lane = tid & 63;
    const int wid = __builtin_amdgcn_readfirstlane(tid >> 6);
    const int wr = wid & 3, wc = wid >> 2;
    const int b = blockIdx.x, xcd = b & 7, idx = b >> 3;
    const int rt = (xcd >> 2) * 4 + (idx & 3), ct = (xcd & 3) * 8 + (idx >> 2);
    asm volatile("" :: "s"(wg), "s"(wu), "s"(ws), "s"(out));

    const char* asrc = ws + WS_H + (size_t)rt * 144 * 2048 + wid * 2048;
    const unsigned aoff = (unsigned)(lane ^ ((wid & 2) << 2)) * 16;
    const unsigned ma0 = lds0 + wid * 2048, mw0 = lds0 + 16384 + wc * 9216 + wr * 2304;
    dma16<0>(aoff, asrc, ma0); dma16<1024>(aoff, asrc, ma0);
    const char* wsrc = (const char*)(wc ? wu : wg) + ct * 288 - 2048;
    auto wofs = [&](int i) -> unsigned {
        const int q = lane + 64 * i, lrow = 8 * wr + q / 18, pos = q % 18;
        const int krow = ((lrow >> 2) & 3) * 8 + (lrow >> 4) * 4 + (lrow & 3);
        const int col = pos * 8 + (pos < 8 ? 0 : (pos < 16 ? 8 : (((pos & 1) ^ wc) ? 136 : 64) - pos * 8));
        return (unsigned)(krow * (F_ * 2) + col * 2 + 2048 - 1024 * i);
    };
    const unsigned w0 = wofs(0), w1 = wofs(1), w2 = wofs(2);
    auto piece = [&](int P, int s) {
        const unsigned bo = (s & 3) * ST_BYTES;
        const char* wp = wsrc + (size_t)s * (32 * F_ * 2);
        if (P == 0) dma16<0>(aoff, asrc + (size_t)s * 16384, ma0 + bo);
        if (P == 1) dma16<1024>(aoff, asrc + (size_t)s * 16384, ma0 + bo);
        if (P == 2) dma16<0>(w0, wp, mw0 + bo);
        if (P == 3) dma16<1024>(w1, wp, mw0 + bo);
        if (P == 4) dma16_q<2048>(w2, wp, mw0 + bo);
    };
#define K3_PIECE(P, s) piece(P, s)
#define K3_STAGE(s) do { K3_PIECE(0, s); K3_PIECE(1, s); K3_PIECE(2, s); K3_PIECE(3, s); K3_PIECE(4, s); } while (0)
    K3_PIECE(2, 0); K3_PIECE(3, 0); K3_PIECE(4, 0);
    BAR();
    K3_STAGE(1);
    u32x2 hs[4];
    {
        const char* hsrc = ws + WS_H + (size_t)rt * 144 * 2048 + ct * 64;
        const unsigned o = (unsigned)((wid * 18 + (lane >> 3)) * 2048 + (lane & 7) * 8);
        asm volatile("global_load_dwordx2 %0, %1, %2" : "=v"(hs[0]) : "v"(o), "s"(hsrc) : "memory");
        asm volatile("global_load_dwordx2 %0, %1, %2" : "=v"(hs[1]) : "v"(o + 16384), "s"(hsrc) : "memory");
        asm volatile("global_load_dwordx2 %0, %1, %2" : "=v"(hs[2]) : "v"((unsigned)((wid * 18 + 16 + ((lane >> 3) & 1)) * 2048 + (lane & 7) * 8)), "s"(hsrc) : "memory");
        asm volatile("global_load_dwordx2 %0, %1, %2" : "=v"(hs[3]) : "v"((unsigned)(lane & 7) * 8), "s"(hsrc) : "memory");
    }

    const int g4 = lane >> 4, q4 = (lane & 15) >> 2, p4 = lane & 3;
    const int rowoff = (4 * g4 + q4) * 288;
    const int tbase = g4 * 4096 + wr * 512 + (lane & 15) * 8;
    const int toff0 = tbase + ((2 * wc) ^ (g4 & 1)) * 128, toff1 = tbase + ((2 * wc + 1) ^ (g4 & 1)) * 128;
    const int tstep = wc ? -256 : 256;
    const int foff = 16384 + rowoff + wc * 128 + p4 * 8;
    const int moff = 16384 + rowoff + 256 + (p4 < 2 ? 16 * wc + 8 * p4 : 9216 + 16 * (1 - wc) + 8 * (p4 - 2));

    f32x4 acc[9][4];
    s16x8 wf[9], tf[2][4];
    auto rd_w = [&](const LAS char* sb, int j) -> s16x8 {
        return j < 8 ? tr_frag(sb + foff + (j >> 2) * 9216 + (j & 3) * 32) : tr_frag(sb + moff);
    };
    auto rd_t = [&](const LAS char* sb, int i) -> s16x8 {
        const LAS char* p = sb + ((i & 1) ? toff1 : toff0) + (i >> 1) * tstep;
        const s16x4 lo = *(const volatile LAS s16x4*)p, hi = *(const volatile LAS s16x4*)(p + 2048);
        return __builtin_shufflevector(lo, hi, 0, 1, 2, 3, 4, 5, 6, 7);
    };
    WAIT_V(9); BAR();
    wf[0] = rd_w(lds, 0);
#pragma unroll
    for (int i = 0; i < 4; ++i) tf[0][i] = rd_t(lds, i);
#pragma unroll
    for (int j = 1; j < 9; ++j) wf[j] = rd_w(lds, j);
    SCHED();
    const int mt2 = (xcd >> 2) * 8 + (idx & 7), ks2 = xcd & 3, nt2 = idx >> 3;
    const rsrc_t rfill = mk_rsrc(ws + WS_X + (size_t)((mt2 * 4 + nt2) * 4 + ks2) * 27648, 27648);
    auto fill = [&](int k) { st16_wt(rfill, (unsigned)(k * 8192 + tid * 16), (u32x4){~0u, ~0u, ~0u, ~0u}); };
    unsigned sgr;
    {
        const LAS char* sb = lds + ST_BYTES;
        float hc = 0.f;
#pragma unroll
        for (int j = 0; j < 9; ++j) {
#pragma unroll
            for (int i = 0; i < 4; ++i) mfma_first(acc[j][i], wf[j], tf[0][i]);
            SCHED();
            if (j == 0) { K3_PIECE(0, 2); K3_PIECE(1, 2); }
            if (j == 1) { K3_PIECE(2, 2); K3_PIECE(3, 2); }
            if (j == 2) K3_PIECE(4, 2);
            if (j == 3) {
                asm volatile("s_waitcnt vmcnt(5)" : "+v"(hs[0]), "+v"(hs[1]), "+v"(hs[2]), "+v"(hs[3]) :: "memory");
                hc = bf_lo(hs[3].x);
                float s1 = 0.f, s2 = 0.f;
#pragma unroll
                for (int i = 0; i < 3; ++i) {
                    const float d[4] = {bf_lo(hs[i].x) - hc, bf_hi(hs[i].x) - hc, bf_lo(hs[i].y) - hc, bf_hi(hs[i].y) - hc};
                    float a = (d[0] + d[1]) + (d[2] + d[3]), q = __builtin_fmaf(d[0], d[0], d[1] * d[1]) + __builtin_fmaf(d[2], d[2], d[3] * d[3]);
                    if (i == 2 && lane >= 16) { a = 0.f; q = 0.f; }
                    s1 += a; s2 += q;
                }
                s1 = x32_add(x16_add(dpp_add<0x128>(s1))); s2 = x32_add(x16_add(dpp_add<0x128>(s2)));
                if (lane < 8) *(LAS f32x2*)(lds + K3_SP + (lane * 8 + wid) * 8) = (f32x2){s1, s2};
                asm volatile("s_waitcnt lgkmcnt(0)" ::: "memory");
                BAR();
                wf[0] = rd_w(sb, 0); wf[1] = rd_w(sb, 1);
                tf[1][0] = rd_t(sb, 0); tf[1][1] = rd_t(sb, 1);
            }
            if (j == 4) { tf[1][2] = rd_t(sb, 2); tf[1][3] = rd_t(sb, 3); wf[2] = rd_w(sb, 2); wf[3] = rd_w(sb, 3); }
            if (j >= 4) { wf[j] = rd_w(sb, j); K3_PIECE(j - 4, 3); }
            if (j == 8) {
                const f32x2 pp = *(const LAS f32x2*)(lds + K3_SP + (wid * 8 + (lane & 7)) * 8);
                const float s1 = dpp_add<0x141>(dpp_add<0x4E>(dpp_add<0xB1>(pp.x))) * (1.f / D_);
                const float s2 = dpp_add<0x141>(dpp_add<0x4E>(dpp_add<0xB1>(pp.y))) * (1.f / D_);
                const float c0 = __uint_as_float(__builtin_amdgcn_readlane(__float_as_uint(hc), wid));
                const float sd = ln_sd(__builtin_fmaxf(__builtin_fmaf(-s1, s1, s2), 0.f));
                sgr = pk_bf16(c0 + s1, sd);
                fill(0); fill(1);
            }
            SCHED();
        }
    }

    const rsrc_t rg = mk_rsrc(ws + WS_G2 + (size_t)(rt * 256 + wr * 64) * 8, 512);
    const char* w2src = ws + WS_WT + (size_t)(ks2 * 4 + nt2) * 18 * 18432 + wid * 2304;
    u32x2 gr[4];
    s16x4 tl;
    constexpr int TP = 1, DPJ0 = 0, DPJ1 = 5;
    auto iter = [&](auto mode, auto vm, int s, s16x8 (&tc)[4], s16x8 (&tn)[4]) {
        constexpr int MODE = decltype(mode)::v;
        if constexpr (MODE != 4) {
            if constexpr (MODE == 3) WAIT_V(0); else WAIT_VI(5 + decltype(vm)::v);
            BAR();
            SCHED();
        }
        const LAS char* sb = lds + ((s + 1) & 3) * ST_BYTES;
#pragma unroll
        for (int j = 0; j < 9; ++j) {
            s16x4 wl, wh;
#pragma unroll
            for (int i = 0; i < 4; ++i) {
                mfma_acc(acc[j][i], wf[j], tc[i]);
                SCHED();
                if constexpr (MODE != 4) {
                    if (j >= 1) {
                        const LAS char* p = (j - 1 < 8) ? sb + foff + ((j - 1) >> 2) * 9216 + ((j - 1) & 3) * 32 : sb + moff;
                        if (i == 0) wl = __builtin_amdgcn_ds_read_tr16_b64_v4i16((LAS s16x4*)p);
                        if (i == 1) { wh = __builtin_amdgcn_ds_read_tr16_b64_v4i16((LAS s16x4*)(p + 16 * 288)); wf[j - 1] = __builtin_shufflevector(wl, wh, 0, 1, 2, 3, 4, 5, 6, 7); }
                    }
                    if (j >= TP && j < TP + 4) {
                        const int ti = j - TP;
                        const LAS char* p = sb + ((ti & 1) ? toff1 : toff0) + (ti >> 1) * tstep;
                        if (i == 2) tl = *(const volatile LAS s16x4*)p;
                        if (i == 3) tn[ti] = __builtin_shufflevector(tl, *(const volatile LAS s16x4*)(p + 2048), 0, 1, 2, 3, 4, 5, 6, 7);
                    }
                    if (j == 8 && i == 3) wf[8] = rd_w(sb, 8);
                }
                if constexpr (MODE < 2 || MODE == 5) {
                    if (j == DPJ0 && i == 1) K3_PIECE(0, s + 3);
                    if (j == DPJ0 && i == 3) K3_PIECE(1, s + 3);
                    if (j == DPJ1 && i == 3) K3_PIECE(2, s + 3);
                    if (j == DPJ1 + 1 && i == 3) K3_PIECE(3, s + 3);
                    if (j == DPJ1 + 2 && i == 3) K3_PIECE(4, s + 3);
                }
                SCHED();
            }
            if constexpr (MODE == 0) { if (j == 8) { fill(2); fill(3); } }
            if constexpr (MODE == 5) {
                if (j == 0 && s == 4) st8_wt(mk_rsrc(ws + WS_G2, S_ * 8), lane == 0 ? (unsigned)((rt * 256 + ct * 8 + wid) * 8) : 0x80000000u, (u32x2){sgr, 1u});
            }
            if constexpr (MODE == 2) {
                if (j == 0) {
#pragma unroll
                    for (int i = 0; i < 4; ++i) gr[i] = __builtin_amdgcn_raw_buffer_load_b64(rg, (unsigned)(((i ^ (2 * wc)) * 16 + (lane & 15)) * 8), 0, 16);
                }
            }
            if constexpr (MODE == 3) {
                if (j == 0) {
                    asm volatile("" : "+v"(gr[0]), "+v"(gr[1]), "+v"(gr[2]), "+v"(gr[3]));
                    const unsigned co = (unsigned)((lane >= 18 ? F_ * 4 + (lane - 18) * 16 : lane * 16));
                    dma_m36(co, ws + WS_CS + (size_t)(ct * 144 + wc * 72) * 4, lds0 + K3_CS + wid * 1152);
                    dma_m36(co, ws + WS_CS + (size_t)(2 * F_ + ct * 144 + wc * 72) * 4, lds0 + K3_CS + wid * 1152 + 576);
                }
            }
            if constexpr (MODE == 3 || MODE == 4) {
                if (j >= 2 && j < 8) g2_wpiece((j - 2) % 3, (MODE - 3) * 2 + (j - 2) / 3, w2src, lane * 16, lds0 + G2_W0 + wid * 2304);
            }
            SCHED();
        }
    };
    iter(IC<0>{}, IC<2>{}, 1, tf[1], tf[0]);
    iter(IC<1>{}, IC<4>{}, 2, tf[0], tf[1]);
    iter(IC<1>{}, IC<2>{}, 3, tf[1], tf[0]);
#pragma unroll 1
    for (int s = 4; s < 14; s += 2) {
        iter(IC<5>{}, IC<0>{}, s, tf[0], tf[1]);
        iter(IC<1>{}, IC<0>{}, s + 1, tf[1], tf[0]);
    }
    iter(IC<1>{}, IC<0>{}, 14, tf[0], tf[1]);
    iter(IC<2>{}, IC<0>{}, 15, tf[1], tf[0]);
    iter(IC<3>{}, IC<0>{}, 16, tf[0], tf[1]);
    iter(IC<4>{}, IC<0>{}, 17, tf[1], tf[0]);
    MFMA_DONE();
    WAIT_V(12);
    __builtin_amdgcn_s_setprio(1);
    if (__builtin_expect(!__all((gr[0].y & gr[1].y & gr[2].y & gr[3].y) == 1u), 0)) {
        for (int spin = 0; spin < (1 << 20); ++spin) {
            __builtin_amdgcn_s_sleep(2);
#pragma unroll
            for (int i = 0; i < 4; ++i) gr[i] = __builtin_amdgcn_raw_buffer_load_b64(rg, (unsigned)(((i ^ (2 * wc)) * 16 + (lane & 15)) * 8), 0, 16);
            if (__all((gr[0].y & gr[1].y & gr[2].y & gr[3].y) == 1u)) break;
        }
    }
    const int mt = rt * 2 + (wr >> 1), ks = ct >> 3, lct = ct & 7;
    const rsrc_t rhh = mk_rsrc(ws + WS_HH, S_ * F_ * 2);
    const unsigned hb = (unsigned)(((mt * 4 + ks) * 36) * 8192 + ((wr & 1) * 4) * 1024);
    const LAS char* csb = lds + K3_CS + wid * 1152 + 16 * g4;
    TokC tcs[4];
#pragma unroll
    for (int i = 0; i < 4; ++i) {
        const float sd = bf_hi(gr[i].x), r = __builtin_amdgcn_rcpf(sd);
        tcs[i] = (TokC){-bf_lo(gr[i].x), -2.30220819f * r, -0.10294324f * (r * r * r), sd * sd};
    }
    {
        const LAS char* cm = lds + K3_CS + wid * 1152 + 256 + 16 * (g4 & 1);
        const f32x4 cgm = *(const LAS f32x4*)cm + *(const LAS f32x4*)(cm + 576), cum = *(const LAS f32x4*)(cm + 288) + *(const LAS f32x4*)(cm + 864);
#pragma unroll
        for (int ip = 0; ip < 2; ++ip) {
            const int ia = ((2 * ip) ^ (2 * wc)) + (lane >> 5);
            const bool up = lane >= 32;
            const TokC tc = {up ? tcs[2 * ip + 1].nm : tcs[2 * ip].nm, up ? tcs[2 * ip + 1].c0 : tcs[2 * ip].c0,
                             up ? tcs[2 * ip + 1].c1 : tcs[2 * ip].c1, up ? tcs[2 * ip + 1].q : tcs[2 * ip].q};
            f32x4 gm, um;
#pragma unroll
            for (int r = 0; r < 4; ++r) {
                auto sw = __builtin_amdgcn_permlane32_swap(__float_as_uint(acc[8][2 * ip][r]), __float_as_uint(acc[8][2 * ip + 1][r]), false, false);
                gm[r] = __uint_as_float(sw[0]); um[r] = __uint_as_float(sw[1]);
            }
            const int kgs = (lct & 1) * 2 + wc;
            st8_wt(rhh, hb + (unsigned)((lct >> 1) * 8192 + ia * 1024 + (kgs * 16 + (lane & 15)) * 16 + (g4 & 1) * 8),
                   (u32x2){geglu2(LO2(gm), LO2(um), LO2(cgm), LO2(cum), tc), geglu2(HI2(gm), HI2(um), HI2(cgm), HI2(cum), tc)});
        }
    }
    SCHED();
    constexpr int FLAGPOS = 1;
    const rsrc_t rfb = mk_rsrc(ws + WS_FB, 16384);
    const unsigned fbo = (unsigned)((mt * 4 + ks) * 256 + (lct * 4 + (wr & 1) * 2 + wc) * 4);
    u32x4 ob[4];
#pragma unroll
    for (int jp = 0; jp < 2; ++jp) {
        f32x4 cg[2], cu[2];
#pragma unroll
        for (int e = 0; e < 2; ++e) {
            cg[e] = *(const LAS f32x4*)(csb + (2 * jp + e) * 64) + *(const LAS f32x4*)(csb + 576 + (2 * jp + e) * 64);
            cu[e] = *(const LAS f32x4*)(csb + 288 + (2 * jp + e) * 64) + *(const LAS f32x4*)(csb + 864 + (2 * jp + e) * 64);
        }
#pragma unroll
        for (int i = 0; i < 4; ++i) {
            unsigned o[4];
#pragma unroll
            for (int e = 0; e < 2; ++e) {
                const f32x4 g = acc[2 * jp + e][i], u = acc[4 + 2 * jp + e][i];
                o[2 * e] = geglu2(LO2(g), LO2(u), LO2(cg[e]), LO2(cu[e]), tcs[i]);
                o[2 * e + 1] = geglu2(HI2(g), HI2(u), HI2(cg[e]), HI2(cu[e]), tcs[i]);
            }
            ob[i] = (u32x4){o[0], o[1], o[2], o[3]};
            st16_wt(rhh, (jp == 1 && wid == 7) ? 0x80000000u : hb + (unsigned)(((2 + jp * 8 + lct) * 2 + wc) * 8192 + (i ^ (2 * wc)) * 1024 + lane * 16), ob[i]);
            if (jp == 1 && i == FLAGPOS) {
                SCHED();
                WAIT_VI(FLAGPOS + 1);
                __builtin_amdgcn_raw_buffer_store_b32(1u, rfb, lane == 0 ? fbo : 0x80000000u, 0, 16);
                SCHED();
            }
        }
        SCHED();
        if (jp == 0) __builtin_amdgcn_s_setprio(0);
    }
    if (wid == 7) {
        for (int spin = 0; spin < (1 << 20); ++spin) {
            const unsigned f = __builtin_amdgcn_raw_buffer_load_b32(rfb, (unsigned)((mt2 * 4 + ks2) * 256 + (lane & 31) * 4), 0, 16);
            if (__all(f == 1u)) break;
            __builtin_amdgcn_s_sleep(4);
        }
#pragma unroll
        for (int i = 0; i < 4; ++i) st16_wt(rhh, hb + (unsigned)(((10 + lct) * 2 + wc) * 8192 + (i ^ (2 * wc)) * 1024 + lane * 16), ob[i]);
    }
    BAR();
    if (wid < 4) g2_body<5>(ws, out, lds, lane, wid, mt2, ks2, nt2, w2src, fbo + 128);
    else g2_body<4>(ws, out, lds, lane, wid, mt2, ks2, nt2, w2src, fbo + 128);
}

extern "C" __global__ void k9_end() {}

__global__ __launch_bounds__(512) void k_pre(int mode, const u32x4* q, const u32x4* k, const u32x4* v, const u32x4* sk, const u32x4* wg, const u32x4* wu, const u32x4* wd, u32x4* ws, u32x4* out) {
    const unsigned long long t0 = __builtin_amdgcn_s_memrealtime();
    const int g = blockIdx.x * 512 + threadIdx.x; unsigned a = 0;
    if (mode & 1) {
        for (int i = g; i < 147456; i += 131072) a ^= q[i].x ^ k[i].x ^ v[i].x ^ sk[i].x;
        for (int i = g; i < 331776; i += 131072) a ^= wg[i].x ^ wu[i].x ^ wd[i].x;
    }
    if (mode & 2) { for (int i = g; i < 2293760; i += 131072) a ^= ws[i].x; for (int i = g; i < 147456; i += 131072) a ^= out[i].x; }
    if (mode & 8) { const char *c1, *ce; SYM_ADDR(c1, k1_conv); SYM_ADDR(ce, k9_end); const int o = ((blockIdx.x >> 3) * 512 + threadIdx.x) * 64; if (c1 + o < ce) a ^= *(const unsigned*)(c1 + o); }
    asm volatile("" :: "v"(a));
    while (__builtin_amdgcn_s_memrealtime() - t0 < 1500ull) __builtin_amdgcn_s_sleep(4);
}
extern "C" void kernel_launch(void* const* d_in, const int* in_sizes, int n_in, void* d_out, int out_size, void* d_ws,
                              size_t ws_size, hipStream_t stream) {
    const u16* q = (const u16*)d_in[0];
    const u16* k = (const u16*)d_in[1];
    const u16* v = (const u16*)d_in[2];
    const u16* shift_k = (const u16*)d_in[3];
    const u16* ssm_k = (const u16*)d_in[4];
    const u16* ssm_D = (const u16*)d_in[5];
    const u16* wg = (const u16*)d_in[7];
    const u16* wu = (const u16*)d_in[8];
    const u16* wd = (const u16*)d_in[9];
    char* ws = (char*)d_ws;
    (void)hipFuncSetAttribute((const void*)k1_conv, hipFuncAttributeMaxDynamicSharedMemorySize, C1_LDS);
    (void)hipFuncSetAttribute((const void*)k3_gemm1, hipFuncAttributeMaxDynamicSharedMemorySize, K3_LDS);
    k_pre<<<256, 512, 0, stream>>>(11, (const u32x4*)q, (const u32x4*)k, (const u32x4*)v, (const u32x4*)ssm_k, (const u32x4*)wg, (const u32x4*)wu, (const u32x4*)wd, (u32x4*)ws, (u32x4*)d_out);
    k1_conv<<<256, 512, C1_LDS, stream>>>(q, k, v, shift_k, ssm_k, ssm_D, wg, wu, wd, ws);
    k3_gemm1<<<256, 512, K3_LDS, stream>>>(wg, wu, ws, (u16*)d_out);
}
```

```cpp
#include <hip/hip_runtime.h>
#include <hip/hip_bf16.h>

#define LAS __attribute__((address_space(3)))
typedef unsigned short u16;
typedef short s16x4 __attribute__((ext_vector_type(4)));
typedef short s16x8 __attribute__((ext_vector_type(8)));
typedef float f32x2 __attribute__((ext_vector_type(2)));
typedef float f32x4 __attribute__((ext_vector_type(4)));
typedef unsigned u32x2 __attribute__((ext_vector_type(2)));
typedef unsigned u32x4 __attribute__((ext_vector_type(4)));
typedef __bf16 bf16x2_t __attribute__((ext_vector_type(2)));

constexpr int S_ = 2048, D_ = 576, F_ = 4608;

__device__ __forceinline__ unsigned pk_bf16(float lo, float hi) {
    f32x2 v = {lo, hi};
    bf16x2_t b = __builtin_convertvector(v, bf16x2_t);
    return __builtin_bit_cast(unsigned, b);
}
__device__ __forceinline__ float bf_lo(unsigned w) { return __uint_as_float(w << 16); }
__device__ __forceinline__ float bf_hi(unsigned w) { return __uint_as_float(w & 0xffff0000u); }
__device__ __forceinline__ float rbf(float x) { return bf_lo(pk_bf16(x, 0.f)); }
#define WAIT_V(n) asm volatile("s_waitcnt vmcnt(" #n ")" ::: "memory")
#define BAR() do { __builtin_amdgcn_s_barrier(); asm volatile("" ::: "memory"); } while (0)
#define SCHED() __builtin_amdgcn_sched_barrier(0)
#define DIAG_DECL
#define MARK()
#define STAMP(i)
#define DIAG_END(wsbase, kid)
__device__ __forceinline__ void mfma_acc(f32x4& c, s16x8 a, s16x8 b) {
    asm volatile("v_mfma_f32_16x16x32_bf16 %0, %1, %2, %0" : "+v"(c) : "v"(a), "v"(b));
}
__device__ __forceinline__ void mfma_first(f32x4& c, s16x8 a, s16x8 b) {
    asm volatile("v_mfma_f32_16x16x32_bf16 %0, %1, %2, 0" : "=&v"(c) : "v"(a), "v"(b));
}
#define MFMA_DONE() do { asm volatile("s_nop 7\n\ts_nop 7" ::: "memory"); SCHED(); } while (0)
constexpr size_t WS_KV = 0;
constexpr size_t WS_H = WS_KV + (size_t)S_ * D_ * 2;
constexpr size_t WS_ST = WS_H + (size_t)S_ * D_ * 2;
constexpr size_t WS_CS = WS_ST + (size_t)S_ * 8;
constexpr size_t WS_G2 = WS_CS + (size_t)4 * F_ * 4;
constexpr size_t WS_F0 = WS_G2 + (size_t)S_ * 8;
constexpr int FSP = 128;
constexpr size_t WS_EP = WS_F0 + 1280 * FSP;
constexpr size_t WS_HH = WS_EP + 1024;
constexpr size_t WS_X = WS_HH + (size_t)S_ * F_ * 2;
constexpr unsigned X_BYTES = 64 * 4 * 3 * 9216;
constexpr size_t WS_FB = WS_X + X_BYTES;
constexpr size_t WS_WT = WS_FB + 16384;

template <int N> struct IC { static constexpr int v = N; };
typedef __amdgpu_buffer_rsrc_t rsrc_t;
__device__ __forceinline__ rsrc_t mk_rsrc(const void* p, unsigned bytes) { return __builtin_amdgcn_make_buffer_rsrc((void*)p, 0, bytes, 0x00020000); }
__device__ __forceinline__ void st16_wt(rsrc_t r, unsigned off, u32x4 v) { __builtin_amdgcn_raw_buffer_store_b128(v, r, off, 0, 16); }
__device__ __forceinline__ void st8_wt(rsrc_t r, unsigned off, u32x2 v) { __builtin_amdgcn_raw_buffer_store_b64(v, r, off, 0, 16); }
template <int CTRL> __device__ __forceinline__ float dpp_add(float x) {
    return x + __uint_as_float(__builtin_amdgcn_update_dpp(0, (int)__float_as_uint(x), CTRL, 0xf, 0xf, true));
}
__device__ __forceinline__ float x16_add(float x) {
    auto r = __builtin_amdgcn_permlane16_swap(__float_as_uint(x), __float_as_uint(x), false, false);
    return __uint_as_float(r[0]) + __uint_as_float(r[1]);
}
__device__ __forceinline__ float x32_add(float x) {
    auto r = __builtin_amdgcn_permlane32_swap(__float_as_uint(x), __float_as_uint(x), false, false);
    return __uint_as_float(r[0]) + __uint_as_float(r[1]);
}
__device__ __forceinline__ float sum16(float x) {
    x = dpp_add<0xB1>(x); x = dpp_add<0x4E>(x); x = dpp_add<0x141>(x);
    return dpp_add<0x140>(x);
}
__device__ __forceinline__ float ln_sd(float var) { return rbf(__builtin_amdgcn_sqrtf(rbf(rbf(var) + 1e-5f))); }

template <int OFF>
__device__ __forceinline__ void dma16(unsigned voff, const void* sbase, unsigned m0v) {
    asm volatile("s_mov_b32 m0, %2\n\ts_nop 0\n\tglobal_load_lds_dwordx4 %0, %1 offset:%3"
                 :: "v"(voff), "s"(sbase), "s"(m0v), "i"(OFF) : "memory");
}
template <int OFF>
__device__ __forceinline__ void dma16_sc1(unsigned voff, const void* sbase, unsigned m0v) {
    asm volatile("s_mov_b32 m0, %2\n\ts_nop 0\n\tglobal_load_lds_dwordx4 %0, %1 offset:%3 sc1"
                 :: "v"(voff), "s"(sbase), "s"(m0v), "i"(OFF) : "memory");
}
template <int OFF>
__device__ __forceinline__ void dma16_q(unsigned voff, const void* sbase, unsigned m0v) {
    asm volatile("s_mov_b32 m0, %2\n\ts_mov_b64 exec, 0xffff\n\tglobal_load_lds_dwordx4 %0, %1 offset:%3\n\ts_mov_b64 exec, -1"
                 :: "v"(voff), "s"(sbase), "s"(m0v), "i"(OFF) : "memory");
}
__device__ __forceinline__ void dma_m36(unsigned voff, const void* sbase, unsigned m0v) {
    asm volatile("s_mov_b32 m0, %2\n\ts_mov_b32 exec_hi, 15\n\tglobal_load_lds_dwordx4 %0, %1\n\ts_mov_b64 exec, -1"
                 :: "v"(voff), "s"(sbase), "s"(m0v) : "memory");
}
__device__ __forceinline__ void dma4(unsigned voff, const void* sbase, unsigned m0v) {
    asm volatile("s_mov_b32 m0, %2\n\ts_nop 0\n\tglobal_load_lds_dword %0, %1" :: "v"(voff), "s"(sbase), "s"(m0v) : "memory");
}
__device__ __forceinline__ void dma4_sc1(unsigned voff, const void* sbase, unsigned m0v) {
    asm volatile("s_mov_b32 m0, %2\n\ts_nop 0\n\tglobal_load_lds_dword %0, %1 sc1" :: "v"(voff), "s"(sbase), "s"(m0v) : "memory");
}
#define SYM_ADDR(out, sym) asm volatile("s_getpc_b64 s[40:41]\n\ts_add_u32 s40, s40, " #sym "@rel32@lo+4\n\ts_addc_u32 s41, s41, " #sym "@rel32@hi+12\n\ts_mov_b64 %0, s[40:41]" \
                                        : "=s"(out) :: "s40", "s41", "scc")
template <int NI>
__device__ __forceinline__ void warm_code(const char* a, const char* e, int l0, int lane, unsigned m0v) {
    const unsigned last = e > a ? (unsigned)(e - a) - 4u : 0u;
#pragma unroll
    for (int i = 0; i < NI; ++i) dma4(min((unsigned)(l0 + i * 64 + lane) * 128u, last), a, m0v);
}
__device__ __forceinline__ s16x8 tr_frag(const LAS char* p) {
    const s16x4 lo = __builtin_amdgcn_ds_read_tr16_b64_v4i16((LAS s16x4*)p);
    const s16x4 hi = __builtin_amdgcn_ds_read_tr16_b64_v4i16((LAS s16x4*)(p + 16 * 288));
    return __builtin_shufflevector(lo, hi, 0, 1, 2, 3, 4, 5, 6, 7);
}
__device__ __forceinline__ void wd_unit_load(const u16* wd, int un, int lane, unsigned m0v) {
    const int uB = un / 3, ut = un % 3, kh = uB & 1, st = (uB >> 1) % 18, kn = uB / 36;
    const bool full = st >= 2;
    const int f0 = (kn >> 2) * 1152 + (full ? ((st - 2) & 7) * 144 + kh * 72 + ((st - 2) >> 3) * 32 : (st * 2 + kh) * 288);
    const char* src = (const char*)wd + (size_t)f0 * (D_ * 2) + (kn & 3) * 288 + ut * 96;
#pragma unroll
    for (int i = 0; i < 3; ++i) {
        const int q = lane + 64 * i, r = q / 6, pos = q % 6, kg = (r & 15) >> 2;
        const int f = full ? r : (kg >> 1) * 144 + (kg & 1) * 72 + 64 + (r & 3) + 4 * (r >> 4);
        dma16<0>((unsigned)(f * (D_ * 2) + pos * 16), src, m0v + i * 1024);
    }
}
__device__ __forceinline__ void wd_unit_store(char* ws, int un, int lane, const LAS char* l) {
    const rsrc_t r = mk_rsrc(ws + WS_WT, F_ * D_ * 2);
#pragma unroll
    for (int t = 0; t < 3; ++t) {
        const LAS char* tp = l + (lane >> 2) * 96 + (lane & 3) * 8 + t * 32;
        const s16x4 lo = __builtin_amdgcn_ds_read_tr16_b64_v4i16((LAS s16x4*)tp), hi = __builtin_amdgcn_ds_read_tr16_b64_v4i16((LAS s16x4*)(tp + 16 * 96));
        st16_wt(r, (unsigned)(un * 3072 + t * 1024 + lane * 16), __builtin_bit_cast(u32x4, __builtin_shufflevector(lo, hi, 0, 1, 2, 3, 4, 5, 6, 7)));
    }
}
constexpr int WU4 = 3;
constexpr int HELPER_DELAY = 48, HELPER_DELAY2 = 24, POLL0 = 16, POLL1 = 1;
constexpr int C1_COPY = 4288;
constexpr int C1_FILT = 4 * C1_COPY;
constexpr int C1_KV = 2304 * 2;
constexpr int C1_KVOFF = 4 * C1_FILT + 64;
constexpr int C1_YOFF = C1_KVOFF + 4 * C1_KV;
constexpr int C1_XOFF = C1_YOFF + 16384;
constexpr int C1_SOFF = C1_XOFF + 16384;
constexpr int C1_TOFF = C1_SOFF + 16384;
constexpr int C1_WOFF = C1_TOFF + 2048;
constexpr int C1_LDS = C1_WOFF + 24576;

extern "C" __global__ __launch_bounds__(512, 2) void k1_conv(const u16* __restrict__ x, const u16* __restrict__ kin, const u16* __restrict__ vin,
                                                  const u16* __restrict__ shift_k, const u16* __restrict__ ssm_k, const u16* __restrict__ ssm_D,
                                                  const u16* __restrict__ wg, const u16* __restrict__ wu, const u16* __restrict__ wd,
                                                  char* __restrict__ ws) {
    extern __shared__ __attribute__((aligned(16))) char smem[];
    const int tid = threadIdx.x, lane = tid & 63;
    const int wid = __builtin_amdgcn_readfirstlane(tid >> 6);
    const int b = blockIdx.x, xcd = b & 7, idx = b >> 3;
    {
        const char *c1, *c3;
        SYM_ADDR(c1, k1_conv); SYM_ADDR(c3, k3_gemm1);
        if (wid >= 4) warm_code<1>(c1, c3, (wid & 3) * 64, lane, (unsigned)(size_t)(LAS char*)smem + C1_TOFF + 256 + (wid & 3) * 256);
    }
    DIAG_DECL;
    asm volatile("" :: "s"(x), "s"(kin), "s"(vin), "s"(shift_k), "s"(ssm_k), "s"(ssm_D), "s"(wg), "s"(wu), "s"(wd), "s"(ws));
    const bool conv = idx < 18;
    const int cg = xcd * 18 + idx, ch0 = cg * 4;
    const unsigned lds0 = (unsigned)(size_t)(LAS char*)smem;
    LAS unsigned* tagp = (LAS unsigned*)(smem + C1_TOFF);
    auto get_tag = [&]() -> unsigned { BAR(); return __builtin_amdgcn_readfirstlane(*(volatile LAS unsigned*)tagp); };

    const int h = (idx - 18) * 8 + xcd, hc_ = h < 108 ? h : 0;
    const int m3 = hc_ % 3, lcA = (hc_ / 3) * 4 + m3, rb0 = m3 * 3;
    constexpr int HU = (1728 - 144 * WU4 + 111) / 112;
    const int hbase = 144 * WU4 + h * HU, hun = hbase + wid * 2, hend = min(hbase + HU, 1728);
    auto cs_load = [&](u32x4 (&v)[12]) {
        const int rs = tid >> 3, c8 = tid & 7;
#pragma unroll
        for (int p = 0; p < 12; ++p) {
            const int rb = rb0 + p, lc = lcA + (rb >= 9), r = (rb >= 9 ? rb - 9 : rb) * 64 + rs;
            v[p] = *(const u32x4*)((lc >= 72 ? wu : wg) + (size_t)r * F_ + (lc % 72) * 64 + c8 * 8);
        }
        SCHED();
#pragma unroll
        for (int u = 0; u < 2; ++u) if (hun + u < hend) wd_unit_load(wd, hun + u, lane, lds0 + 16384 + (wid * 2 + u) * 3072);
        st16_wt(mk_rsrc(ws + WS_FB, 16384), (unsigned)(h * 8192 + tid * 16), (u32x4){0u, 0u, 0u, 0u});
        SCHED();
    };
    auto cs_finish = [&](u32x4 (&v)[12]) {
        float g[4][8];
#pragma unroll
        for (int q = 0; q < 4; ++q) {
#pragma unroll
            for (int e = 0; e < 4; ++e) {
                g[q][2 * e] = (bf_lo(v[3 * q][e]) + bf_lo(v[3 * q + 1][e])) + bf_lo(v[3 * q + 2][e]);
                g[q][2 * e + 1] = (bf_hi(v[3 * q][e]) + bf_hi(v[3 * q + 1][e])) + bf_hi(v[3 * q + 2][e]);
            }
        }
        LAS float* red = (LAS float*)smem;
#pragma unroll
        for (int l = 0; l < 2; ++l) {
            float a[8];
#pragma unroll
            for (int e = 0; e < 8; ++e) {
                a[e] = l == 0 ? g[0][e] + (m3 <= 1 ? g[1][e] : 0.f) + (m3 == 0 ? g[2][e] : 0.f) : g[3][e] + (m3 >= 1 ? g[2][e] : 0.f) + (m3 == 2 ? g[1][e] : 0.f);
                a[e] = x32_add(x16_add(dpp_add<0x128>(a[e])));
            }
            if (lane < 8) {
                *(LAS f32x4*)(red + (wid * 2 + l) * 64 + lane * 8) = (f32x4){a[0], a[1], a[2], a[3]};
                *(LAS f32x4*)(red + (wid * 2 + l) * 64 + lane * 8 + 4) = (f32x4){a[4], a[5], a[6], a[7]};
            }
        }
        WAIT_V(1);
#pragma unroll
        for (int u = 0; u < 2; ++u) if (hun + u < hend) wd_unit_store(ws, hun + u, lane, (const LAS char*)smem + 16384 + (wid * 2 + u) * 3072);
        asm volatile("s_waitcnt lgkmcnt(0)" ::: "memory"); BAR();
        if (tid < 192 && h < 108) {
            float s = 0.f;
            if (tid < 128) {
#pragma unroll
                for (int w = 0; w < 8; ++w) s += red[w * 128 + tid];
            }
            const int part = tid >> 6;
            const int lc = part == 0 ? lcA : (part == 1 ? lcA + 1 : (m3 == 0 ? lcA : lcA + 1));
            const int slot = part == 0 ? (m3 != 0) : (part == 1 ? 0 : 1);
            if (part < 2 || m3 != 1)
                __builtin_amdgcn_raw_buffer_store_b32(__float_as_uint(s), mk_rsrc(ws + WS_CS, 4 * F_ * 4), (unsigned)((slot * 2 * F_ + lc * 64 + (tid & 63)) * 4), 0, 16);
        }
    };
    auto wait_kv = [&](unsigned tag) {
        const rsrc_t rf = mk_rsrc(ws + WS_F0 + ((idx >> 2) * 8 + xcd) * 32 * FSP, 32 * FSP);
        __builtin_amdgcn_s_sleep(POLL0);
        for (int spin = 0; spin < (1 << 20); ++spin) {
            const unsigned f = __builtin_amdgcn_raw_buffer_load_b32(rf, (lane & 31) * FSP, 0, 16);
            if (__all(f == tag)) break;
            __builtin_amdgcn_s_sleep(POLL1);
        }
    };

    auto x_rows = [&]() {
        u32x2 xr[8];
#pragma unroll
        for (int i = 0; i < 8; ++i) xr[i] = *(const u32x2*)(x + (size_t)((tid - 256) + (wid - 4) * 448 + i * 64) * D_ + ch0);
#pragma unroll
        for (int i = 0; i < 8; ++i) *(LAS u32x2*)(smem + C1_XOFF + ((tid - 256) + (wid - 4) * 448 + i * 64) * 8) = xr[i];
        SCHED();
    };
    unsigned tag;
    unsigned dvv = 0;
    auto upper = [&](auto is_stat, auto is_conv) {
        constexpr bool STAT = decltype(is_stat)::v, CONV = decltype(is_conv)::v;
        const int srow = b * 8 + (wid - 5) * 4 + (lane >> 4), t3 = tid - 320;
        unsigned xw[18], epv = 0;
        if constexpr (STAT) {
            const unsigned* xr = (const unsigned*)(x + (size_t)srow * D_) + (lane & 15);
#pragma unroll
            for (int j = 0; j < 18; ++j) xw[j] = xr[16 * j];
        } else {
            epv = __hip_atomic_load((const unsigned*)(ws + WS_EP), __ATOMIC_RELAXED, __HIP_MEMORY_SCOPE_AGENT);
        }
        SCHED();
        if constexpr (STAT) {
            const char *c3, *c4;
            SYM_ADDR(c3, k3_gemm1); SYM_ADDR(c4, k9_end);
            warm_code<1>(c3, c4, ((idx & 3) * 2 + wid - 5) * 64, lane, lds0 + C1_TOFF + 256 + (wid - 5) * 256);
        }
        SCHED();
        u32x4 v[12], fa[6]; u32x2 fb[6];
        if constexpr (CONV) {
#pragma unroll
            for (int i = 0; i < 6; ++i) {
                const int task = min(t3 + 192 * i, 1023), u = task & 255;
                const u16* src = ssm_k + (size_t)(ch0 + (task >> 8)) * S_ + u * 8;
                fa[i] = *(const u32x4*)src;
                fb[i] = *(const u32x2*)(src + (u < 255 ? 8 : 0));
            }
            SCHED();
        }
        if constexpr (STAT) {
            f32x2 xf[18], s = {0.f, 0.f};
#pragma unroll
            for (int j = 0; j < 18; ++j) { xf[j] = (f32x2){bf_lo(xw[j]), bf_hi(xw[j])}; s += xf[j]; }
            const float mean = sum16(s.x + s.y) * (1.f / D_);
            f32x2 q = {0.f, 0.f};
#pragma unroll
            for (int j = 0; j < 18; ++j) { const f32x2 d = xf[j] - mean; q = __builtin_elementwise_fma(d, d, q); }
            const unsigned sgr = pk_bf16(mean, ln_sd(sum16(q.x + q.y) * (1.f / D_)));
            tag = get_tag();
            const unsigned so = (lane & 15) == 0 ? (unsigned)srow * 8 : 0x80000000u;
            st8_wt(mk_rsrc(ws + WS_ST, S_ * 8), so, (u32x2){sgr, tag});
            st8_wt(mk_rsrc(ws + WS_G2, S_ * 8), so, (u32x2){0u, 0u});
        } else {
            *tagp = epv + 1u;
            asm volatile("s_waitcnt lgkmcnt(0)" ::: "memory");
            tag = get_tag();
        }
        SCHED();
        if constexpr (!CONV) {
            __builtin_amdgcn_s_sleep(HELPER_DELAY);
            cs_load(v);
            if (!STAT && h == 111) {
                const rsrc_t rf = mk_rsrc(ws + WS_F0, 256 * FSP);
                for (int spin = 0; spin < (1 << 20); ++spin) {
                    bool ok = true;
#pragma unroll
                    for (int i = 0; i < 4; ++i) ok &= __builtin_amdgcn_raw_buffer_load_b32(rf, (i * 64 + lane) * FSP, 0, 16) == tag;
                    if (__all(ok)) break;
                    __builtin_amdgcn_s_sleep(8);
                }
                if (lane == 0) __hip_atomic_store((unsigned*)(ws + WS_EP), tag, __ATOMIC_RELAXED, __HIP_MEMORY_SCOPE_AGENT);
            }
            cs_finish(v);
            return;
        }
        if (t3 < 80) {
            const int cpy = t3 / 5, e = t3 % 5, q = e < 3 ? e : 257 + e;
            *(LAS u32x4*)(smem + cpy * C1_COPY + q * 16) = (u32x4){0u, 0u, 0u, 0u};
        } else if (t3 < 84) {
            *(LAS u32x4*)(smem + C1_KVOFF - 64 + (t3 - 80) * 16) = (u32x4){0u, 0u, 0u, 0u};
        }
        if (t3 < 128) *(LAS u32x4*)(smem + C1_KVOFF + (t3 >> 5) * C1_KV + 4096 + (t3 & 31) * 16) = (u32x4){0u, 0u, 0u, 0u};
#pragma unroll
        for (int i = 0; i < 6; ++i) {
            const int task = min(t3 + 192 * i, 1023), u = task & 255;
            const unsigned d[6] = {fa[i].x, fa[i].y, fa[i].z, fa[i].w, u < 255 ? fb[i].x : 0u, u < 255 ? fb[i].y : 0u};
            unsigned o[5];
#pragma unroll
            for (int e = 0; e < 5; ++e) o[e] = __builtin_amdgcn_alignbit(d[e + 1], d[e], 16);
            LAS char* base = (LAS char*)smem + (task >> 8) * C1_FILT + (u + 4) * 16;
            *(LAS u32x4*)(base) = (u32x4){o[0], o[1], o[2], o[3]};
            *(LAS u32x4*)(base + C1_COPY) = (u32x4){d[1], d[2], d[3], d[4]};
            *(LAS u32x4*)(base + 2 * C1_COPY) = (u32x4){o[1], o[2], o[3], o[4]};
            *(LAS u32x4*)(base + 3 * C1_COPY) = (u32x4){d[2], d[3], d[4], d[5]};
            if (u == 0) {
                *(LAS u32x4*)(base - 16) = (u32x4){0u, 0u, 0u, d[0] << 16};
                *(LAS u32x4*)(base - 16 + C1_COPY) = (u32x4){0u, 0u, 0u, d[0]};
                *(LAS u32x4*)(base - 16 + 2 * C1_COPY) = (u32x4){0u, 0u, d[0] << 16, o[0]};
                *(LAS u32x4*)(base - 16 + 3 * C1_COPY) = (u32x4){0u, 0u, d[0], d[1]};
            }
        }
        asm volatile("s_waitcnt lgkmcnt(0)" ::: "memory"); BAR();
        x_rows();
    };
    if (wid >= 5) {
        if (!conv) {
            if (wid < 7) upper(IC<1>{}, IC<0>{}); else upper(IC<0>{}, IC<0>{});
            DIAG_END(ws, 0);
            return;
        }
        if (wid < 7) upper(IC<1>{}, IC<1>{}); else upper(IC<0>{}, IC<1>{});
    } else {
        {
            unsigned kw[11], vw[8];
            const int t0 = idx * 64 + (lane >> 3) * 8, dc = wid * 8 + (lane & 7), kch = xcd * 72 + (dc < 36 ? dc : 35) * 2;
            const rsrc_t rk = mk_rsrc(kin, S_ * D_ * 2), rv = mk_rsrc(vin, S_ * D_ * 2);
            const int off = t0 * (D_ * 2) + kch * 2;
#pragma unroll
            for (int j = 0; j < 11; ++j) kw[j] = __builtin_amdgcn_raw_buffer_load_b32(rk, (unsigned)(off + (j - 3) * (D_ * 2)), 0, 0);
#pragma unroll
            for (int j = 0; j < 8; ++j) vw[j] = __builtin_amdgcn_raw_buffer_load_b32(rv, (unsigned)(off + j * (D_ * 2)), 0, 0);
            const u32x4 sw = *(const u32x4*)(shift_k + kch * 4);
            dvv = ssm_D[(conv ? ch0 : 0) + (wid & 3)];
            SCHED();
            STAMP(1);
            const f32x2 w0 = {bf_lo(sw.x), bf_lo(sw.z)}, w1 = {bf_hi(sw.x), bf_hi(sw.z)}, w2 = {bf_lo(sw.y), bf_lo(sw.w)}, w3 = {bf_hi(sw.y), bf_hi(sw.w)};
            f32x2 kf[11], o[8];
#pragma unroll
            for (int j = 0; j < 11; ++j) kf[j] = (f32x2){bf_lo(kw[j]), bf_hi(kw[j])};
#pragma unroll
            for (int j = 0; j < 8; ++j) {
                f32x2 s = kf[j] * w3;
                s = __builtin_elementwise_fma(kf[j + 1], w2, s);
                s = __builtin_elementwise_fma(kf[j + 2], w1, s);
                s = __builtin_elementwise_fma(kf[j + 3], w0, s);
                o[j] = s * (f32x2){bf_lo(vw[j]), bf_hi(vw[j])};
            }
            const rsrc_t ro = mk_rsrc(ws + WS_KV, S_ * D_ * 2);
            const unsigned oo = dc < 36 ? (unsigned)(kch * (S_ * 2) + (2040 - t0) * 2) : 0x80000000u;
            st16_wt(ro, oo, (u32x4){pk_bf16(o[7].x, o[6].x), pk_bf16(o[5].x, o[4].x), pk_bf16(o[3].x, o[2].x), pk_bf16(o[1].x, o[0].x)});
            st16_wt(ro, oo + S_ * 2, (u32x4){pk_bf16(o[7].y, o[6].y), pk_bf16(o[5].y, o[4].y), pk_bf16(o[3].y, o[2].y), pk_bf16(o[1].y, o[0].y)});
        }
        tag = get_tag();
        WAIT_V(0);
        if (lane == 0) __hip_atomic_store((unsigned*)(ws + WS_F0 + ((wid * 8 + xcd) * 32 + idx) * FSP), tag, __ATOMIC_RELAXED, __HIP_MEMORY_SCOPE_AGENT);
        MARK();
        STAMP(2);
        if (!conv) {
            u32x4 v[12];
            __builtin_amdgcn_s_sleep(HELPER_DELAY2);
            cs_load(v);
            cs_finish(v);
            DIAG_END(ws, 0);
            return;
        }
        if (wid == 4) {
            x_rows();
            BAR();
#pragma unroll 1
            for (int u = 0; u < WU4; ++u) wd_unit_load(wd, cg * WU4 + u, lane, lds0 + C1_WOFF + u * 3072);
            __builtin_amdgcn_s_sleep(24);
            const char* gs = ws + WS_ST;
#pragma unroll
            for (int i = 0; i < 4; ++i) {
                dma16_sc1<0>(lane * 16, gs + i * 4096, lds0 + C1_SOFF + i * 4096); dma16_sc1<1024>(lane * 16, gs + i * 4096, lds0 + C1_SOFF + i * 4096);
                dma16_sc1<2048>(lane * 16, gs + i * 4096, lds0 + C1_SOFF + i * 4096); dma16_sc1<3072>(lane * 16, gs + i * 4096, lds0 + C1_SOFF + i * 4096);
            }
            WAIT_V(0);
#pragma unroll 1
            for (int u = 0; u < WU4; ++u) wd_unit_store(ws, cg * WU4 + u, lane, (const LAS char*)smem + C1_WOFF + u * 3072);
        } else {
        {
            wait_kv(tag);
            STAMP(7);
            const char* kvs = ws + WS_KV + (size_t)(ch0 + wid) * (S_ * 2);
            const unsigned m = lds0 + C1_KVOFF + wid * C1_KV;
            dma16_sc1<0>(lane * 16, kvs, m); dma16_sc1<1024>(lane * 16, kvs, m); dma16_sc1<2048>(lane * 16, kvs, m); dma16_sc1<3072>(lane * 16, kvs, m);
            WAIT_V(0);
            STAMP(9);
            BAR();
            STAMP(4);
        }
        const int row = lane & 15, kg = lane >> 4;
        const LAS char* abase = (const LAS char*)smem + wid * C1_FILT + (row & 3) * C1_COPY + (24 + 4 * (row >> 2) - 8 * kg) * 2;
        const LAS char* bbase = (const LAS char*)smem + C1_KVOFF + wid * C1_KV + (2040 - 16 * row - 8 * kg) * 2 - 512 * 7 - 448;
        auto rd_ah = [&](int r, int L, int hf) -> s16x4 { return *(const volatile LAS s16x4*)(abase + 512 * L - 64 * r + 8 * hf); };
        auto rd_b = [&](int r, int m) -> s16x8 { return *(const LAS s16x8*)(bbase + 448 - 64 * r + 512 * (7 - m)); };
        auto cat = [&](s16x4 lo, s16x4 hi) -> s16x8 { return __builtin_shufflevector(lo, hi, 0, 1, 2, 3, 4, 5, 6, 7); };
        f32x4 acc[8];
        s16x4 al[2][8], ah[2][8];
        s16x8 bf[2][8], bd[8];
        auto rd_k = [&](int r, int k) {
            if (k < 2) { if (k == 0) al[r & 1][0] = rd_ah(r, 1, 0); else ah[r & 1][0] = rd_ah(r, 1, 1); }
            else if (k < 10) bf[r & 1][9 - k] = rd_b(r, 8 - k);
            else { const int L = (k - 10) / 2 + 2; if (k & 1) ah[r & 1][L - 1] = rd_ah(r, L, 1); else al[r & 1][L - 1] = rd_ah(r, L, 0); }
        };
        const s16x8 a0 = cat(rd_ah(0, 0, 0), rd_ah(0, 0, 1));
#pragma unroll
        for (int e = 0; e < 8; ++e) bd[e] = rd_b(0, e);
        s16x8 dfr;
        {
            const int j = 8 * kg + 7 - row;
            u32x4 w;
#pragma unroll
            for (int e = 0; e < 4; ++e) w[e] = j == 2 * e ? dvv : (j == 2 * e + 1 ? dvv << 16 : 0u);
            dfr = __builtin_bit_cast(s16x8, w);
            asm volatile("s_nop 1" : "+v"(dfr));
        }
        SCHED();
#pragma unroll
        for (int n = 0; n < 16; ++n) {
            if (n < 8) mfma_first(acc[n], a0, bd[n]); else mfma_acc(acc[n - 8], dfr, bd[n - 8]);
            SCHED();
            if (n == 0) { rd_k(0, 0); rd_k(0, 1); }
            else if (n == 1) rd_k(0, 9);
            else rd_k(0, n + 8);
            SCHED();
        }
#pragma unroll
        for (int r = 0; r < 8; ++r) {
            int k = 0;
#pragma unroll
            for (int L = 1; L <= 8; ++L) {
#pragma unroll
                for (int n = 7; n >= L - 1; --n) {
                    const int e = n - L + 1;
                    mfma_acc(acc[n], cat(al[r & 1][L - 1], ah[r & 1][L - 1]), (r == 0 && e > 0) ? bd[e - 1] : bf[r & 1][e]);
                    SCHED();
                    if (r < 7 && k < 24) rd_k(r + 1, k);
                    ++k;
                    SCHED();
                }
            }
        }
        MFMA_DONE();
#pragma unroll
        for (int n = 0; n < 8; ++n)
            *(LAS u32x2*)(smem + C1_YOFF + wid * 4096 + (256 * n + 16 * row + 4 * kg) * 2) = (u32x2){pk_bf16(acc[n][0], acc[n][1]), pk_bf16(acc[n][2], acc[n][3])};
        }
    }
    STAMP(5);
    asm volatile("s_waitcnt lgkmcnt(0)" ::: "memory"); BAR();
    STAMP(6);
    {
        u32x2 yc[4];
#pragma unroll
        for (int c = 0; c < 4; ++c) yc[c] = *(const LAS u32x2*)(smem + C1_YOFF + c * 4096 + tid * 8);
        const u32x4 xa = *(const LAS u32x4*)(smem + C1_XOFF + tid * 32), xb = *(const LAS u32x4*)(smem + C1_XOFF + tid * 32 + 16);
        const u32x2 xr[4] = {{xa.x, xa.y}, {xa.z, xa.w}, {xb.x, xb.y}, {xb.z, xb.w}};
        u32x4 s01 = *(const LAS u32x4*)(smem + C1_SOFF + tid * 32), s23 = *(const LAS u32x4*)(smem + C1_SOFF + tid * 32 + 16);
        if (__builtin_expect(!__all(s01.y == tag && s01.w == tag && s23.y == tag && s23.w == tag), 0)) {
            const rsrc_t rs = mk_rsrc(ws + WS_ST, S_ * 8);
            for (int spin = 0; spin < (1 << 20); ++spin) {
                s01 = __builtin_amdgcn_raw_buffer_load_b128(rs, tid * 32, 0, 16);
                s23 = __builtin_amdgcn_raw_buffer_load_b128(rs, tid * 32 + 16, 0, 16);
                if (__all(s01.y == tag && s01.w == tag && s23.y == tag && s23.w == tag)) break;
                __builtin_amdgcn_s_sleep(2);
            }
        }
        const unsigned sv[4] = {s01.x, s01.z, s23.x, s23.z};
        float mm[4], rr[4];
#pragma unroll
        for (int j = 0; j < 4; ++j) { mm[j] = bf_lo(sv[j]); rr[j] = __builtin_amdgcn_rcpf(bf_hi(sv[j])); }
        unsigned hw[8];
#pragma unroll
        for (int j = 0; j < 4; ++j) {
#pragma unroll
            for (int e = 0; e < 2; ++e) {
                const unsigned xw = e ? xr[j].y : xr[j].x;
                const unsigned ya = (j >> 1) ? yc[2 * e].y : yc[2 * e].x, yb = (j >> 1) ? yc[2 * e + 1].y : yc[2 * e + 1].x;
                const float y0 = (j & 1) ? bf_hi(ya) : bf_lo(ya), y1 = (j & 1) ? bf_hi(yb) : bf_lo(yb);
                const float x0 = bf_lo(xw), x1 = bf_hi(xw);
                hw[2 * j + e] = pk_bf16(__builtin_fmaf((x0 - mm[j]) * rr[j], y0, x0), __builtin_fmaf((x1 - mm[j]) * rr[j], y1, x1));
            }
        }
        const rsrc_t rh = mk_rsrc(ws + WS_H, S_ * D_ * 2);
        const unsigned ho = (unsigned)(((tid >> 6) * 144 + cg) * 2048 + (tid & 63) * 32);
        st16_wt(rh, ho, (u32x4){hw[0], hw[1], hw[2], hw[3]});
        st16_wt(rh, ho + 16, (u32x4){hw[4], hw[5], hw[6], hw[7]});
    }
    STAMP(8);
    DIAG_END(ws, 0);
}

constexpr int ST_BYTES = 16384 + 2 * 9216;
constexpr int NBUF = 4;
constexpr int G_LDS = NBUF * ST_BYTES;

struct TokC { float nm, c0, c1, q; };
__device__ __forceinline__ unsigned geglu2(f32x2 ag, f32x2 au, f32x2 csg, f32x2 csu, const TokC& t) {
    const f32x2 nm = {t.nm, t.nm}, c0 = {t.c0, t.c0}, c1 = {t.c1, t.c1}, q = {t.q, t.q};
    const f32x2 g = __builtin_elementwise_fma(nm, csg, ag), u = __builtin_elementwise_fma(nm, csu, au);
    const f32x2 z = g * __builtin_elementwise_fma(g * g, c1, c0);
    const f32x2 e = {__builtin_amdgcn_exp2f(z.x), __builtin_amdgcn_exp2f(z.y)};
    const f32x2 d = __builtin_elementwise_fma(e, q, q);
    const f32x2 r = (g * u) * (f32x2){__builtin_amdgcn_rcpf(d.x), __builtin_amdgcn_rcpf(d.y)};
    return pk_bf16(r.x, r.y);
}
#define LO2(v) __builtin_shufflevector(v, v, 0, 1)
#define HI2(v) __builtin_shufflevector(v, v, 2, 3)
#define WAIT_VI(N) asm volatile("s_waitcnt vmcnt(%0)" :: "i"(N) : "memory")

constexpr int G2_W0 = 71680, G2_FL = 69632;
__device__ __forceinline__ void g2_wpiece(int P, int s, const char* wsrc, unsigned aoff, unsigned mw0) {
    const unsigned m = mw0 + (s % 5) * 18432;
    if (P == 0) dma16<0>(aoff, wsrc + (size_t)s * 18432, m);
    if (P == 1) dma16<1024>(aoff, wsrc + (size_t)s * 18432, m);
    if (P == 2) dma16_q<2048>(aoff, wsrc + (size_t)s * 18432, m);
}
constexpr int g2_n(int S) { return (S + 3 < 18 ? 2 : 0) + (S + 4 < 18 ? 3 : 0) + (S == 4 ? 1 : 0); }
constexpr int g2_vm(int S) {
    return S == 0 ? 3 : (S == 1 ? 1 + g2_n(0) : g2_n(S - 2) - (S + 1 < 18 ? 2 : 0) + g2_n(S - 1));
}
#define DIAG_ARG
#define DIAG_PASS
template <int NT>
__device__ __forceinline__ void g2_body(char* __restrict__ ws, u16* __restrict__ out, LAS char* lds, int lane, int wid, int mt, int ks, int nt,
                                        const char* wsrc, unsigned flag2 DIAG_ARG) {
    constexpr int dh = NT == 4;
    const unsigned lds0 = (unsigned)(size_t)lds;
    const int wr = wid & 3;
    const char* asrc = ws + WS_HH + ((size_t)(mt * 4 + ks) * 36) * 8192 + wid * 2048;
    const unsigned aoff = lane * 16;
    const unsigned ma0 = lds0 + wid * 2048, mw0 = lds0 + G2_W0 + wid * 2304;
    auto hpiece = [&](int P, int s) {
        if (P == 0) dma16_sc1<0>(aoff, asrc + (size_t)s * 16384, ma0 + (s & 3) * 16384);
        if (P == 1) dma16_sc1<1024>(aoff, asrc + (size_t)s * 16384, ma0 + (s & 3) * 16384);
    };
    hpiece(0, 0); hpiece(1, 0); hpiece(0, 1); hpiece(1, 1); hpiece(0, 2); hpiece(1, 2);

    const int g4 = lane >> 4;
    const int toff = wr * 2048 + lane * 16;
    const int foff = G2_W0 + dh * 5120 + lane * 16;
    auto rdf = [&](const LAS char* p) -> s16x8 { return *(const LAS s16x8*)p; };
    const rsrc_t rfb = mk_rsrc(ws + WS_FB, 16384);

    f32x4 acc[NT][2];
    s16x8 wf[2 * NT], tf[2][4];
    WAIT_V(4);
    __builtin_amdgcn_raw_buffer_store_b32(1u, rfb, lane == 0 ? flag2 : 0x80000000u, 0, 16);
    BAR();
    STAMP(8);
#pragma unroll
    for (int e = 0; e < 4; ++e) tf[0][e] = rdf(lds + toff + (e >> 1) * 8192 + (e & 1) * 1024);
#pragma unroll
    for (int e = 0; e < 2 * NT; ++e) wf[e] = rdf(lds + foff + (e / NT) * 9216 + (e % NT) * 1024);
    SCHED();

    auto iter = [&](auto sc, s16x8 (&tc)[4], s16x8 (&tn)[4]) {
        constexpr int S = decltype(sc)::v;
        if constexpr (S < 17) {
            WAIT_VI(g2_vm(S));
            BAR();
            if constexpr (S == 7) {
                unsigned fb2 = *(const volatile LAS unsigned*)(lds + G2_FL + wid * 256 + lane * 4);
                if (__builtin_expect(!__all(fb2 == 1u), 0)) {
                    for (int spin = 0; spin < (1 << 20); ++spin) {
                        fb2 = __builtin_amdgcn_raw_buffer_load_b32(rfb, (unsigned)(((mt * 4 + ks) * 2 + 1) * 128 + (lane & 31) * 4), 0, 16);
                        if (__all(fb2 == 1u)) break;
                        __builtin_amdgcn_s_sleep(2);
                    }
                }
            }
            SCHED();
        }
        const LAS char* tb = lds + ((S + 1) & 3) * 16384;
        const LAS char* wb = lds + ((S + 1) % 5) * 18432;
#pragma unroll
        for (int e = 0; e < 2 * NT; ++e) {
#pragma unroll
            for (int i = 0; i < 2; ++i) {
                if (S == 0 && e < NT) mfma_first(acc[e % NT][i], wf[e], tc[(e / NT) * 2 + i]);
                else mfma_acc(acc[e % NT][i], wf[e], tc[(e / NT) * 2 + i]);
            }
            SCHED();
            if constexpr (S < 17) {
                wf[e] = rdf(wb + foff + (e / NT) * 9216 + (e % NT) * 1024);
                if (e >= 2 && e < 6) tn[e - 2] = rdf(tb + toff + ((e - 2) >> 1) * 8192 + ((e - 2) & 1) * 1024);
            }
            if constexpr (S + 3 < 18) { if (e < 2) hpiece(e, S + 3); }
            if constexpr (S + 4 < 18) {
                if (e == 3) g2_wpiece(0, S + 4, wsrc, aoff, mw0);
                if (e == 5) g2_wpiece(1, S + 4, wsrc, aoff, mw0);
                if (e == 6) g2_wpiece(2, S + 4, wsrc, aoff, mw0);
            }
            if constexpr (S == 4) {
                if (e == 7) dma4_sc1((unsigned)(lane & 31) * 4, ws + WS_FB + ((mt * 4 + ks) * 2 + 1) * 128, lds0 + G2_FL + wid * 256);
            }
            SCHED();
        }
    };
#define G2_IT2(S) iter(IC<S>{}, tf[0], tf[1]); iter(IC<S + 1>{}, tf[1], tf[0])
    G2_IT2(0); G2_IT2(2); G2_IT2(4); G2_IT2(6); G2_IT2(8); G2_IT2(10); G2_IT2(12);
    STAMP(9);
    G2_IT2(14);
    iter(IC<16>{}, tf[0], tf[1]);
    const int tok0 = mt * 128 + wr * 32 + (lane & 15);
    u32x2 hv[2][5];
    if (wr == ks) {
#pragma unroll
        for (int i = 0; i < 2; ++i) {
            const int t = tok0 + i * 16;
            const char* hp = ws + WS_H + ((size_t)((t >> 8) * 144 + nt * 36 + dh * 20) * 256 + (t & 255)) * 8;
#pragma unroll
            for (int e = 0; e < 4; ++e) hv[i][e] = *(const u32x2*)(hp + ((e >> 1) * 8 + (g4 & 1) * 4 + (g4 >> 1) * 2 + (e & 1)) * 2048);
            if (NT == 5) hv[i][4] = *(const u32x2*)(hp + (16 + g4) * 2048);
        }
    }
    SCHED();
    iter(IC<17>{}, tf[1], tf[0]);
    MFMA_DONE();
    STAMP(10);

    const rsrc_t rx = mk_rsrc(ws + WS_X, X_BYTES);
    const unsigned xt = (unsigned)((mt * 4 + nt) * 4 * 27648 + dh * 4096);
    auto inb = [&](int s) -> unsigned { return xt + (unsigned)(((ks + 1 + s) & 3) * 27648 + (2 - s) * 9216); };
    if (wr != ks) {
        const unsigned o = xt + (unsigned)(ks * 27648 + ((wr - ks - 1) & 3) * 9216);
#pragma unroll
        for (int i = 0; i < 2; ++i) {
            unsigned w[2 * NT];
#pragma unroll
            for (int j = 0; j < NT; ++j) { w[2 * j] = pk_bf16(acc[j][i][0], acc[j][i][1]); w[2 * j + 1] = pk_bf16(acc[j][i][2], acc[j][i][3]); }
#pragma unroll
            for (int e = 0; e < 2 * NT; ++e) w[e] = (w[e] == ~0u) ? 0x7fc07fc0u : w[e];
            if constexpr (NT == 5) st8_wt(rx, o + 8192 + i * 512 + lane * 8, (u32x2){w[8], w[9]});
            st16_wt(rx, o + i * 2048 + lane * 16, (u32x4){w[0], w[1], w[2], w[3]});
            st16_wt(rx, o + i * 2048 + 1024 + lane * 16, (u32x4){w[4], w[5], w[6], w[7]});
        }
    } else {
        u32x4 pa[3][4]; u32x2 pb[3][2];
        __builtin_amdgcn_s_sleep(16);
        for (int spin = 0; spin < (1 << 18); ++spin) {
            const u32x4 t = __builtin_amdgcn_raw_buffer_load_b128(rx, lane < 3 ? inb(lane) + 4080 : 0x80000000u, 0, 16);
            if (__all(t.w != ~0u)) {
                STAMP(11);
                unsigned m = 0u;
#pragma unroll
                for (int s = 0; s < 3; ++s) {
#pragma unroll
                    for (int p = 0; p < 4; ++p) pa[s][p] = __builtin_amdgcn_raw_buffer_load_b128(rx, inb(s) + p * 1024 + lane * 16, 0, 16);
                    if constexpr (NT == 5) {
#pragma unroll
                        for (int i = 0; i < 2; ++i) pb[s][i] = __builtin_amdgcn_raw_buffer_load_b64(rx, inb(s) + 8192 + i * 512 + lane * 8, 0, 16);
                    }
                }
#pragma unroll
                for (int s = 0; s < 3; ++s) {
#pragma unroll
                    for (int p = 0; p < 4; ++p) m = max(max(max(m, pa[s][p].x), max(pa[s][p].y, pa[s][p].z)), pa[s][p].w);
                    if constexpr (NT == 5) m = max(max(max(m, pb[s][0].x), max(pb[s][0].y, pb[s][1].x)), pb[s][1].y);
                }
                if (__all(m != ~0u)) break;
            }
            __builtin_amdgcn_s_sleep(1);
        }
        STAMP(12);
        const rsrc_t ro = mk_rsrc(out, S_ * D_ * 2);
#pragma unroll
        for (int i = 0; i < 2; ++i) {
            const unsigned ob = (unsigned)(((tok0 + i * 16) * D_ + nt * 144 + dh * 80) * 2);
            unsigned r[NT][2];
#pragma unroll
            for (int j = 0; j < NT; ++j) {
                f32x4 t = acc[j][i];
#pragma unroll
                for (int s = 0; s < 3; ++s) {
                    const unsigned w0 = j < 4 ? pa[s][i * 2 + (j >> 1)][(j & 1) * 2] : pb[s][i].x, w1 = j < 4 ? pa[s][i * 2 + (j >> 1)][(j & 1) * 2 + 1] : pb[s][i].y;
                    t += (f32x4){bf_lo(w0), bf_hi(w0), bf_lo(w1), bf_hi(w1)};
                }
                r[j][0] = pk_bf16(t[0], t[1]); r[j][1] = pk_bf16(t[2], t[3]);
            }
            auto addh = [&](unsigned a, unsigned h) -> unsigned { return pk_bf16(bf_lo(h) + bf_lo(a), bf_hi(h) + bf_hi(a)); };
#pragma unroll
            for (int p = 0; p < 2; ++p) {
                auto s0 = __builtin_amdgcn_permlane16_swap(r[2 * p][0], r[2 * p + 1][0], false, false);
                auto s1 = __builtin_amdgcn_permlane16_swap(r[2 * p][1], r[2 * p + 1][1], false, false);
                st16_wt(ro, ob + (unsigned)((p * 32 + (g4 & 1) * 16 + (g4 >> 1) * 8) * 2),
                        (u32x4){addh(s0[0], hv[i][2 * p].x), addh(s1[0], hv[i][2 * p].y), addh(s0[1], hv[i][2 * p + 1].x), addh(s1[1], hv[i][2 * p + 1].y)});
            }
            if constexpr (NT == 5) st8_wt(ro, ob + (unsigned)((64 + g4 * 4) * 2), (u32x2){addh(r[4][0], hv[i][4].x), addh(r[4][1], hv[i][4].y)});
        }
    }
    STAMP(13);
}

constexpr int K3_LDS = 163840;
constexpr int K3_SP = G_LDS;
constexpr int K3_CS = G2_W0 + 4 * 18432;
extern "C" __global__ __launch_bounds__(512, 2) void k3_gemm1(const u16* __restrict__ wg, const u16* __restrict__ wu,
                                                   char* __restrict__ ws, u16* __restrict__ out) {
    extern __shared__ __attribute__((aligned(16))) char smem[];
    LAS char* lds = (LAS char*)smem;
    const unsigned lds0 = (unsigned)(size_t)lds;
    const int tid = threadIdx.x, lane = tid & 63;
    const int wid = __builtin_amdgcn_readfirstlane(tid >> 6);
    const int wr = wid & 3, wc = wid >> 2;
    const int b = blockIdx.x, xcd = b & 7, idx = b >> 3;
    const int rt = (xcd >> 2) * 4 + (idx & 3), ct = (xcd & 3) * 8 + (idx >> 2);
    asm volatile("" :: "s"(wg), "s"(wu), "s"(ws), "s"(out));
    DIAG_DECL;

    const char* asrc = ws + WS_H + (size_t)rt * 144 * 2048 + wid * 2048;
    const unsigned aoff = (unsigned)(lane ^ ((wid & 2) << 2)) * 16;
    const unsigned ma0 = lds0 + wid * 2048, mw0 = lds0 + 16384 + wc * 9216 + wr * 2304;
    dma16<0>(aoff, asrc, ma0); dma16<1024>(aoff, asrc, ma0);
    const char* wsrc = (const char*)(wc ? wu : wg) + ct * 288 - 2048;
    auto wofs = [&](int i) -> unsigned {
        const int q = lane + 64 * i, lrow = 8 * wr + q / 18, pos = q % 18;
        const int krow = ((lrow >> 2) & 3) * 8 + (lrow >> 4) * 4 + (lrow & 3);
        const int col = pos * 8 + (pos < 8 ? 0 : (pos < 16 ? 8 : (((pos & 1) ^ wc) ? 136 : 64) - pos * 8));
        return (unsigned)(krow * (F_ * 2) + col * 2 + 2048 - 1024 * i);
    };
    const unsigned w0 = wofs(0), w1 = wofs(1), w2 = wofs(2);
    auto piece = [&](int P, int s) {
        const unsigned bo = (s & 3) * ST_BYTES;
        const char* wp = wsrc + (size_t)s * (32 * F_ * 2);
        if (P == 0) dma16<0>(aoff, asrc + (size_t)s * 16384, ma0 + bo);
        if (P == 1) dma16<1024>(aoff, asrc + (size_t)s * 16384, ma0 + bo);
        if (P == 2) dma16<0>(w0, wp, mw0 + bo);
        if (P == 3) dma16<1024>(w1, wp, mw0 + bo);
        if (P == 4) dma16_q<2048>(w2, wp, mw0 + bo);
    };
#define K3_PIECE(P, s) piece(P, s)
#define K3_STAGE(s) do { K3_PIECE(0, s); K3_PIECE(1, s); K3_PIECE(2, s); K3_PIECE(3, s); K3_PIECE(4, s); } while (0)
    K3_PIECE(2, 0); K3_PIECE(3, 0); K3_PIECE(4, 0);
    BAR();
    K3_STAGE(1);
    u32x2 hs[4];
    {
        const char* hsrc = ws + WS_H + (size_t)rt * 144 * 2048 + ct * 64;
        const unsigned o = (unsigned)((wid * 18 + (lane >> 3)) * 2048 + (lane & 7) * 8);
        asm volatile("global_load_dwordx2 %0, %1, %2" : "=v"(hs[0]) : "v"(o), "s"(hsrc) : "memory");
        asm volatile("global_load_dwordx2 %0, %1, %2" : "=v"(hs[1]) : "v"(o + 16384), "s"(hsrc) : "memory");
        asm volatile("global_load_dwordx2 %0, %1, %2" : "=v"(hs[2]) : "v"((unsigned)((wid * 18 + 16 + ((lane >> 3) & 1)) * 2048 + (lane & 7) * 8)), "s"(hsrc) : "memory");
        asm volatile("global_load_dwordx2 %0, %1, %2" : "=v"(hs[3]) : "v"((unsigned)(lane & 7) * 8), "s"(hsrc) : "memory");
    }

    const int g4 = lane >> 4, q4 = (lane & 15) >> 2, p4 = lane & 3;
    const int rowoff = (4 * g4 + q4) * 288;
    const int tbase = g4 * 4096 + wr * 512 + (lane & 15) * 8;
    const int toff0 = tbase + ((2 * wc) ^ (g4 & 1)) * 128, toff1 = tbase + ((2 * wc + 1) ^ (g4 & 1)) * 128;
    const int tstep = wc ? -256 : 256;
    const int foff = 16384 + rowoff + wc * 128 + p4 * 8;
    const int moff = 16384 + rowoff + 256 + (p4 < 2 ? 16 * wc + 8 * p4 : 9216 + 16 * (1 - wc) + 8 * (p4 - 2));

    f32x4 acc[9][4];
    s16x8 wf[9], tf[2][4];
    auto rd_w = [&](const LAS char* sb, int j) -> s16x8 {
        return j < 8 ? tr_frag(sb + foff + (j >> 2) * 9216 + (j & 3) * 32) : tr_frag(sb + moff);
    };
    auto rd_t = [&](const LAS char* sb, int i) -> s16x8 {
        const LAS char* p = sb + ((i & 1) ? toff1 : toff0) + (i >> 1) * tstep;
        const s16x4 lo = *(const volatile LAS s16x4*)p, hi = *(const volatile LAS s16x4*)(p + 2048);
        return __builtin_shufflevector(lo, hi, 0, 1, 2, 3, 4, 5, 6, 7);
    };

    STAMP(1);
    WAIT_V(9); BAR();
    STAMP(2);
    wf[0] = rd_w(lds, 0);
#pragma unroll
    for (int i = 0; i < 4; ++i) tf[0][i] = rd_t(lds, i);
#pragma unroll
    for (int j = 1; j < 9; ++j) wf[j] = rd_w(lds, j);
    SCHED();
    const int mt2 = (xcd >> 2) * 8 + (idx & 7), ks2 = xcd & 3, nt2 = idx >> 3;
    const rsrc_t rfill = mk_rsrc(ws + WS_X + (size_t)((mt2 * 4 + nt2) * 4 + ks2) * 27648, 27648);
    auto fill = [&](int k) { st16_wt(rfill, (unsigned)(k * 8192 + tid * 16), (u32x4){~0u, ~0u, ~0u, ~0u}); };
    unsigned sgr;
    {
        const LAS char* sb = lds + ST_BYTES;
        float hc = 0.f;
#pragma unroll
        for (int j = 0; j < 9; ++j) {
#pragma unroll
            for (int i = 0; i < 4; ++i) mfma_first(acc[j][i], wf[j], tf[0][i]);
            SCHED();
            if (j == 0) { K3_PIECE(0, 2); K3_PIECE(1, 2); }
            if (j == 1) { K3_PIECE(2, 2); K3_PIECE(3, 2); }
            if (j == 2) K3_PIECE(4, 2);
            if (j == 3) {
                asm volatile("s_waitcnt vmcnt(5)" : "+v"(hs[0]), "+v"(hs[1]), "+v"(hs[2]), "+v"(hs[3]) :: "memory");
                hc = bf_lo(hs[3].x);
                float s1 = 0.f, s2 = 0.f;
#pragma unroll
                for (int i = 0; i < 3; ++i) {
                    const float d[4] = {bf_lo(hs[i].x) - hc, bf_hi(hs[i].x) - hc, bf_lo(hs[i].y) - hc, bf_hi(hs[i].y) - hc};
                    float a = (d[0] + d[1]) + (d[2] + d[3]), q = __builtin_fmaf(d[0], d[0], d[1] * d[1]) + __builtin_fmaf(d[2], d[2], d[3] * d[3]);
                    if (i == 2 && lane >= 16) { a = 0.f; q = 0.f; }
                    s1 += a; s2 += q;
                }
                s1 = x32_add(x16_add(dpp_add<0x128>(s1))); s2 = x32_add(x16_add(dpp_add<0x128>(s2)));
                if (lane < 8) *(LAS f32x2*)(lds + K3_SP + (lane * 8 + wid) * 8) = (f32x2){s1, s2};
                asm volatile("s_waitcnt lgkmcnt(0)" ::: "memory");
                BAR();
                wf[0] = rd_w(sb, 0); wf[1] = rd_w(sb, 1);
                tf[1][0] = rd_t(sb, 0); tf[1][1] = rd_t(sb, 1);
            }
            if (j == 4) { tf[1][2] = rd_t(sb, 2); tf[1][3] = rd_t(sb, 3); wf[2] = rd_w(sb, 2); wf[3] = rd_w(sb, 3); }
            if (j >= 4) { wf[j] = rd_w(sb, j); K3_PIECE(j - 4, 3); }
            if (j == 8) {
                const f32x2 pp = *(const LAS f32x2*)(lds + K3_SP + (wid * 8 + (lane & 7)) * 8);
                const float s1 = dpp_add<0x141>(dpp_add<0x4E>(dpp_add<0xB1>(pp.x))) * (1.f / D_);
                const float s2 = dpp_add<0x141>(dpp_add<0x4E>(dpp_add<0xB1>(pp.y))) * (1.f / D_);
                const float c0 = __uint_as_float(__builtin_amdgcn_readlane(__float_as_uint(hc), wid));
                const float sd = ln_sd(__builtin_fmaxf(__builtin_fmaf(-s1, s1, s2), 0.f));
                sgr = pk_bf16(c0 + s1, sd);
                fill(0); fill(1);
            }
            SCHED();
        }
    }

    const rsrc_t rg = mk_rsrc(ws + WS_G2 + (size_t)(rt * 256 + wr * 64) * 8, 512);
    const char* w2src = ws + WS_WT + (size_t)(ks2 * 4 + nt2) * 18 * 18432 + wid * 2304;
    u32x2 gr[4];
    s16x4 tl;
    constexpr int TP = 1, DPJ0 = 0, DPJ1 = 5;
    auto iter = [&](auto mode, auto vm, int s, s16x8 (&tc)[4], s16x8 (&tn)[4]) {
        constexpr int MODE = decltype(mode)::v;
        if constexpr (MODE != 4) {
            if constexpr (MODE == 3) WAIT_V(0); else WAIT_VI(5 + decltype(vm)::v);
            BAR();
            SCHED();
        }
        const LAS char* sb = lds + ((s + 1) & 3) * ST_BYTES;
#pragma unroll
        for (int j = 0; j < 9; ++j) {
            s16x4 wl, wh;
#pragma unroll
            for (int i = 0; i < 4; ++i) {
                mfma_acc(acc[j][i], wf[j], tc[i]);
                SCHED();
                if constexpr (MODE != 4) {
                    if (j >= 1) {
                        const LAS char* p = (j - 1 < 8) ? sb + foff + ((j - 1) >> 2) * 9216 + ((j - 1) & 3) * 32 : sb + moff;
                        if (i == 0) wl = __builtin_amdgcn_ds_read_tr16_b64_v4i16((LAS s16x4*)p);
                        if (i == 1) { wh = __builtin_amdgcn_ds_read_tr16_b64_v4i16((LAS s16x4*)(p + 16 * 288)); wf[j - 1] = __builtin_shufflevector(wl, wh, 0, 1, 2, 3, 4, 5, 6, 7); }
                    }
                    if (j >= TP && j < TP + 4) {
                        const int ti = j - TP;
                        const LAS char* p = sb + ((ti & 1) ? toff1 : toff0) + (ti >> 1) * tstep;
                        if (i == 2) tl = *(const volatile LAS s16x4*)p;
                        if (i == 3) tn[ti] = __builtin_shufflevector(tl, *(const volatile LAS s16x4*)(p + 2048), 0, 1, 2, 3, 4, 5, 6, 7);
                    }
                    if (j == 8 && i == 3) wf[8] = rd_w(sb, 8);
                }
                if constexpr (MODE < 2 || MODE == 5) {
                    if (j == DPJ0 && i == 1) K3_PIECE(0, s + 3);
                    if (j == DPJ0 && i == 3) K3_PIECE(1, s + 3);
                    if (j == DPJ1 && i == 3) K3_PIECE(2, s + 3);
                    if (j == DPJ1 + 1 && i == 3) K3_PIECE(3, s + 3);
                    if (j == DPJ1 + 2 && i == 3) K3_PIECE(4, s + 3);
                }
                SCHED();
            }
            if constexpr (MODE == 0) { if (j == 8) { fill(2); fill(3); } }
            if constexpr (MODE == 5) {
                if (j == 0 && s == 4) st8_wt(mk_rsrc(ws + WS_G2, S_ * 8), lane == 0 ? (unsigned)((rt * 256 + ct * 8 + wid) * 8) : 0x80000000u, (u32x2){sgr, 1u});
            }
            if constexpr (MODE == 2) {
                if (j == 0) {
#pragma unroll
                    for (int i = 0; i < 4; ++i) gr[i] = __builtin_amdgcn_raw_buffer_load_b64(rg, (unsigned)(((i ^ (2 * wc)) * 16 + (lane & 15)) * 8), 0, 16);
                }
            }
            if constexpr (MODE == 3) {
                if (j == 0) {
                    asm volatile("" : "+v"(gr[0]), "+v"(gr[1]), "+v"(gr[2]), "+v"(gr[3]));
                    const unsigned co = (unsigned)((lane >= 18 ? F_ * 4 + (lane - 18) * 16 : lane * 16));
                    dma_m36(co, ws + WS_CS + (size_t)(ct * 144 + wc * 72) * 4, lds0 + K3_CS + wid * 1152);
                    dma_m36(co, ws + WS_CS + (size_t)(2 * F_ + ct * 144 + wc * 72) * 4, lds0 + K3_CS + wid * 1152 + 576);
                }
            }
            if constexpr (MODE == 3 || MODE == 4) {
                if (j >= 2 && j < 8) g2_wpiece((j - 2) % 3, (MODE - 3) * 2 + (j - 2) / 3, w2src, lane * 16, lds0 + G2_W0 + wid * 2304);
            }
            SCHED();
        }
    };
    iter(IC<0>{}, IC<2>{}, 1, tf[1], tf[0]);
    iter(IC<1>{}, IC<4>{}, 2, tf[0], tf[1]);
    iter(IC<1>{}, IC<2>{}, 3, tf[1], tf[0]);
#pragma unroll 1
    for (int s = 4; s < 14; s += 2) {
        iter(IC<5>{}, IC<0>{}, s, tf[0], tf[1]);
        iter(IC<1>{}, IC<0>{}, s + 1, tf[1], tf[0]);
    }
    STAMP(3);
    iter(IC<1>{}, IC<0>{}, 14, tf[0], tf[1]);
    iter(IC<2>{}, IC<0>{}, 15, tf[1], tf[0]);
    iter(IC<3>{}, IC<0>{}, 16, tf[0], tf[1]);
    iter(IC<4>{}, IC<0>{}, 17, tf[1], tf[0]);
    MFMA_DONE();
    STAMP(4);
    WAIT_V(12);
    __builtin_amdgcn_s_setprio(1);
    if (__builtin_expect(!__all((gr[0].y & gr[1].y & gr[2].y & gr[3].y) == 1u), 0)) {
        for (int spin = 0; spin < (1 << 20); ++spin) {
            __builtin_amdgcn_s_sleep(2);
#pragma unroll
            for (int i = 0; i < 4; ++i) gr[i] = __builtin_amdgcn_raw_buffer_load_b64(rg, (unsigned)(((i ^ (2 * wc)) * 16 + (lane & 15)) * 8), 0, 16);
            if (__all((gr[0].y & gr[1].y & gr[2].y & gr[3].y) == 1u)) break;
        }
    }
    const int mt = rt * 2 + (wr >> 1), ks = ct >> 3, lct = ct & 7;
    const rsrc_t rhh = mk_rsrc(ws + WS_HH, S_ * F_ * 2);
    const unsigned hb = (unsigned)(((mt * 4 + ks) * 36) * 8192 + ((wr & 1) * 4) * 1024);
    const LAS char* csb = lds + K3_CS + wid * 1152 + 16 * g4;
    TokC tcs[4];
#pragma unroll
    for (int i = 0; i < 4; ++i) {
        const float sd = bf_hi(gr[i].x), r = __builtin_amdgcn_rcpf(sd);
        tcs[i] = (TokC){-bf_lo(gr[i].x), -2.30220819f * r, -0.10294324f * (r * r * r), sd * sd};
    }
    {
        const LAS char* cm = lds + K3_CS + wid * 1152 + 256 + 16 * (g4 & 1);
        const f32x4 cgm = *(const LAS f32x4*)cm + *(const LAS f32x4*)(cm + 576), cum = *(const LAS f32x4*)(cm + 288) + *(const LAS f32x4*)(cm + 864);
#pragma unroll
        for (int ip = 0; ip < 2; ++ip) {
            const int ia = ((2 * ip) ^ (2 * wc)) + (lane >> 5);
            const bool up = lane >= 32;
            const TokC tc = {up ? tcs[2 * ip + 1].nm : tcs[2 * ip].nm, up ? tcs[2 * ip + 1].c0 : tcs[2 * ip].c0,
                             up ? tcs[2 * ip + 1].c1 : tcs[2 * ip].c1, up ? tcs[2 * ip + 1].q : tcs[2 * ip].q};
            f32x4 gm, um;
#pragma unroll
            for (int r = 0; r < 4; ++r) {
                auto sw = __builtin_amdgcn_permlane32_swap(__float_as_uint(acc[8][2 * ip][r]), __float_as_uint(acc[8][2 * ip + 1][r]), false, false);
                gm[r] = __uint_as_float(sw[0]); um[r] = __uint_as_float(sw[1]);
            }
            const int kgs = (lct & 1) * 2 + wc;
            st8_wt(rhh, hb + (unsigned)((lct >> 1) * 8192 + ia * 1024 + (kgs * 16 + (lane & 15)) * 16 + (g4 & 1) * 8),
                   (u32x2){geglu2(LO2(gm), LO2(um), LO2(cgm), LO2(cum), tc), geglu2(HI2(gm), HI2(um), HI2(cgm), HI2(cum), tc)});
        }
    }
    SCHED();
    constexpr int FLAGPOS = 1;
    const rsrc_t rfb = mk_rsrc(ws + WS_FB, 16384);
    const unsigned fbo = (unsigned)((mt * 4 + ks) * 256 + (lct * 4 + (wr & 1) * 2 + wc) * 4);
    u32x4 ob[4];
#pragma unroll
    for (int jp = 0; jp < 2; ++jp) {
        f32x4 cg[2], cu[2];
#pragma unroll
        for (int e = 0; e < 2; ++e) {
            cg[e] = *(const LAS f32x4*)(csb + (2 * jp + e) * 64) + *(const LAS f32x4*)(csb + 576 + (2 * jp + e) * 64);
            cu[e] = *(const LAS f32x4*)(csb + 288 + (2 * jp + e) * 64) + *(const LAS f32x4*)(csb + 864 + (2 * jp + e) * 64);
        }
#pragma unroll
        for (int i = 0; i < 4; ++i) {
            unsigned o[4];
#pragma unroll
            for (int e = 0; e < 2; ++e) {
                const f32x4 g = acc[2 * jp + e][i], u = acc[4 + 2 * jp + e][i];
                o[2 * e] = geglu2(LO2(g), LO2(u), LO2(cg[e]), LO2(cu[e]), tcs[i]);
                o[2 * e + 1] = geglu2(HI2(g), HI2(u), HI2(cg[e]), HI2(cu[e]), tcs[i]);
            }
            ob[i] = (u32x4){o[0], o[1], o[2], o[3]};
            st16_wt(rhh, (jp == 1 && wid == 7) ? 0x80000000u : hb + (unsigned)(((2 + jp * 8 + lct) * 2 + wc) * 8192 + (i ^ (2 * wc)) * 1024 + lane * 16), ob[i]);
            if (jp == 1 && i == FLAGPOS) {
                SCHED();
                WAIT_VI(FLAGPOS + 1);
                __builtin_amdgcn_raw_buffer_store_b32(1u, rfb, lane == 0 ? fbo : 0x80000000u, 0, 16);
                SCHED();
            }
        }
        SCHED();
        if (jp == 0) __builtin_amdgcn_s_setprio(0);
    }
    STAMP(5);
    if (wid == 7) {
        for (int spin = 0; spin < (1 << 20); ++spin) {
            const unsigned f = __builtin_amdgcn_raw_buffer_load_b32(rfb, (unsigned)((mt2 * 4 + ks2) * 256 + (lane & 31) * 4), 0, 16);
            if (__all(f == 1u)) break;
            __builtin_amdgcn_s_sleep(4);
        }
#pragma unroll
        for (int i = 0; i < 4; ++i) st16_wt(rhh, hb + (unsigned)(((10 + lct) * 2 + wc) * 8192 + (i ^ (2 * wc)) * 1024 + lane * 16), ob[i]);
    }
    BAR();
    STAMP(7);
    if (wid < 4) g2_body<5>(ws, out, lds, lane, wid, mt2, ks2, nt2, w2src, fbo + 128 DIAG_PASS);
    else g2_body<4>(ws, out, lds, lane, wid, mt2, ks2, nt2, w2src, fbo + 128 DIAG_PASS);
    DIAG_END(ws, 1);
}

extern "C" __global__ void k9_end() {}

extern "C" void kernel_launch(void* const* d_in, const int* in_sizes, int n_in, void* d_out, int out_size, void* d_ws,
                              size_t ws_size, hipStream_t stream) {
    const u16* q = (const u16*)d_in[0];
    const u16* k = (const u16*)d_in[1];
    const u16* v = (const u16*)d_in[2];
    const u16* shift_k = (const u16*)d_in[3];
    const u16* ssm_k = (const u16*)d_in[4];
    const u16* ssm_D = (const u16*)d_in[5];
    const u16* wg = (const u16*)d_in[7];
    const u16* wu = (const u16*)d_in[8];
    const u16* wd = (const u16*)d_in[9];
    char* ws = (char*)d_ws;
    (void)hipFuncSetAttribute((const void*)k1_conv, hipFuncAttributeMaxDynamicSharedMemorySize, C1_LDS);
    (void)hipFuncSetAttribute((const void*)k3_gemm1, hipFuncAttributeMaxDynamicSharedMemorySize, K3_LDS);
    k1_conv<<<256, 512, C1_LDS, stream>>>(q, k, v, shift_k, ssm_k, ssm_D, wg, wu, wd, ws);
    k3_gemm1<<<256, 512, K3_LDS, stream>>>(wg, wu, ws, (u16*)d_out);
}
```

```cpp
#include <hip/hip_runtime.h>
#include <hip/hip_bf16.h>

#define LAS __attribute__((address_space(3)))
typedef unsigned short u16;
typedef short s16x4 __attribute__((ext_vector_type(4)));
typedef short s16x8 __attribute__((ext_vector_type(8)));
typedef float f32x2 __attribute__((ext_vector_type(2)));
typedef float f32x4 __attribute__((ext_vector_type(4)));
typedef unsigned u32x2 __attribute__((ext_vector_type(2)));
typedef unsigned u32x4 __attribute__((ext_vector_type(4)));
typedef __bf16 bf16x2_t __attribute__((ext_vector_type(2)));

constexpr int S_ = 2048, D_ = 576, F_ = 4608;

__device__ __forceinline__ unsigned pk_bf16(float lo, float hi) {
    f32x2 v = {lo, hi};
    bf16x2_t b = __builtin_convertvector(v, bf16x2_t);
    return __builtin_bit_cast(unsigned, b);
}
__device__ __forceinline__ float bf_lo(unsigned w) { return __uint_as_float(w << 16); }
__device__ __forceinline__ float bf_hi(unsigned w) { return __uint_as_float(w & 0xffff0000u); }
__device__ __forceinline__ float rbf(float x) { return bf_lo(pk_bf16(x, 0.f)); }
#define WAIT_V(n) asm volatile("s_waitcnt vmcnt(" #n ")" ::: "memory")
#define BAR() do { __builtin_amdgcn_s_barrier(); asm volatile("" ::: "memory"); } while (0)
#define SCHED() __builtin_amdgcn_sched_barrier(0)
__device__ __forceinline__ void mfma_acc(f32x4& c, s16x8 a, s16x8 b) {
    asm volatile("v_mfma_f32_16x16x32_bf16 %0, %1, %2, %0" : "+v"(c) : "v"(a), "v"(b));
}
__device__ __forceinline__ void mfma_first(f32x4& c, s16x8 a, s16x8 b) {
    asm volatile("v_mfma_f32_16x16x32_bf16 %0, %1, %2, 0" : "=&v"(c) : "v"(a), "v"(b));
}
#define MFMA_DONE() do { asm volatile("s_nop 7\n\ts_nop 7" ::: "memory"); SCHED(); } while (0)
constexpr size_t WS_KV = 0;
constexpr size_t WS_H = WS_KV + (size_t)S_ * D_ * 2;
constexpr size_t WS_ST = WS_H + (size_t)S_ * D_ * 2;
constexpr size_t WS_CS = WS_ST + (size_t)S_ * 8;
constexpr size_t WS_G2 = WS_CS + (size_t)4 * F_ * 4;
constexpr size_t WS_F0 = WS_G2 + (size_t)S_ * 8;
constexpr int FSP = 128;
constexpr size_t WS_EP = WS_F0 + 1280 * FSP;
constexpr size_t WS_HH = WS_EP + 1024;
constexpr size_t WS_X = WS_HH + (size_t)S_ * F_ * 2;
constexpr unsigned X_BYTES = 64 * 4 * 3 * 9216;
constexpr size_t WS_FB = WS_X + X_BYTES;
constexpr size_t WS_WT = WS_FB + 16384;

template <int N> struct IC { static constexpr int v = N; };
typedef __amdgpu_buffer_rsrc_t rsrc_t;
__device__ __forceinline__ rsrc_t mk_rsrc(const void* p, unsigned bytes) { return __builtin_amdgcn_make_buffer_rsrc((void*)p, 0, bytes, 0x00020000); }
__device__ __forceinline__ void st16_wt(rsrc_t r, unsigned off, u32x4 v) { __builtin_amdgcn_raw_buffer_store_b128(v, r, off, 0, 16); }
__device__ __forceinline__ void st8_wt(rsrc_t r, unsigned off, u32x2 v) { __builtin_amdgcn_raw_buffer_store_b64(v, r, off, 0, 16); }
template <int CTRL> __device__ __forceinline__ float dpp_add(float x) {
    return x + __uint_as_float(__builtin_amdgcn_update_dpp(0, (int)__float_as_uint(x), CTRL, 0xf, 0xf, true));
}
__device__ __forceinline__ float x16_add(float x) {
    auto r = __builtin_amdgcn_permlane16_swap(__float_as_uint(x), __float_as_uint(x), false, false);
    return __uint_as_float(r[0]) + __uint_as_float(r[1]);
}
__device__ __forceinline__ float x32_add(float x) {
    auto r = __builtin_amdgcn_permlane32_swap(__float_as_uint(x), __float_as_uint(x), false, false);
    return __uint_as_float(r[0]) + __uint_as_float(r[1]);
}
__device__ __forceinline__ float sum16(float x) {
    x = dpp_add<0xB1>(x); x = dpp_add<0x4E>(x); x = dpp_add<0x141>(x);
    return dpp_add<0x140>(x);
}
__device__ __forceinline__ float ln_sd(float var) { return rbf(__builtin_amdgcn_sqrtf(rbf(rbf(var) + 1e-5f))); }

template <int OFF>
__device__ __forceinline__ void dma16(unsigned voff, const void* sbase, unsigned m0v) {
    asm volatile("s_mov_b32 m0, %2\n\ts_nop 0\n\tglobal_load_lds_dwordx4 %0, %1 offset:%3"
                 :: "v"(voff), "s"(sbase), "s"(m0v), "i"(OFF) : "memory");
}
template <int OFF>
__device__ __forceinline__ void dma16_sc1(unsigned voff, const void* sbase, unsigned m0v) {
    asm volatile("s_mov_b32 m0, %2\n\ts_nop 0\n\tglobal_load_lds_dwordx4 %0, %1 offset:%3 sc1"
                 :: "v"(voff), "s"(sbase), "s"(m0v), "i"(OFF) : "memory");
}
template <int OFF>
__device__ __forceinline__ void dma16_q(unsigned voff, const void* sbase, unsigned m0v) {
    asm volatile("s_mov_b32 m0, %2\n\ts_mov_b64 exec, 0xffff\n\tglobal_load_lds_dwordx4 %0, %1 offset:%3\n\ts_mov_b64 exec, -1"
                 :: "v"(voff), "s"(sbase), "s"(m0v), "i"(OFF) : "memory");
}
__device__ __forceinline__ void dma_m36(unsigned voff, const void* sbase, unsigned m0v) {
    asm volatile("s_mov_b32 m0, %2\n\ts_mov_b32 exec_hi, 15\n\tglobal_load_lds_dwordx4 %0, %1\n\ts_mov_b64 exec, -1"
                 :: "v"(voff), "s"(sbase), "s"(m0v) : "memory");
}
__device__ __forceinline__ void dma4(unsigned voff, const void* sbase, unsigned m0v) {
    asm volatile("s_mov_b32 m0, %2\n\ts_nop 0\n\tglobal_load_lds_dword %0, %1" :: "v"(voff), "s"(sbase), "s"(m0v) : "memory");
}
__device__ __forceinline__ void dma4_sc1(unsigned voff, const void* sbase, unsigned m0v) {
    asm volatile("s_mov_b32 m0, %2\n\ts_nop 0\n\tglobal_load_lds_dword %0, %1 sc1" :: "v"(voff), "s"(sbase), "s"(m0v) : "memory");
}
#define SYM_ADDR(out, sym) asm volatile("s_getpc_b64 s[40:41]\n\ts_add_u32 s40, s40, " #sym "@rel32@lo+4\n\ts_addc_u32 s41, s41, " #sym "@rel32@hi+12\n\ts_mov_b64 %0, s[40:41]" \
                                        : "=s"(out) :: "s40", "s41", "scc")
template <int NI>
__device__ __forceinline__ void warm_code(const char* a, const char* e, int l0, int lane, unsigned m0v) {
    const unsigned last = e > a ? (unsigned)(e - a) - 4u : 0u;
#pragma unroll
    for (int i = 0; i < NI; ++i) dma4(min((unsigned)(l0 + i * 64 + lane) * 128u, last), a, m0v);
}
__device__ __forceinline__ s16x8 tr_frag(const LAS char* p) {
    const s16x4 lo = __builtin_amdgcn_ds_read_tr16_b64_v4i16((LAS s16x4*)p);
    const s16x4 hi = __builtin_amdgcn_ds_read_tr16_b64_v4i16((LAS s16x4*)(p + 16 * 288));
    return __builtin_shufflevector(lo, hi, 0, 1, 2, 3, 4, 5, 6, 7);
}
__device__ __forceinline__ void wd_unit_load(const u16* wd, int un, int lane, unsigned m0v) {
    const int uB = un / 3, ut = un % 3, kh = uB & 1, st = (uB >> 1) % 18, kn = uB / 36;
    const bool full = st >= 2;
    const int f0 = (kn >> 2) * 1152 + (full ? ((st - 2) & 7) * 144 + kh * 72 + ((st - 2) >> 3) * 32 : (st * 2 + kh) * 288);
    const char* src = (const char*)wd + (size_t)f0 * (D_ * 2) + (kn & 3) * 288 + ut * 96;
#pragma unroll
    for (int i = 0; i < 3; ++i) {
        const int q = lane + 64 * i, r = q / 6, pos = q % 6, kg = (r & 15) >> 2;
        const int f = full ? r : (kg >> 1) * 144 + (kg & 1) * 72 + 64 + (r & 3) + 4 * (r >> 4);
        dma16<0>((unsigned)(f * (D_ * 2) + pos * 16), src, m0v + i * 1024);
    }
}
__device__ __forceinline__ void wd_unit_store(char* ws, int un, int lane, const LAS char* l) {
    const rsrc_t r = mk_rsrc(ws + WS_WT, F_ * D_ * 2);
#pragma unroll
    for (int t = 0; t < 3; ++t) {
        const LAS char* tp = l + (lane >> 2) * 96 + (lane & 3) * 8 + t * 32;
        const s16x4 lo = __builtin_amdgcn_ds_read_tr16_b64_v4i16((LAS s16x4*)tp), hi = __builtin_amdgcn_ds_read_tr16_b64_v4i16((LAS s16x4*)(tp + 16 * 96));
        st16_wt(r, (unsigned)(un * 3072 + t * 1024 + lane * 16), __builtin_bit_cast(u32x4, __builtin_shufflevector(lo, hi, 0, 1, 2, 3, 4, 5, 6, 7)));
    }
}
constexpr int WU4 = 3;
constexpr int HELPER_DELAY = 48, HELPER_DELAY2 = 24, POLL0 = 16, POLL1 = 1;
constexpr int C1_COPY = 4288;
constexpr int C1_FILT = 4 * C1_COPY;
constexpr int C1_KV = 2304 * 2;
constexpr int C1_KVOFF = 4 * C1_FILT + 64;
constexpr int C1_YOFF = C1_KVOFF + 4 * C1_KV;
constexpr int C1_XOFF = C1_YOFF + 16384;
constexpr int C1_SOFF = C1_XOFF + 16384;
constexpr int C1_TOFF = C1_SOFF + 16384;
constexpr int C1_WOFF = C1_TOFF + 2048;
constexpr int C1_LDS = C1_WOFF + 24576;

extern "C" __global__ __launch_bounds__(512, 2) void k1_conv(const u16* __restrict__ x, const u16* __restrict__ kin, const u16* __restrict__ vin,
                                                  const u16* __restrict__ shift_k, const u16* __restrict__ ssm_k, const u16* __restrict__ ssm_D,
                                                  const u16* __restrict__ wg, const u16* __restrict__ wu, const u16* __restrict__ wd,
                                                  char* __restrict__ ws) {
    extern __shared__ __attribute__((aligned(16))) char smem[];
    const int tid = threadIdx.x, lane = tid & 63;
    const int wid = __builtin_amdgcn_readfirstlane(tid >> 6);
    const int b = blockIdx.x, xcd = b & 7, idx = b >> 3;
    {
        const char *c1, *c3;
        SYM_ADDR(c1, k1_conv); SYM_ADDR(c3, k3_gemm1);
        if (wid >= 4) warm_code<1>(c1, c3, (wid & 3) * 64, lane, (unsigned)(size_t)(LAS char*)smem + C1_TOFF + 256 + (wid & 3) * 256);
    }
    asm volatile("" :: "s"(x), "s"(kin), "s"(vin), "s"(shift_k), "s"(ssm_k), "s"(ssm_D), "s"(wg), "s"(wu), "s"(wd), "s"(ws));
    const bool conv = idx < 18;
    const int cg = xcd * 18 + idx, ch0 = cg * 4;
    const unsigned lds0 = (unsigned)(size_t)(LAS char*)smem;
    LAS unsigned* tagp = (LAS unsigned*)(smem + C1_TOFF);
    auto get_tag = [&]() -> unsigned { BAR(); return __builtin_amdgcn_readfirstlane(*(volatile LAS unsigned*)tagp); };

    const int h = (idx - 18) * 8 + xcd, hc_ = h < 108 ? h : 0;
    const int m3 = hc_ % 3, lcA = (hc_ / 3) * 4 + m3, rb0 = m3 * 3;
    constexpr int HU = (1728 - 144 * WU4 + 111) / 112;
    const int hbase = 144 * WU4 + h * HU, hun = hbase + wid * 2, hend = min(hbase + HU, 1728);
    auto cs_load = [&](u32x4 (&v)[12]) {
        const int rs = tid >> 3, c8 = tid & 7;
#pragma unroll
        for (int p = 0; p < 12; ++p) {
            const int rb = rb0 + p, lc = lcA + (rb >= 9), r = (rb >= 9 ? rb - 9 : rb) * 64 + rs;
            v[p] = *(const u32x4*)((lc >= 72 ? wu : wg) + (size_t)r * F_ + (lc % 72) * 64 + c8 * 8);
        }
        SCHED();
#pragma unroll
        for (int u = 0; u < 2; ++u) if (hun + u < hend) wd_unit_load(wd, hun + u, lane, lds0 + 16384 + (wid * 2 + u) * 3072);
        st16_wt(mk_rsrc(ws + WS_FB, 16384), (unsigned)(h * 8192 + tid * 16), (u32x4){0u, 0u, 0u, 0u});
        SCHED();
    };
    auto cs_finish = [&](u32x4 (&v)[12]) {
        float g[4][8];
#pragma unroll
        for (int q = 0; q < 4; ++q) {
#pragma unroll
            for (int e = 0; e < 4; ++e) {
                g[q][2 * e] = (bf_lo(v[3 * q][e]) + bf_lo(v[3 * q + 1][e])) + bf_lo(v[3 * q + 2][e]);
                g[q][2 * e + 1] = (bf_hi(v[3 * q][e]) + bf_hi(v[3 * q + 1][e])) + bf_hi(v[3 * q + 2][e]);
            }
        }
        LAS float* red = (LAS float*)smem;
#pragma unroll
        for (int l = 0; l < 2; ++l) {
            float a[8];
#pragma unroll
            for (int e = 0; e < 8; ++e) {
                a[e] = l == 0 ? g[0][e] + (m3 <= 1 ? g[1][e] : 0.f) + (m3 == 0 ? g[2][e] : 0.f) : g[3][e] + (m3 >= 1 ? g[2][e] : 0.f) + (m3 == 2 ? g[1][e] : 0.f);
                a[e] = x32_add(x16_add(dpp_add<0x128>(a[e])));
            }
            if (lane < 8) {
                *(LAS f32x4*)(red + (wid * 2 + l) * 64 + lane * 8) = (f32x4){a[0], a[1], a[2], a[3]};
                *(LAS f32x4*)(red + (wid * 2 + l) * 64 + lane * 8 + 4) = (f32x4){a[4], a[5], a[6], a[7]};
            }
        }
        WAIT_V(1);
#pragma unroll
        for (int u = 0; u < 2; ++u) if (hun + u < hend) wd_unit_store(ws, hun + u, lane, (const LAS char*)smem + 16384 + (wid * 2 + u) * 3072);
        asm volatile("s_waitcnt lgkmcnt(0)" ::: "memory"); BAR();
        if (tid < 192 && h < 108) {
            float s = 0.f;
            if (tid < 128) {
#pragma unroll
                for (int w = 0; w < 8; ++w) s += red[w * 128 + tid];
            }
            const int part = tid >> 6;
            const int lc = part == 0 ? lcA : (part == 1 ? lcA + 1 : (m3 == 0 ? lcA : lcA + 1));
            const int slot = part == 0 ? (m3 != 0) : (part == 1 ? 0 : 1);
            if (part < 2 || m3 != 1)
                __builtin_amdgcn_raw_buffer_store_b32(__float_as_uint(s), mk_rsrc(ws + WS_CS, 4 * F_ * 4), (unsigned)((slot * 2 * F_ + lc * 64 + (tid & 63)) * 4), 0, 16);
        }
    };
    auto wait_kv = [&](unsigned tag) {
        const rsrc_t rf = mk_rsrc(ws + WS_F0 + ((idx >> 2) * 8 + xcd) * 32 * FSP, 32 * FSP);
        __builtin_amdgcn_s_sleep(POLL0);
        for (int spin = 0; spin < (1 << 20); ++spin) {
            const unsigned f = __builtin_amdgcn_raw_buffer_load_b32(rf, (lane & 31) * FSP, 0, 16);
            if (__all(f == tag)) break;
            __builtin_amdgcn_s_sleep(POLL1);
        }
    };

    auto x_rows = [&]() {
        u32x2 xr[8];
#pragma unroll
        for (int i = 0; i < 8; ++i) xr[i] = *(const u32x2*)(x + (size_t)((tid - 256) + (wid - 4) * 448 + i * 64) * D_ + ch0);
#pragma unroll
        for (int i = 0; i < 8; ++i) *(LAS u32x2*)(smem + C1_XOFF + ((tid - 256) + (wid - 4) * 448 + i * 64) * 8) = xr[i];
        SCHED();
    };
    unsigned tag;
    unsigned dvv = 0;
    auto upper = [&](auto is_stat, auto is_conv) {
        constexpr bool STAT = decltype(is_stat)::v, CONV = decltype(is_conv)::v;
        const int srow = b * 8 + (wid - 5) * 4 + (lane >> 4), t3 = tid - 320;
        unsigned xw[18], epv = 0;
        if constexpr (STAT) {
            const unsigned* xr = (const unsigned*)(x + (size_t)srow * D_) + (lane & 15);
#pragma unroll
            for (int j = 0; j < 18; ++j) xw[j] = xr[16 * j];
        } else {
            epv = __hip_atomic_load((const unsigned*)(ws + WS_EP), __ATOMIC_RELAXED, __HIP_MEMORY_SCOPE_AGENT);
        }
        SCHED();
        if constexpr (STAT) {
            const char *c3, *c4;
            SYM_ADDR(c3, k3_gemm1); SYM_ADDR(c4, k9_end);
            warm_code<1>(c3, c4, ((idx & 3) * 2 + wid - 5) * 64, lane, lds0 + C1_TOFF + 256 + (wid - 5) * 256);
        }
        SCHED();
        u32x4 v[12], fa[6]; u32x2 fb[6];
        if constexpr (CONV) {
#pragma unroll
            for (int i = 0; i < 6; ++i) {
                const int task = min(t3 + 192 * i, 1023), u = task & 255;
                const u16* src = ssm_k + (size_t)(ch0 + (task >> 8)) * S_ + u * 8;
                fa[i] = *(const u32x4*)src;
                fb[i] = *(const u32x2*)(src + (u < 255 ? 8 : 0));
            }
            SCHED();
        }
        if constexpr (STAT) {
            f32x2 xf[18], s = {0.f, 0.f};
#pragma unroll
            for (int j = 0; j < 18; ++j) { xf[j] = (f32x2){bf_lo(xw[j]), bf_hi(xw[j])}; s += xf[j]; }
            const float mean = sum16(s.x + s.y) * (1.f / D_);
            f32x2 q = {0.f, 0.f};
#pragma unroll
            for (int j = 0; j < 18; ++j) { const f32x2 d = xf[j] - mean; q = __builtin_elementwise_fma(d, d, q); }
            const unsigned sgr = pk_bf16(mean, ln_sd(sum16(q.x + q.y) * (1.f / D_)));
            tag = get_tag();
            const unsigned so = (lane & 15) == 0 ? (unsigned)srow * 8 : 0x80000000u;
            st8_wt(mk_rsrc(ws + WS_ST, S_ * 8), so, (u32x2){sgr, tag});
            st8_wt(mk_rsrc(ws + WS_G2, S_ * 8), so, (u32x2){0u, 0u});
        } else {
            *tagp = epv + 1u;
            asm volatile("s_waitcnt lgkmcnt(0)" ::: "memory");
            tag = get_tag();
        }
        SCHED();
        if constexpr (!CONV) {
            __builtin_amdgcn_s_sleep(HELPER_DELAY);
            cs_load(v);
            if (!STAT && h == 111) {
                const rsrc_t rf = mk_rsrc(ws + WS_F0, 256 * FSP);
                for (int spin = 0; spin < (1 << 20); ++spin) {
                    bool ok = true;
#pragma unroll
                    for (int i = 0; i < 4; ++i) ok &= __builtin_amdgcn_raw_buffer_load_b32(rf, (i * 64 + lane) * FSP, 0, 16) == tag;
                    if (__all(ok)) break;
                    __builtin_amdgcn_s_sleep(8);
                }
                if (lane == 0) __hip_atomic_store((unsigned*)(ws + WS_EP), tag, __ATOMIC_RELAXED, __HIP_MEMORY_SCOPE_AGENT);
            }
            cs_finish(v);
            return;
        }
        if (t3 < 80) {
            const int cpy = t3 / 5, e = t3 % 5, q = e < 3 ? e : 257 + e;
            *(LAS u32x4*)(smem + cpy * C1_COPY + q * 16) = (u32x4){0u, 0u, 0u, 0u};
        } else if (t3 < 84) {
            *(LAS u32x4*)(smem + C1_KVOFF - 64 + (t3 - 80) * 16) = (u32x4){0u, 0u, 0u, 0u};
        }
        if (t3 < 128) *(LAS u32x4*)(smem + C1_KVOFF + (t3 >> 5) * C1_KV + 4096 + (t3 & 31) * 16) = (u32x4){0u, 0u, 0u, 0u};
#pragma unroll
        for (int i = 0; i < 6; ++i) {
            const int task = min(t3 + 192 * i, 1023), u = task & 255;
            const unsigned d[6] = {fa[i].x, fa[i].y, fa[i].z, fa[i].w, u < 255 ? fb[i].x : 0u, u < 255 ? fb[i].y : 0u};
            unsigned o[5];
#pragma unroll
            for (int e = 0; e < 5; ++e) o[e] = __builtin_amdgcn_alignbit(d[e + 1], d[e], 16);
            LAS char* base = (LAS char*)smem + (task >> 8) * C1_FILT + (u + 4) * 16;
            *(LAS u32x4*)(base) = (u32x4){o[0], o[1], o[2], o[3]};
            *(LAS u32x4*)(base + C1_COPY) = (u32x4){d[1], d[2], d[3], d[4]};
            *(LAS u32x4*)(base + 2 * C1_COPY) = (u32x4){o[1], o[2], o[3], o[4]};
            *(LAS u32x4*)(base + 3 * C1_COPY) = (u32x4){d[2], d[3], d[4], d[5]};
            if (u == 0) {
                *(LAS u32x4*)(base - 16) = (u32x4){0u, 0u, 0u, d[0] << 16};
                *(LAS u32x4*)(base - 16 + C1_COPY) = (u32x4){0u, 0u, 0u, d[0]};
                *(LAS u32x4*)(base - 16 + 2 * C1_COPY) = (u32x4){0u, 0u, d[0] << 16, o[0]};
                *(LAS u32x4*)(base - 16 + 3 * C1_COPY) = (u32x4){0u, 0u, d[0], d[1]};
            }
        }
        asm volatile("s_waitcnt lgkmcnt(0)" ::: "memory"); BAR();
        x_rows();
    };
    if (wid >= 5) {
        if (!conv) {
            if (wid < 7) upper(IC<1>{}, IC<0>{}); else upper(IC<0>{}, IC<0>{});
            return;
        }
        if (wid < 7) upper(IC<1>{}, IC<1>{}); else upper(IC<0>{}, IC<1>{});
    } else {
        {
            unsigned kw[11], vw[8];
            const int t0 = idx * 64 + (lane >> 3) * 8, dc = wid * 8 + (lane & 7), kch = xcd * 72 + (dc < 36 ? dc : 35) * 2;
            const rsrc_t rk = mk_rsrc(kin, S_ * D_ * 2), rv = mk_rsrc(vin, S_ * D_ * 2);
            const int off = t0 * (D_ * 2) + kch * 2;
#pragma unroll
            for (int j = 0; j < 11; ++j) kw[j] = __builtin_amdgcn_raw_buffer_load_b32(rk, (unsigned)(off + (j - 3) * (D_ * 2)), 0, 0);
#pragma unroll
            for (int j = 0; j < 8; ++j) vw[j] = __builtin_amdgcn_raw_buffer_load_b32(rv, (unsigned)(off + j * (D_ * 2)), 0, 0);
            const u32x4 sw = *(const u32x4*)(shift_k + kch * 4);
            dvv = ssm_D[(conv ? ch0 : 0) + (wid & 3)];
            SCHED();
            const f32x2 w0 = {bf_lo(sw.x), bf_lo(sw.z)}, w1 = {bf_hi(sw.x), bf_hi(sw.z)}, w2 = {bf_lo(sw.y), bf_lo(sw.w)}, w3 = {bf_hi(sw.y), bf_hi(sw.w)};
            f32x2 kf[11], o[8];
#pragma unroll
            for (int j = 0; j < 11; ++j) kf[j] = (f32x2){bf_lo(kw[j]), bf_hi(kw[j])};
#pragma unroll
            for (int j = 0; j < 8; ++j) {
                f32x2 s = kf[j] * w3;
                s = __builtin_elementwise_fma(kf[j + 1], w2, s);
                s = __builtin_elementwise_fma(kf[j + 2], w1, s);
                s = __builtin_elementwise_fma(kf[j + 3], w0, s);
                o[j] = s * (f32x2){bf_lo(vw[j]), bf_hi(vw[j])};
            }
            const rsrc_t ro = mk_rsrc(ws + WS_KV, S_ * D_ * 2);
            const unsigned oo = dc < 36 ? (unsigned)(kch * (S_ * 2) + (2040 - t0) * 2) : 0x80000000u;
            st16_wt(ro, oo, (u32x4){pk_bf16(o[7].x, o[6].x), pk_bf16(o[5].x, o[4].x), pk_bf16(o[3].x, o[2].x), pk_bf16(o[1].x, o[0].x)});
            st16_wt(ro, oo + S_ * 2, (u32x4){pk_bf16(o[7].y, o[6].y), pk_bf16(o[5].y, o[4].y), pk_bf16(o[3].y, o[2].y), pk_bf16(o[1].y, o[0].y)});
        }
        tag = get_tag();
        WAIT_V(0);
        if (lane == 0) __hip_atomic_store((unsigned*)(ws + WS_F0 + ((wid * 8 + xcd) * 32 + idx) * FSP), tag, __ATOMIC_RELAXED, __HIP_MEMORY_SCOPE_AGENT);
        if (!conv) {
            u32x4 v[12];
            __builtin_amdgcn_s_sleep(HELPER_DELAY2);
            cs_load(v);
            cs_finish(v);
            return;
        }
        if (wid == 4) {
            x_rows();
            BAR();
#pragma unroll 1
            for (int u = 0; u < WU4; ++u) wd_unit_load(wd, cg * WU4 + u, lane, lds0 + C1_WOFF + u * 3072);
            __builtin_amdgcn_s_sleep(24);
            const char* gs = ws + WS_ST;
#pragma unroll
            for (int i = 0; i < 4; ++i) {
                dma16_sc1<0>(lane * 16, gs + i * 4096, lds0 + C1_SOFF + i * 4096); dma16_sc1<1024>(lane * 16, gs + i * 4096, lds0 + C1_SOFF + i * 4096);
                dma16_sc1<2048>(lane * 16, gs + i * 4096, lds0 + C1_SOFF + i * 4096); dma16_sc1<3072>(lane * 16, gs + i * 4096, lds0 + C1_SOFF + i * 4096);
            }
            WAIT_V(0);
#pragma unroll 1
            for (int u = 0; u < WU4; ++u) wd_unit_store(ws, cg * WU4 + u, lane, (const LAS char*)smem + C1_WOFF + u * 3072);
        } else {
        {
            wait_kv(tag);
            const char* kvs = ws + WS_KV + (size_t)(ch0 + wid) * (S_ * 2);
            const unsigned m = lds0 + C1_KVOFF + wid * C1_KV;
            dma16_sc1<0>(lane * 16, kvs, m); dma16_sc1<1024>(lane * 16, kvs, m); dma16_sc1<2048>(lane * 16, kvs, m); dma16_sc1<3072>(lane * 16, kvs, m);
            WAIT_V(0);
            BAR();
        }
        const int row = lane & 15, kg = lane >> 4;
        const LAS char* abase = (const LAS char*)smem + wid * C1_FILT + (row & 3) * C1_COPY + (24 + 4 * (row >> 2) - 8 * kg) * 2;
        const LAS char* bbase = (const LAS char*)smem + C1_KVOFF + wid * C1_KV + (2040 - 16 * row - 8 * kg) * 2 - 512 * 7 - 448;
        auto rd_ah = [&](int r, int L, int hf) -> s16x4 { return *(const volatile LAS s16x4*)(abase + 512 * L - 64 * r + 8 * hf); };
        auto rd_b = [&](int r, int m) -> s16x8 { return *(const LAS s16x8*)(bbase + 448 - 64 * r + 512 * (7 - m)); };
        auto cat = [&](s16x4 lo, s16x4 hi) -> s16x8 { return __builtin_shufflevector(lo, hi, 0, 1, 2, 3, 4, 5, 6, 7); };
        f32x4 acc[8];
        s16x4 al[2][8], ah[2][8];
        s16x8 bf[2][8], bd[8];
        auto rd_k = [&](int r, int k) {
            if (k < 2) { if (k == 0) al[r & 1][0] = rd_ah(r, 1, 0); else ah[r & 1][0] = rd_ah(r, 1, 1); }
            else if (k < 10) bf[r & 1][9 - k] = rd_b(r, 8 - k);
            else { const int L = (k - 10) / 2 + 2; if (k & 1) ah[r & 1][L - 1] = rd_ah(r, L, 1); else al[r & 1][L - 1] = rd_ah(r, L, 0); }
        };
        const s16x8 a0 = cat(rd_ah(0, 0, 0), rd_ah(0, 0, 1));
#pragma unroll
        for (int e = 0; e < 8; ++e) bd[e] = rd_b(0, e);
        s16x8 dfr;
        {
            const int j = 8 * kg + 7 - row;
            u32x4 w;
#pragma unroll
            for (int e = 0; e < 4; ++e) w[e] = j == 2 * e ? dvv : (j == 2 * e + 1 ? dvv << 16 : 0u);
            dfr = __builtin_bit_cast(s16x8, w);
            asm volatile("s_nop 1" : "+v"(dfr));
        }
        SCHED();
#pragma unroll
        for (int n = 0; n < 16; ++n) {
            if (n < 8) mfma_first(acc[n], a0, bd[n]); else mfma_acc(acc[n - 8], dfr, bd[n - 8]);
            SCHED();
            if (n == 0) { rd_k(0, 0); rd_k(0, 1); }
            else if (n == 1) rd_k(0, 9);
            else rd_k(0, n + 8);
            SCHED();
        }
#pragma unroll
        for (int r = 0; r < 8; ++r) {
            int k = 0;
#pragma unroll
            for (int L = 1; L <= 8; ++L) {
#pragma unroll
                for (int n = 7; n >= L - 1; --n) {
                    const int e = n - L + 1;
                    mfma_acc(acc[n], cat(al[r & 1][L - 1], ah[r & 1][L - 1]), (r == 0 && e > 0) ? bd[e - 1] : bf[r & 1][e]);
                    SCHED();
                    if (r < 7 && k < 24) rd_k(r + 1, k);
                    ++k;
                    SCHED();
                }
            }
        }
        MFMA_DONE();
#pragma unroll
        for (int n = 0; n < 8; ++n)
            *(LAS u32x2*)(smem + C1_YOFF + wid * 4096 + (256 * n + 16 * row + 4 * kg) * 2) = (u32x2){pk_bf16(acc[n][0], acc[n][1]), pk_bf16(acc[n][2], acc[n][3])};
        }
    }
    asm volatile("s_waitcnt lgkmcnt(0)" ::: "memory"); BAR();
    {
        u32x2 yc[4];
#pragma unroll
        for (int c = 0; c < 4; ++c) yc[c] = *(const LAS u32x2*)(smem + C1_YOFF + c * 4096 + tid * 8);
        const u32x4 xa = *(const LAS u32x4*)(smem + C1_XOFF + tid * 32), xb = *(const LAS u32x4*)(smem + C1_XOFF + tid * 32 + 16);
        const u32x2 xr[4] = {{xa.x, xa.y}, {xa.z, xa.w}, {xb.x, xb.y}, {xb.z, xb.w}};
        u32x4 s01 = *(const LAS u32x4*)(smem + C1_SOFF + tid * 32), s23 = *(const LAS u32x4*)(smem + C1_SOFF + tid * 32 + 16);
        if (__builtin_expect(!__all(s01.y == tag && s01.w == tag && s23.y == tag && s23.w == tag), 0)) {
            const rsrc_t rs = mk_rsrc(ws + WS_ST, S_ * 8);
            for (int spin = 0; spin < (1 << 20); ++spin) {
                s01 = __builtin_amdgcn_raw_buffer_load_b128(rs, tid * 32, 0, 16);
                s23 = __builtin_amdgcn_raw_buffer_load_b128(rs, tid * 32 + 16, 0, 16);
                if (__all(s01.y == tag && s01.w == tag && s23.y == tag && s23.w == tag)) break;
                __builtin_amdgcn_s_sleep(2);
            }
        }
        const unsigned sv[4] = {s01.x, s01.z, s23.x, s23.z};
        float mm[4], rr[4];
#pragma unroll
        for (int j = 0; j < 4; ++j) { mm[j] = bf_lo(sv[j]); rr[j] = __builtin_amdgcn_rcpf(bf_hi(sv[j])); }
        unsigned hw[8];
#pragma unroll
        for (int j = 0; j < 4; ++j) {
#pragma unroll
            for (int e = 0; e < 2; ++e) {
                const unsigned xw = e ? xr[j].y : xr[j].x;
                const unsigned ya = (j >> 1) ? yc[2 * e].y : yc[2 * e].x, yb = (j >> 1) ? yc[2 * e + 1].y : yc[2 * e + 1].x;
                const float y0 = (j & 1) ? bf_hi(ya) : bf_lo(ya), y1 = (j & 1) ? bf_hi(yb) : bf_lo(yb);
                const float x0 = bf_lo(xw), x1 = bf_hi(xw);
                hw[2 * j + e] = pk_bf16(__builtin_fmaf((x0 - mm[j]) * rr[j], y0, x0), __builtin_fmaf((x1 - mm[j]) * rr[j], y1, x1));
            }
        }
        const rsrc_t rh = mk_rsrc(ws + WS_H, S_ * D_ * 2);
        const unsigned ho = (unsigned)(((tid >> 6) * 144 + cg) * 2048 + (tid & 63) * 32);
        st16_wt(rh, ho, (u32x4){hw[0], hw[1], hw[2], hw[3]});
        st16_wt(rh, ho + 16, (u32x4){hw[4], hw[5], hw[6], hw[7]});
    }
}

constexpr int ST_BYTES = 16384 + 2 * 9216;
constexpr int G_LDS = 4 * ST_BYTES;

struct TokC { float nm, c0, c1, q; };
__device__ __forceinline__ unsigned geglu2(f32x2 ag, f32x2 au, f32x2 csg, f32x2 csu, const TokC& t) {
    const f32x2 nm = {t.nm, t.nm}, c0 = {t.c0, t.c0}, c1 = {t.c1, t.c1}, q = {t.q, t.q};
    const f32x2 g = __builtin_elementwise_fma(nm, csg, ag), u = __builtin_elementwise_fma(nm, csu, au);
    const f32x2 z = g * __builtin_elementwise_fma(g * g, c1, c0);
    const f32x2 e = {__builtin_amdgcn_exp2f(z.x), __builtin_amdgcn_exp2f(z.y)};
    const f32x2 d = __builtin_elementwise_fma(e, q, q);
    const f32x2 r = (g * u) * (f32x2){__builtin_amdgcn_rcpf(d.x), __builtin_amdgcn_rcpf(d.y)};
    return pk_bf16(r.x, r.y);
}
#define LO2(v) __builtin_shufflevector(v, v, 0, 1)
#define HI2(v) __builtin_shufflevector(v, v, 2, 3)
#define WAIT_VI(N) asm volatile("s_waitcnt vmcnt(%0)" :: "i"(N) : "memory")

constexpr int G2_W0 = 71680, G2_FL = 69632;
__device__ __forceinline__ void g2_wpiece(int P, int s, const char* wsrc, unsigned aoff, unsigned mw0) {
    const unsigned m = mw0 + (s % 5) * 18432;
    if (P == 0) dma16<0>(aoff, wsrc + (size_t)s * 18432, m);
    if (P == 1) dma16<1024>(aoff, wsrc + (size_t)s * 18432, m);
    if (P == 2) dma16_q<2048>(aoff, wsrc + (size_t)s * 18432, m);
}
constexpr int g2_n(int S) { return (S + 3 < 18 ? 2 : 0) + (S + 4 < 18 ? 3 : 0) + (S == 4 ? 1 : 0); }
constexpr int g2_vm(int S) {
    return S == 0 ? 3 : (S == 1 ? 1 + g2_n(0) : g2_n(S - 2) - (S + 1 < 18 ? 2 : 0) + g2_n(S - 1));
}
template <int NT>
__device__ __forceinline__ void g2_body(char* __restrict__ ws, u16* __restrict__ out, LAS char* lds, int lane, int wid, int mt, int ks, int nt,
                                        const char* wsrc, unsigned flag2) {
    constexpr int dh = NT == 4;
    const unsigned lds0 = (unsigned)(size_t)lds;
    const int wr = wid & 3;
    const char* asrc = ws + WS_HH + ((size_t)(mt * 4 + ks) * 36) * 8192 + wid * 2048;
    const unsigned aoff = lane * 16;
    const unsigned ma0 = lds0 + wid * 2048, mw0 = lds0 + G2_W0 + wid * 2304;
    auto hpiece = [&](int P, int s) {
        if (P == 0) dma16_sc1<0>(aoff, asrc + (size_t)s * 16384, ma0 + (s & 3) * 16384);
        if (P == 1) dma16_sc1<1024>(aoff, asrc + (size_t)s * 16384, ma0 + (s & 3) * 16384);
    };
    hpiece(0, 0); hpiece(1, 0); hpiece(0, 1); hpiece(1, 1); hpiece(0, 2); hpiece(1, 2);

    const int g4 = lane >> 4;
    const int toff = wr * 2048 + lane * 16;
    const int foff = G2_W0 + dh * 5120 + lane * 16;
    auto rdf = [&](const LAS char* p) -> s16x8 { return *(const LAS s16x8*)p; };
    const rsrc_t rfb = mk_rsrc(ws + WS_FB, 16384);

    f32x4 acc[NT][2];
    s16x8 wf[2 * NT], tf[2][4];
    WAIT_V(4);
    __builtin_amdgcn_raw_buffer_store_b32(1u, rfb, lane == 0 ? flag2 : 0x80000000u, 0, 16);
    BAR();
#pragma unroll
    for (int e = 0; e < 4; ++e) tf[0][e] = rdf(lds + toff + (e >> 1) * 8192 + (e & 1) * 1024);
#pragma unroll
    for (int e = 0; e < 2 * NT; ++e) wf[e] = rdf(lds + foff + (e / NT) * 9216 + (e % NT) * 1024);
    SCHED();

    auto iter = [&](auto sc, s16x8 (&tc)[4], s16x8 (&tn)[4]) {
        constexpr int S = decltype(sc)::v;
        if constexpr (S < 17) {
            WAIT_VI(g2_vm(S));
            BAR();
            if constexpr (S == 7) {
                unsigned fb2 = *(const volatile LAS unsigned*)(lds + G2_FL + wid * 256 + lane * 4);
                if (__builtin_expect(!__all(fb2 == 1u), 0)) {
                    for (int spin = 0; spin < (1 << 20); ++spin) {
                        fb2 = __builtin_amdgcn_raw_buffer_load_b32(rfb, (unsigned)(((mt * 4 + ks) * 2 + 1) * 128 + (lane & 31) * 4), 0, 16);
                        if (__all(fb2 == 1u)) break;
                        __builtin_amdgcn_s_sleep(2);
                    }
                }
            }
            SCHED();
        }
        const LAS char* tb = lds + ((S + 1) & 3) * 16384;
        const LAS char* wb = lds + ((S + 1) % 5) * 18432;
#pragma unroll
        for (int e = 0; e < 2 * NT; ++e) {
#pragma unroll
            for (int i = 0; i < 2; ++i) {
                if (S == 0 && e < NT) mfma_first(acc[e % NT][i], wf[e], tc[(e / NT) * 2 + i]);
                else mfma_acc(acc[e % NT][i], wf[e], tc[(e / NT) * 2 + i]);
            }
            SCHED();
            if constexpr (S < 17) {
                wf[e] = rdf(wb + foff + (e / NT) * 9216 + (e % NT) * 1024);
                if (e >= 2 && e < 6) tn[e - 2] = rdf(tb + toff + ((e - 2) >> 1) * 8192 + ((e - 2) & 1) * 1024);
            }
            if constexpr (S + 3 < 18) { if (e < 2) hpiece(e, S + 3); }
            if constexpr (S + 4 < 18) {
                if (e == 3) g2_wpiece(0, S + 4, wsrc, aoff, mw0);
                if (e == 5) g2_wpiece(1, S + 4, wsrc, aoff, mw0);
                if (e == 6) g2_wpiece(2, S + 4, wsrc, aoff, mw0);
            }
            if constexpr (S == 4) {
                if (e == 7) dma4_sc1((unsigned)(lane & 31) * 4, ws + WS_FB + ((mt * 4 + ks) * 2 + 1) * 128, lds0 + G2_FL + wid * 256);
            }
            SCHED();
        }
    };
#define G2_IT2(S) iter(IC<S>{}, tf[0], tf[1]); iter(IC<S + 1>{}, tf[1], tf[0])
    G2_IT2(0); G2_IT2(2); G2_IT2(4); G2_IT2(6); G2_IT2(8); G2_IT2(10); G2_IT2(12);
    G2_IT2(14);
    iter(IC<16>{}, tf[0], tf[1]);
    const int tok0 = mt * 128 + wr * 32 + (lane & 15);
    u32x2 hv[2][5];
    if (wr == ks) {
#pragma unroll
        for (int i = 0; i < 2; ++i) {
            const int t = tok0 + i * 16;
            const char* hp = ws + WS_H + ((size_t)((t >> 8) * 144 + nt * 36 + dh * 20) * 256 + (t & 255)) * 8;
#pragma unroll
            for (int e = 0; e < 4; ++e) hv[i][e] = *(const u32x2*)(hp + ((e >> 1) * 8 + (g4 & 1) * 4 + (g4 >> 1) * 2 + (e & 1)) * 2048);
            if (NT == 5) hv[i][4] = *(const u32x2*)(hp + (16 + g4) * 2048);
        }
    }
    SCHED();
    iter(IC<17>{}, tf[1], tf[0]);
    MFMA_DONE();

    const rsrc_t rx = mk_rsrc(ws + WS_X, X_BYTES);
    const unsigned xt = (unsigned)((mt * 4 + nt) * 4 * 27648 + dh * 4096);
    auto inb = [&](int s) -> unsigned { return xt + (unsigned)(((ks + 1 + s) & 3) * 27648 + (2 - s) * 9216); };
    if (wr != ks) {
        const unsigned o = xt + (unsigned)(ks * 27648 + ((wr - ks - 1) & 3) * 9216);
#pragma unroll
        for (int i = 0; i < 2; ++i) {
            unsigned w[2 * NT];
#pragma unroll
            for (int j = 0; j < NT; ++j) { w[2 * j] = pk_bf16(acc[j][i][0], acc[j][i][1]); w[2 * j + 1] = pk_bf16(acc[j][i][2], acc[j][i][3]); }
#pragma unroll
            for (int e = 0; e < 2 * NT; ++e) w[e] = (w[e] == ~0u) ? 0x7fc07fc0u : w[e];
            if constexpr (NT == 5) st8_wt(rx, o + 8192 + i * 512 + lane * 8, (u32x2){w[8], w[9]});
            st16_wt(rx, o + i * 2048 + lane * 16, (u32x4){w[0], w[1], w[2], w[3]});
            st16_wt(rx, o + i * 2048 + 1024 + lane * 16, (u32x4){w[4], w[5], w[6], w[7]});
        }
    } else {
        u32x4 pa[3][4]; u32x2 pb[3][2];
        __builtin_amdgcn_s_sleep(16);
        for (int spin = 0; spin < (1 << 18); ++spin) {
            const u32x4 t = __builtin_amdgcn_raw_buffer_load_b128(rx, lane < 3 ? inb(lane) + 4080 : 0x80000000u, 0, 16);
            if (__all(t.w != ~0u)) {
                unsigned m = 0u;
#pragma unroll
                for (int s = 0; s < 3; ++s) {
#pragma unroll
                    for (int p = 0; p < 4; ++p) pa[s][p] = __builtin_amdgcn_raw_buffer_load_b128(rx, inb(s) + p * 1024 + lane * 16, 0, 16);
                    if constexpr (NT == 5) {
#pragma unroll
                        for (int i = 0; i < 2; ++i) pb[s][i] = __builtin_amdgcn_raw_buffer_load_b64(rx, inb(s) + 8192 + i * 512 + lane * 8, 0, 16);
                    }
                }
#pragma unroll
                for (int s = 0; s < 3; ++s) {
#pragma unroll
                    for (int p = 0; p < 4; ++p) m = max(max(max(m, pa[s][p].x), max(pa[s][p].y, pa[s][p].z)), pa[s][p].w);
                    if constexpr (NT == 5) m = max(max(max(m, pb[s][0].x), max(pb[s][0].y, pb[s][1].x)), pb[s][1].y);
                }
                if (__all(m != ~0u)) break;
            }
            __builtin_amdgcn_s_sleep(1);
        }
        const rsrc_t ro = mk_rsrc(out, S_ * D_ * 2);
#pragma unroll
        for (int i = 0; i < 2; ++i) {
            const unsigned ob = (unsigned)(((tok0 + i * 16) * D_ + nt * 144 + dh * 80) * 2);
            unsigned r[NT][2];
#pragma unroll
            for (int j = 0; j < NT; ++j) {
                f32x4 t = acc[j][i];
#pragma unroll
                for (int s = 0; s < 3; ++s) {
                    const unsigned w0 = j < 4 ? pa[s][i * 2 + (j >> 1)][(j & 1) * 2] : pb[s][i].x, w1 = j < 4 ? pa[s][i * 2 + (j >> 1)][(j & 1) * 2 + 1] : pb[s][i].y;
                    t += (f32x4){bf_lo(w0), bf_hi(w0), bf_lo(w1), bf_hi(w1)};
                }
                r[j][0] = pk_bf16(t[0], t[1]); r[j][1] = pk_bf16(t[2], t[3]);
            }
            auto addh = [&](unsigned a, unsigned h) -> unsigned { return pk_bf16(bf_lo(h) + bf_lo(a), bf_hi(h) + bf_hi(a)); };
#pragma unroll
            for (int p = 0; p < 2; ++p) {
                auto s0 = __builtin_amdgcn_permlane16_swap(r[2 * p][0], r[2 * p + 1][0], false, false);
                auto s1 = __builtin_amdgcn_permlane16_swap(r[2 * p][1], r[2 * p + 1][1], false, false);
                st16_wt(ro, ob + (unsigned)((p * 32 + (g4 & 1) * 16 + (g4 >> 1) * 8) * 2),
                        (u32x4){addh(s0[0], hv[i][2 * p].x), addh(s1[0], hv[i][2 * p].y), addh(s0[1], hv[i][2 * p + 1].x), addh(s1[1], hv[i][2 * p + 1].y)});
            }
            if constexpr (NT == 5) st8_wt(ro, ob + (unsigned)((64 + g4 * 4) * 2), (u32x2){addh(r[4][0], hv[i][4].x), addh(r[4][1], hv[i][4].y)});
        }
    }
}

constexpr int K3_LDS = 163840;
constexpr int K3_SP = G_LDS;
constexpr int K3_CS = G2_W0 + 4 * 18432;
extern "C" __global__ __launch_bounds__(512, 2) void k3_gemm1(const u16* __restrict__ wg, const u16* __restrict__ wu,
                                                   char* __restrict__ ws, u16* __restrict__ out) {
    extern __shared__ __attribute__((aligned(16))) char smem[];
    LAS char* lds = (LAS char*)smem;
    const unsigned lds0 = (unsigned)(size_t)lds;
    const int tid = threadIdx.x, lane = tid & 63;
    const int wid = __builtin_amdgcn_readfirstlane(tid >> 6);
    const int wr = wid & 3, wc = wid >> 2;
    const int b = blockIdx.x, xcd = b & 7, idx = b >> 3;
    const int rt = (xcd >> 2) * 4 + (idx & 3), ct = (xcd & 3) * 8 + (idx >> 2);
    asm volatile("" :: "s"(wg), "s"(wu), "s"(ws), "s"(out));

    const char* asrc = ws + WS_H + (size_t)rt * 144 * 2048 + wid * 2048;
    const unsigned aoff = (unsigned)(lane ^ ((wid & 2) << 2)) * 16;
    const unsigned ma0 = lds0 + wid * 2048, mw0 = lds0 + 16384 + wc * 9216 + wr * 2304;
    dma16<0>(aoff, asrc, ma0); dma16<1024>(aoff, asrc, ma0);
    const char* wsrc = (const char*)(wc ? wu : wg) + ct * 288 - 2048;
    auto wofs = [&](int i) -> unsigned {
        const int q = lane + 64 * i, lrow = 8 * wr + q / 18, pos = q % 18;
        const int krow = ((lrow >> 2) & 3) * 8 + (lrow >> 4) * 4 + (lrow & 3);
        const int col = pos * 8 + (pos < 8 ? 0 : (pos < 16 ? 8 : (((pos & 1) ^ wc) ? 136 : 64) - pos * 8));
        return (unsigned)(krow * (F_ * 2) + col * 2 + 2048 - 1024 * i);
    };
    const unsigned w0 = wofs(0), w1 = wofs(1), w2 = wofs(2);
    auto piece = [&](int P, int s) {
        const unsigned bo = (s & 3) * ST_BYTES;
        const char* wp = wsrc + (size_t)s * (32 * F_ * 2);
        if (P == 0) dma16<0>(aoff, asrc + (size_t)s * 16384, ma0 + bo);
        if (P == 1) dma16<1024>(aoff, asrc + (size_t)s * 16384, ma0 + bo);
        if (P == 2) dma16<0>(w0, wp, mw0 + bo);
        if (P == 3) dma16<1024>(w1, wp, mw0 + bo);
        if (P == 4) dma16_q<2048>(w2, wp, mw0 + bo);
    };
#define K3_PIECE(P, s) piece(P, s)
#define K3_STAGE(s) do { K3_PIECE(0, s); K3_PIECE(1, s); K3_PIECE(2, s); K3_PIECE(3, s); K3_PIECE(4, s); } while (0)
    K3_PIECE(2, 0); K3_PIECE(3, 0); K3_PIECE(4, 0);
    BAR();
    K3_STAGE(1);
    u32x2 hs[4];
    {
        const char* hsrc = ws + WS_H + (size_t)rt * 144 * 2048 + ct * 64;
        const unsigned o = (unsigned)((wid * 18 + (lane >> 3)) * 2048 + (lane & 7) * 8);
        asm volatile("global_load_dwordx2 %0, %1, %2" : "=v"(hs[0]) : "v"(o), "s"(hsrc) : "memory");
        asm volatile("global_load_dwordx2 %0, %1, %2" : "=v"(hs[1]) : "v"(o + 16384), "s"(hsrc) : "memory");
        asm volatile("global_load_dwordx2 %0, %1, %2" : "=v"(hs[2]) : "v"((unsigned)((wid * 18 + 16 + ((lane >> 3) & 1)) * 2048 + (lane & 7) * 8)), "s"(hsrc) : "memory");
        asm volatile("global_load_dwordx2 %0, %1, %2" : "=v"(hs[3]) : "v"((unsigned)(lane & 7) * 8), "s"(hsrc) : "memory");
    }

    const int g4 = lane >> 4, q4 = (lane & 15) >> 2, p4 = lane & 3;
    const int rowoff = (4 * g4 + q4) * 288;
    const int tbase = g4 * 4096 + wr * 512 + (lane & 15) * 8;
    const int toff0 = tbase + ((2 * wc) ^ (g4 & 1)) * 128, toff1 = tbase + ((2 * wc + 1) ^ (g4 & 1)) * 128;
    const int tstep = wc ? -256 : 256;
    const int foff = 16384 + rowoff + wc * 128 + p4 * 8;
    const int moff = 16384 + rowoff + 256 + (p4 < 2 ? 16 * wc + 8 * p4 : 9216 + 16 * (1 - wc) + 8 * (p4 - 2));

    f32x4 acc[9][4];
    s16x8 wf[9], tf[2][4];
    auto rd_w = [&](const LAS char* sb, int j) -> s16x8 {
        return j < 8 ? tr_frag(sb + foff + (j >> 2) * 9216 + (j & 3) * 32) : tr_frag(sb + moff);
    };
    auto rd_t = [&](const LAS char* sb, int i) -> s16x8 {
        const LAS char* p = sb + ((i & 1) ? toff1 : toff0) + (i >> 1) * tstep;
        const s16x4 lo = *(const volatile LAS s16x4*)p, hi = *(const volatile LAS s16x4*)(p + 2048);
        return __builtin_shufflevector(lo, hi, 0, 1, 2, 3, 4, 5, 6, 7);
    };
    WAIT_V(9); BAR();
    wf[0] = rd_w(lds, 0);
#pragma unroll
    for (int i = 0; i < 4; ++i) tf[0][i] = rd_t(lds, i);
#pragma unroll
    for (int j = 1; j < 9; ++j) wf[j] = rd_w(lds, j);
    SCHED();
    const int mt2 = (xcd >> 2) * 8 + (idx & 7), ks2 = xcd & 3, nt2 = idx >> 3;
    const rsrc_t rfill = mk_rsrc(ws + WS_X + (size_t)((mt2 * 4 + nt2) * 4 + ks2) * 27648, 27648);
    auto fill = [&](int k) { st16_wt(rfill, (unsigned)(k * 8192 + tid * 16), (u32x4){~0u, ~0u, ~0u, ~0u}); };
    unsigned sgr;
    {
        const LAS char* sb = lds + ST_BYTES;
        float hc = 0.f;
#pragma unroll
        for (int j = 0; j < 9; ++j) {
#pragma unroll
            for (int i = 0; i < 4; ++i) mfma_first(acc[j][i], wf[j], tf[0][i]);
            SCHED();
            if (j == 0) { K3_PIECE(0, 2); K3_PIECE(1, 2); }
            if (j == 1) { K3_PIECE(2, 2); K3_PIECE(3, 2); }
            if (j == 2) K3_PIECE(4, 2);
            if (j == 3) {
                asm volatile("s_waitcnt vmcnt(5)" : "+v"(hs[0]), "+v"(hs[1]), "+v"(hs[2]), "+v"(hs[3]) :: "memory");
                hc = bf_lo(hs[3].x);
                float s1 = 0.f, s2 = 0.f;
#pragma unroll
                for (int i = 0; i < 3; ++i) {
                    const float d[4] = {bf_lo(hs[i].x) - hc, bf_hi(hs[i].x) - hc, bf_lo(hs[i].y) - hc, bf_hi(hs[i].y) - hc};
                    float a = (d[0] + d[1]) + (d[2] + d[3]), q = __builtin_fmaf(d[0], d[0], d[1] * d[1]) + __builtin_fmaf(d[2], d[2], d[3] * d[3]);
                    if (i == 2 && lane >= 16) { a = 0.f; q = 0.f; }
                    s1 += a; s2 += q;
                }
                s1 = x32_add(x16_add(dpp_add<0x128>(s1))); s2 = x32_add(x16_add(dpp_add<0x128>(s2)));
                if (lane < 8) *(LAS f32x2*)(lds + K3_SP + (lane * 8 + wid) * 8) = (f32x2){s1, s2};
                asm volatile("s_waitcnt lgkmcnt(0)" ::: "memory");
                BAR();
                wf[0] = rd_w(sb, 0); wf[1] = rd_w(sb, 1);
                tf[1][0] = rd_t(sb, 0); tf[1][1] = rd_t(sb, 1);
            }
            if (j == 4) { tf[1][2] = rd_t(sb, 2); tf[1][3] = rd_t(sb, 3); wf[2] = rd_w(sb, 2); wf[3] = rd_w(sb, 3); }
            if (j >= 4) { wf[j] = rd_w(sb, j); K3_PIECE(j - 4, 3); }
            if (j == 8) {
                const f32x2 pp = *(const LAS f32x2*)(lds + K3_SP + (wid * 8 + (lane & 7)) * 8);
                const float s1 = dpp_add<0x141>(dpp_add<0x4E>(dpp_add<0xB1>(pp.x))) * (1.f / D_);
                const float s2 = dpp_add<0x141>(dpp_add<0x4E>(dpp_add<0xB1>(pp.y))) * (1.f / D_);
                const float c0 = __uint_as_float(__builtin_amdgcn_readlane(__float_as_uint(hc), wid));
                const float sd = ln_sd(__builtin_fmaxf(__builtin_fmaf(-s1, s1, s2), 0.f));
                sgr = pk_bf16(c0 + s1, sd);
                fill(0); fill(1);
            }
            SCHED();
        }
    }

    const rsrc_t rg = mk_rsrc(ws + WS_G2 + (size_t)(rt * 256 + wr * 64) * 8, 512);
    const char* w2src = ws + WS_WT + (size_t)(ks2 * 4 + nt2) * 18 * 18432 + wid * 2304;
    u32x2 gr[4];
    s16x4 tl;
    constexpr int TP = 1, DPJ0 = 0, DPJ1 = 5;
    auto iter = [&](auto mode, auto vm, int s, s16x8 (&tc)[4], s16x8 (&tn)[4]) {
        constexpr int MODE = decltype(mode)::v;
        if constexpr (MODE != 4) {
            if constexpr (MODE == 3) WAIT_V(0); else WAIT_VI(5 + decltype(vm)::v);
            BAR();
            SCHED();
        }
        const LAS char* sb = lds + ((s + 1) & 3) * ST_BYTES;
#pragma unroll
        for (int j = 0; j < 9; ++j) {
            s16x4 wl, wh;
#pragma unroll
            for (int i = 0; i < 4; ++i) {
                mfma_acc(acc[j][i], wf[j], tc[i]);
                SCHED();
                if constexpr (MODE != 4) {
                    if (j >= 1) {
                        const LAS char* p = (j - 1 < 8) ? sb + foff + ((j - 1) >> 2) * 9216 + ((j - 1) & 3) * 32 : sb + moff;
                        if (i == 0) wl = __builtin_amdgcn_ds_read_tr16_b64_v4i16((LAS s16x4*)p);
                        if (i == 1) { wh = __builtin_amdgcn_ds_read_tr16_b64_v4i16((LAS s16x4*)(p + 16 * 288)); wf[j - 1] = __builtin_shufflevector(wl, wh, 0, 1, 2, 3, 4, 5, 6, 7); }
                    }
                    if (j >= TP && j < TP + 4) {
                        const int ti = j - TP;
                        const LAS char* p = sb + ((ti & 1) ? toff1 : toff0) + (ti >> 1) * tstep;
                        if (i == 2) tl = *(const volatile LAS s16x4*)p;
                        if (i == 3) tn[ti] = __builtin_shufflevector(tl, *(const volatile LAS s16x4*)(p + 2048), 0, 1, 2, 3, 4, 5, 6, 7);
                    }
                    if (j == 8 && i == 3) wf[8] = rd_w(sb, 8);
                }
                if constexpr (MODE < 2 || MODE == 5) {
                    if (j == DPJ0 && i == 1) K3_PIECE(0, s + 3);
                    if (j == DPJ0 && i == 3) K3_PIECE(1, s + 3);
                    if (j == DPJ1 && i == 3) K3_PIECE(2, s + 3);
                    if (j == DPJ1 + 1 && i == 3) K3_PIECE(3, s + 3);
                    if (j == DPJ1 + 2 && i == 3) K3_PIECE(4, s + 3);
                }
                SCHED();
            }
            if constexpr (MODE == 0) { if (j == 8) { fill(2); fill(3); } }
            if constexpr (MODE == 5) {
                if (j == 0 && s == 4) st8_wt(mk_rsrc(ws + WS_G2, S_ * 8), lane == 0 ? (unsigned)((rt * 256 + ct * 8 + wid) * 8) : 0x80000000u, (u32x2){sgr, 1u});
            }
            if constexpr (MODE == 2) {
                if (j == 0) {
#pragma unroll
                    for (int i = 0; i < 4; ++i) gr[i] = __builtin_amdgcn_raw_buffer_load_b64(rg, (unsigned)(((i ^ (2 * wc)) * 16 + (lane & 15)) * 8), 0, 16);
                }
            }
            if constexpr (MODE == 3) {
                if (j == 0) {
                    asm volatile("" : "+v"(gr[0]), "+v"(gr[1]), "+v"(gr[2]), "+v"(gr[3]));
                    const unsigned co = (unsigned)((lane >= 18 ? F_ * 4 + (lane - 18) * 16 : lane * 16));
                    dma_m36(co, ws + WS_CS + (size_t)(ct * 144 + wc * 72) * 4, lds0 + K3_CS + wid * 1152);
                    dma_m36(co, ws + WS_CS + (size_t)(2 * F_ + ct * 144 + wc * 72) * 4, lds0 + K3_CS + wid * 1152 + 576);
                }
            }
            if constexpr (MODE == 3 || MODE == 4) {
                if (j >= 2 && j < 8) g2_wpiece((j - 2) % 3, (MODE - 3) * 2 + (j - 2) / 3, w2src, lane * 16, lds0 + G2_W0 + wid * 2304);
            }
            SCHED();
        }
    };
    iter(IC<0>{}, IC<2>{}, 1, tf[1], tf[0]);
    iter(IC<1>{}, IC<4>{}, 2, tf[0], tf[1]);
    iter(IC<1>{}, IC<2>{}, 3, tf[1], tf[0]);
#pragma unroll 1
    for (int s = 4; s < 14; s += 2) {
        iter(IC<5>{}, IC<0>{}, s, tf[0], tf[1]);
        iter(IC<1>{}, IC<0>{}, s + 1, tf[1], tf[0]);
    }
    iter(IC<1>{}, IC<0>{}, 14, tf[0], tf[1]);
    iter(IC<2>{}, IC<0>{}, 15, tf[1], tf[0]);
    iter(IC<3>{}, IC<0>{}, 16, tf[0], tf[1]);
    iter(IC<4>{}, IC<0>{}, 17, tf[1], tf[0]);
    MFMA_DONE();
    WAIT_V(12);
    __builtin_amdgcn_s_setprio(1);
    if (__builtin_expect(!__all((gr[0].y & gr[1].y & gr[2].y & gr[3].y) == 1u), 0)) {
        for (int spin = 0; spin < (1 << 20); ++spin) {
            __builtin_amdgcn_s_sleep(2);
#pragma unroll
            for (int i = 0; i < 4; ++i) gr[i] = __builtin_amdgcn_raw_buffer_load_b64(rg, (unsigned)(((i ^ (2 * wc)) * 16 + (lane & 15)) * 8), 0, 16);
            if (__all((gr[0].y & gr[1].y & gr[2].y & gr[3].y) == 1u)) break;
        }
    }
    const int mt = rt * 2 + (wr >> 1), ks = ct >> 3, lct = ct & 7;
    const rsrc_t rhh = mk_rsrc(ws + WS_HH, S_ * F_ * 2);
    const unsigned hb = (unsigned)(((mt * 4 + ks) * 36) * 8192 + ((wr & 1) * 4) * 1024);
    const LAS char* csb = lds + K3_CS + wid * 1152 + 16 * g4;
    TokC tcs[4];
#pragma unroll
    for (int i = 0; i < 4; ++i) {
        const float sd = bf_hi(gr[i].x), r = __builtin_amdgcn_rcpf(sd);
        tcs[i] = (TokC){-bf_lo(gr[i].x), -2.30220819f * r, -0.10294324f * (r * r * r), sd * sd};
    }
    {
        const LAS char* cm = lds + K3_CS + wid * 1152 + 256 + 16 * (g4 & 1);
        const f32x4 cgm = *(const LAS f32x4*)cm + *(const LAS f32x4*)(cm + 576), cum = *(const LAS f32x4*)(cm + 288) + *(const LAS f32x4*)(cm + 864);
#pragma unroll
        for (int ip = 0; ip < 2; ++ip) {
            const int ia = ((2 * ip) ^ (2 * wc)) + (lane >> 5);
            const bool up = lane >= 32;
            const TokC tc = {up ? tcs[2 * ip + 1].nm : tcs[2 * ip].nm, up ? tcs[2 * ip + 1].c0 : tcs[2 * ip].c0,
                             up ? tcs[2 * ip + 1].c1 : tcs[2 * ip].c1, up ? tcs[2 * ip + 1].q : tcs[2 * ip].q};
            f32x4 gm, um;
#pragma unroll
            for (int r = 0; r < 4; ++r) {
                auto sw = __builtin_amdgcn_permlane32_swap(__float_as_uint(acc[8][2 * ip][r]), __float_as_uint(acc[8][2 * ip + 1][r]), false, false);
                gm[r] = __uint_as_float(sw[0]); um[r] = __uint_as_float(sw[1]);
            }
            const int kgs = (lct & 1) * 2 + wc;
            st8_wt(rhh, hb + (unsigned)((lct >> 1) * 8192 + ia * 1024 + (kgs * 16 + (lane & 15)) * 16 + (g4 & 1) * 8),
                   (u32x2){geglu2(LO2(gm), LO2(um), LO2(cgm), LO2(cum), tc), geglu2(HI2(gm), HI2(um), HI2(cgm), HI2(cum), tc)});
        }
    }
    SCHED();
    constexpr int FLAGPOS = 1;
    const rsrc_t rfb = mk_rsrc(ws + WS_FB, 16384);
    const unsigned fbo = (unsigned)((mt * 4 + ks) * 256 + (lct * 4 + (wr & 1) * 2 + wc) * 4);
    u32x4 ob[4];
#pragma unroll
    for (int jp = 0; jp < 2; ++jp) {
        f32x4 cg[2], cu[2];
#pragma unroll
        for (int e = 0; e < 2; ++e) {
            cg[e] = *(const LAS f32x4*)(csb + (2 * jp + e) * 64) + *(const LAS f32x4*)(csb + 576 + (2 * jp + e) * 64);
            cu[e] = *(const LAS f32x4*)(csb + 288 + (2 * jp + e) * 64) + *(const LAS f32x4*)(csb + 864 + (2 * jp + e) * 64);
        }
#pragma unroll
        for (int i = 0; i < 4; ++i) {
            unsigned o[4];
#pragma unroll
            for (int e = 0; e < 2; ++e) {
                const f32x4 g = acc[2 * jp + e][i], u = acc[4 + 2 * jp + e][i];
                o[2 * e] = geglu2(LO2(g), LO2(u), LO2(cg[e]), LO2(cu[e]), tcs[i]);
                o[2 * e + 1] = geglu2(HI2(g), HI2(u), HI2(cg[e]), HI2(cu[e]), tcs[i]);
            }
            ob[i] = (u32x4){o[0], o[1], o[2], o[3]};
            st16_wt(rhh, (jp == 1 && wid == 7) ? 0x80000000u : hb + (unsigned)(((2 + jp * 8 + lct) * 2 + wc) * 8192 + (i ^ (2 * wc)) * 1024 + lane * 16), ob[i]);
            if (jp == 1 && i == FLAGPOS) {
                SCHED();
                WAIT_VI(FLAGPOS + 1);
                __builtin_amdgcn_raw_buffer_store_b32(1u, rfb, lane == 0 ? fbo : 0x80000000u, 0, 16);
                SCHED();
            }
        }
        SCHED();
        if (jp == 0) __builtin_amdgcn_s_setprio(0);
    }
    if (wid == 7) {
        for (int spin = 0; spin < (1 << 20); ++spin) {
            const unsigned f = __builtin_amdgcn_raw_buffer_load_b32(rfb, (unsigned)((mt2 * 4 + ks2) * 256 + (lane & 31) * 4), 0, 16);
            if (__all(f == 1u)) break;
            __builtin_amdgcn_s_sleep(4);
        }
#pragma unroll
        for (int i = 0; i < 4; ++i) st16_wt(rhh, hb + (unsigned)(((10 + lct) * 2 + wc) * 8192 + (i ^ (2 * wc)) * 1024 + lane * 16), ob[i]);
    }
    BAR();
    if (wid < 4) g2_body<5>(ws, out, lds, lane, wid, mt2, ks2, nt2, w2src, fbo + 128);
    else g2_body<4>(ws, out, lds, lane, wid, mt2, ks2, nt2, w2src, fbo + 128);
}

extern "C" __global__ void k9_end() {}

__global__ __launch_bounds__(512) void k_pre(int mode, const u32x4* q, const u32x4* k, const u32x4* v, const u32x4* sk, const u32x4* wg, const u32x4* wu, const u32x4* wd, u32x4* ws, u32x4* out) {
    const unsigned long long t0 = __builtin_amdgcn_s_memrealtime();
    const int g = blockIdx.x * 512 + threadIdx.x; unsigned a = 0;
    if (mode & 1) for (int i = g; i < 147456; i += 131072) a ^= q[i].x ^ k[i].x ^ v[i].x ^ sk[i].x;
    if (mode & 16) for (int i = g; i < 331776; i += 131072) a ^= wg[i].x ^ wu[i].x;
    if (mode & 32) for (int i = g; i < 331776; i += 131072) a ^= wd[i].x;
    if (mode & 2) { for (int i = g; i < 2293760; i += 131072) a ^= ws[i].x; for (int i = g; i < 147456; i += 131072) a ^= out[i].x; }
    if (mode & 8) { const char *c1, *ce; SYM_ADDR(c1, k1_conv); SYM_ADDR(ce, k9_end); const int o = ((blockIdx.x >> 3) * 512 + threadIdx.x) * 64; if (c1 + o < ce) a ^= *(const unsigned*)(c1 + o); }
    asm volatile("" :: "v"(a));
    while (__builtin_amdgcn_s_memrealtime() - t0 < 1500ull) __builtin_amdgcn_s_sleep(4);
}
extern "C" void kernel_launch(void* const* d_in, const int* in_sizes, int n_in, void* d_out, int out_size, void* d_ws,
                              size_t ws_size, hipStream_t stream) {
    const u16* q = (const u16*)d_in[0];
    const u16* k = (const u16*)d_in[1];
    const u16* v = (const u16*)d_in[2];
    const u16* shift_k = (const u16*)d_in[3];
    const u16* ssm_k = (const u16*)d_in[4];
    const u16* ssm_D = (const u16*)d_in[5];
    const u16* wg = (const u16*)d_in[7];
    const u16* wu = (const u16*)d_in[8];
    const u16* wd = (const u16*)d_in[9];
    char* ws = (char*)d_ws;
    (void)hipFuncSetAttribute((const void*)k1_conv, hipFuncAttributeMaxDynamicSharedMemorySize, C1_LDS);
    (void)hipFuncSetAttribute((const void*)k3_gemm1, hipFuncAttributeMaxDynamicSharedMemorySize, K3_LDS);
    k_pre<<<256, 512, 0, stream>>>(48, (const u32x4*)q, (const u32x4*)k, (const u32x4*)v, (const u32x4*)ssm_k, (const u32x4*)wg, (const u32x4*)wu, (const u32x4*)wd, (u32x4*)ws, (u32x4*)d_out);
    k1_conv<<<256, 512, C1_LDS, stream>>>(q, k, v, shift_k, ssm_k, ssm_D, wg, wu, wd, ws);
    k3_gemm1<<<256, 512, K3_LDS, stream>>>(wg, wu, ws, (u16*)d_out);
}
```

```cpp
#include <hip/hip_runtime.h>
#include <hip/hip_bf16.h>

#define LAS __attribute__((address_space(3)))
typedef unsigned short u16;
typedef short s16x4 __attribute__((ext_vector_type(4)));
typedef short s16x8 __attribute__((ext_vector_type(8)));
typedef float f32x2 __attribute__((ext_vector_type(2)));
typedef float f32x4 __attribute__((ext_vector_type(4)));
typedef unsigned u32x2 __attribute__((ext_vector_type(2)));
typedef unsigned u32x4 __attribute__((ext_vector_type(4)));
typedef __bf16 bf16x2_t __attribute__((ext_vector_type(2)));

constexpr int S_ = 2048, D_ = 576, F_ = 4608;

__device__ __forceinline__ unsigned pk_bf16(float lo, float hi) {
    f32x2 v = {lo, hi};
    bf16x2_t b = __builtin_convertvector(v, bf16x2_t);
    return __builtin_bit_cast(unsigned, b);
}
__device__ __forceinline__ float bf_lo(unsigned w) { return __uint_as_float(w << 16); }
__device__ __forceinline__ float bf_hi(unsigned w) { return __uint_as_float(w & 0xffff0000u); }
__device__ __forceinline__ float rbf(float x) { return bf_lo(pk_bf16(x, 0.f)); }
#define WAIT_V(n) asm volatile("s_waitcnt vmcnt(" #n ")" ::: "memory")
#define BAR() do { __builtin_amdgcn_s_barrier(); asm volatile("" ::: "memory"); } while (0)
#define SCHED() __builtin_amdgcn_sched_barrier(0)
#define DIAG_DECL
#define MARK()
#define STAMP(i)
#define DIAG_END(wsbase, kid)
__device__ __forceinline__ void mfma_acc(f32x4& c, s16x8 a, s16x8 b) {
    asm volatile("v_mfma_f32_16x16x32_bf16 %0, %1, %2, %0" : "+v"(c) : "v"(a), "v"(b));
}
__device__ __forceinline__ void mfma_first(f32x4& c, s16x8 a, s16x8 b) {
    asm volatile("v_mfma_f32_16x16x32_bf16 %0, %1, %2, 0" : "=&v"(c) : "v"(a), "v"(b));
}
#define MFMA_DONE() do { asm volatile("s_nop 7\n\ts_nop 7" ::: "memory"); SCHED(); } while (0)
constexpr size_t WS_KV = 0;
constexpr size_t WS_H = WS_KV + (size_t)S_ * D_ * 2;
constexpr size_t WS_ST = WS_H + (size_t)S_ * D_ * 2;
constexpr size_t WS_CS = WS_ST + (size_t)S_ * 8;
constexpr size_t WS_G2 = WS_CS + (size_t)4 * F_ * 4;
constexpr size_t WS_F0 = WS_G2 + (size_t)S_ * 8;
constexpr int FSP = 128;
constexpr size_t WS_EP = WS_F0 + 1280 * FSP;
constexpr size_t WS_HH = WS_EP + 1024;
constexpr size_t WS_X = WS_HH + (size_t)S_ * F_ * 2;
constexpr unsigned X_BYTES = 64 * 4 * 3 * 9216;
constexpr size_t WS_FB = WS_X + X_BYTES;
constexpr size_t WS_WT = WS_FB + 16384;

template <int N> struct IC { static constexpr int v = N; };
typedef __amdgpu_buffer_rsrc_t rsrc_t;
__device__ __forceinline__ rsrc_t mk_rsrc(const void* p, unsigned bytes) { return __builtin_amdgcn_make_buffer_rsrc((void*)p, 0, bytes, 0x00020000); }
__device__ __forceinline__ void st16_wt(rsrc_t r, unsigned off, u32x4 v) { __builtin_amdgcn_raw_buffer_store_b128(v, r, off, 0, 16); }
__device__ __forceinline__ void st8_wt(rsrc_t r, unsigned off, u32x2 v) { __builtin_amdgcn_raw_buffer_store_b64(v, r, off, 0, 16); }
template <int CTRL> __device__ __forceinline__ float dpp_add(float x) {
    return x + __uint_as_float(__builtin_amdgcn_update_dpp(0, (int)__float_as_uint(x), CTRL, 0xf, 0xf, true));
}
__device__ __forceinline__ float x16_add(float x) {
    auto r = __builtin_amdgcn_permlane16_swap(__float_as_uint(x), __float_as_uint(x), false, false);
    return __uint_as_float(r[0]) + __uint_as_float(r[1]);
}
__device__ __forceinline__ float x32_add(float x) {
    auto r = __builtin_amdgcn_permlane32_swap(__float_as_uint(x), __float_as_uint(x), false, false);
    return __uint_as_float(r[0]) + __uint_as_float(r[1]);
}
__device__ __forceinline__ float sum16(float x) {
    x = dpp_add<0xB1>(x); x = dpp_add<0x4E>(x); x = dpp_add<0x141>(x);
    return dpp_add<0x140>(x);
}
__device__ __forceinline__ float ln_sd(float var) { return rbf(__builtin_amdgcn_sqrtf(rbf(rbf(var) + 1e-5f))); }

template <int OFF>
__device__ __forceinline__ void dma16(unsigned voff, const void* sbase, unsigned m0v) {
    asm volatile("s_mov_b32 m0, %2\n\ts_nop 0\n\tglobal_load_lds_dwordx4 %0, %1 offset:%3"
                 :: "v"(voff), "s"(sbase), "s"(m0v), "i"(OFF) : "memory");
}
template <int OFF>
__device__ __forceinline__ void dma16_sc1(unsigned voff, const void* sbase, unsigned m0v) {
    asm volatile("s_mov_b32 m0, %2\n\ts_nop 0\n\tglobal_load_lds_dwordx4 %0, %1 offset:%3 sc1"
                 :: "v"(voff), "s"(sbase), "s"(m0v), "i"(OFF) : "memory");
}
template <int OFF>
__device__ __forceinline__ void dma16_q(unsigned voff, const void* sbase, unsigned m0v) {
    asm volatile("s_mov_b32 m0, %2\n\ts_mov_b64 exec, 0xffff\n\tglobal_load_lds_dwordx4 %0, %1 offset:%3\n\ts_mov_b64 exec, -1"
                 :: "v"(voff), "s"(sbase), "s"(m0v), "i"(OFF) : "memory");
}
__device__ __forceinline__ void dma_m36(unsigned voff, const void* sbase, unsigned m0v) {
    asm volatile("s_mov_b32 m0, %2\n\ts_mov_b32 exec_hi, 15\n\tglobal_load_lds_dwordx4 %0, %1\n\ts_mov_b64 exec, -1"
                 :: "v"(voff), "s"(sbase), "s"(m0v) : "memory");
}
__device__ __forceinline__ void dma4(unsigned voff, const void* sbase, unsigned m0v) {
    asm volatile("s_mov_b32 m0, %2\n\ts_nop 0\n\tglobal_load_lds_dword %0, %1" :: "v"(voff), "s"(sbase), "s"(m0v) : "memory");
}
__device__ __forceinline__ void dma4_sc1(unsigned voff, const void* sbase, unsigned m0v) {
    asm volatile("s_mov_b32 m0, %2\n\ts_nop 0\n\tglobal_load_lds_dword %0, %1 sc1" :: "v"(voff), "s"(sbase), "s"(m0v) : "memory");
}
#define SYM_ADDR(out, sym) asm volatile("s_getpc_b64 s[40:41]\n\ts_add_u32 s40, s40, " #sym "@rel32@lo+4\n\ts_addc_u32 s41, s41, " #sym "@rel32@hi+12\n\ts_mov_b64 %0, s[40:41]" \
                                        : "=s"(out) :: "s40", "s41", "scc")
template <int NI>
__device__ __forceinline__ void warm_code(const char* a, const char* e, int l0, int lane, unsigned m0v) {
    const unsigned last = e > a ? (unsigned)(e - a) - 4u : 0u;
#pragma unroll
    for (int i = 0; i < NI; ++i) dma4(min((unsigned)(l0 + i * 64 + lane) * 128u, last), a, m0v);
}
__device__ __forceinline__ s16x8 tr_frag(const LAS char* p) {
    const s16x4 lo = __builtin_amdgcn_ds_read_tr16_b64_v4i16((LAS s16x4*)p);
    const s16x4 hi = __builtin_amdgcn_ds_read_tr16_b64_v4i16((LAS s16x4*)(p + 16 * 288));
    return __builtin_shufflevector(lo, hi, 0, 1, 2, 3, 4, 5, 6, 7);
}
__device__ __forceinline__ void wd_unit_load(const u16* wd, int un, int lane, unsigned m0v) {
    const int uB = un / 3, ut = un % 3, kh = uB & 1, st = (uB >> 1) % 18, kn = uB / 36;
    const bool full = st >= 2;
    const int f0 = (kn >> 2) * 1152 + (full ? ((st - 2) & 7) * 144 + kh * 72 + ((st - 2) >> 3) * 32 : (st * 2 + kh) * 288);
    const char* src = (const char*)wd + (size_t)f0 * (D_ * 2) + (kn & 3) * 288 + ut * 96;
#pragma unroll
    for (int i = 0; i < 3; ++i) {
        const int q = lane + 64 * i, r = q / 6, pos = q % 6, kg = (r & 15) >> 2;
        const int f = full ? r : (kg >> 1) * 144 + (kg & 1) * 72 + 64 + (r & 3) + 4 * (r >> 4);
        dma16<0>((unsigned)(f * (D_ * 2) + pos * 16), src, m0v + i * 1024);
    }
}
__device__ __forceinline__ void wd_unit_store(char* ws, int un, int lane, const LAS char* l) {
    const rsrc_t r = mk_rsrc(ws + WS_WT, F_ * D_ * 2);
#pragma unroll
    for (int t = 0; t < 3; ++t) {
        const LAS char* tp = l + (lane >> 2) * 96 + (lane & 3) * 8 + t * 32;
        const s16x4 lo = __builtin_amdgcn_ds_read_tr16_b64_v4i16((LAS s16x4*)tp), hi = __builtin_amdgcn_ds_read_tr16_b64_v4i16((LAS s16x4*)(tp + 16 * 96));
        st16_wt(r, (unsigned)(un * 3072 + t * 1024 + lane * 16), __builtin_bit_cast(u32x4, __builtin_shufflevector(lo, hi, 0, 1, 2, 3, 4, 5, 6, 7)));
    }
}
constexpr int WU4 = 3;
constexpr int HELPER_DELAY = 48, HELPER_DELAY2 = 24, POLL0 = 16, POLL1 = 1;
constexpr int C1_COPY = 4288;
constexpr int C1_FILT = 4 * C1_COPY;
constexpr int C1_KV = 2304 * 2;
constexpr int C1_KVOFF = 4 * C1_FILT + 64;
constexpr int C1_YOFF = C1_KVOFF + 4 * C1_KV;
constexpr int C1_XOFF = C1_YOFF + 16384;
constexpr int C1_SOFF = C1_XOFF + 16384;
constexpr int C1_TOFF = C1_SOFF + 16384;
constexpr int C1_WOFF = C1_TOFF + 2048;
constexpr int C1_LDS = C1_WOFF + 24576;

extern "C" __global__ __launch_bounds__(512, 2) void k1_conv(const u16* __restrict__ x, const u16* __restrict__ kin, const u16* __restrict__ vin,
                                                  const u16* __restrict__ shift_k, const u16* __restrict__ ssm_k, const u16* __restrict__ ssm_D,
                                                  const u16* __restrict__ wg, const u16* __restrict__ wu, const u16* __restrict__ wd,
                                                  char* __restrict__ ws) {
    extern __shared__ __attribute__((aligned(16))) char smem[];
    const int tid = threadIdx.x, lane = tid & 63;
    const int wid = __builtin_amdgcn_readfirstlane(tid >> 6);
    const int b = blockIdx.x, xcd = b & 7, idx = b >> 3;
    {
        const char *c1, *c3;
        SYM_ADDR(c1, k1_conv); SYM_ADDR(c3, k3_gemm1);
        if (wid >= 4) warm_code<1>(c1, c3, (wid & 3) * 64, lane, (unsigned)(size_t)(LAS char*)smem + C1_TOFF + 256 + (wid & 3) * 256);
    }
    DIAG_DECL;
    asm volatile("" :: "s"(x), "s"(kin), "s"(vin), "s"(shift_k), "s"(ssm_k), "s"(ssm_D), "s"(wg), "s"(wu), "s"(wd), "s"(ws));
    const bool conv = idx < 18;
    const int cg = xcd * 18 + idx, ch0 = cg * 4;
    const unsigned lds0 = (unsigned)(size_t)(LAS char*)smem;
    LAS unsigned* tagp = (LAS unsigned*)(smem + C1_TOFF);
    auto get_tag = [&]() -> unsigned { BAR(); return __builtin_amdgcn_readfirstlane(*(volatile LAS unsigned*)tagp); };

    const int h = (idx - 18) * 8 + xcd, hc_ = h < 108 ? h : 0;
    const int m3 = hc_ % 3, lcA = (hc_ / 3) * 4 + m3, rb0 = m3 * 3;
    constexpr int HU = (1728 - 144 * WU4 + 111) / 112;
    const int hbase = 144 * WU4 + h * HU, hun = hbase + wid * 2, hend = min(hbase + HU, 1728);
    auto cs_load = [&](u32x4 (&v)[12]) {
        const int rs = tid >> 3, c8 = tid & 7;
#pragma unroll
        for (int p = 0; p < 12; ++p) {
            const int rb = rb0 + p, lc = lcA + (rb >= 9), r = (rb >= 9 ? rb - 9 : rb) * 64 + rs;
            v[p] = *(const u32x4*)((lc >= 72 ? wu : wg) + (size_t)r * F_ + (lc % 72) * 64 + c8 * 8);
        }
        SCHED();
#pragma unroll
        for (int u = 0; u < 2; ++u) if (hun + u < hend) wd_unit_load(wd, hun + u, lane, lds0 + 16384 + (wid * 2 + u) * 3072);
        st16_wt(mk_rsrc(ws + WS_FB, 16384), (unsigned)(h * 8192 + tid * 16), (u32x4){0u, 0u, 0u, 0u});
        SCHED();
    };
    auto cs_finish = [&](u32x4 (&v)[12]) {
        float g[4][8];
#pragma unroll
        for (int q = 0; q < 4; ++q) {
#pragma unroll
            for (int e = 0; e < 4; ++e) {
                g[q][2 * e] = (bf_lo(v[3 * q][e]) + bf_lo(v[3 * q + 1][e])) + bf_lo(v[3 * q + 2][e]);
                g[q][2 * e + 1] = (bf_hi(v[3 * q][e]) + bf_hi(v[3 * q + 1][e])) + bf_hi(v[3 * q + 2][e]);
            }
        }
        LAS float* red = (LAS float*)smem;
#pragma unroll
        for (int l = 0; l < 2; ++l) {
            float a[8];
#pragma unroll
            for (int e = 0; e < 8; ++e) {
                a[e] = l == 0 ? g[0][e] + (m3 <= 1 ? g[1][e] : 0.f) + (m3 == 0 ? g[2][e] : 0.f) : g[3][e] + (m3 >= 1 ? g[2][e] : 0.f) + (m3 == 2 ? g[1][e] : 0.f);
                a[e] = x32_add(x16_add(dpp_add<0x128>(a[e])));
            }
            if (lane < 8) {
                *(LAS f32x4*)(red + (wid * 2 + l) * 64 + lane * 8) = (f32x4){a[0], a[1], a[2], a[3]};
                *(LAS f32x4*)(red + (wid * 2 + l) * 64 + lane * 8 + 4) = (f32x4){a[4], a[5], a[6], a[7]};
            }
        }
        WAIT_V(1);
#pragma unroll
        for (int u = 0; u < 2; ++u) if (hun + u < hend) wd_unit_store(ws, hun + u, lane, (const LAS char*)smem + 16384 + (wid * 2 + u) * 3072);
        asm volatile("s_waitcnt lgkmcnt(0)" ::: "memory"); BAR();
        if (tid < 192 && h < 108) {
            float s = 0.f;
            if (tid < 128) {
#pragma unroll
                for (int w = 0; w < 8; ++w) s += red[w * 128 + tid];
            }
            const int part = tid >> 6;
            const int lc = part == 0 ? lcA : (part == 1 ? lcA + 1 : (m3 == 0 ? lcA : lcA + 1));
            const int slot = part == 0 ? (m3 != 0) : (part == 1 ? 0 : 1);
            if (part < 2 || m3 != 1)
                __builtin_amdgcn_raw_buffer_store_b32(__float_as_uint(s), mk_rsrc(ws + WS_CS, 4 * F_ * 4), (unsigned)((slot * 2 * F_ + lc * 64 + (tid & 63)) * 4), 0, 16);
        }
    };
    auto wait_kv = [&](unsigned tag) {
        const rsrc_t rf = mk_rsrc(ws + WS_F0 + ((idx >> 2) * 8 + xcd) * 32 * FSP, 32 * FSP);
        __builtin_amdgcn_s_sleep(POLL0);
        for (int spin = 0; spin < (1 << 20); ++spin) {
            const unsigned f = __builtin_amdgcn_raw_buffer_load_b32(rf, (lane & 31) * FSP, 0, 16);
            if (__all(f == tag)) break;
            __builtin_amdgcn_s_sleep(POLL1);
        }
    };

    auto x_rows = [&]() {
        u32x2 xr[8];
#pragma unroll
        for (int i = 0; i < 8; ++i) xr[i] = *(const u32x2*)(x + (size_t)((tid - 256) + (wid - 4) * 448 + i * 64) * D_ + ch0);
#pragma unroll
        for (int i = 0; i < 8; ++i) *(LAS u32x2*)(smem + C1_XOFF + ((tid - 256) + (wid - 4) * 448 + i * 64) * 8) = xr[i];
        SCHED();
    };
    unsigned tag;
    unsigned dvv = 0;
    auto upper = [&](auto is_stat, auto is_conv) {
        constexpr bool STAT = decltype(is_stat)::v, CONV = decltype(is_conv)::v;
        const int srow = b * 8 + (wid - 5) * 4 + (lane >> 4), t3 = tid - 320;
        unsigned xw[18], epv = 0;
        if constexpr (STAT) {
            const unsigned* xr = (const unsigned*)(x + (size_t)srow * D_) + (lane & 15);
#pragma unroll
            for (int j = 0; j < 18; ++j) xw[j] = xr[16 * j];
        } else {
            epv = __hip_atomic_load((const unsigned*)(ws + WS_EP), __ATOMIC_RELAXED, __HIP_MEMORY_SCOPE_AGENT);
        }
        SCHED();
        if constexpr (STAT) {
            const char *c3, *c4;
            SYM_ADDR(c3, k3_gemm1); SYM_ADDR(c4, k9_end);
            warm_code<1>(c3, c4, ((idx & 3) * 2 + wid - 5) * 64, lane, lds0 + C1_TOFF + 256 + (wid - 5) * 256);
        }
        SCHED();
        u32x4 v[12], fa[6]; u32x2 fb[6];
        if constexpr (CONV) {
#pragma unroll
            for (int i = 0; i < 6; ++i) {
                const int task = min(t3 + 192 * i, 1023), u = task & 255;
                const u16* src = ssm_k + (size_t)(ch0 + (task >> 8)) * S_ + u * 8;
                fa[i] = *(const u32x4*)src;
                fb[i] = *(const u32x2*)(src + (u < 255 ? 8 : 0));
            }
            SCHED();
        }
        if constexpr (STAT) {
            f32x2 xf[18], s = {0.f, 0.f};
#pragma unroll
            for (int j = 0; j < 18; ++j) { xf[j] = (f32x2){bf_lo(xw[j]), bf_hi(xw[j])}; s += xf[j]; }
            const float mean = sum16(s.x + s.y) * (1.f / D_);
            f32x2 q = {0.f, 0.f};
#pragma unroll
            for (int j = 0; j < 18; ++j) { const f32x2 d = xf[j] - mean; q = __builtin_elementwise_fma(d, d, q); }
            const unsigned sgr = pk_bf16(mean, ln_sd(sum16(q.x + q.y) * (1.f / D_)));
            tag = get_tag();
            const unsigned so = (lane & 15) == 0 ? (unsigned)srow * 8 : 0x80000000u;
            st8_wt(mk_rsrc(ws + WS_ST, S_ * 8), so, (u32x2){sgr, tag});
            st8_wt(mk_rsrc(ws + WS_G2, S_ * 8), so, (u32x2){0u, 0u});
        } else {
            *tagp = epv + 1u;
            asm volatile("s_waitcnt lgkmcnt(0)" ::: "memory");
            tag = get_tag();
        }
        SCHED();
        if constexpr (!CONV) {
            __builtin_amdgcn_s_sleep(HELPER_DELAY);
            cs_load(v);
            if (!STAT && h == 111) {
                const rsrc_t rf = mk_rsrc(ws + WS_F0, 256 * FSP);
                for (int spin = 0; spin < (1 << 20); ++spin) {
                    bool ok = true;
#pragma unroll
                    for (int i = 0; i < 4; ++i) ok &= __builtin_amdgcn_raw_buffer_load_b32(rf, (i * 64 + lane) * FSP, 0, 16) == tag;
                    if (__all(ok)) break;
                    __builtin_amdgcn_s_sleep(8);
                }
                if (lane == 0) __hip_atomic_store((unsigned*)(ws + WS_EP), tag, __ATOMIC_RELAXED, __HIP_MEMORY_SCOPE_AGENT);
            }
            cs_finish(v);
            return;
        }
        if (t3 < 80) {
            const int cpy = t3 / 5, e = t3 % 5, q = e < 3 ? e : 257 + e;
            *(LAS u32x4*)(smem + cpy * C1_COPY + q * 16) = (u32x4){0u, 0u, 0u, 0u};
        } else if (t3 < 84) {
            *(LAS u32x4*)(smem + C1_KVOFF - 64 + (t3 - 80) * 16) = (u32x4){0u, 0u, 0u, 0u};
        }
        if (t3 < 128) *(LAS u32x4*)(smem + C1_KVOFF + (t3 >> 5) * C1_KV + 4096 + (t3 & 31) * 16) = (u32x4){0u, 0u, 0u, 0u};
#pragma unroll
        for (int i = 0; i < 6; ++i) {
            const int task = min(t3 + 192 * i, 1023), u = task & 255;
            const unsigned d[6] = {fa[i].x, fa[i].y, fa[i].z, fa[i].w, u < 255 ? fb[i].x : 0u, u < 255 ? fb[i].y : 0u};
            unsigned o[5];
#pragma unroll
            for (int e = 0; e < 5; ++e) o[e] = __builtin_amdgcn_alignbit(d[e + 1], d[e], 16);
            LAS char* base = (LAS char*)smem + (task >> 8) * C1_FILT + (u + 4) * 16;
            *(LAS u32x4*)(base) = (u32x4){o[0], o[1], o[2], o[3]};
            *(LAS u32x4*)(base + C1_COPY) = (u32x4){d[1], d[2], d[3], d[4]};
            *(LAS u32x4*)(base + 2 * C1_COPY) = (u32x4){o[1], o[2], o[3], o[4]};
            *(LAS u32x4*)(base + 3 * C1_COPY) = (u32x4){d[2], d[3], d[4], d[5]};
            if (u == 0) {
                *(LAS u32x4*)(base - 16) = (u32x4){0u, 0u, 0u, d[0] << 16};
                *(LAS u32x4*)(base - 16 + C1_COPY) = (u32x4){0u, 0u, 0u, d[0]};
                *(LAS u32x4*)(base - 16 + 2 * C1_COPY) = (u32x4){0u, 0u, d[0] << 16, o[0]};
                *(LAS u32x4*)(base - 16 + 3 * C1_COPY) = (u32x4){0u, 0u, d[0], d[1]};
            }
        }
        asm volatile("s_waitcnt lgkmcnt(0)" ::: "memory"); BAR();
        x_rows();
    };
    if (wid >= 5) {
        if (!conv) {
            if (wid < 7) upper(IC<1>{}, IC<0>{}); else upper(IC<0>{}, IC<0>{});
            DIAG_END(ws, 0);
            return;
        }
        if (wid < 7) upper(IC<1>{}, IC<1>{}); else upper(IC<0>{}, IC<1>{});
    } else {
        {
            unsigned kw[11], vw[8];
            const int t0 = idx * 64 + (lane >> 3) * 8, dc = wid * 8 + (lane & 7), kch = xcd * 72 + (dc < 36 ? dc : 35) * 2;
            const rsrc_t rk = mk_rsrc(kin, S_ * D_ * 2), rv = mk_rsrc(vin, S_ * D_ * 2);
            const int off = t0 * (D_ * 2) + kch * 2;
#pragma unroll
            for (int j = 0; j < 11; ++j) kw[j] = __builtin_amdgcn_raw_buffer_load_b32(rk, (unsigned)(off + (j - 3) * (D_ * 2)), 0, 0);
#pragma unroll
            for (int j = 0; j < 8; ++j) vw[j] = __builtin_amdgcn_raw_buffer_load_b32(rv, (unsigned)(off + j * (D_ * 2)), 0, 0);
            const u32x4 sw = *(const u32x4*)(shift_k + kch * 4);
            dvv = ssm_D[(conv ? ch0 : 0) + (wid & 3)];
            SCHED();
            STAMP(1);
            const f32x2 w0 = {bf_lo(sw.x), bf_lo(sw.z)}, w1 = {bf_hi(sw.x), bf_hi(sw.z)}, w2 = {bf_lo(sw.y), bf_lo(sw.w)}, w3 = {bf_hi(sw.y), bf_hi(sw.w)};
            f32x2 kf[11], o[8];
#pragma unroll
            for (int j = 0; j < 11; ++j) kf[j] = (f32x2){bf_lo(kw[j]), bf_hi(kw[j])};
#pragma unroll
            for (int j = 0; j < 8; ++j) {
                f32x2 s = kf[j] * w3;
                s = __builtin_elementwise_fma(kf[j + 1], w2, s);
                s = __builtin_elementwise_fma(kf[j + 2], w1, s);
                s = __builtin_elementwise_fma(kf[j + 3], w0, s);
                o[j] = s * (f32x2){bf_lo(vw[j]), bf_hi(vw[j])};
            }
            const rsrc_t ro = mk_rsrc(ws + WS_KV, S_ * D_ * 2);
            const unsigned oo = dc < 36 ? (unsigned)(kch * (S_ * 2) + (2040 - t0) * 2) : 0x80000000u;
            st16_wt(ro, oo, (u32x4){pk_bf16(o[7].x, o[6].x), pk_bf16(o[5].x, o[4].x), pk_bf16(o[3].x, o[2].x), pk_bf16(o[1].x, o[0].x)});
            st16_wt(ro, oo + S_ * 2, (u32x4){pk_bf16(o[7].y, o[6].y), pk_bf16(o[5].y, o[4].y), pk_bf16(o[3].y, o[2].y), pk_bf16(o[1].y, o[0].y)});
        }
        tag = get_tag();
        WAIT_V(0);
        if (lane == 0) __hip_atomic_store((unsigned*)(ws + WS_F0 + ((wid * 8 + xcd) * 32 + idx) * FSP), tag, __ATOMIC_RELAXED, __HIP_MEMORY_SCOPE_AGENT);
        MARK();
        STAMP(2);
        if (!conv) {
            u32x4 v[12];
            __builtin_amdgcn_s_sleep(HELPER_DELAY2);
            cs_load(v);
            cs_finish(v);
            DIAG_END(ws, 0);
            return;
        }
        if (wid == 4) {
            x_rows();
            BAR();
#pragma unroll 1
            for (int u = 0; u < WU4; ++u) wd_unit_load(wd, cg * WU4 + u, lane, lds0 + C1_WOFF + u * 3072);
            __builtin_amdgcn_s_sleep(24);
            const char* gs = ws + WS_ST;
#pragma unroll
            for (int i = 0; i < 4; ++i) {
                dma16_sc1<0>(lane * 16, gs + i * 4096, lds0 + C1_SOFF + i * 4096); dma16_sc1<1024>(lane * 16, gs + i * 4096, lds0 + C1_SOFF + i * 4096);
                dma16_sc1<2048>(lane * 16, gs + i * 4096, lds0 + C1_SOFF + i * 4096); dma16_sc1<3072>(lane * 16, gs + i * 4096, lds0 + C1_SOFF + i * 4096);
            }
            WAIT_V(0);
#pragma unroll 1
            for (int u = 0; u < WU4; ++u) wd_unit_store(ws, cg * WU4 + u, lane, (const LAS char*)smem + C1_WOFF + u * 3072);
        } else {
        {
            wait_kv(tag);
            STAMP(7);
            const char* kvs = ws + WS_KV + (size_t)(ch0 + wid) * (S_ * 2);
            const unsigned m = lds0 + C1_KVOFF + wid * C1_KV;
            dma16_sc1<0>(lane * 16, kvs, m); dma16_sc1<1024>(lane * 16, kvs, m); dma16_sc1<2048>(lane * 16, kvs, m); dma16_sc1<3072>(lane * 16, kvs, m);
            WAIT_V(0);
            STAMP(9);
            BAR();
            STAMP(4);
        }
        const int row = lane & 15, kg = lane >> 4;
        const LAS char* abase = (const LAS char*)smem + wid * C1_FILT + (row & 3) * C1_COPY + (24 + 4 * (row >> 2) - 8 * kg) * 2;
        const LAS char* bbase = (const LAS char*)smem + C1_KVOFF + wid * C1_KV + (2040 - 16 * row - 8 * kg) * 2 - 512 * 7 - 448;
        auto rd_ah = [&](int r, int L, int hf) -> s16x4 { return *(const volatile LAS s16x4*)(abase + 512 * L - 64 * r + 8 * hf); };
        auto rd_b = [&](int r, int m) -> s16x8 { return *(const LAS s16x8*)(bbase + 448 - 64 * r + 512 * (7 - m)); };
        auto cat = [&](s16x4 lo, s16x4 hi) -> s16x8 { return __builtin_shufflevector(lo, hi, 0, 1, 2, 3, 4, 5, 6, 7); };
        f32x4 acc[8];
        s16x4 al[2][8], ah[2][8];
        s16x8 bf[2][8], bd[8];
        auto rd_k = [&](int r, int k) {
            if (k < 2) { if (k == 0) al[r & 1][0] = rd_ah(r, 1, 0); else ah[r & 1][0] = rd_ah(r, 1, 1); }
            else if (k < 10) bf[r & 1][9 - k] = rd_b(r, 8 - k);
            else { const int L = (k - 10) / 2 + 2; if (k & 1) ah[r & 1][L - 1] = rd_ah(r, L, 1); else al[r & 1][L - 1] = rd_ah(r, L, 0); }
        };
        const s16x8 a0 = cat(rd_ah(0, 0, 0), rd_ah(0, 0, 1));
#pragma unroll
        for (int e = 0; e < 8; ++e) bd[e] = rd_b(0, e);
        s16x8 dfr;
        {
            const int j = 8 * kg + 7 - row;
            u32x4 w;
#pragma unroll
            for (int e = 0; e < 4; ++e) w[e] = j == 2 * e ? dvv : (j == 2 * e + 1 ? dvv << 16 : 0u);
            dfr = __builtin_bit_cast(s16x8, w);
            asm volatile("s_nop 1" : "+v"(dfr));
        }
        SCHED();
#pragma unroll
        for (int n = 0; n < 16; ++n) {
            if (n < 8) mfma_first(acc[n], a0, bd[n]); else mfma_acc(acc[n - 8], dfr, bd[n - 8]);
            SCHED();
            if (n == 0) { rd_k(0, 0); rd_k(0, 1); }
            else if (n == 1) rd_k(0, 9);
            else rd_k(0, n + 8);
            SCHED();
        }
#pragma unroll
        for (int r = 0; r < 8; ++r) {
            int k = 0;
#pragma unroll
            for (int L = 1; L <= 8; ++L) {
#pragma unroll
                for (int n = 7; n >= L - 1; --n) {
                    const int e = n - L + 1;
                    mfma_acc(acc[n], cat(al[r & 1][L - 1], ah[r & 1][L - 1]), (r == 0 && e > 0) ? bd[e - 1] : bf[r & 1][e]);
                    SCHED();
                    if (r < 7 && k < 24) rd_k(r + 1, k);
                    ++k;
                    SCHED();
                }
            }
        }
        MFMA_DONE();
#pragma unroll
        for (int n = 0; n < 8; ++n)
            *(LAS u32x2*)(smem + C1_YOFF + wid * 4096 + (256 * n + 16 * row + 4 * kg) * 2) = (u32x2){pk_bf16(acc[n][0], acc[n][1]), pk_bf16(acc[n][2], acc[n][3])};
        }
    }
    STAMP(5);
    asm volatile("s_waitcnt lgkmcnt(0)" ::: "memory"); BAR();
    STAMP(6);
    {
        u32x2 yc[4];
#pragma unroll
        for (int c = 0; c < 4; ++c) yc[c] = *(const LAS u32x2*)(smem + C1_YOFF + c * 4096 + tid * 8);
        const u32x4 xa = *(const LAS u32x4*)(smem + C1_XOFF + tid * 32), xb = *(const LAS u32x4*)(smem + C1_XOFF + tid * 32 + 16);
        const u32x2 xr[4] = {{xa.x, xa.y}, {xa.z, xa.w}, {xb.x, xb.y}, {xb.z, xb.w}};
        u32x4 s01 = *(const LAS u32x4*)(smem + C1_SOFF + tid * 32), s23 = *(const LAS u32x4*)(smem + C1_SOFF + tid * 32 + 16);
        if (__builtin_expect(!__all(s01.y == tag && s01.w == tag && s23.y == tag && s23.w == tag), 0)) {
            const rsrc_t rs = mk_rsrc(ws + WS_ST, S_ * 8);
            for (int spin = 0; spin < (1 << 20); ++spin) {
                s01 = __builtin_amdgcn_raw_buffer_load_b128(rs, tid * 32, 0, 16);
                s23 = __builtin_amdgcn_raw_buffer_load_b128(rs, tid * 32 + 16, 0, 16);
                if (__all(s01.y == tag && s01.w == tag && s23.y == tag && s23.w == tag)) break;
                __builtin_amdgcn_s_sleep(2);
            }
        }
        const unsigned sv[4] = {s01.x, s01.z, s23.x, s23.z};
        float mm[4], rr[4];
#pragma unroll
        for (int j = 0; j < 4; ++j) { mm[j] = bf_lo(sv[j]); rr[j] = __builtin_amdgcn_rcpf(bf_hi(sv[j])); }
        unsigned hw[8];
#pragma unroll
        for (int j = 0; j < 4; ++j) {
#pragma unroll
            for (int e = 0; e < 2; ++e) {
                const unsigned xw = e ? xr[j].y : xr[j].x;
                const unsigned ya = (j >> 1) ? yc[2 * e].y : yc[2 * e].x, yb = (j >> 1) ? yc[2 * e + 1].y : yc[2 * e + 1].x;
                const float y0 = (j & 1) ? bf_hi(ya) : bf_lo(ya), y1 = (j & 1) ? bf_hi(yb) : bf_lo(yb);
                const float x0 = bf_lo(xw), x1 = bf_hi(xw);
                hw[2 * j + e] = pk_bf16(__builtin_fmaf((x0 - mm[j]) * rr[j], y0, x0), __builtin_fmaf((x1 - mm[j]) * rr[j], y1, x1));
            }
        }
        const rsrc_t rh = mk_rsrc(ws + WS_H, S_ * D_ * 2);
        const unsigned ho = (unsigned)(((tid >> 6) * 144 + cg) * 2048 + (tid & 63) * 32);
        st16_wt(rh, ho, (u32x4){hw[0], hw[1], hw[2], hw[3]});
        st16_wt(rh, ho + 16, (u32x4){hw[4], hw[5], hw[6], hw[7]});
    }
    STAMP(8);
    DIAG_END(ws, 0);
}

constexpr int ST_BYTES = 16384 + 2 * 9216;
constexpr int NBUF = 4;
constexpr int G_LDS = NBUF * ST_BYTES;

struct TokC { float nm, c0, c1, q; };
__device__ __forceinline__ unsigned geglu2(f32x2 ag, f32x2 au, f32x2 csg, f32x2 csu, const TokC& t) {
    const f32x2 nm = {t.nm, t.nm}, c0 = {t.c0, t.c0}, c1 = {t.c1, t.c1}, q = {t.q, t.q};
    const f32x2 g = __builtin_elementwise_fma(nm, csg, ag), u = __builtin_elementwise_fma(nm, csu, au);
    const f32x2 z = g * __builtin_elementwise_fma(g * g, c1, c0);
    const f32x2 e = {__builtin_amdgcn_exp2f(z.x), __builtin_amdgcn_exp2f(z.y)};
    const f32x2 d = __builtin_elementwise_fma(e, q, q);
    const f32x2 r = (g * u) * (f32x2){__builtin_amdgcn_rcpf(d.x), __builtin_amdgcn_rcpf(d.y)};
    return pk_bf16(r.x, r.y);
}
#define LO2(v) __builtin_shufflevector(v, v, 0, 1)
#define HI2(v) __builtin_shufflevector(v, v, 2, 3)
#define WAIT_VI(N) asm volatile("s_waitcnt vmcnt(%0)" :: "i"(N) : "memory")

constexpr int G2_W0 = 71680, G2_FL = 69632;
__device__ __forceinline__ void g2_wpiece(int P, int s, const char* wsrc, unsigned aoff, unsigned mw0) {
    const unsigned m = mw0 + (s % 5) * 18432;
    if (P == 0) dma16<0>(aoff, wsrc + (size_t)s * 18432, m);
    if (P == 1) dma16<1024>(aoff, wsrc + (size_t)s * 18432, m);
    if (P == 2) dma16_q<2048>(aoff, wsrc + (size_t)s * 18432, m);
}
constexpr int g2_n(int S) { return (S + 3 < 18 ? 2 : 0) + (S + 4 < 18 ? 3 : 0) + (S == 4 ? 1 : 0); }
constexpr int g2_vm(int S) {
    return S == 0 ? 3 : (S == 1 ? 1 + g2_n(0) : g2_n(S - 2) - (S + 1 < 18 ? 2 : 0) + g2_n(S - 1));
}
#define DIAG_ARG
#define DIAG_PASS
template <int NT>
__device__ __forceinline__ void g2_body(char* __restrict__ ws, u16* __restrict__ out, LAS char* lds, int lane, int wid, int mt, int ks, int nt,
                                        const char* wsrc, unsigned flag2 DIAG_ARG) {
    constexpr int dh = NT == 4;
    const unsigned lds0 = (unsigned)(size_t)lds;
    const int wr = wid & 3;
    const char* asrc = ws + WS_HH + ((size_t)(mt * 4 + ks) * 36) * 8192 + wid * 2048;
    const unsigned aoff = lane * 16;
    const unsigned ma0 = lds0 + wid * 2048, mw0 = lds0 + G2_W0 + wid * 2304;
    auto hpiece = [&](int P, int s) {
        if (P == 0) dma16_sc1<0>(aoff, asrc + (size_t)s * 16384, ma0 + (s & 3) * 16384);
        if (P == 1) dma16_sc1<1024>(aoff, asrc + (size_t)s * 16384, ma0 + (s & 3) * 16384);
    };
    hpiece(0, 0); hpiece(1, 0); hpiece(0, 1); hpiece(1, 1); hpiece(0, 2); hpiece(1, 2);

    const int g4 = lane >> 4;
    const int toff = wr * 2048 + lane * 16;
    const int foff = G2_W0 + dh * 5120 + lane * 16;
    auto rdf = [&](const LAS char* p) -> s16x8 { return *(const LAS s16x8*)p; };
    const rsrc_t rfb = mk_rsrc(ws + WS_FB, 16384);

    f32x4 acc[NT][2];
    s16x8 wf[2 * NT], tf[2][4];
    WAIT_V(4);
    __builtin_amdgcn_raw_buffer_store_b32(1u, rfb, lane == 0 ? flag2 : 0x80000000u, 0, 16);
    BAR();
    STAMP(8);
#pragma unroll
    for (int e = 0; e < 4; ++e) tf[0][e] = rdf(lds + toff + (e >> 1) * 8192 + (e & 1) * 1024);
#pragma unroll
    for (int e = 0; e < 2 * NT; ++e) wf[e] = rdf(lds + foff + (e / NT) * 9216 + (e % NT) * 1024);
    SCHED();

    auto iter = [&](auto sc, s16x8 (&tc)[4], s16x8 (&tn)[4]) {
        constexpr int S = decltype(sc)::v;
        if constexpr (S < 17) {
            WAIT_VI(g2_vm(S));
            BAR();
            if constexpr (S == 7) {
                unsigned fb2 = *(const volatile LAS unsigned*)(lds + G2_FL + wid * 256 + lane * 4);
                if (__builtin_expect(!__all(fb2 == 1u), 0)) {
                    for (int spin = 0; spin < (1 << 20); ++spin) {
                        fb2 = __builtin_amdgcn_raw_buffer_load_b32(rfb, (unsigned)(((mt * 4 + ks) * 2 + 1) * 128 + (lane & 31) * 4), 0, 16);
                        if (__all(fb2 == 1u)) break;
                        __builtin_amdgcn_s_sleep(2);
                    }
                }
            }
            SCHED();
        }
        const LAS char* tb = lds + ((S + 1) & 3) * 16384;
        const LAS char* wb = lds + ((S + 1) % 5) * 18432;
#pragma unroll
        for (int e = 0; e < 2 * NT; ++e) {
#pragma unroll
            for (int i = 0; i < 2; ++i) {
                if (S == 0 && e < NT) mfma_first(acc[e % NT][i], wf[e], tc[(e / NT) * 2 + i]);
                else mfma_acc(acc[e % NT][i], wf[e], tc[(e / NT) * 2 + i]);
            }
            SCHED();
            if constexpr (S < 17) {
                wf[e] = rdf(wb + foff + (e / NT) * 9216 + (e % NT) * 1024);
                if (e >= 2 && e < 6) tn[e - 2] = rdf(tb + toff + ((e - 2) >> 1) * 8192 + ((e - 2) & 1) * 1024);
            }
            if constexpr (S + 3 < 18) { if (e < 2) hpiece(e, S + 3); }
            if constexpr (S + 4 < 18) {
                if (e == 3) g2_wpiece(0, S + 4, wsrc, aoff, mw0);
                if (e == 5) g2_wpiece(1, S + 4, wsrc, aoff, mw0);
                if (e == 6) g2_wpiece(2, S + 4, wsrc, aoff, mw0);
            }
            if constexpr (S == 4) {
                if (e == 7) dma4_sc1((unsigned)(lane & 31) * 4, ws + WS_FB + ((mt * 4 + ks) * 2 + 1) * 128, lds0 + G2_FL + wid * 256);
            }
            SCHED();
        }
    };
#define G2_IT2(S) iter(IC<S>{}, tf[0], tf[1]); iter(IC<S + 1>{}, tf[1], tf[0])
    G2_IT2(0); G2_IT2(2); G2_IT2(4); G2_IT2(6); G2_IT2(8); G2_IT2(10); G2_IT2(12);
    STAMP(9);
    G2_IT2(14);
    iter(IC<16>{}, tf[0], tf[1]);
    const int tok0 = mt * 128 + wr * 32 + (lane & 15);
    u32x2 hv[2][5];
    if (wr == ks) {
#pragma unroll
        for (int i = 0; i < 2; ++i) {
            const int t = tok0 + i * 16;
            const char* hp = ws + WS_H + ((size_t)((t >> 8) * 144 + nt * 36 + dh * 20) * 256 + (t & 255)) * 8;
#pragma unroll
            for (int e = 0; e < 4; ++e) hv[i][e] = *(const u32x2*)(hp + ((e >> 1) * 8 + (g4 & 1) * 4 + (g4 >> 1) * 2 + (e & 1)) * 2048);
            if (NT == 5) hv[i][4] = *(const u32x2*)(hp + (16 + g4) * 2048);
        }
    }
    SCHED();
    iter(IC<17>{}, tf[1], tf[0]);
    MFMA_DONE();
    STAMP(10);

    const rsrc_t rx = mk_rsrc(ws + WS_X, X_BYTES);
    const unsigned xt = (unsigned)((mt * 4 + nt) * 4 * 27648 + dh * 4096);
    auto inb = [&](int s) -> unsigned { return xt + (unsigned)(((ks + 1 + s) & 3) * 27648 + (2 - s) * 9216); };
    if (wr != ks) {
        const unsigned o = xt + (unsigned)(ks * 27648 + ((wr - ks - 1) & 3) * 9216);
#pragma unroll
        for (int i = 0; i < 2; ++i) {
            unsigned w[2 * NT];
#pragma unroll
            for (int j = 0; j < NT; ++j) { w[2 * j] = pk_bf16(acc[j][i][0], acc[j][i][1]); w[2 * j + 1] = pk_bf16(acc[j][i][2], acc[j][i][3]); }
#pragma unroll
            for (int e = 0; e < 2 * NT; ++e) w[e] = (w[e] == ~0u) ? 0x7fc07fc0u : w[e];
            if constexpr (NT == 5) st8_wt(rx, o + 8192 + i * 512 + lane * 8, (u32x2){w[8], w[9]});
            st16_wt(rx, o + i * 2048 + lane * 16, (u32x4){w[0], w[1], w[2], w[3]});
            st16_wt(rx, o + i * 2048 + 1024 + lane * 16, (u32x4){w[4], w[5], w[6], w[7]});
        }
    } else {
        u32x4 pa[3][4]; u32x2 pb[3][2];
        __builtin_amdgcn_s_sleep(16);
        for (int spin = 0; spin < (1 << 18); ++spin) {
            const u32x4 t = __builtin_amdgcn_raw_buffer_load_b128(rx, lane < 3 ? inb(lane) + 4080 : 0x80000000u, 0, 16);
            if (__all(t.w != ~0u)) {
                STAMP(11);
                unsigned m = 0u;
#pragma unroll
                for (int s = 0; s < 3; ++s) {
#pragma unroll
                    for (int p = 0; p < 4; ++p) pa[s][p] = __builtin_amdgcn_raw_buffer_load_b128(rx, inb(s) + p * 1024 + lane * 16, 0, 16);
                    if constexpr (NT == 5) {
#pragma unroll
                        for (int i = 0; i < 2; ++i) pb[s][i] = __builtin_amdgcn_raw_buffer_load_b64(rx, inb(s) + 8192 + i * 512 + lane * 8, 0, 16);
                    }
                }
#pragma unroll
                for (int s = 0; s < 3; ++s) {
#pragma unroll
                    for (int p = 0; p < 4; ++p) m = max(max(max(m, pa[s][p].x), max(pa[s][p].y, pa[s][p].z)), pa[s][p].w);
                    if constexpr (NT == 5) m = max(max(max(m, pb[s][0].x), max(pb[s][0].y, pb[s][1].x)), pb[s][1].y);
                }
                if (__all(m != ~0u)) break;
            }
            __builtin_amdgcn_s_sleep(1);
        }
        STAMP(12);
        const rsrc_t ro = mk_rsrc(out, S_ * D_ * 2);
#pragma unroll
        for (int i = 0; i < 2; ++i) {
            const unsigned ob = (unsigned)(((tok0 + i * 16) * D_ + nt * 144 + dh * 80) * 2);
            unsigned r[NT][2];
#pragma unroll
            for (int j = 0; j < NT; ++j) {
                f32x4 t = acc[j][i];
#pragma unroll
                for (int s = 0; s < 3; ++s) {
                    const unsigned w0 = j < 4 ? pa[s][i * 2 + (j >> 1)][(j & 1) * 2] : pb[s][i].x, w1 = j < 4 ? pa[s][i * 2 + (j >> 1)][(j & 1) * 2 + 1] : pb[s][i].y;
                    t += (f32x4){bf_lo(w0), bf_hi(w0), bf_lo(w1), bf_hi(w1)};
                }
                r[j][0] = pk_bf16(t[0], t[1]); r[j][1] = pk_bf16(t[2], t[3]);
            }
            auto addh = [&](unsigned a, unsigned h) -> unsigned { return pk_bf16(bf_lo(h) + bf_lo(a), bf_hi(h) + bf_hi(a)); };
#pragma unroll
            for (int p = 0; p < 2; ++p) {
                auto s0 = __builtin_amdgcn_permlane16_swap(r[2 * p][0], r[2 * p + 1][0], false, false);
                auto s1 = __builtin_amdgcn_permlane16_swap(r[2 * p][1], r[2 * p + 1][1], false, false);
                st16_wt(ro, ob + (unsigned)((p * 32 + (g4 & 1) * 16 + (g4 >> 1) * 8) * 2),
                        (u32x4){addh(s0[0], hv[i][2 * p].x), addh(s1[0], hv[i][2 * p].y), addh(s0[1], hv[i][2 * p + 1].x), addh(s1[1], hv[i][2 * p + 1].y)});
            }
            if constexpr (NT == 5) st8_wt(ro, ob + (unsigned)((64 + g4 * 4) * 2), (u32x2){addh(r[4][0], hv[i][4].x), addh(r[4][1], hv[i][4].y)});
        }
    }
    STAMP(13);
}

constexpr int K3_LDS = 163840;
constexpr int K3_SP = G_LDS;
constexpr int K3_CS = G2_W0 + 4 * 18432;
extern "C" __global__ __launch_bounds__(512, 2) void k3_gemm1(const u16* __restrict__ wg, const u16* __restrict__ wu,
                                                   char* __restrict__ ws, u16* __restrict__ out) {
    extern __shared__ __attribute__((aligned(16))) char smem[];
    LAS char* lds = (LAS char*)smem;
    const unsigned lds0 = (unsigned)(size_t)lds;
    const int tid = threadIdx.x, lane = tid & 63;
    const int wid = __builtin_amdgcn_readfirstlane(tid >> 6);
    const int wr = wid & 3, wc = wid >> 2;
    const int b = blockIdx.x, xcd = b & 7, idx = b >> 3;
    const int rt = (xcd >> 2) * 4 + (idx & 3), ct = (xcd & 3) * 8 + (idx >> 2);
    asm volatile("" :: "s"(wg), "s"(wu), "s"(ws), "s"(out));
    DIAG_DECL;

    const char* asrc = ws + WS_H + (size_t)rt * 144 * 2048 + wid * 2048;
    const unsigned aoff = (unsigned)(lane ^ ((wid & 2) << 2)) * 16;
    const unsigned ma0 = lds0 + wid * 2048, mw0 = lds0 + 16384 + wc * 9216 + wr * 2304;
    dma16<0>(aoff, asrc, ma0); dma16<1024>(aoff, asrc, ma0);
    const char* wsrc = (const char*)(wc ? wu : wg) + ct * 288 - 2048;
    auto wofs = [&](int i) -> unsigned {
        const int q = lane + 64 * i, lrow = 8 * wr + q / 18, pos = q % 18;
        const int krow = ((lrow >> 2) & 3) * 8 + (lrow >> 4) * 4 + (lrow & 3);
        const int col = pos * 8 + (pos < 8 ? 0 : (pos < 16 ? 8 : (((pos & 1) ^ wc) ? 136 : 64) - pos * 8));
        return (unsigned)(krow * (F_ * 2) + col * 2 + 2048 - 1024 * i);
    };
    const unsigned w0 = wofs(0), w1 = wofs(1), w2 = wofs(2);
    auto piece = [&](int P, int s) {
        const unsigned bo = (s & 3) * ST_BYTES;
        const char* wp = wsrc + (size_t)s * (32 * F_ * 2);
        if (P == 0) dma16<0>(aoff, asrc + (size_t)s * 16384, ma0 + bo);
        if (P == 1) dma16<1024>(aoff, asrc + (size_t)s * 16384, ma0 + bo);
        if (P == 2) dma16<0>(w0, wp, mw0 + bo);
        if (P == 3) dma16<1024>(w1, wp, mw0 + bo);
        if (P == 4) dma16_q<2048>(w2, wp, mw0 + bo);
    };
#define K3_PIECE(P, s) piece(P, s)
#define K3_STAGE(s) do { K3_PIECE(0, s); K3_PIECE(1, s); K3_PIECE(2, s); K3_PIECE(3, s); K3_PIECE(4, s); } while (0)
    K3_PIECE(2, 0); K3_PIECE(3, 0); K3_PIECE(4, 0);
    BAR();
    K3_STAGE(1);
    u32x2 hs[4];
    {
        const char* hsrc = ws + WS_H + (size_t)rt * 144 * 2048 + ct * 64;
        const unsigned o = (unsigned)((wid * 18 + (lane >> 3)) * 2048 + (lane & 7) * 8);
        asm volatile("global_load_dwordx2 %0, %1, %2" : "=v"(hs[0]) : "v"(o), "s"(hsrc) : "memory");
        asm volatile("global_load_dwordx2 %0, %1, %2" : "=v"(hs[1]) : "v"(o + 16384), "s"(hsrc) : "memory");
        asm volatile("global_load_dwordx2 %0, %1, %2" : "=v"(hs[2]) : "v"((unsigned)((wid * 18 + 16 + ((lane >> 3) & 1)) * 2048 + (lane & 7) * 8)), "s"(hsrc) : "memory");
        asm volatile("global_load_dwordx2 %0, %1, %2" : "=v"(hs[3]) : "v"((unsigned)(lane & 7) * 8), "s"(hsrc) : "memory");
    }

    const int g4 = lane >> 4, q4 = (lane & 15) >> 2, p4 = lane & 3;
    const int rowoff = (4 * g4 + q4) * 288;
    const int tbase = g4 * 4096 + wr * 512 + (lane & 15) * 8;
    const int toff0 = tbase + ((2 * wc) ^ (g4 & 1)) * 128, toff1 = tbase + ((2 * wc + 1) ^ (g4 & 1)) * 128;
    const int tstep = wc ? -256 : 256;
    const int foff = 16384 + rowoff + wc * 128 + p4 * 8;
    const int moff = 16384 + rowoff + 256 + (p4 < 2 ? 16 * wc + 8 * p4 : 9216 + 16 * (1 - wc) + 8 * (p4 - 2));

    f32x4 acc[9][4];
    s16x8 wf[9], tf[2][4];
    auto rd_w = [&](const LAS char* sb, int j) -> s16x8 {
        return j < 8 ? tr_frag(sb + foff + (j >> 2) * 9216 + (j & 3) * 32) : tr_frag(sb + moff);
    };
    auto rd_t = [&](const LAS char* sb, int i) -> s16x8 {
        const LAS char* p = sb + ((i & 1) ? toff1 : toff0) + (i >> 1) * tstep;
        const s16x4 lo = *(const volatile LAS s16x4*)p, hi = *(const volatile LAS s16x4*)(p + 2048);
        return __builtin_shufflevector(lo, hi, 0, 1, 2, 3, 4, 5, 6, 7);
    };

    STAMP(1);
    WAIT_V(9); BAR();
    STAMP(2);
    wf[0] = rd_w(lds, 0);
#pragma unroll
    for (int i = 0; i < 4; ++i) tf[0][i] = rd_t(lds, i);
#pragma unroll
    for (int j = 1; j < 9; ++j) wf[j] = rd_w(lds, j);
    SCHED();
    const int mt2 = (xcd >> 2) * 8 + (idx & 7), ks2 = xcd & 3, nt2 = idx >> 3;
    const rsrc_t rfill = mk_rsrc(ws + WS_X + (size_t)((mt2 * 4 + nt2) * 4 + ks2) * 27648, 27648);
    auto fill = [&](int k) { st16_wt(rfill, (unsigned)(k * 8192 + tid * 16), (u32x4){~0u, ~0u, ~0u, ~0u}); };
    unsigned sgr;
    {
        const LAS char* sb = lds + ST_BYTES;
        float hc = 0.f;
#pragma unroll
        for (int j = 0; j < 9; ++j) {
#pragma unroll
            for (int i = 0; i < 4; ++i) mfma_first(acc[j][i], wf[j], tf[0][i]);
            SCHED();
            if (j == 0) { K3_PIECE(0, 2); K3_PIECE(1, 2); }
            if (j == 1) { K3_PIECE(2, 2); K3_PIECE(3, 2); }
            if (j == 2) K3_PIECE(4, 2);
            if (j == 3) {
                asm volatile("s_waitcnt vmcnt(5)" : "+v"(hs[0]), "+v"(hs[1]), "+v"(hs[2]), "+v"(hs[3]) :: "memory");
                hc = bf_lo(hs[3].x);
                float s1 = 0.f, s2 = 0.f;
#pragma unroll
                for (int i = 0; i < 3; ++i) {
                    const float d[4] = {bf_lo(hs[i].x) - hc, bf_hi(hs[i].x) - hc, bf_lo(hs[i].y) - hc, bf_hi(hs[i].y) - hc};
                    float a = (d[0] + d[1]) + (d[2] + d[3]), q = __builtin_fmaf(d[0], d[0], d[1] * d[1]) + __builtin_fmaf(d[2], d[2], d[3] * d[3]);
                    if (i == 2 && lane >= 16) { a = 0.f; q = 0.f; }
                    s1 += a; s2 += q;
                }
                s1 = x32_add(x16_add(dpp_add<0x128>(s1))); s2 = x32_add(x16_add(dpp_add<0x128>(s2)));
                if (lane < 8) *(LAS f32x2*)(lds + K3_SP + (lane * 8 + wid) * 8) = (f32x2){s1, s2};
                asm volatile("s_waitcnt lgkmcnt(0)" ::: "memory");
                BAR();
                wf[0] = rd_w(sb, 0); wf[1] = rd_w(sb, 1);
                tf[1][0] = rd_t(sb, 0); tf[1][1] = rd_t(sb, 1);
            }
            if (j == 4) { tf[1][2] = rd_t(sb, 2); tf[1][3] = rd_t(sb, 3); wf[2] = rd_w(sb, 2); wf[3] = rd_w(sb, 3); }
            if (j >= 4) { wf[j] = rd_w(sb, j); K3_PIECE(j - 4, 3); }
            if (j == 8) {
                const f32x2 pp = *(const LAS f32x2*)(lds + K3_SP + (wid * 8 + (lane & 7)) * 8);
                const float s1 = dpp_add<0x141>(dpp_add<0x4E>(dpp_add<0xB1>(pp.x))) * (1.f / D_);
                const float s2 = dpp_add<0x141>(dpp_add<0x4E>(dpp_add<0xB1>(pp.y))) * (1.f / D_);
                const float c0 = __uint_as_float(__builtin_amdgcn_readlane(__float_as_uint(hc), wid));
                const float sd = ln_sd(__builtin_fmaxf(__builtin_fmaf(-s1, s1, s2), 0.f));
                sgr = pk_bf16(c0 + s1, sd);
                fill(0); fill(1);
            }
            SCHED();
        }
    }

    const rsrc_t rg = mk_rsrc(ws + WS_G2 + (size_t)(rt * 256 + wr * 64) * 8, 512);
    const char* w2src = ws + WS_WT + (size_t)(ks2 * 4 + nt2) * 18 * 18432 + wid * 2304;
    u32x2 gr[4];
    s16x4 tl;
    constexpr int TP = 1, DPJ0 = 0, DPJ1 = 5;
    auto iter = [&](auto mode, auto vm, int s, s16x8 (&tc)[4], s16x8 (&tn)[4]) {
        constexpr int MODE = decltype(mode)::v;
        if constexpr (MODE != 4) {
            if constexpr (MODE == 3) WAIT_V(0); else WAIT_VI(5 + decltype(vm)::v);
            BAR();
            SCHED();
        }
        const LAS char* sb = lds + ((s + 1) & 3) * ST_BYTES;
#pragma unroll
        for (int j = 0; j < 9; ++j) {
            s16x4 wl, wh;
#pragma unroll
            for (int i = 0; i < 4; ++i) {
                mfma_acc(acc[j][i], wf[j], tc[i]);
                SCHED();
                if constexpr (MODE != 4) {
                    if (j >= 1) {
                        const LAS char* p = (j - 1 < 8) ? sb + foff + ((j - 1) >> 2) * 9216 + ((j - 1) & 3) * 32 : sb + moff;
                        if (i == 0) wl = __builtin_amdgcn_ds_read_tr16_b64_v4i16((LAS s16x4*)p);
                        if (i == 1) { wh = __builtin_amdgcn_ds_read_tr16_b64_v4i16((LAS s16x4*)(p + 16 * 288)); wf[j - 1] = __builtin_shufflevector(wl, wh, 0, 1, 2, 3, 4, 5, 6, 7); }
                    }
                    if (j >= TP && j < TP + 4) {
                        const int ti = j - TP;
                        const LAS char* p = sb + ((ti & 1) ? toff1 : toff0) + (ti >> 1) * tstep;
                        if (i == 2) tl = *(const volatile LAS s16x4*)p;
                        if (i == 3) tn[ti] = __builtin_shufflevector(tl, *(const volatile LAS s16x4*)(p + 2048), 0, 1, 2, 3, 4, 5, 6, 7);
                    }
                    if (j == 8 && i == 3) wf[8] = rd_w(sb, 8);
                }
                if constexpr (MODE < 2 || MODE == 5) {
                    if (j == DPJ0 && i == 1) K3_PIECE(0, s + 3);
                    if (j == DPJ0 && i == 3) K3_PIECE(1, s + 3);
                    if (j == DPJ1 && i == 3) K3_PIECE(2, s + 3);
                    if (j == DPJ1 + 1 && i == 3) K3_PIECE(3, s + 3);
                    if (j == DPJ1 + 2 && i == 3) K3_PIECE(4, s + 3);
                }
                SCHED();
            }
            if constexpr (MODE == 0) { if (j == 8) { fill(2); fill(3); } }
            if constexpr (MODE == 5) {
                if (j == 0 && s == 4) st8_wt(mk_rsrc(ws + WS_G2, S_ * 8), lane == 0 ? (unsigned)((rt * 256 + ct * 8 + wid) * 8) : 0x80000000u, (u32x2){sgr, 1u});
            }
            if constexpr (MODE == 2) {
                if (j == 0) {
#pragma unroll
                    for (int i = 0; i < 4; ++i) gr[i] = __builtin_amdgcn_raw_buffer_load_b64(rg, (unsigned)(((i ^ (2 * wc)) * 16 + (lane & 15)) * 8), 0, 16);
                    const char *c3, *ce;
                    SYM_ADDR(c3, k3_gemm1); SYM_ADDR(ce, k9_end);
                    warm_code<1>(c3, ce, 64 + wid * 64, lane, lds0 + K3_CS + 12288 + wid * 256);
                }
            }
            if constexpr (MODE == 3) {
                if (j == 0) {
                    asm volatile("" : "+v"(gr[0]), "+v"(gr[1]), "+v"(gr[2]), "+v"(gr[3]));
                    const unsigned co = (unsigned)((lane >= 18 ? F_ * 4 + (lane - 18) * 16 : lane * 16));
                    dma_m36(co, ws + WS_CS + (size_t)(ct * 144 + wc * 72) * 4, lds0 + K3_CS + wid * 1152);
                    dma_m36(co, ws + WS_CS + (size_t)(2 * F_ + ct * 144 + wc * 72) * 4, lds0 + K3_CS + wid * 1152 + 576);
                }
            }
            if constexpr (MODE == 3 || MODE == 4) {
                if (j >= 2 && j < 8) g2_wpiece((j - 2) % 3, (MODE - 3) * 2 + (j - 2) / 3, w2src, lane * 16, lds0 + G2_W0 + wid * 2304);
            }
            SCHED();
        }
    };
    iter(IC<0>{}, IC<2>{}, 1, tf[1], tf[0]);
    iter(IC<1>{}, IC<4>{}, 2, tf[0], tf[1]);
    iter(IC<1>{}, IC<2>{}, 3, tf[1], tf[0]);
#pragma unroll 1
    for (int s = 4; s < 14; s += 2) {
        iter(IC<5>{}, IC<0>{}, s, tf[0], tf[1]);
        iter(IC<1>{}, IC<0>{}, s + 1, tf[1], tf[0]);
    }
    STAMP(3);
    iter(IC<1>{}, IC<0>{}, 14, tf[0], tf[1]);
    iter(IC<2>{}, IC<0>{}, 15, tf[1], tf[0]);
    iter(IC<3>{}, IC<0>{}, 16, tf[0], tf[1]);
    iter(IC<4>{}, IC<0>{}, 17, tf[1], tf[0]);
    MFMA_DONE();
    STAMP(4);
    WAIT_V(12);
    __builtin_amdgcn_s_setprio(1);
    if (__builtin_expect(!__all((gr[0].y & gr[1].y & gr[2].y & gr[3].y) == 1u), 0)) {
        for (int spin = 0; spin < (1 << 20); ++spin) {
            __builtin_amdgcn_s_sleep(2);
#pragma unroll
            for (int i = 0; i < 4; ++i) gr[i] = __builtin_amdgcn_raw_buffer_load_b64(rg, (unsigned)(((i ^ (2 * wc)) * 16 + (lane & 15)) * 8), 0, 16);
            if (__all((gr[0].y & gr[1].y & gr[2].y & gr[3].y) == 1u)) break;
        }
    }
    const int mt = rt * 2 + (wr >> 1), ks = ct >> 3, lct = ct & 7;
    const rsrc_t rhh = mk_rsrc(ws + WS_HH, S_ * F_ * 2);
    const unsigned hb = (unsigned)(((mt * 4 + ks) * 36) * 8192 + ((wr & 1) * 4) * 1024);
    const LAS char* csb = lds + K3_CS + wid * 1152 + 16 * g4;
    TokC tcs[4];
#pragma unroll
    for (int i = 0; i < 4; ++i) {
        const float sd = bf_hi(gr[i].x), r = __builtin_amdgcn_rcpf(sd);
        tcs[i] = (TokC){-bf_lo(gr[i].x), -2.30220819f * r, -0.10294324f * (r * r * r), sd * sd};
    }
    {
        const LAS char* cm = lds + K3_CS + wid * 1152 + 256 + 16 * (g4 & 1);
        const f32x4 cgm = *(const LAS f32x4*)cm + *(const LAS f32x4*)(cm + 576), cum = *(const LAS f32x4*)(cm + 288) + *(const LAS f32x4*)(cm + 864);
#pragma unroll
        for (int ip = 0; ip < 2; ++ip) {
            const int ia = ((2 * ip) ^ (2 * wc)) + (lane >> 5);
            const bool up = lane >= 32;
            const TokC tc = {up ? tcs[2 * ip + 1].nm : tcs[2 * ip].nm, up ? tcs[2 * ip + 1].c0 : tcs[2 * ip].c0,
                             up ? tcs[2 * ip + 1].c1 : tcs[2 * ip].c1, up ? tcs[2 * ip + 1].q : tcs[2 * ip].q};
            f32x4 gm, um;
#pragma unroll
            for (int r = 0; r < 4; ++r) {
                auto sw = __builtin_amdgcn_permlane32_swap(__float_as_uint(acc[8][2 * ip][r]), __float_as_uint(acc[8][2 * ip + 1][r]), false, false);
                gm[r] = __uint_as_float(sw[0]); um[r] = __uint_as_float(sw[1]);
            }
            const int kgs = (lct & 1) * 2 + wc;
            st8_wt(rhh, hb + (unsigned)((lct >> 1) * 8192 + ia * 1024 + (kgs * 16 + (lane & 15)) * 16 + (g4 & 1) * 8),
                   (u32x2){geglu2(LO2(gm), LO2(um), LO2(cgm), LO2(cum), tc), geglu2(HI2(gm), HI2(um), HI2(cgm), HI2(cum), tc)});
        }
    }
    SCHED();
    constexpr int FLAGPOS = 1;
    const rsrc_t rfb = mk_rsrc(ws + WS_FB, 16384);
    const unsigned fbo = (unsigned)((mt * 4 + ks) * 256 + (lct * 4 + (wr & 1) * 2 + wc) * 4);
    u32x4 ob[4];
#pragma unroll
    for (int jp = 0; jp < 2; ++jp) {
        f32x4 cg[2], cu[2];
#pragma unroll
        for (int e = 0; e < 2; ++e) {
            cg[e] = *(const LAS f32x4*)(csb + (2 * jp + e) * 64) + *(const LAS f32x4*)(csb + 576 + (2 * jp + e) * 64);
            cu[e] = *(const LAS f32x4*)(csb + 288 + (2 * jp + e) * 64) + *(const LAS f32x4*)(csb + 864 + (2 * jp + e) * 64);
        }
#pragma unroll
        for (int i = 0; i < 4; ++i) {
            unsigned o[4];
#pragma unroll
            for (int e = 0; e < 2; ++e) {
                const f32x4 g = acc[2 * jp + e][i], u = acc[4 + 2 * jp + e][i];
                o[2 * e] = geglu2(LO2(g), LO2(u), LO2(cg[e]), LO2(cu[e]), tcs[i]);
                o[2 * e + 1] = geglu2(HI2(g), HI2(u), HI2(cg[e]), HI2(cu[e]), tcs[i]);
            }
            ob[i] = (u32x4){o[0], o[1], o[2], o[3]};
            st16_wt(rhh, (jp == 1 && wid == 7) ? 0x80000000u : hb + (unsigned)(((2 + jp * 8 + lct) * 2 + wc) * 8192 + (i ^ (2 * wc)) * 1024 + lane * 16), ob[i]);
            if (jp == 1 && i == FLAGPOS) {
                SCHED();
                WAIT_VI(FLAGPOS + 1);
                __builtin_amdgcn_raw_buffer_store_b32(1u, rfb, lane == 0 ? fbo : 0x80000000u, 0, 16);
                SCHED();
            }
        }
        SCHED();
        if (jp == 0) __builtin_amdgcn_s_setprio(0);
    }
    STAMP(5);
    if (wid == 7) {
        for (int spin = 0; spin < (1 << 20); ++spin) {
            const unsigned f = __builtin_amdgcn_raw_buffer_load_b32(rfb, (unsigned)((mt2 * 4 + ks2) * 256 + (lane & 31) * 4), 0, 16);
            if (__all(f == 1u)) break;
            __builtin_amdgcn_s_sleep(4);
        }
#pragma unroll
        for (int i = 0; i < 4; ++i) st16_wt(rhh, hb + (unsigned)(((10 + lct) * 2 + wc) * 8192 + (i ^ (2 * wc)) * 1024 + lane * 16), ob[i]);
    }
    BAR();
    STAMP(7);
    if (wid < 4) g2_body<5>(ws, out, lds, lane, wid, mt2, ks2, nt2, w2src, fbo + 128 DIAG_PASS);
    else g2_body<4>(ws, out, lds, lane, wid, mt2, ks2, nt2, w2src, fbo + 128 DIAG_PASS);
    DIAG_END(ws, 1);
}

extern "C" __global__ void k9_end() {}

extern "C" void kernel_launch(void* const* d_in, const int* in_sizes, int n_in, void* d_out, int out_size, void* d_ws,
                              size_t ws_size, hipStream_t stream) {
    const u16* q = (const u16*)d_in[0];
    const u16* k = (const u16*)d_in[1];
    const u16* v = (const u16*)d_in[2];
    const u16* shift_k = (const u16*)d_in[3];
    const u16* ssm_k = (const u16*)d_in[4];
    const u16* ssm_D = (const u16*)d_in[5];
    const u16* wg = (const u16*)d_in[7];
    const u16* wu = (const u16*)d_in[8];
    const u16* wd = (const u16*)d_in[9];
    char* ws = (char*)d_ws;
    (void)hipFuncSetAttribute((const void*)k1_conv, hipFuncAttributeMaxDynamicSharedMemorySize, C1_LDS);
    (void)hipFuncSetAttribute((const void*)k3_gemm1, hipFuncAttributeMaxDynamicSharedMemorySize, K3_LDS);
    k1_conv<<<256, 512, C1_LDS, stream>>>(q, k, v, shift_k, ssm_k, ssm_D, wg, wu, wd, ws);
    k3_gemm1<<<256, 512, K3_LDS, stream>>>(wg, wu, ws, (u16*)d_out);
}
```

```cpp
#include <hip/hip_runtime.h>
#include <hip/hip_bf16.h>

#define LAS __attribute__((address_space(3)))
typedef unsigned short u16;
typedef short s16x4 __attribute__((ext_vector_type(4)));
typedef short s16x8 __attribute__((ext_vector_type(8)));
typedef float f32x2 __attribute__((ext_vector_type(2)));
typedef float f32x4 __attribute__((ext_vector_type(4)));
typedef unsigned u32x2 __attribute__((ext_vector_type(2)));
typedef unsigned u32x4 __attribute__((ext_vector_type(4)));
typedef __bf16 bf16x2_t __attribute__((ext_vector_type(2)));

constexpr int S_ = 2048, D_ = 576, F_ = 4608;

__device__ __forceinline__ unsigned pk_bf16(float lo, float hi) {
    f32x2 v = {lo, hi};
    bf16x2_t b = __builtin_convertvector(v, bf16x2_t);
    return __builtin_bit_cast(unsigned, b);
}
__device__ __forceinline__ float bf_lo(unsigned w) { return __uint_as_float(w << 16); }
__device__ __forceinline__ float bf_hi(unsigned w) { return __uint_as_float(w & 0xffff0000u); }
__device__ __forceinline__ float rbf(float x) { return bf_lo(pk_bf16(x, 0.f)); }
#define WAIT_V(n) asm volatile("s_waitcnt vmcnt(" #n ")" ::: "memory")
#define BAR() do { __builtin_amdgcn_s_barrier(); asm volatile("" ::: "memory"); } while (0)
#define SCHED() __builtin_amdgcn_sched_barrier(0)
#define SLEEP(n) do { __builtin_amdgcn_s_sleep(n); asm volatile("" ::: "memory"); } while (0)
#define DIAG_DECL
#define MARK()
#define STAMP(i)
#define DIAG_END(wsbase, kid)
__device__ __forceinline__ void mfma_acc(f32x4& c, s16x8 a, s16x8 b) {
    asm volatile("v_mfma_f32_16x16x32_bf16 %0, %1, %2, %0" : "+v"(c) : "v"(a), "v"(b));
}
__device__ __forceinline__ void mfma_first(f32x4& c, s16x8 a, s16x8 b) {
    asm volatile("v_mfma_f32_16x16x32_bf16 %0, %1, %2, 0" : "=&v"(c) : "v"(a), "v"(b));
}
#define MFMA_DONE() do { asm volatile("s_nop 7\n\ts_nop 7" ::: "memory"); SCHED(); } while (0)
constexpr size_t WS_KV = 0;
constexpr size_t WS_H = WS_KV + (size_t)S_ * D_ * 2;
constexpr size_t WS_ST = WS_H + (size_t)S_ * D_ * 2;
constexpr size_t WS_CS = WS_ST + (size_t)S_ * 8;
constexpr size_t WS_G2 = WS_CS + (size_t)4 * F_ * 4;
constexpr size_t WS_F0 = WS_G2 + (size_t)S_ * 8;
constexpr int FSP = 128;
constexpr size_t WS_EP = WS_F0 + 1280 * FSP;
constexpr size_t WS_HH = WS_EP + 1024;
constexpr size_t WS_X = WS_HH + (size_t)S_ * F_ * 2;
constexpr unsigned X_BYTES = 64 * 4 * 3 * 9216;
constexpr size_t WS_FB = WS_X + X_BYTES;
constexpr size_t WS_WT = WS_FB + 16384;

template <int N> struct IC { static constexpr int v = N; };
constexpr int AUX_RD = (int)(16u | (1u << 31));
typedef __amdgpu_buffer_rsrc_t rsrc_t;
__device__ __forceinline__ rsrc_t mk_rsrc(const void* p, unsigned bytes) { return __builtin_amdgcn_make_buffer_rsrc((void*)p, 0, bytes, 0x00020000); }
__device__ __forceinline__ void st16_wt(rsrc_t r, unsigned off, u32x4 v) { __builtin_amdgcn_raw_buffer_store_b128(v, r, off, 0, 16); }
__device__ __forceinline__ void st8_wt(rsrc_t r, unsigned off, u32x2 v) { __builtin_amdgcn_raw_buffer_store_b64(v, r, off, 0, 16); }
template <int CTRL> __device__ __forceinline__ float dpp_add(float x) {
    return x + __uint_as_float(__builtin_amdgcn_update_dpp(0, (int)__float_as_uint(x), CTRL, 0xf, 0xf, true));
}
__device__ __forceinline__ float x16_add(float x) {
    auto r = __builtin_amdgcn_permlane16_swap(__float_as_uint(x), __float_as_uint(x), false, false);
    return __uint_as_float(r[0]) + __uint_as_float(r[1]);
}
__device__ __forceinline__ float x32_add(float x) {
    auto r = __builtin_amdgcn_permlane32_swap(__float_as_uint(x), __float_as_uint(x), false, false);
    return __uint_as_float(r[0]) + __uint_as_float(r[1]);
}
__device__ __forceinline__ float sum16(float x) {
    x = dpp_add<0xB1>(x); x = dpp_add<0x4E>(x); x = dpp_add<0x141>(x);
    return dpp_add<0x140>(x);
}
__device__ __forceinline__ float ln_sd(float var) { return rbf(__builtin_amdgcn_sqrtf(rbf(rbf(var) + 1e-5f))); }

template <int OFF>
__device__ __forceinline__ void dma16(unsigned voff, const void* sbase, unsigned m0v) {
    asm volatile("s_mov_b32 m0, %2\n\ts_nop 0\n\tglobal_load_lds_dwordx4 %0, %1 offset:%3"
                 :: "v"(voff), "s"(sbase), "s"(m0v), "i"(OFF) : "memory");
}
template <int OFF>
__device__ __forceinline__ void dma16_sc1(unsigned voff, const void* sbase, unsigned m0v) {
    asm volatile("s_mov_b32 m0, %2\n\ts_nop 0\n\tglobal_load_lds_dwordx4 %0, %1 offset:%3 sc1"
                 :: "v"(voff), "s"(sbase), "s"(m0v), "i"(OFF) : "memory");
}
template <int OFF>
__device__ __forceinline__ void dma16_q(unsigned voff, const void* sbase, unsigned m0v) {
    asm volatile("s_mov_b32 m0, %2\n\ts_mov_b64 exec, 0xffff\n\tglobal_load_lds_dwordx4 %0, %1 offset:%3\n\ts_mov_b64 exec, -1"
                 :: "v"(voff), "s"(sbase), "s"(m0v), "i"(OFF) : "memory");
}
__device__ __forceinline__ void dma_m36(unsigned voff, const void* sbase, unsigned m0v) {
    asm volatile("s_mov_b32 m0, %2\n\ts_mov_b32 exec_hi, 15\n\tglobal_load_lds_dwordx4 %0, %1\n\ts_mov_b64 exec, -1"
                 :: "v"(voff), "s"(sbase), "s"(m0v) : "memory");
}
__device__ __forceinline__ void dma4(unsigned voff, const void* sbase, unsigned m0v) {
    asm volatile("s_mov_b32 m0, %2\n\ts_nop 0\n\tglobal_load_lds_dword %0, %1" :: "v"(voff), "s"(sbase), "s"(m0v) : "memory");
}
__device__ __forceinline__ void dma4_sc1(unsigned voff, const void* sbase, unsigned m0v) {
    asm volatile("s_mov_b32 m0, %2\n\ts_nop 0\n\tglobal_load_lds_dword %0, %1 sc1" :: "v"(voff), "s"(sbase), "s"(m0v) : "memory");
}
#define SYM_ADDR(out, sym) asm volatile("s_getpc_b64 s[40:41]\n\ts_add_u32 s40, s40, " #sym "@rel32@lo+4\n\ts_addc_u32 s41, s41, " #sym "@rel32@hi+12\n\ts_mov_b64 %0, s[40:41]" \
                                        : "=s"(out) :: "s40", "s41", "scc")
template <int NI>
__device__ __forceinline__ void warm_code(const char* a, const char* e, int l0, int lane, unsigned m0v) {
    const unsigned last = e > a ? (unsigned)(e - a) - 4u : 0u;
#pragma unroll
    for (int i = 0; i < NI; ++i) dma4(min((unsigned)(l0 + i * 64 + lane) * 128u, last), a, m0v);
}
__device__ __forceinline__ s16x8 tr_frag(const LAS char* p) {
    const s16x4 lo = __builtin_amdgcn_ds_read_tr16_b64_v4i16((LAS s16x4*)p);
    const s16x4 hi = __builtin_amdgcn_ds_read_tr16_b64_v4i16((LAS s16x4*)(p + 16 * 288));
    return __builtin_shufflevector(lo, hi, 0, 1, 2, 3, 4, 5, 6, 7);
}
__device__ __forceinline__ void wd_unit_load(const u16* wd, int un, int lane, unsigned m0v) {
    const int uB = un / 3, ut = un % 3, kh = uB & 1, st = (uB >> 1) % 18, kn = uB / 36;
    const bool full = st >= 2;
    const int f0 = (kn >> 2) * 1152 + (full ? ((st - 2) & 7) * 144 + kh * 72 + ((st - 2) >> 3) * 32 : (st * 2 + kh) * 288);
    const char* src = (const char*)wd + (size_t)f0 * (D_ * 2) + (kn & 3) * 288 + ut * 96;
#pragma unroll
    for (int i = 0; i < 3; ++i) {
        const int q = lane + 64 * i, r = q / 6, pos = q % 6, kg = (r & 15) >> 2;
        const int f = full ? r : (kg >> 1) * 144 + (kg & 1) * 72 + 64 + (r & 3) + 4 * (r >> 4);
        dma16<0>((unsigned)(f * (D_ * 2) + pos * 16), src, m0v + i * 1024);
    }
}
__device__ __forceinline__ void wd_unit_store(char* ws, int un, int lane, const LAS char* l) {
    const rsrc_t r = mk_rsrc(ws + WS_WT, F_ * D_ * 2);
#pragma unroll
    for (int t = 0; t < 3; ++t) {
        const LAS char* tp = l + (lane >> 2) * 96 + (lane & 3) * 8 + t * 32;
        const s16x4 lo = __builtin_amdgcn_ds_read_tr16_b64_v4i16((LAS s16x4*)tp), hi = __builtin_amdgcn_ds_read_tr16_b64_v4i16((LAS s16x4*)(tp + 16 * 96));
        st16_wt(r, (unsigned)(un * 3072 + t * 1024 + lane * 16), __builtin_bit_cast(u32x4, __builtin_shufflevector(lo, hi, 0, 1, 2, 3, 4, 5, 6, 7)));
    }
}
constexpr int WU4 = 3;
constexpr int HELPER_DELAY = 48, HELPER_DELAY2 = 24, POLL0 = 16, POLL1 = 1;
constexpr int C1_COPY = 4288;
constexpr int C1_FILT = 4 * C1_COPY;
constexpr int C1_KV = 2304 * 2;
constexpr int C1_KVOFF = 4 * C1_FILT + 64;
constexpr int C1_YOFF = C1_KVOFF + 4 * C1_KV;
constexpr int C1_XOFF = C1_YOFF + 16384;
constexpr int C1_SOFF = C1_XOFF + 16384;
constexpr int C1_TOFF = C1_SOFF + 16384;
constexpr int C1_WOFF = C1_TOFF + 2048;
constexpr int C1_LDS = C1_WOFF + 24576;

extern "C" __global__ __launch_bounds__(512, 2) void k1_conv(const u16* __restrict__ x, const u16* __restrict__ kin, const u16* __restrict__ vin,
                                                  const u16* __restrict__ shift_k, const u16* __restrict__ ssm_k, const u16* __restrict__ ssm_D,
                                                  const u16* __restrict__ wg, const u16* __restrict__ wu, const u16* __restrict__ wd,
                                                  char* ws) {
    extern __shared__ __attribute__((aligned(16))) char smem[];
    const int tid = threadIdx.x, lane = tid & 63;
    const int wid = __builtin_amdgcn_readfirstlane(tid >> 6);
    const int b = blockIdx.x, xcd = b & 7, idx = b >> 3;
    {
        const char *c1, *c3;
        SYM_ADDR(c1, k1_conv); SYM_ADDR(c3, k3_gemm1);
        if (wid >= 4) warm_code<1>(c1, c3, (wid & 3) * 64, lane, (unsigned)(size_t)(LAS char*)smem + C1_TOFF + 256 + (wid & 3) * 256);
    }
    DIAG_DECL;
    asm volatile("" :: "s"(x), "s"(kin), "s"(vin), "s"(shift_k), "s"(ssm_k), "s"(ssm_D), "s"(wg), "s"(wu), "s"(wd), "s"(ws));
    const bool conv = idx < 18;
    const int cg = xcd * 18 + idx, ch0 = cg * 4;
    const unsigned lds0 = (unsigned)(size_t)(LAS char*)smem;
    LAS unsigned* tagp = (LAS unsigned*)(smem + C1_TOFF);
    auto get_tag = [&]() -> unsigned { BAR(); return __builtin_amdgcn_readfirstlane(*(volatile LAS unsigned*)tagp); };

    const int h = (idx - 18) * 8 + xcd, hc_ = h < 108 ? h : 0;
    const int m3 = hc_ % 3, lcA = (hc_ / 3) * 4 + m3, rb0 = m3 * 3;
    constexpr int HU = (1728 - 144 * WU4 + 111) / 112;
    const int hbase = 144 * WU4 + h * HU, hun = hbase + wid * 2, hend = min(hbase + HU, 1728);
    auto cs_load = [&](u32x4 (&v)[12]) {
        const int rs = tid >> 3, c8 = tid & 7;
#pragma unroll
        for (int p = 0; p < 12; ++p) {
            const int rb = rb0 + p, lc = lcA + (rb >= 9), r = (rb >= 9 ? rb - 9 : rb) * 64 + rs;
            v[p] = *(const u32x4*)((lc >= 72 ? wu : wg) + (size_t)r * F_ + (lc % 72) * 64 + c8 * 8);
        }
        SCHED();
#pragma unroll
        for (int u = 0; u < 2; ++u) if (hun + u < hend) wd_unit_load(wd, hun + u, lane, lds0 + 16384 + (wid * 2 + u) * 3072);
        st16_wt(mk_rsrc(ws + WS_FB, 16384), (unsigned)(h * 8192 + tid * 16), (u32x4){0u, 0u, 0u, 0u});
        SCHED();
    };
    auto cs_finish = [&](u32x4 (&v)[12]) {
        float g[4][8];
#pragma unroll
        for (int q = 0; q < 4; ++q) {
#pragma unroll
            for (int e = 0; e < 4; ++e) {
                g[q][2 * e] = (bf_lo(v[3 * q][e]) + bf_lo(v[3 * q + 1][e])) + bf_lo(v[3 * q + 2][e]);
                g[q][2 * e + 1] = (bf_hi(v[3 * q][e]) + bf_hi(v[3 * q + 1][e])) + bf_hi(v[3 * q + 2][e]);
            }
        }
        LAS float* red = (LAS float*)smem;
#pragma unroll
        for (int l = 0; l < 2; ++l) {
            float a[8];
#pragma unroll
            for (int e = 0; e < 8; ++e) {
                a[e] = l == 0 ? g[0][e] + (m3 <= 1 ? g[1][e] : 0.f) + (m3 == 0 ? g[2][e] : 0.f) : g[3][e] + (m3 >= 1 ? g[2][e] : 0.f) + (m3 == 2 ? g[1][e] : 0.f);
                a[e] = x32_add(x16_add(dpp_add<0x128>(a[e])));
            }
            if (lane < 8) {
                *(LAS f32x4*)(red + (wid * 2 + l) * 64 + lane * 8) = (f32x4){a[0], a[1], a[2], a[3]};
                *(LAS f32x4*)(red + (wid * 2 + l) * 64 + lane * 8 + 4) = (f32x4){a[4], a[5], a[6], a[7]};
            }
        }
        WAIT_V(1);
#pragma unroll
        for (int u = 0; u < 2; ++u) if (hun + u < hend) wd_unit_store(ws, hun + u, lane, (const LAS char*)smem + 16384 + (wid * 2 + u) * 3072);
        asm volatile("s_waitcnt lgkmcnt(0)" ::: "memory"); BAR();
        if (tid < 192 && h < 108) {
            float s = 0.f;
            if (tid < 128) {
#pragma unroll
                for (int w = 0; w < 8; ++w) s += red[w * 128 + tid];
            }
            const int part = tid >> 6;
            const int lc = part == 0 ? lcA : (part == 1 ? lcA + 1 : (m3 == 0 ? lcA : lcA + 1));
            const int slot = part == 0 ? (m3 != 0) : (part == 1 ? 0 : 1);
            if (part < 2 || m3 != 1)
                __builtin_amdgcn_raw_buffer_store_b32(__float_as_uint(s), mk_rsrc(ws + WS_CS, 4 * F_ * 4), (unsigned)((slot * 2 * F_ + lc * 64 + (tid & 63)) * 4), 0, 16);
        }
    };
    auto wait_kv = [&](unsigned tag) {
        const rsrc_t rf = mk_rsrc(ws + WS_F0 + ((idx >> 2) * 8 + xcd) * 32 * FSP, 32 * FSP);
        SLEEP(POLL0);
        for (int spin = 0; spin < (1 << 20); ++spin) {
            const unsigned f = __builtin_amdgcn_raw_buffer_load_b32(rf, (lane & 31) * FSP, 0, AUX_RD);
            if (__all(f == tag)) break;
            SLEEP(POLL1);
        }
    };

    auto x_rows = [&]() {
        u32x2 xr[8];
#pragma unroll
        for (int i = 0; i < 8; ++i) xr[i] = *(const u32x2*)(x + (size_t)((tid - 256) + (wid - 4) * 448 + i * 64) * D_ + ch0);
#pragma unroll
        for (int i = 0; i < 8; ++i) *(LAS u32x2*)(smem + C1_XOFF + ((tid - 256) + (wid - 4) * 448 + i * 64) * 8) = xr[i];
        SCHED();
    };
    unsigned tag;
    unsigned dvv = 0;
    auto upper = [&](auto is_stat, auto is_conv) {
        constexpr bool STAT = decltype(is_stat)::v, CONV = decltype(is_conv)::v;
        const int srow = b * 8 + (wid - 5) * 4 + (lane >> 4), t3 = tid - 320;
        unsigned xw[18], epv = 0;
        if constexpr (STAT) {
            const unsigned* xr = (const unsigned*)(x + (size_t)srow * D_) + (lane & 15);
#pragma unroll
            for (int j = 0; j < 18; ++j) xw[j] = xr[16 * j];
        } else {
            epv = __hip_atomic_load((const unsigned*)(ws + WS_EP), __ATOMIC_RELAXED, __HIP_MEMORY_SCOPE_AGENT);
        }
        SCHED();
        if constexpr (STAT) {
            const char *c3, *c4;
            SYM_ADDR(c3, k3_gemm1); SYM_ADDR(c4, k9_end);
            warm_code<1>(c3, c4, ((idx & 3) * 2 + wid - 5) * 64, lane, lds0 + C1_TOFF + 256 + (wid - 5) * 256);
        }
        SCHED();
        u32x4 v[12], fa[6]; u32x2 fb[6];
        if constexpr (CONV) {
#pragma unroll
            for (int i = 0; i < 6; ++i) {
                const int task = min(t3 + 192 * i, 1023), u = task & 255;
                const u16* src = ssm_k + (size_t)(ch0 + (task >> 8)) * S_ + u * 8;
                fa[i] = *(const u32x4*)src;
                fb[i] = *(const u32x2*)(src + (u < 255 ? 8 : 0));
            }
            SCHED();
        }
        if constexpr (STAT) {
            f32x2 xf[18], s = {0.f, 0.f};
#pragma unroll
            for (int j = 0; j < 18; ++j) { xf[j] = (f32x2){bf_lo(xw[j]), bf_hi(xw[j])}; s += xf[j]; }
            const float mean = sum16(s.x + s.y) * (1.f / D_);
            f32x2 q = {0.f, 0.f};
#pragma unroll
            for (int j = 0; j < 18; ++j) { const f32x2 d = xf[j] - mean; q = __builtin_elementwise_fma(d, d, q); }
            const unsigned sgr = pk_bf16(mean, ln_sd(sum16(q.x + q.y) * (1.f / D_)));
            tag = get_tag();
            const unsigned so = (lane & 15) == 0 ? (unsigned)srow * 8 : 0x80000000u;
            st8_wt(mk_rsrc(ws + WS_ST, S_ * 8), so, (u32x2){sgr, tag});
            st8_wt(mk_rsrc(ws + WS_G2, S_ * 8), so, (u32x2){0u, 0u});
        } else {
            *tagp = epv + 1u;
            asm volatile("s_waitcnt lgkmcnt(0)" ::: "memory");
            tag = get_tag();
        }
        SCHED();
        if constexpr (!CONV) {
            SLEEP(HELPER_DELAY);
            cs_load(v);
            if (!STAT && h == 111) {
                const rsrc_t rf = mk_rsrc(ws + WS_F0, 256 * FSP);
                for (int spin = 0; spin < (1 << 20); ++spin) {
                    bool ok = true;
#pragma unroll
                    for (int i = 0; i < 4; ++i) ok &= __builtin_amdgcn_raw_buffer_load_b32(rf, (i * 64 + lane) * FSP, 0, AUX_RD) == tag;
                    if (__all(ok)) break;
                    SLEEP(8);
                }
                if (lane == 0) __hip_atomic_store((unsigned*)(ws + WS_EP), tag, __ATOMIC_RELAXED, __HIP_MEMORY_SCOPE_AGENT);
            }
            cs_finish(v);
            return;
        }
        if (t3 < 80) {
            const int cpy = t3 / 5, e = t3 % 5, q = e < 3 ? e : 257 + e;
            *(LAS u32x4*)(smem + cpy * C1_COPY + q * 16) = (u32x4){0u, 0u, 0u, 0u};
        } else if (t3 < 84) {
            *(LAS u32x4*)(smem + C1_KVOFF - 64 + (t3 - 80) * 16) = (u32x4){0u, 0u, 0u, 0u};
        }
        if (t3 < 128) *(LAS u32x4*)(smem + C1_KVOFF + (t3 >> 5) * C1_KV + 4096 + (t3 & 31) * 16) = (u32x4){0u, 0u, 0u, 0u};
#pragma unroll
        for (int i = 0; i < 6; ++i) {
            const int task = min(t3 + 192 * i, 1023), u = task & 255;
            const unsigned d[6] = {fa[i].x, fa[i].y, fa[i].z, fa[i].w, u < 255 ? fb[i].x : 0u, u < 255 ? fb[i].y : 0u};
            unsigned o[5];
#pragma unroll
            for (int e = 0; e < 5; ++e) o[e] = __builtin_amdgcn_alignbit(d[e + 1], d[e], 16);
            LAS char* base = (LAS char*)smem + (task >> 8) * C1_FILT + (u + 4) * 16;
            *(LAS u32x4*)(base) = (u32x4){o[0], o[1], o[2], o[3]};
            *(LAS u32x4*)(base + C1_COPY) = (u32x4){d[1], d[2], d[3], d[4]};
            *(LAS u32x4*)(base + 2 * C1_COPY) = (u32x4){o[1], o[2], o[3], o[4]};
            *(LAS u32x4*)(base + 3 * C1_COPY) = (u32x4){d[2], d[3], d[4], d[5]};
            if (u == 0) {
                *(LAS u32x4*)(base - 16) = (u32x4){0u, 0u, 0u, d[0] << 16};
                *(LAS u32x4*)(base - 16 + C1_COPY) = (u32x4){0u, 0u, 0u, d[0]};
                *(LAS u32x4*)(base - 16 + 2 * C1_COPY) = (u32x4){0u, 0u, d[0] << 16, o[0]};
                *(LAS u32x4*)(base - 16 + 3 * C1_COPY) = (u32x4){0u, 0u, d[0], d[1]};
            }
        }
        asm volatile("s_waitcnt lgkmcnt(0)" ::: "memory"); BAR();
        x_rows();
    };
    if (wid >= 5) {
        if (!conv) {
            if (wid < 7) upper(IC<1>{}, IC<0>{}); else upper(IC<0>{}, IC<0>{});
            DIAG_END(ws, 0);
            return;
        }
        if (wid < 7) upper(IC<1>{}, IC<1>{}); else upper(IC<0>{}, IC<1>{});
    } else {
        {
            unsigned kw[11], vw[8];
            const int t0 = idx * 64 + (lane >> 3) * 8, dc = wid * 8 + (lane & 7), kch = xcd * 72 + (dc < 36 ? dc : 35) * 2;
            const rsrc_t rk = mk_rsrc(kin, S_ * D_ * 2), rv = mk_rsrc(vin, S_ * D_ * 2);
            const int off = t0 * (D_ * 2) + kch * 2;
#pragma unroll
            for (int j = 0; j < 11; ++j) kw[j] = __builtin_amdgcn_raw_buffer_load_b32(rk, (unsigned)(off + (j - 3) * (D_ * 2)), 0, 0);
#pragma unroll
            for (int j = 0; j < 8; ++j) vw[j] = __builtin_amdgcn_raw_buffer_load_b32(rv, (unsigned)(off + j * (D_ * 2)), 0, 0);
            const u32x4 sw = *(const u32x4*)(shift_k + kch * 4);
            dvv = ssm_D[(conv ? ch0 : 0) + (wid & 3)];
            SCHED();
            STAMP(1);
            const f32x2 w0 = {bf_lo(sw.x), bf_lo(sw.z)}, w1 = {bf_hi(sw.x), bf_hi(sw.z)}, w2 = {bf_lo(sw.y), bf_lo(sw.w)}, w3 = {bf_hi(sw.y), bf_hi(sw.w)};
            f32x2 kf[11], o[8];
#pragma unroll
            for (int j = 0; j < 11; ++j) kf[j] = (f32x2){bf_lo(kw[j]), bf_hi(kw[j])};
#pragma unroll
            for (int j = 0; j < 8; ++j) {
                f32x2 s = kf[j] * w3;
                s = __builtin_elementwise_fma(kf[j + 1], w2, s);
                s = __builtin_elementwise_fma(kf[j + 2], w1, s);
                s = __builtin_elementwise_fma(kf[j + 3], w0, s);
                o[j] = s * (f32x2){bf_lo(vw[j]), bf_hi(vw[j])};
            }
            const rsrc_t ro = mk_rsrc(ws + WS_KV, S_ * D_ * 2);
            const unsigned oo = dc < 36 ? (unsigned)(kch * (S_ * 2) + (2040 - t0) * 2) : 0x80000000u;
            st16_wt(ro, oo, (u32x4){pk_bf16(o[7].x, o[6].x), pk_bf16(o[5].x, o[4].x), pk_bf16(o[3].x, o[2].x), pk_bf16(o[1].x, o[0].x)});
            st16_wt(ro, oo + S_ * 2, (u32x4){pk_bf16(o[7].y, o[6].y), pk_bf16(o[5].y, o[4].y), pk_bf16(o[3].y, o[2].y), pk_bf16(o[1].y, o[0].y)});
        }
        tag = get_tag();
        WAIT_V(0);
        if (lane == 0) __hip_atomic_store((unsigned*)(ws + WS_F0 + ((wid * 8 + xcd) * 32 + idx) * FSP), tag, __ATOMIC_RELAXED, __HIP_MEMORY_SCOPE_AGENT);
        MARK();
        STAMP(2);
        if (!conv) {
            u32x4 v[12];
            SLEEP(HELPER_DELAY2);
            cs_load(v);
            cs_finish(v);
            DIAG_END(ws, 0);
            return;
        }
        if (wid == 4) {
            x_rows();
            BAR();
#pragma unroll 1
            for (int u = 0; u < WU4; ++u) wd_unit_load(wd, cg * WU4 + u, lane, lds0 + C1_WOFF + u * 3072);
            SLEEP(24);
            const char* gs = ws + WS_ST;
#pragma unroll
            for (int i = 0; i < 4; ++i) {
                dma16_sc1<0>(lane * 16, gs + i * 4096, lds0 + C1_SOFF + i * 4096); dma16_sc1<1024>(lane * 16, gs + i * 4096, lds0 + C1_SOFF + i * 4096);
                dma16_sc1<2048>(lane * 16, gs + i * 4096, lds0 + C1_SOFF + i * 4096); dma16_sc1<3072>(lane * 16, gs + i * 4096, lds0 + C1_SOFF + i * 4096);
            }
            WAIT_V(0);
#pragma unroll 1
            for (int u = 0; u < WU4; ++u) wd_unit_store(ws, cg * WU4 + u, lane, (const LAS char*)smem + C1_WOFF + u * 3072);
        } else {
        {
            wait_kv(tag);
            STAMP(7);
            const char* kvs = ws + WS_KV + (size_t)(ch0 + wid) * (S_ * 2);
            const unsigned m = lds0 + C1_KVOFF + wid * C1_KV;
            dma16_sc1<0>(lane * 16, kvs, m); dma16_sc1<1024>(lane * 16, kvs, m); dma16_sc1<2048>(lane * 16, kvs, m); dma16_sc1<3072>(lane * 16, kvs, m);
            WAIT_V(0);
            STAMP(9);
            BAR();
            STAMP(4);
        }
        const int row = lane & 15, kg = lane >> 4;
        const LAS char* abase = (const LAS char*)smem + wid * C1_FILT + (row & 3) * C1_COPY + (24 + 4 * (row >> 2) - 8 * kg) * 2;
        const LAS char* bbase = (const LAS char*)smem + C1_KVOFF + wid * C1_KV + (2040 - 16 * row - 8 * kg) * 2 - 512 * 7 - 448;
        auto rd_ah = [&](int r, int L, int hf) -> s16x4 { return *(const volatile LAS s16x4*)(abase + 512 * L - 64 * r + 8 * hf); };
        auto rd_b = [&](int r, int m) -> s16x8 { return *(const LAS s16x8*)(bbase + 448 - 64 * r + 512 * (7 - m)); };
        auto cat = [&](s16x4 lo, s16x4 hi) -> s16x8 { return __builtin_shufflevector(lo, hi, 0, 1, 2, 3, 4, 5, 6, 7); };
        f32x4 acc[8];
        s16x4 al[2][8], ah[2][8];
        s16x8 bf[2][8], bd[8];
        auto rd_k = [&](int r, int k) {
            if (k < 2) { if (k == 0) al[r & 1][0] = rd_ah(r, 1, 0); else ah[r & 1][0] = rd_ah(r, 1, 1); }
            else if (k < 10) bf[r & 1][9 - k] = rd_b(r, 8 - k);
            else { const int L = (k - 10) / 2 + 2; if (k & 1) ah[r & 1][L - 1] = rd_ah(r, L, 1); else al[r & 1][L - 1] = rd_ah(r, L, 0); }
        };
        const s16x8 a0 = cat(rd_ah(0, 0, 0), rd_ah(0, 0, 1));
#pragma unroll
        for (int e = 0; e < 8; ++e) bd[e] = rd_b(0, e);
        s16x8 dfr;
        {
            const int j = 8 * kg + 7 - row;
            u32x4 w;
#pragma unroll
            for (int e = 0; e < 4; ++e) w[e] = j == 2 * e ? dvv : (j == 2 * e + 1 ? dvv << 16 : 0u);
            dfr = __builtin_bit_cast(s16x8, w);
            asm volatile("s_nop 1" : "+v"(dfr));
        }
        SCHED();
#pragma unroll
        for (int n = 0; n < 16; ++n) {
            if (n < 8) mfma_first(acc[n], a0, bd[n]); else mfma_acc(acc[n - 8], dfr, bd[n - 8]);
            SCHED();
            if (n == 0) { rd_k(0, 0); rd_k(0, 1); }
            else if (n == 1) rd_k(0, 9);
            else rd_k(0, n + 8);
            SCHED();
        }
#pragma unroll
        for (int r = 0; r < 8; ++r) {
            int k = 0;
#pragma unroll
            for (int L = 1; L <= 8; ++L) {
#pragma unroll
                for (int n = 7; n >= L - 1; --n) {
                    const int e = n - L + 1;
                    mfma_acc(acc[n], cat(al[r & 1][L - 1], ah[r & 1][L - 1]), (r == 0 && e > 0) ? bd[e - 1] : bf[r & 1][e]);
                    SCHED();
                    if (r < 7 && k < 24) rd_k(r + 1, k);
                    ++k;
                    SCHED();
                }
            }
        }
        MFMA_DONE();
#pragma unroll
        for (int n = 0; n < 8; ++n)
            *(LAS u32x2*)(smem + C1_YOFF + wid * 4096 + (256 * n + 16 * row + 4 * kg) * 2) = (u32x2){pk_bf16(acc[n][0], acc[n][1]), pk_bf16(acc[n][2], acc[n][3])};
        }
    }
    STAMP(5);
    asm volatile("s_waitcnt lgkmcnt(0)" ::: "memory"); BAR();
    STAMP(6);
    {
        u32x2 yc[4];
#pragma unroll
        for (int c = 0; c < 4; ++c) yc[c] = *(const LAS u32x2*)(smem + C1_YOFF + c * 4096 + tid * 8);
        const u32x4 xa = *(const LAS u32x4*)(smem + C1_XOFF + tid * 32), xb = *(const LAS u32x4*)(smem + C1_XOFF + tid * 32 + 16);
        const u32x2 xr[4] = {{xa.x, xa.y}, {xa.z, xa.w}, {xb.x, xb.y}, {xb.z, xb.w}};
        u32x4 s01 = *(const LAS u32x4*)(smem + C1_SOFF + tid * 32), s23 = *(const LAS u32x4*)(smem + C1_SOFF + tid * 32 + 16);
        if (__builtin_expect(!__all(s01.y == tag && s01.w == tag && s23.y == tag && s23.w == tag), 0)) {
            const rsrc_t rs = mk_rsrc(ws + WS_ST, S_ * 8);
            for (int spin = 0; spin < (1 << 20); ++spin) {
                s01 = __builtin_amdgcn_raw_buffer_load_b128(rs, tid * 32, 0, AUX_RD);
                s23 = __builtin_amdgcn_raw_buffer_load_b128(rs, tid * 32 + 16, 0, AUX_RD);
                if (__all(s01.y == tag && s01.w == tag && s23.y == tag && s23.w == tag)) break;
                SLEEP(2);
            }
        }
        const unsigned sv[4] = {s01.x, s01.z, s23.x, s23.z};
        float mm[4], rr[4];
#pragma unroll
        for (int j = 0; j < 4; ++j) { mm[j] = bf_lo(sv[j]); rr[j] = __builtin_amdgcn_rcpf(bf_hi(sv[j])); }
        unsigned hw[8];
#pragma unroll
        for (int j = 0; j < 4; ++j) {
#pragma unroll
            for (int e = 0; e < 2; ++e) {
                const unsigned xw = e ? xr[j].y : xr[j].x;
                const unsigned ya = (j >> 1) ? yc[2 * e].y : yc[2 * e].x, yb = (j >> 1) ? yc[2 * e + 1].y : yc[2 * e + 1].x;
                const float y0 = (j & 1) ? bf_hi(ya) : bf_lo(ya), y1 = (j & 1) ? bf_hi(yb) : bf_lo(yb);
                const float x0 = bf_lo(xw), x1 = bf_hi(xw);
                hw[2 * j + e] = pk_bf16(__builtin_fmaf((x0 - mm[j]) * rr[j], y0, x0), __builtin_fmaf((x1 - mm[j]) * rr[j], y1, x1));
            }
        }
        const rsrc_t rh = mk_rsrc(ws + WS_H, S_ * D_ * 2);
        const unsigned ho = (unsigned)(((tid >> 6) * 144 + cg) * 2048 + (tid & 63) * 32);
        st16_wt(rh, ho, (u32x4){hw[0], hw[1], hw[2], hw[3]});
        st16_wt(rh, ho + 16, (u32x4){hw[4], hw[5], hw[6], hw[7]});
    }
    STAMP(8);
    DIAG_END(ws, 0);
}

constexpr int ST_BYTES = 16384 + 2 * 9216;
constexpr int NBUF = 4;
constexpr int G_LDS = NBUF * ST_BYTES;

struct TokC { float nm, c0, c1, q; };
__device__ __forceinline__ unsigned geglu2(f32x2 ag, f32x2 au, f32x2 csg, f32x2 csu, const TokC& t) {
    const f32x2 nm = {t.nm, t.nm}, c0 = {t.c0, t.c0}, c1 = {t.c1, t.c1}, q = {t.q, t.q};
    const f32x2 g = __builtin_elementwise_fma(nm, csg, ag), u = __builtin_elementwise_fma(nm, csu, au);
    const f32x2 z = g * __builtin_elementwise_fma(g * g, c1, c0);
    const f32x2 e = {__builtin_amdgcn_exp2f(z.x), __builtin_amdgcn_exp2f(z.y)};
    const f32x2 d = __builtin_elementwise_fma(e, q, q);
    const f32x2 r = (g * u) * (f32x2){__builtin_amdgcn_rcpf(d.x), __builtin_amdgcn_rcpf(d.y)};
    return pk_bf16(r.x, r.y);
}
#define LO2(v) __builtin_shufflevector(v, v, 0, 1)
#define HI2(v) __builtin_shufflevector(v, v, 2, 3)
#define WAIT_VI(N) asm volatile("s_waitcnt vmcnt(%0)" :: "i"(N) : "memory")

constexpr int G2_W0 = 71680, G2_FL = 69632;
__device__ __forceinline__ void g2_wpiece(int P, int s, const char* wsrc, unsigned aoff, unsigned mw0) {
    const unsigned m = mw0 + (s % 5) * 18432;
    if (P == 0) dma16<0>(aoff, wsrc + (size_t)s * 18432, m);
    if (P == 1) dma16<1024>(aoff, wsrc + (size_t)s * 18432, m);
    if (P == 2) dma16_q<2048>(aoff, wsrc + (size_t)s * 18432, m);
}
constexpr int g2_n(int S) { return (S + 3 < 18 ? 2 : 0) + (S + 4 < 18 ? 3 : 0) + (S == 4 ? 1 : 0); }
constexpr int g2_vm(int S) {
    return S == 0 ? 3 : (S == 1 ? 1 + g2_n(0) : g2_n(S - 2) - (S + 1 < 18 ? 2 : 0) + g2_n(S - 1));
}
#define DIAG_ARG
#define DIAG_PASS
template <int NT>
__device__ __forceinline__ void g2_body(char* ws, u16* __restrict__ out, LAS char* lds, int lane, int wid, int mt, int ks, int nt,
                                        const char* wsrc, unsigned flag2 DIAG_ARG) {
    constexpr int dh = NT == 4;
    const unsigned lds0 = (unsigned)(size_t)lds;
    const int wr = wid & 3;
    const char* asrc = ws + WS_HH + ((size_t)(mt * 4 + ks) * 36) * 8192 + wid * 2048;
    const unsigned aoff = lane * 16;
    const unsigned ma0 = lds0 + wid * 2048, mw0 = lds0 + G2_W0 + wid * 2304;
    auto hpiece = [&](int P, int s) {
        if (P == 0) dma16_sc1<0>(aoff, asrc + (size_t)s * 16384, ma0 + (s & 3) * 16384);
        if (P == 1) dma16_sc1<1024>(aoff, asrc + (size_t)s * 16384, ma0 + (s & 3) * 16384);
    };
    hpiece(0, 0); hpiece(1, 0); hpiece(0, 1); hpiece(1, 1); hpiece(0, 2); hpiece(1, 2);

    const int g4 = lane >> 4;
    const int toff = wr * 2048 + lane * 16;
    const int foff = G2_W0 + dh * 5120 + lane * 16;
    auto rdf = [&](const LAS char* p) -> s16x8 { return *(const LAS s16x8*)p; };
    const rsrc_t rfb = mk_rsrc(ws + WS_FB, 16384);

    f32x4 acc[NT][2];
    s16x8 wf[2 * NT], tf[2][4];
    WAIT_V(4);
    __builtin_amdgcn_raw_buffer_store_b32(1u, rfb, lane == 0 ? flag2 : 0x80000000u, 0, 16);
    BAR();
    STAMP(8);
#pragma unroll
    for (int e = 0; e < 4; ++e) tf[0][e] = rdf(lds + toff + (e >> 1) * 8192 + (e & 1) * 1024);
#pragma unroll
    for (int e = 0; e < 2 * NT; ++e) wf[e] = rdf(lds + foff + (e / NT) * 9216 + (e % NT) * 1024);
    SCHED();

    auto iter = [&](auto sc, s16x8 (&tc)[4], s16x8 (&tn)[4]) {
        constexpr int S = decltype(sc)::v;
        if constexpr (S < 17) {
            WAIT_VI(g2_vm(S));
            BAR();
            if constexpr (S == 7) {
                unsigned fb2 = *(const volatile LAS unsigned*)(lds + G2_FL + wid * 256 + lane * 4);
                if (__builtin_expect(!__all(fb2 == 1u), 0)) {
                    for (int spin = 0; spin < (1 << 20); ++spin) {
                        fb2 = __builtin_amdgcn_raw_buffer_load_b32(rfb, (unsigned)(((mt * 4 + ks) * 2 + 1) * 128 + (lane & 31) * 4), 0, AUX_RD);
                        if (__all(fb2 == 1u)) break;
                        SLEEP(2);
                    }
                }
            }
            SCHED();
        }
        const LAS char* tb = lds + ((S + 1) & 3) * 16384;
        const LAS char* wb = lds + ((S + 1) % 5) * 18432;
#pragma unroll
        for (int e = 0; e < 2 * NT; ++e) {
#pragma unroll
            for (int i = 0; i < 2; ++i) {
                if (S == 0 && e < NT) mfma_first(acc[e % NT][i], wf[e], tc[(e / NT) * 2 + i]);
                else mfma_acc(acc[e % NT][i], wf[e], tc[(e / NT) * 2 + i]);
            }
            SCHED();
            if constexpr (S < 17) {
                wf[e] = rdf(wb + foff + (e / NT) * 9216 + (e % NT) * 1024);
                if (e >= 2 && e < 6) tn[e - 2] = rdf(tb + toff + ((e - 2) >> 1) * 8192 + ((e - 2) & 1) * 1024);
            }
            if constexpr (S + 3 < 18) { if (e < 2) hpiece(e, S + 3); }
            if constexpr (S + 4 < 18) {
                if (e == 3) g2_wpiece(0, S + 4, wsrc, aoff, mw0);
                if (e == 5) g2_wpiece(1, S + 4, wsrc, aoff, mw0);
                if (e == 6) g2_wpiece(2, S + 4, wsrc, aoff, mw0);
            }
            if constexpr (S == 4) {
                if (e == 7) dma4_sc1((unsigned)(lane & 31) * 4, ws + WS_FB + ((mt * 4 + ks) * 2 + 1) * 128, lds0 + G2_FL + wid * 256);
            }
            SCHED();
        }
    };
#define G2_IT2(S) iter(IC<S>{}, tf[0], tf[1]); iter(IC<S + 1>{}, tf[1], tf[0])
    G2_IT2(0); G2_IT2(2); G2_IT2(4); G2_IT2(6); G2_IT2(8); G2_IT2(10); G2_IT2(12);
    STAMP(9);
    G2_IT2(14);
    iter(IC<16>{}, tf[0], tf[1]);
    const int tok0 = mt * 128 + wr * 32 + (lane & 15);
    u32x2 hv[2][5];
    if (wr == ks) {
#pragma unroll
        for (int i = 0; i < 2; ++i) {
            const int t = tok0 + i * 16;
            const char* hp = ws + WS_H + ((size_t)((t >> 8) * 144 + nt * 36 + dh * 20) * 256 + (t & 255)) * 8;
#pragma unroll
            for (int e = 0; e < 4; ++e) hv[i][e] = *(const u32x2*)(hp + ((e >> 1) * 8 + (g4 & 1) * 4 + (g4 >> 1) * 2 + (e & 1)) * 2048);
            if (NT == 5) hv[i][4] = *(const u32x2*)(hp + (16 + g4) * 2048);
        }
    }
    SCHED();
    iter(IC<17>{}, tf[1], tf[0]);
    MFMA_DONE();
    STAMP(10);

    const rsrc_t rx = mk_rsrc(ws + WS_X, X_BYTES);
    const unsigned xt = (unsigned)((mt * 4 + nt) * 4 * 27648 + dh * 4096);
    auto inb = [&](int s) -> unsigned { return xt + (unsigned)(((ks + 1 + s) & 3) * 27648 + (2 - s) * 9216); };
    if (wr != ks) {
        const unsigned o = xt + (unsigned)(ks * 27648 + ((wr - ks - 1) & 3) * 9216);
#pragma unroll
        for (int i = 0; i < 2; ++i) {
            unsigned w[2 * NT];
#pragma unroll
            for (int j = 0; j < NT; ++j) { w[2 * j] = pk_bf16(acc[j][i][0], acc[j][i][1]); w[2 * j + 1] = pk_bf16(acc[j][i][2], acc[j][i][3]); }
#pragma unroll
            for (int e = 0; e < 2 * NT; ++e) w[e] = (w[e] == ~0u) ? 0x7fc07fc0u : w[e];
            if constexpr (NT == 5) st8_wt(rx, o + 8192 + i * 512 + lane * 8, (u32x2){w[8], w[9]});
            st16_wt(rx, o + i * 2048 + lane * 16, (u32x4){w[0], w[1], w[2], w[3]});
            st16_wt(rx, o + i * 2048 + 1024 + lane * 16, (u32x4){w[4], w[5], w[6], w[7]});
        }
    } else {
        u32x4 pa[3][4]; u32x2 pb[3][2];
        SLEEP(16);
        for (int spin = 0; spin < (1 << 18); ++spin) {
            const unsigned t = __builtin_amdgcn_raw_buffer_load_b32(rx, lane < 3 ? inb(lane) + 4092 : 0x80000000u, 0, AUX_RD);
            if (__all(t != ~0u)) {
                STAMP(11);
                unsigned m = 0u;
#pragma unroll
                for (int s = 0; s < 3; ++s) {
#pragma unroll
                    for (int p = 0; p < 4; ++p) pa[s][p] = __builtin_amdgcn_raw_buffer_load_b128(rx, inb(s) + p * 1024 + lane * 16, 0, 16);
                    if constexpr (NT == 5) {
#pragma unroll
                        for (int i = 0; i < 2; ++i) pb[s][i] = __builtin_amdgcn_raw_buffer_load_b64(rx, inb(s) + 8192 + i * 512 + lane * 8, 0, 16);
                    }
                }
#pragma unroll
                for (int s = 0; s < 3; ++s) {
#pragma unroll
                    for (int p = 0; p < 4; ++p) m = max(max(max(m, pa[s][p].x), max(pa[s][p].y, pa[s][p].z)), pa[s][p].w);
                    if constexpr (NT == 5) m = max(max(max(m, pb[s][0].x), max(pb[s][0].y, pb[s][1].x)), pb[s][1].y);
                }
                if (__all(m != ~0u)) break;
            }
            SLEEP(1);
        }
        STAMP(12);
        const rsrc_t ro = mk_rsrc(out, S_ * D_ * 2);
#pragma unroll
        for (int i = 0; i < 2; ++i) {
            const unsigned ob = (unsigned)(((tok0 + i * 16) * D_ + nt * 144 + dh * 80) * 2);
            unsigned r[NT][2];
#pragma unroll
            for (int j = 0; j < NT; ++j) {
                f32x4 t = acc[j][i];
#pragma unroll
                for (int s = 0; s < 3; ++s) {
                    const unsigned w0 = j < 4 ? pa[s][i * 2 + (j >> 1)][(j & 1) * 2] : pb[s][i].x, w1 = j < 4 ? pa[s][i * 2 + (j >> 1)][(j & 1) * 2 + 1] : pb[s][i].y;
                    t += (f32x4){bf_lo(w0), bf_hi(w0), bf_lo(w1), bf_hi(w1)};
                }
                r[j][0] = pk_bf16(t[0], t[1]); r[j][1] = pk_bf16(t[2], t[3]);
            }
            auto addh = [&](unsigned a, unsigned h) -> unsigned { return pk_bf16(bf_lo(h) + bf_lo(a), bf_hi(h) + bf_hi(a)); };
#pragma unroll
            for (int p = 0; p < 2; ++p) {
                auto s0 = __builtin_amdgcn_permlane16_swap(r[2 * p][0], r[2 * p + 1][0], false, false);
                auto s1 = __builtin_amdgcn_permlane16_swap(r[2 * p][1], r[2 * p + 1][1], false, false);
                st16_wt(ro, ob + (unsigned)((p * 32 + (g4 & 1) * 16 + (g4 >> 1) * 8) * 2),
                        (u32x4){addh(s0[0], hv[i][2 * p].x), addh(s1[0], hv[i][2 * p].y), addh(s0[1], hv[i][2 * p + 1].x), addh(s1[1], hv[i][2 * p + 1].y)});
            }
            if constexpr (NT == 5) st8_wt(ro, ob + (unsigned)((64 + g4 * 4) * 2), (u32x2){addh(r[4][0], hv[i][4].x), addh(r[4][1], hv[i][4].y)});
        }
    }
    STAMP(13);
}

constexpr int K3_LDS = 163840;
constexpr int K3_SP = G_LDS;
constexpr int K3_CS = G2_W0 + 4 * 18432;
extern "C" __global__ __launch_bounds__(512, 2) void k3_gemm1(const u16* __restrict__ wg, const u16* __restrict__ wu,
                                                   char* ws, u16* __restrict__ out) {
    extern __shared__ __attribute__((aligned(16))) char smem[];
    LAS char* lds = (LAS char*)smem;
    const unsigned lds0 = (unsigned)(size_t)lds;
    const int tid = threadIdx.x, lane = tid & 63;
    const int wid = __builtin_amdgcn_readfirstlane(tid >> 6);
    const int wr = wid & 3, wc = wid >> 2;
    const int b = blockIdx.x, xcd = b & 7, idx = b >> 3;
    const int rt = (xcd >> 2) * 4 + (idx & 3), ct = (xcd & 3) * 8 + (idx >> 2);
    asm volatile("" :: "s"(wg), "s"(wu), "s"(ws), "s"(out));
    DIAG_DECL;

    const char* asrc = ws + WS_H + (size_t)rt * 144 * 2048 + wid * 2048;
    const unsigned aoff = (unsigned)(lane ^ ((wid & 2) << 2)) * 16;
    const unsigned ma0 = lds0 + wid * 2048, mw0 = lds0 + 16384 + wc * 9216 + wr * 2304;
    dma16<0>(aoff, asrc, ma0); dma16<1024>(aoff, asrc, ma0);
    const char* wsrc = (const char*)(wc ? wu : wg) + ct * 288 - 2048;
    auto wofs = [&](int i) -> unsigned {
        const int q = lane + 64 * i, lrow = 8 * wr + q / 18, pos = q % 18;
        const int krow = ((lrow >> 2) & 3) * 8 + (lrow >> 4) * 4 + (lrow & 3);
        const int col = pos * 8 + (pos < 8 ? 0 : (pos < 16 ? 8 : (((pos & 1) ^ wc) ? 136 : 64) - pos * 8));
        return (unsigned)(krow * (F_ * 2) + col * 2 + 2048 - 1024 * i);
    };
    const unsigned w0 = wofs(0), w1 = wofs(1), w2 = wofs(2);
    auto piece = [&](int P, int s) {
        const unsigned bo = (s & 3) * ST_BYTES;
        const char* wp = wsrc + (size_t)s * (32 * F_ * 2);
        if (P == 0) dma16<0>(aoff, asrc + (size_t)s * 16384, ma0 + bo);
        if (P == 1) dma16<1024>(aoff, asrc + (size_t)s * 16384, ma0 + bo);
        if (P == 2) dma16<0>(w0, wp, mw0 + bo);
        if (P == 3) dma16<1024>(w1, wp, mw0 + bo);
        if (P == 4) dma16_q<2048>(w2, wp, mw0 + bo);
    };
#define K3_PIECE(P, s) piece(P, s)
#define K3_STAGE(s) do { K3_PIECE(0, s); K3_PIECE(1, s); K3_PIECE(2, s); K3_PIECE(3, s); K3_PIECE(4, s); } while (0)
    K3_PIECE(2, 0); K3_PIECE(3, 0); K3_PIECE(4, 0);
    BAR();
    K3_STAGE(1);
    u32x2 hs[4];
    {
        const char* hsrc = ws + WS_H + (size_t)rt * 144 * 2048 + ct * 64;
        const unsigned o = (unsigned)((wid * 18 + (lane >> 3)) * 2048 + (lane & 7) * 8);
        asm volatile("global_load_dwordx2 %0, %1, %2" : "=v"(hs[0]) : "v"(o), "s"(hsrc) : "memory");
        asm volatile("global_load_dwordx2 %0, %1, %2" : "=v"(hs[1]) : "v"(o + 16384), "s"(hsrc) : "memory");
        asm volatile("global_load_dwordx2 %0, %1, %2" : "=v"(hs[2]) : "v"((unsigned)((wid * 18 + 16 + ((lane >> 3) & 1)) * 2048 + (lane & 7) * 8)), "s"(hsrc) : "memory");
        asm volatile("global_load_dwordx2 %0, %1, %2" : "=v"(hs[3]) : "v"((unsigned)(lane & 7) * 8), "s"(hsrc) : "memory");
    }

    const int g4 = lane >> 4, q4 = (lane & 15) >> 2, p4 = lane & 3;
    const int rowoff = (4 * g4 + q4) * 288;
    const int tbase = g4 * 4096 + wr * 512 + (lane & 15) * 8;
    const int toff0 = tbase + ((2 * wc) ^ (g4 & 1)) * 128, toff1 = tbase + ((2 * wc + 1) ^ (g4 & 1)) * 128;
    const int tstep = wc ? -256 : 256;
    const int foff = 16384 + rowoff + wc * 128 + p4 * 8;
    const int moff = 16384 + rowoff + 256 + (p4 < 2 ? 16 * wc + 8 * p4 : 9216 + 16 * (1 - wc) + 8 * (p4 - 2));

    f32x4 acc[9][4];
    s16x8 wf[9], tf[2][4];
    auto rd_w = [&](const LAS char* sb, int j) -> s16x8 {
        return j < 8 ? tr_frag(sb + foff + (j >> 2) * 9216 + (j & 3) * 32) : tr_frag(sb + moff);
    };
    auto rd_t = [&](const LAS char* sb, int i) -> s16x8 {
        const LAS char* p = sb + ((i & 1) ? toff1 : toff0) + (i >> 1) * tstep;
        const s16x4 lo = *(const volatile LAS s16x4*)p, hi = *(const volatile LAS s16x4*)(p + 2048);
        return __builtin_shufflevector(lo, hi, 0, 1, 2, 3, 4, 5, 6, 7);
    };

    STAMP(1);
    WAIT_V(9); BAR();
    STAMP(2);
    wf[0] = rd_w(lds, 0);
#pragma unroll
    for (int i = 0; i < 4; ++i) tf[0][i] = rd_t(lds, i);
#pragma unroll
    for (int j = 1; j < 9; ++j) wf[j] = rd_w(lds, j);
    SCHED();
    const int mt2 = (xcd >> 2) * 8 + (idx & 7), ks2 = xcd & 3, nt2 = idx >> 3;
    const rsrc_t rfill = mk_rsrc(ws + WS_X + (size_t)((mt2 * 4 + nt2) * 4 + ks2) * 27648, 27648);
    auto fill = [&](int k) { st16_wt(rfill, (unsigned)(k * 8192 + tid * 16), (u32x4){~0u, ~0u, ~0u, ~0u}); };
    unsigned sgr;
    {
        const LAS char* sb = lds + ST_BYTES;
        float hc = 0.f;
#pragma unroll
        for (int j = 0; j < 9; ++j) {
#pragma unroll
            for (int i = 0; i < 4; ++i) mfma_first(acc[j][i], wf[j], tf[0][i]);
            SCHED();
            if (j == 0) { K3_PIECE(0, 2); K3_PIECE(1, 2); }
            if (j == 1) { K3_PIECE(2, 2); K3_PIECE(3, 2); }
            if (j == 2) K3_PIECE(4, 2);
            if (j == 3) {
                asm volatile("s_waitcnt vmcnt(5)" : "+v"(hs[0]), "+v"(hs[1]), "+v"(hs[2]), "+v"(hs[3]) :: "memory");
                hc = bf_lo(hs[3].x);
                float s1 = 0.f, s2 = 0.f;
#pragma unroll
                for (int i = 0; i < 3; ++i) {
                    const float d[4] = {bf_lo(hs[i].x) - hc, bf_hi(hs[i].x) - hc, bf_lo(hs[i].y) - hc, bf_hi(hs[i].y) - hc};
                    float a = (d[0] + d[1]) + (d[2] + d[3]), q = __builtin_fmaf(d[0], d[0], d[1] * d[1]) + __builtin_fmaf(d[2], d[2], d[3] * d[3]);
                    if (i == 2 && lane >= 16) { a = 0.f; q = 0.f; }
                    s1 += a; s2 += q;
                }
                s1 = x32_add(x16_add(dpp_add<0x128>(s1))); s2 = x32_add(x16_add(dpp_add<0x128>(s2)));
                if (lane < 8) *(LAS f32x2*)(lds + K3_SP + (lane * 8 + wid) * 8) = (f32x2){s1, s2};
                asm volatile("s_waitcnt lgkmcnt(0)" ::: "memory");
                BAR();
                wf[0] = rd_w(sb, 0); wf[1] = rd_w(sb, 1);
                tf[1][0] = rd_t(sb, 0); tf[1][1] = rd_t(sb, 1);
            }
            if (j == 4) { tf[1][2] = rd_t(sb, 2); tf[1][3] = rd_t(sb, 3); wf[2] = rd_w(sb, 2); wf[3] = rd_w(sb, 3); }
            if (j >= 4) { wf[j] = rd_w(sb, j); K3_PIECE(j - 4, 3); }
            if (j == 8) {
                const f32x2 pp = *(const LAS f32x2*)(lds + K3_SP + (wid * 8 + (lane & 7)) * 8);
                const float s1 = dpp_add<0x141>(dpp_add<0x4E>(dpp_add<0xB1>(pp.x))) * (1.f / D_);
                const float s2 = dpp_add<0x141>(dpp_add<0x4E>(dpp_add<0xB1>(pp.y))) * (1.f / D_);
                const float c0 = __uint_as_float(__builtin_amdgcn_readlane(__float_as_uint(hc), wid));
                const float sd = ln_sd(__builtin_fmaxf(__builtin_fmaf(-s1, s1, s2), 0.f));
                sgr = pk_bf16(c0 + s1, sd);
                fill(0); fill(1);
            }
            SCHED();
        }
    }

    const rsrc_t rg = mk_rsrc(ws + WS_G2 + (size_t)(rt * 256 + wr * 64) * 8, 512);
    const char* w2src = ws + WS_WT + (size_t)(ks2 * 4 + nt2) * 18 * 18432 + wid * 2304;
    u32x2 gr[4];
    s16x4 tl;
    constexpr int TP = 1, DPJ0 = 0, DPJ1 = 5;
    auto iter = [&](auto mode, auto vm, int s, s16x8 (&tc)[4], s16x8 (&tn)[4]) {
        constexpr int MODE = decltype(mode)::v;
        if constexpr (MODE != 4) {
            if constexpr (MODE == 3) WAIT_V(0); else WAIT_VI(5 + decltype(vm)::v);
            BAR();
            SCHED();
        }
        const LAS char* sb = lds + ((s + 1) & 3) * ST_BYTES;
#pragma unroll
        for (int j = 0; j < 9; ++j) {
            s16x4 wl, wh;
#pragma unroll
            for (int i = 0; i < 4; ++i) {
                mfma_acc(acc[j][i], wf[j], tc[i]);
                SCHED();
                if constexpr (MODE != 4) {
                    if (j >= 1) {
                        const LAS char* p = (j - 1 < 8) ? sb + foff + ((j - 1) >> 2) * 9216 + ((j - 1) & 3) * 32 : sb + moff;
                        if (i == 0) wl = __builtin_amdgcn_ds_read_tr16_b64_v4i16((LAS s16x4*)p);
                        if (i == 1) { wh = __builtin_amdgcn_ds_read_tr16_b64_v4i16((LAS s16x4*)(p + 16 * 288)); wf[j - 1] = __builtin_shufflevector(wl, wh, 0, 1, 2, 3, 4, 5, 6, 7); }
                    }
                    if (j >= TP && j < TP + 4) {
                        const int ti = j - TP;
                        const LAS char* p = sb + ((ti & 1) ? toff1 : toff0) + (ti >> 1) * tstep;
                        if (i == 2) tl = *(const volatile LAS s16x4*)p;
                        if (i == 3) tn[ti] = __builtin_shufflevector(tl, *(const volatile LAS s16x4*)(p + 2048), 0, 1, 2, 3, 4, 5, 6, 7);
                    }
                    if (j == 8 && i == 3) wf[8] = rd_w(sb, 8);
                }
                if constexpr (MODE < 2 || MODE == 5) {
                    if (j == DPJ0 && i == 1) K3_PIECE(0, s + 3);
                    if (j == DPJ0 && i == 3) K3_PIECE(1, s + 3);
                    if (j == DPJ1 && i == 3) K3_PIECE(2, s + 3);
                    if (j == DPJ1 + 1 && i == 3) K3_PIECE(3, s + 3);
                    if (j == DPJ1 + 2 && i == 3) K3_PIECE(4, s + 3);
                }
                SCHED();
            }
            if constexpr (MODE == 0) { if (j == 8) { fill(2); fill(3); } }
            if constexpr (MODE == 5) {
                if (j == 0 && s == 4) st8_wt(mk_rsrc(ws + WS_G2, S_ * 8), lane == 0 ? (unsigned)((rt * 256 + ct * 8 + wid) * 8) : 0x80000000u, (u32x2){sgr, 1u});
            }
            if constexpr (MODE == 2) {
                if (j == 0) {
#pragma unroll
                    for (int i = 0; i < 4; ++i) gr[i] = __builtin_amdgcn_raw_buffer_load_b64(rg, (unsigned)(((i ^ (2 * wc)) * 16 + (lane & 15)) * 8), 0, 16);
                }
            }
            if constexpr (MODE == 3) {
                if (j == 0) {
                    asm volatile("" : "+v"(gr[0]), "+v"(gr[1]), "+v"(gr[2]), "+v"(gr[3]));
                    const unsigned co = (unsigned)((lane >= 18 ? F_ * 4 + (lane - 18) * 16 : lane * 16));
                    dma_m36(co, ws + WS_CS + (size_t)(ct * 144 + wc * 72) * 4, lds0 + K3_CS + wid * 1152);
                    dma_m36(co, ws + WS_CS + (size_t)(2 * F_ + ct * 144 + wc * 72) * 4, lds0 + K3_CS + wid * 1152 + 576);
                }
            }
            if constexpr (MODE == 3 || MODE == 4) {
                if (j >= 2 && j < 8) g2_wpiece((j - 2) % 3, (MODE - 3) * 2 + (j - 2) / 3, w2src, lane * 16, lds0 + G2_W0 + wid * 2304);
            }
            SCHED();
        }
    };
    iter(IC<0>{}, IC<2>{}, 1, tf[1], tf[0]);
    iter(IC<1>{}, IC<4>{}, 2, tf[0], tf[1]);
    iter(IC<1>{}, IC<2>{}, 3, tf[1], tf[0]);
#pragma unroll 1
    for (int s = 4; s < 14; s += 2) {
        iter(IC<5>{}, IC<0>{}, s, tf[0], tf[1]);
        iter(IC<1>{}, IC<0>{}, s + 1, tf[1], tf[0]);
    }
    STAMP(3);
    iter(IC<1>{}, IC<0>{}, 14, tf[0], tf[1]);
    iter(IC<2>{}, IC<0>{}, 15, tf[1], tf[0]);
    iter(IC<3>{}, IC<0>{}, 16, tf[0], tf[1]);
    iter(IC<4>{}, IC<0>{}, 17, tf[1], tf[0]);
    MFMA_DONE();
    STAMP(4);
    WAIT_V(12);
    __builtin_amdgcn_s_setprio(1);
    if (__builtin_expect(!__all((gr[0].y & gr[1].y & gr[2].y & gr[3].y) == 1u), 0)) {
        for (int spin = 0; spin < (1 << 20); ++spin) {
            SLEEP(2);
#pragma unroll
            for (int i = 0; i < 4; ++i) gr[i] = __builtin_amdgcn_raw_buffer_load_b64(rg, (unsigned)(((i ^ (2 * wc)) * 16 + (lane & 15)) * 8), 0, AUX_RD);
            if (__all((gr[0].y & gr[1].y & gr[2].y & gr[3].y) == 1u)) break;
        }
    }
    const int mt = rt * 2 + (wr >> 1), ks = ct >> 3, lct = ct & 7;
    const rsrc_t rhh = mk_rsrc(ws + WS_HH, S_ * F_ * 2);
    const unsigned hb = (unsigned)(((mt * 4 + ks) * 36) * 8192 + ((wr & 1) * 4) * 1024);
    const LAS char* csb = lds + K3_CS + wid * 1152 + 16 * g4;
    TokC tcs[4];
#pragma unroll
    for (int i = 0; i < 4; ++i) {
        const float sd = bf_hi(gr[i].x), r = __builtin_amdgcn_rcpf(sd);
        tcs[i] = (TokC){-bf_lo(gr[i].x), -2.30220819f * r, -0.10294324f * (r * r * r), sd * sd};
    }
    {
        const LAS char* cm = lds + K3_CS + wid * 1152 + 256 + 16 * (g4 & 1);
        const f32x4 cgm = *(const LAS f32x4*)cm + *(const LAS f32x4*)(cm + 576), cum = *(const LAS f32x4*)(cm + 288) + *(const LAS f32x4*)(cm + 864);
#pragma unroll
        for (int ip = 0; ip < 2; ++ip) {
            const int ia = ((2 * ip) ^ (2 * wc)) + (lane >> 5);
            const bool up = lane >= 32;
            const TokC tc = {up ? tcs[2 * ip + 1].nm : tcs[2 * ip].nm, up ? tcs[2 * ip + 1].c0 : tcs[2 * ip].c0,
                             up ? tcs[2 * ip + 1].c1 : tcs[2 * ip].c1, up ? tcs[2 * ip + 1].q : tcs[2 * ip].q};
            f32x4 gm, um;
#pragma unroll
            for (int r = 0; r < 4; ++r) {
                auto sw = __builtin_amdgcn_permlane32_swap(__float_as_uint(acc[8][2 * ip][r]), __float_as_uint(acc[8][2 * ip + 1][r]), false, false);
                gm[r] = __uint_as_float(sw[0]); um[r] = __uint_as_float(sw[1]);
            }
            const int kgs = (lct & 1) * 2 + wc;
            st8_wt(rhh, hb + (unsigned)((lct >> 1) * 8192 + ia * 1024 + (kgs * 16 + (lane & 15)) * 16 + (g4 & 1) * 8),
                   (u32x2){geglu2(LO2(gm), LO2(um), LO2(cgm), LO2(cum), tc), geglu2(HI2(gm), HI2(um), HI2(cgm), HI2(cum), tc)});
        }
    }
    SCHED();
    constexpr int FLAGPOS = 1;
    const rsrc_t rfb = mk_rsrc(ws + WS_FB, 16384);
    const unsigned fbo = (unsigned)((mt * 4 + ks) * 256 + (lct * 4 + (wr & 1) * 2 + wc) * 4);
    u32x4 ob[4];
#pragma unroll
    for (int jp = 0; jp < 2; ++jp) {
        f32x4 cg[2], cu[2];
#pragma unroll
        for (int e = 0; e < 2; ++e) {
            cg[e] = *(const LAS f32x4*)(csb + (2 * jp + e) * 64) + *(const LAS f32x4*)(csb + 576 + (2 * jp + e) * 64);
            cu[e] = *(const LAS f32x4*)(csb + 288 + (2 * jp + e) * 64) + *(const LAS f32x4*)(csb + 864 + (2 * jp + e) * 64);
        }
#pragma unroll
        for (int i = 0; i < 4; ++i) {
            unsigned o[4];
#pragma unroll
            for (int e = 0; e < 2; ++e) {
                const f32x4 g = acc[2 * jp + e][i], u = acc[4 + 2 * jp + e][i];
                o[2 * e] = geglu2(LO2(g), LO2(u), LO2(cg[e]), LO2(cu[e]), tcs[i]);
                o[2 * e + 1] = geglu2(HI2(g), HI2(u), HI2(cg[e]), HI2(cu[e]), tcs[i]);
            }
            ob[i] = (u32x4){o[0], o[1], o[2], o[3]};
            st16_wt(rhh, (jp == 1 && wid == 7) ? 0x80000000u : hb + (unsigned)(((2 + jp * 8 + lct) * 2 + wc) * 8192 + (i ^ (2 * wc)) * 1024 + lane * 16), ob[i]);
            if (jp == 1 && i == FLAGPOS) {
                SCHED();
                WAIT_VI(FLAGPOS + 1);
                __builtin_amdgcn_raw_buffer_store_b32(1u, rfb, lane == 0 ? fbo : 0x80000000u, 0, 16);
                SCHED();
            }
        }
        SCHED();
        if (jp == 0) __builtin_amdgcn_s_setprio(0);
    }
    STAMP(5);
    if (wid == 7) {
        for (int spin = 0; spin < (1 << 20); ++spin) {
            const unsigned f = __builtin_amdgcn_raw_buffer_load_b32(rfb, (unsigned)((mt2 * 4 + ks2) * 256 + (lane & 31) * 4), 0, AUX_RD);
            if (__all(f == 1u)) break;
            SLEEP(4);
        }
#pragma unroll
        for (int i = 0; i < 4; ++i) st16_wt(rhh, hb + (unsigned)(((10 + lct) * 2 + wc) * 8192 + (i ^ (2 * wc)) * 1024 + lane * 16), ob[i]);
    }
    BAR();
    STAMP(7);
    if (wid < 4) g2_body<5>(ws, out, lds, lane, wid, mt2, ks2, nt2, w2src, fbo + 128 DIAG_PASS);
    else g2_body<4>(ws, out, lds, lane, wid, mt2, ks2, nt2, w2src, fbo + 128 DIAG_PASS);
    DIAG_END(ws, 1);
}

extern "C" __global__ void k9_end() {}

extern "C" void kernel_launch(void* const* d_in, const int* in_sizes, int n_in, void* d_out, int out_size, void* d_ws,
                              size_t ws_size, hipStream_t stream) {
    const u16* q = (const u16*)d_in[0];
    const u16* k = (const u16*)d_in[1];
    const u16* v = (const u16*)d_in[2];
    const u16* shift_k = (const u16*)d_in[3];
    const u16* ssm_k = (const u16*)d_in[4];
    const u16* ssm_D = (const u16*)d_in[5];
    const u16* wg = (const u16*)d_in[7];
    const u16* wu = (const u16*)d_in[8];
    const u16* wd = (const u16*)d_in[9];
    char* ws = (char*)d_ws;
    (void)hipFuncSetAttribute((const void*)k1_conv, hipFuncAttributeMaxDynamicSharedMemorySize, C1_LDS);
    (void)hipFuncSetAttribute((const void*)k3_gemm1, hipFuncAttributeMaxDynamicSharedMemorySize, K3_LDS);
    k1_conv<<<256, 512, C1_LDS, stream>>>(q, k, v, shift_k, ssm_k, ssm_D, wg, wu, wd, ws);
    k3_gemm1<<<256, 512, K3_LDS, stream>>>(wg, wu, ws, (u16*)d_out);
}
```

```cpp
#include <hip/hip_runtime.h>
#include <hip/hip_bf16.h>

#define LAS __attribute__((address_space(3)))
typedef unsigned short u16;
typedef short s16x4 __attribute__((ext_vector_type(4)));
typedef short s16x8 __attribute__((ext_vector_type(8)));
typedef float f32x2 __attribute__((ext_vector_type(2)));
typedef float f32x4 __attribute__((ext_vector_type(4)));
typedef unsigned u32x2 __attribute__((ext_vector_type(2)));
typedef unsigned u32x4 __attribute__((ext_vector_type(4)));
typedef __bf16 bf16x2_t __attribute__((ext_vector_type(2)));

constexpr int S_ = 2048, D_ = 576, F_ = 4608;

__device__ __forceinline__ unsigned pk_bf16(float lo, float hi) {
    f32x2 v = {lo, hi};
    bf16x2_t b = __builtin_convertvector(v, bf16x2_t);
    return __builtin_bit_cast(unsigned, b);
}
__device__ __forceinline__ float bf_lo(unsigned w) { return __uint_as_float(w << 16); }
__device__ __forceinline__ float bf_hi(unsigned w) { return __uint_as_float(w & 0xffff0000u); }
__device__ __forceinline__ float rbf(float x) { return bf_lo(pk_bf16(x, 0.f)); }
#define WAIT_V(n) asm volatile("s_waitcnt vmcnt(" #n ")" ::: "memory")
#define BAR() do { __builtin_amdgcn_s_barrier(); asm volatile("" ::: "memory"); } while (0)
#define SCHED() __builtin_amdgcn_sched_barrier(0)
#define SLEEP(n) do { __builtin_amdgcn_s_sleep(n); asm volatile("" ::: "memory"); } while (0)
__device__ __forceinline__ void mfma_acc(f32x4& c, s16x8 a, s16x8 b) {
    asm volatile("v_mfma_f32_16x16x32_bf16 %0, %1, %2, %0" : "+v"(c) : "v"(a), "v"(b));
}
__device__ __forceinline__ void mfma_first(f32x4& c, s16x8 a, s16x8 b) {
    asm volatile("v_mfma_f32_16x16x32_bf16 %0, %1, %2, 0" : "=&v"(c) : "v"(a), "v"(b));
}
#define MFMA_DONE() do { asm volatile("s_nop 7\n\ts_nop 7" ::: "memory"); SCHED(); } while (0)
constexpr size_t WS_KV = 0;
constexpr size_t WS_H = WS_KV + (size_t)S_ * D_ * 2;
constexpr size_t WS_ST = WS_H + (size_t)S_ * D_ * 2;
constexpr size_t WS_CS = WS_ST + (size_t)S_ * 8;
constexpr size_t WS_G2 = WS_CS + (size_t)4 * F_ * 4;
constexpr size_t WS_F0 = WS_G2 + (size_t)S_ * 8;
constexpr int FSP = 128;
constexpr size_t WS_EP = WS_F0 + 1280 * FSP;
constexpr size_t WS_HH = WS_EP + 1024;
constexpr size_t WS_X = WS_HH + (size_t)S_ * F_ * 2;
constexpr unsigned X_BYTES = 64 * 4 * 3 * 9216;
constexpr size_t WS_FB = WS_X + X_BYTES;
constexpr size_t WS_WT = WS_FB + 16384;

template <int N> struct IC { static constexpr int v = N; };
constexpr int AUX_RD = (int)(16u | (1u << 31));
typedef __amdgpu_buffer_rsrc_t rsrc_t;
__device__ __forceinline__ rsrc_t mk_rsrc(const void* p, unsigned bytes) { return __builtin_amdgcn_make_buffer_rsrc((void*)p, 0, bytes, 0x00020000); }
__device__ __forceinline__ void st16_wt(rsrc_t r, unsigned off, u32x4 v) { __builtin_amdgcn_raw_buffer_store_b128(v, r, off, 0, 16); }
__device__ __forceinline__ void st8_wt(rsrc_t r, unsigned off, u32x2 v) { __builtin_amdgcn_raw_buffer_store_b64(v, r, off, 0, 16); }
template <int CTRL> __device__ __forceinline__ float dpp_add(float x) {
    return x + __uint_as_float(__builtin_amdgcn_update_dpp(0, (int)__float_as_uint(x), CTRL, 0xf, 0xf, true));
}
__device__ __forceinline__ float x16_add(float x) {
    auto r = __builtin_amdgcn_permlane16_swap(__float_as_uint(x), __float_as_uint(x), false, false);
    return __uint_as_float(r[0]) + __uint_as_float(r[1]);
}
__device__ __forceinline__ float x32_add(float x) {
    auto r = __builtin_amdgcn_permlane32_swap(__float_as_uint(x), __float_as_uint(x), false, false);
    return __uint_as_float(r[0]) + __uint_as_float(r[1]);
}
__device__ __forceinline__ float sum16(float x) {
    x = dpp_add<0xB1>(x); x = dpp_add<0x4E>(x); x = dpp_add<0x141>(x);
    return dpp_add<0x140>(x);
}
__device__ __forceinline__ float ln_sd(float var) { return rbf(__builtin_amdgcn_sqrtf(rbf(rbf(var) + 1e-5f))); }

template <int OFF>
__device__ __forceinline__ void dma16(unsigned voff, const void* sbase, unsigned m0v) {
    asm volatile("s_mov_b32 m0, %2\n\ts_nop 0\n\tglobal_load_lds_dwordx4 %0, %1 offset:%3"
                 :: "v"(voff), "s"(sbase), "s"(m0v), "i"(OFF) : "memory");
}
template <int OFF>
__device__ __forceinline__ void dma16_sc1(unsigned voff, const void* sbase, unsigned m0v) {
    asm volatile("s_mov_b32 m0, %2\n\ts_nop 0\n\tglobal_load_lds_dwordx4 %0, %1 offset:%3 sc1"
                 :: "v"(voff), "s"(sbase), "s"(m0v), "i"(OFF) : "memory");
}
template <int OFF>
__device__ __forceinline__ void dma16_q(unsigned voff, const void* sbase, unsigned m0v) {
    asm volatile("s_mov_b32 m0, %2\n\ts_mov_b64 exec, 0xffff\n\tglobal_load_lds_dwordx4 %0, %1 offset:%3\n\ts_mov_b64 exec, -1"
                 :: "v"(voff), "s"(sbase), "s"(m0v), "i"(OFF) : "memory");
}
__device__ __forceinline__ void dma_m36(unsigned voff, const void* sbase, unsigned m0v) {
    asm volatile("s_mov_b32 m0, %2\n\ts_mov_b32 exec_hi, 15\n\tglobal_load_lds_dwordx4 %0, %1\n\ts_mov_b64 exec, -1"
                 :: "v"(voff), "s"(sbase), "s"(m0v) : "memory");
}
__device__ __forceinline__ void dma4(unsigned voff, const void* sbase, unsigned m0v) {
    asm volatile("s_mov_b32 m0, %2\n\ts_nop 0\n\tglobal_load_lds_dword %0, %1" :: "v"(voff), "s"(sbase), "s"(m0v) : "memory");
}
__device__ __forceinline__ void dma4_sc1(unsigned voff, const void* sbase, unsigned m0v) {
    asm volatile("s_mov_b32 m0, %2\n\ts_nop 0\n\tglobal_load_lds_dword %0, %1 sc1" :: "v"(voff), "s"(sbase), "s"(m0v) : "memory");
}
#define SYM_ADDR(out, sym) asm volatile("s_getpc_b64 s[40:41]\n\ts_add_u32 s40, s40, " #sym "@rel32@lo+4\n\ts_addc_u32 s41, s41, " #sym "@rel32@hi+12\n\ts_mov_b64 %0, s[40:41]" \
                                        : "=s"(out) :: "s40", "s41", "scc")
template <int NI>
__device__ __forceinline__ void warm_code(const char* a, const char* e, int l0, int lane, unsigned m0v) {
    const unsigned last = e > a ? (unsigned)(e - a) - 4u : 0u;
#pragma unroll
    for (int i = 0; i < NI; ++i) dma4(min((unsigned)(l0 + i * 64 + lane) * 128u, last), a, m0v);
}
__device__ __forceinline__ s16x8 tr_frag(const LAS char* p) {
    const s16x4 lo = __builtin_amdgcn_ds_read_tr16_b64_v4i16((LAS s16x4*)p);
    const s16x4 hi = __builtin_amdgcn_ds_read_tr16_b64_v4i16((LAS s16x4*)(p + 16 * 288));
    return __builtin_shufflevector(lo, hi, 0, 1, 2, 3, 4, 5, 6, 7);
}
__device__ __forceinline__ void wd_unit_load(const u16* wd, int un, int lane, unsigned m0v) {
    const int uB = un / 3, ut = un % 3, kh = uB & 1, st = (uB >> 1) % 18, kn = uB / 36;
    const bool full = st >= 2;
    const int f0 = (kn >> 2) * 1152 + (full ? ((st - 2) & 7) * 144 + kh * 72 + ((st - 2) >> 3) * 32 : (st * 2 + kh) * 288);
    const char* src = (const char*)wd + (size_t)f0 * (D_ * 2) + (kn & 3) * 288 + ut * 96;
#pragma unroll
    for (int i = 0; i < 3; ++i) {
        const int q = lane + 64 * i, r = q / 6, pos = q % 6, kg = (r & 15) >> 2;
        const int f = full ? r : (kg >> 1) * 144 + (kg & 1) * 72 + 64 + (r & 3) + 4 * (r >> 4);
        dma16<0>((unsigned)(f * (D_ * 2) + pos * 16), src, m0v + i * 1024);
    }
}
__device__ __forceinline__ void wd_unit_store(char* ws, int un, int lane, const LAS char* l) {
    const rsrc_t r = mk_rsrc(ws + WS_WT, F_ * D_ * 2);
#pragma unroll
    for (int t = 0; t < 3; ++t) {
        const LAS char* tp = l + (lane >> 2) * 96 + (lane & 3) * 8 + t * 32;
        const s16x4 lo = __builtin_amdgcn_ds_read_tr16_b64_v4i16((LAS s16x4*)tp), hi = __builtin_amdgcn_ds_read_tr16_b64_v4i16((LAS s16x4*)(tp + 16 * 96));
        st16_wt(r, (unsigned)(un * 3072 + t * 1024 + lane * 16), __builtin_bit_cast(u32x4, __builtin_shufflevector(lo, hi, 0, 1, 2, 3, 4, 5, 6, 7)));
    }
}
constexpr int WU4 = 3;
constexpr int HELPER_DELAY = 48, HELPER_DELAY2 = 24, POLL0 = 16, POLL1 = 1;
constexpr int C1_COPY = 4288;
constexpr int C1_FILT = 4 * C1_COPY;
constexpr int C1_KV = 2304 * 2;
constexpr int C1_KVOFF = 4 * C1_FILT + 64;
constexpr int C1_YOFF = C1_KVOFF + 4 * C1_KV;
constexpr int C1_XOFF = C1_YOFF + 16384;
constexpr int C1_SOFF = C1_XOFF + 16384;
constexpr int C1_TOFF = C1_SOFF + 16384;
constexpr int C1_WOFF = C1_TOFF + 2048;
constexpr int C1_LDS = C1_WOFF + 24576;

extern "C" __global__ __launch_bounds__(512, 2) void k1_conv(const u16* __restrict__ x, const u16* __restrict__ kin, const u16* __restrict__ vin,
                                                  const u16* __restrict__ shift_k, const u16* __restrict__ ssm_k, const u16* __restrict__ ssm_D,
                                                  const u16* __restrict__ wg, const u16* __restrict__ wu, const u16* __restrict__ wd,
                                                  char* ws) {
    extern __shared__ __attribute__((aligned(16))) char smem[];
    const int tid = threadIdx.x, lane = tid & 63;
    const int wid = __builtin_amdgcn_readfirstlane(tid >> 6);
    const int b = blockIdx.x, xcd = b & 7, idx = b >> 3;
    {
        const char *c1, *c3;
        SYM_ADDR(c1, k1_conv); SYM_ADDR(c3, k3_gemm1);
        if (wid >= 4) warm_code<1>(c1, c3, (wid & 3) * 64, lane, (unsigned)(size_t)(LAS char*)smem + C1_TOFF + 256 + (wid & 3) * 256);
    }
    asm volatile("" :: "s"(x), "s"(kin), "s"(vin), "s"(shift_k), "s"(ssm_k), "s"(ssm_D), "s"(wg), "s"(wu), "s"(wd), "s"(ws));
    const bool conv = idx < 18;
    const int cg = xcd * 18 + idx, ch0 = cg * 4;
    const unsigned lds0 = (unsigned)(size_t)(LAS char*)smem;
    LAS unsigned* tagp = (LAS unsigned*)(smem + C1_TOFF);
    auto get_tag = [&]() -> unsigned { BAR(); return __builtin_amdgcn_readfirstlane(*(volatile LAS unsigned*)tagp); };

    const int h = (idx - 18) * 8 + xcd, hc_ = h < 108 ? h : 0;
    const int m3 = hc_ % 3, lcA = (hc_ / 3) * 4 + m3, rb0 = m3 * 3;
    constexpr int HU = (1728 - 144 * WU4 + 111) / 112;
    const int hbase = 144 * WU4 + h * HU, hun = hbase + wid * 2, hend = min(hbase + HU, 1728);
    auto cs_load = [&](u32x4 (&v)[12]) {
        const int rs = tid >> 3, c8 = tid & 7;
#pragma unroll
        for (int p = 0; p < 12; ++p) {
            const int rb = rb0 + p, lc = lcA + (rb >= 9), r = (rb >= 9 ? rb - 9 : rb) * 64 + rs;
            v[p] = *(const u32x4*)((lc >= 72 ? wu : wg) + (size_t)r * F_ + (lc % 72) * 64 + c8 * 8);
        }
        SCHED();
#pragma unroll
        for (int u = 0; u < 2; ++u) if (hun + u < hend) wd_unit_load(wd, hun + u, lane, lds0 + 16384 + (wid * 2 + u) * 3072);
        st16_wt(mk_rsrc(ws + WS_FB, 16384), (unsigned)(h * 8192 + tid * 16), (u32x4){0u, 0u, 0u, 0u});
        SCHED();
    };
    auto cs_finish = [&](u32x4 (&v)[12]) {
        float g[4][8];
#pragma unroll
        for (int q = 0; q < 4; ++q) {
#pragma unroll
            for (int e = 0; e < 4; ++e) {
                g[q][2 * e] = (bf_lo(v[3 * q][e]) + bf_lo(v[3 * q + 1][e])) + bf_lo(v[3 * q + 2][e]);
                g[q][2 * e + 1] = (bf_hi(v[3 * q][e]) + bf_hi(v[3 * q + 1][e])) + bf_hi(v[3 * q + 2][e]);
            }
        }
        LAS float* red = (LAS float*)smem;
#pragma unroll
        for (int l = 0; l < 2; ++l) {
            float a[8];
#pragma unroll
            for (int e = 0; e < 8; ++e) {
                a[e] = l == 0 ? g[0][e] + (m3 <= 1 ? g[1][e] : 0.f) + (m3 == 0 ? g[2][e] : 0.f) : g[3][e] + (m3 >= 1 ? g[2][e] : 0.f) + (m3 == 2 ? g[1][e] : 0.f);
                a[e] = x32_add(x16_add(dpp_add<0x128>(a[e])));
            }
            if (lane < 8) {
                *(LAS f32x4*)(red + (wid * 2 + l) * 64 + lane * 8) = (f32x4){a[0], a[1], a[2], a[3]};
                *(LAS f32x4*)(red + (wid * 2 + l) * 64 + lane * 8 + 4) = (f32x4){a[4], a[5], a[6], a[7]};
            }
        }
        WAIT_V(1);
#pragma unroll
        for (int u = 0; u < 2; ++u) if (hun + u < hend) wd_unit_store(ws, hun + u, lane, (const LAS char*)smem + 16384 + (wid * 2 + u) * 3072);
        asm volatile("s_waitcnt lgkmcnt(0)" ::: "memory"); BAR();
        if (tid < 192 && h < 108) {
            float s = 0.f;
            if (tid < 128) {
#pragma unroll
                for (int w = 0; w < 8; ++w) s += red[w * 128 + tid];
            }
            const int part = tid >> 6;
            const int lc = part == 0 ? lcA : (part == 1 ? lcA + 1 : (m3 == 0 ? lcA : lcA + 1));
            const int slot = part == 0 ? (m3 != 0) : (part == 1 ? 0 : 1);
            if (part < 2 || m3 != 1)
                __builtin_amdgcn_raw_buffer_store_b32(__float_as_uint(s), mk_rsrc(ws + WS_CS, 4 * F_ * 4), (unsigned)((slot * 2 * F_ + lc * 64 + (tid & 63)) * 4), 0, 16);
        }
    };
    auto wait_kv = [&](unsigned tag) {
        const rsrc_t rf = mk_rsrc(ws + WS_F0 + ((idx >> 2) * 8 + xcd) * 32 * FSP, 32 * FSP);
        SLEEP(POLL0);
        for (int spin = 0; spin < (1 << 20); ++spin) {
            const unsigned f = __builtin_amdgcn_raw_buffer_load_b32(rf, (lane & 31) * FSP, 0, AUX_RD);
            if (__all(f == tag)) break;
            SLEEP(POLL1);
        }
    };

    auto x_rows = [&]() {
        u32x2 xr[8];
#pragma unroll
        for (int i = 0; i < 8; ++i) xr[i] = *(const u32x2*)(x + (size_t)((tid - 256) + (wid - 4) * 448 + i * 64) * D_ + ch0);
#pragma unroll
        for (int i = 0; i < 8; ++i) *(LAS u32x2*)(smem + C1_XOFF + ((tid - 256) + (wid - 4) * 448 + i * 64) * 8) = xr[i];
        SCHED();
    };
    unsigned tag;
    unsigned dvv = 0;
    auto upper = [&](auto is_stat, auto is_conv) {
        constexpr bool STAT = decltype(is_stat)::v, CONV = decltype(is_conv)::v;
        const int srow = b * 8 + (wid - 5) * 4 + (lane >> 4), t3 = tid - 320;
        unsigned xw[18], epv = 0;
        if constexpr (STAT) {
            const unsigned* xr = (const unsigned*)(x + (size_t)srow * D_) + (lane & 15);
#pragma unroll
            for (int j = 0; j < 18; ++j) xw[j] = xr[16 * j];
        } else {
            epv = __hip_atomic_load((const unsigned*)(ws + WS_EP), __ATOMIC_RELAXED, __HIP_MEMORY_SCOPE_AGENT);
        }
        SCHED();
        if constexpr (STAT) {
            const char *c3, *c4;
            SYM_ADDR(c3, k3_gemm1); SYM_ADDR(c4, k9_end);
            warm_code<1>(c3, c4, ((idx & 3) * 2 + wid - 5) * 64, lane, lds0 + C1_TOFF + 256 + (wid - 5) * 256);
        }
        SCHED();
        u32x4 v[12], fa[6]; u32x2 fb[6];
        if constexpr (CONV) {
#pragma unroll
            for (int i = 0; i < 6; ++i) {
                const int task = min(t3 + 192 * i, 1023), u = task & 255;
                const u16* src = ssm_k + (size_t)(ch0 + (task >> 8)) * S_ + u * 8;
                fa[i] = *(const u32x4*)src;
                fb[i] = *(const u32x2*)(src + (u < 255 ? 8 : 0));
            }
            SCHED();
        }
        if constexpr (STAT) {
            f32x2 xf[18], s = {0.f, 0.f};
#pragma unroll
            for (int j = 0; j < 18; ++j) { xf[j] = (f32x2){bf_lo(xw[j]), bf_hi(xw[j])}; s += xf[j]; }
            const float mean = sum16(s.x + s.y) * (1.f / D_);
            f32x2 q = {0.f, 0.f};
#pragma unroll
            for (int j = 0; j < 18; ++j) { const f32x2 d = xf[j] - mean; q = __builtin_elementwise_fma(d, d, q); }
            const unsigned sgr = pk_bf16(mean, ln_sd(sum16(q.x + q.y) * (1.f / D_)));
            tag = get_tag();
            const unsigned so = (lane & 15) == 0 ? (unsigned)srow * 8 : 0x80000000u;
            st8_wt(mk_rsrc(ws + WS_ST, S_ * 8), so, (u32x2){sgr, tag});
            st8_wt(mk_rsrc(ws + WS_G2, S_ * 8), so, (u32x2){0u, 0u});
        } else {
            *tagp = epv + 1u;
            asm volatile("s_waitcnt lgkmcnt(0)" ::: "memory");
            tag = get_tag();
        }
        SCHED();
        if constexpr (!CONV) {
            SLEEP(HELPER_DELAY);
            cs_load(v);
            if (!STAT && h == 111) {
                const rsrc_t rf = mk_rsrc(ws + WS_F0, 256 * FSP);
                for (int spin = 0; spin < (1 << 20); ++spin) {
                    bool ok = true;
#pragma unroll
                    for (int i = 0; i < 4; ++i) ok &= __builtin_amdgcn_raw_buffer_load_b32(rf, (i * 64 + lane) * FSP, 0, AUX_RD) == tag;
                    if (__all(ok)) break;
                    SLEEP(8);
                }
                if (lane == 0) __hip_atomic_store((unsigned*)(ws + WS_EP), tag, __ATOMIC_RELAXED, __HIP_MEMORY_SCOPE_AGENT);
            }
            cs_finish(v);
            return;
        }
        if (t3 < 80) {
            const int cpy = t3 / 5, e = t3 % 5, q = e < 3 ? e : 257 + e;
            *(LAS u32x4*)(smem + cpy * C1_COPY + q * 16) = (u32x4){0u, 0u, 0u, 0u};
        } else if (t3 < 84) {
            *(LAS u32x4*)(smem + C1_KVOFF - 64 + (t3 - 80) * 16) = (u32x4){0u, 0u, 0u, 0u};
        }
        if (t3 < 128) *(LAS u32x4*)(smem + C1_KVOFF + (t3 >> 5) * C1_KV + 4096 + (t3 & 31) * 16) = (u32x4){0u, 0u, 0u, 0u};
#pragma unroll
        for (int i = 0; i < 6; ++i) {
            const int task = min(t3 + 192 * i, 1023), u = task & 255;
            const unsigned d[6] = {fa[i].x, fa[i].y, fa[i].z, fa[i].w, u < 255 ? fb[i].x : 0u, u < 255 ? fb[i].y : 0u};
            unsigned o[5];
#pragma unroll
            for (int e = 0; e < 5; ++e) o[e] = __builtin_amdgcn_alignbit(d[e + 1], d[e], 16);
            LAS char* base = (LAS char*)smem + (task >> 8) * C1_FILT + (u + 4) * 16;
            *(LAS u32x4*)(base) = (u32x4){o[0], o[1], o[2], o[3]};
            *(LAS u32x4*)(base + C1_COPY) = (u32x4){d[1], d[2], d[3], d[4]};
            *(LAS u32x4*)(base + 2 * C1_COPY) = (u32x4){o[1], o[2], o[3], o[4]};
            *(LAS u32x4*)(base + 3 * C1_COPY) = (u32x4){d[2], d[3], d[4], d[5]};
            if (u == 0) {
                *(LAS u32x4*)(base - 16) = (u32x4){0u, 0u, 0u, d[0] << 16};
                *(LAS u32x4*)(base - 16 + C1_COPY) = (u32x4){0u, 0u, 0u, d[0]};
                *(LAS u32x4*)(base - 16 + 2 * C1_COPY) = (u32x4){0u, 0u, d[0] << 16, o[0]};
                *(LAS u32x4*)(base - 16 + 3 * C1_COPY) = (u32x4){0u, 0u, d[0], d[1]};
            }
        }
        asm volatile("s_waitcnt lgkmcnt(0)" ::: "memory"); BAR();
        x_rows();
    };
    if (wid >= 5) {
        if (!conv) {
            if (wid < 7) upper(IC<1>{}, IC<0>{}); else upper(IC<0>{}, IC<0>{});
            return;
        }
        if (wid < 7) upper(IC<1>{}, IC<1>{}); else upper(IC<0>{}, IC<1>{});
    } else {
        {
            unsigned kw[11], vw[8];
            const int t0 = idx * 64 + (lane >> 3) * 8, dc = wid * 8 + (lane & 7), kch = xcd * 72 + (dc < 36 ? dc : 35) * 2;
            const rsrc_t rk = mk_rsrc(kin, S_ * D_ * 2), rv = mk_rsrc(vin, S_ * D_ * 2);
            const int off = t0 * (D_ * 2) + kch * 2;
#pragma unroll
            for (int j = 0; j < 11; ++j) kw[j] = __builtin_amdgcn_raw_buffer_load_b32(rk, (unsigned)(off + (j - 3) * (D_ * 2)), 0, 0);
#pragma unroll
            for (int j = 0; j < 8; ++j) vw[j] = __builtin_amdgcn_raw_buffer_load_b32(rv, (unsigned)(off + j * (D_ * 2)), 0, 0);
            const u32x4 sw = *(const u32x4*)(shift_k + kch * 4);
            dvv = ssm_D[(conv ? ch0 : 0) + (wid & 3)];
            SCHED();
            const f32x2 w0 = {bf_lo(sw.x), bf_lo(sw.z)}, w1 = {bf_hi(sw.x), bf_hi(sw.z)}, w2 = {bf_lo(sw.y), bf_lo(sw.w)}, w3 = {bf_hi(sw.y), bf_hi(sw.w)};
            f32x2 kf[11], o[8];
#pragma unroll
            for (int j = 0; j < 11; ++j) kf[j] = (f32x2){bf_lo(kw[j]), bf_hi(kw[j])};
#pragma unroll
            for (int j = 0; j < 8; ++j) {
                f32x2 s = kf[j] * w3;
                s = __builtin_elementwise_fma(kf[j + 1], w2, s);
                s = __builtin_elementwise_fma(kf[j + 2], w1, s);
                s = __builtin_elementwise_fma(kf[j + 3], w0, s);
                o[j] = s * (f32x2){bf_lo(vw[j]), bf_hi(vw[j])};
            }
            const rsrc_t ro = mk_rsrc(ws + WS_KV, S_ * D_ * 2);
            const unsigned oo = dc < 36 ? (unsigned)(kch * (S_ * 2) + (2040 - t0) * 2) : 0x80000000u;
            st16_wt(ro, oo, (u32x4){pk_bf16(o[7].x, o[6].x), pk_bf16(o[5].x, o[4].x), pk_bf16(o[3].x, o[2].x), pk_bf16(o[1].x, o[0].x)});
            st16_wt(ro, oo + S_ * 2, (u32x4){pk_bf16(o[7].y, o[6].y), pk_bf16(o[5].y, o[4].y), pk_bf16(o[3].y, o[2].y), pk_bf16(o[1].y, o[0].y)});
        }
        tag = get_tag();
        WAIT_V(0);
        if (lane == 0) __hip_atomic_store((unsigned*)(ws + WS_F0 + ((wid * 8 + xcd) * 32 + idx) * FSP), tag, __ATOMIC_RELAXED, __HIP_MEMORY_SCOPE_AGENT);
        if (!conv) {
            u32x4 v[12];
            SLEEP(HELPER_DELAY2);
            cs_load(v);
            cs_finish(v);
            return;
        }
        if (wid == 4) {
            x_rows();
            BAR();
#pragma unroll 1
            for (int u = 0; u < WU4; ++u) wd_unit_load(wd, cg * WU4 + u, lane, lds0 + C1_WOFF + u * 3072);
            SLEEP(24);
            const char* gs = ws + WS_ST;
#pragma unroll
            for (int i = 0; i < 4; ++i) {
                dma16_sc1<0>(lane * 16, gs + i * 4096, lds0 + C1_SOFF + i * 4096); dma16_sc1<1024>(lane * 16, gs + i * 4096, lds0 + C1_SOFF + i * 4096);
                dma16_sc1<2048>(lane * 16, gs + i * 4096, lds0 + C1_SOFF + i * 4096); dma16_sc1<3072>(lane * 16, gs + i * 4096, lds0 + C1_SOFF + i * 4096);
            }
            WAIT_V(0);
#pragma unroll 1
            for (int u = 0; u < WU4; ++u) wd_unit_store(ws, cg * WU4 + u, lane, (const LAS char*)smem + C1_WOFF + u * 3072);
        } else {
        {
            wait_kv(tag);
            const char* kvs = ws + WS_KV + (size_t)(ch0 + wid) * (S_ * 2);
            const unsigned m = lds0 + C1_KVOFF + wid * C1_KV;
            dma16_sc1<0>(lane * 16, kvs, m); dma16_sc1<1024>(lane * 16, kvs, m); dma16_sc1<2048>(lane * 16, kvs, m); dma16_sc1<3072>(lane * 16, kvs, m);
            WAIT_V(0);
            BAR();
        }
        const int row = lane & 15, kg = lane >> 4;
        const LAS char* abase = (const LAS char*)smem + wid * C1_FILT + (row & 3) * C1_COPY + (24 + 4 * (row >> 2) - 8 * kg) * 2;
        const LAS char* bbase = (const LAS char*)smem + C1_KVOFF + wid * C1_KV + (2040 - 16 * row - 8 * kg) * 2 - 512 * 7 - 448;
        auto rd_ah = [&](int r, int L, int hf) -> s16x4 { return *(const volatile LAS s16x4*)(abase + 512 * L - 64 * r + 8 * hf); };
        auto rd_b = [&](int r, int m) -> s16x8 { return *(const LAS s16x8*)(bbase + 448 - 64 * r + 512 * (7 - m)); };
        auto cat = [&](s16x4 lo, s16x4 hi) -> s16x8 { return __builtin_shufflevector(lo, hi, 0, 1, 2, 3, 4, 5, 6, 7); };
        f32x4 acc[8];
        s16x4 al[2][8], ah[2][8];
        s16x8 bf[2][8], bd[8];
        auto rd_k = [&](int r, int k) {
            if (k < 2) { if (k == 0) al[r & 1][0] = rd_ah(r, 1, 0); else ah[r & 1][0] = rd_ah(r, 1, 1); }
            else if (k < 10) bf[r & 1][9 - k] = rd_b(r, 8 - k);
            else { const int L = (k - 10) / 2 + 2; if (k & 1) ah[r & 1][L - 1] = rd_ah(r, L, 1); else al[r & 1][L - 1] = rd_ah(r, L, 0); }
        };
        const s16x8 a0 = cat(rd_ah(0, 0, 0), rd_ah(0, 0, 1));
#pragma unroll
        for (int e = 0; e < 8; ++e) bd[e] = rd_b(0, e);
        s16x8 dfr;
        {
            const int j = 8 * kg + 7 - row;
            u32x4 w;
#pragma unroll
            for (int e = 0; e < 4; ++e) w[e] = j == 2 * e ? dvv : (j == 2 * e + 1 ? dvv << 16 : 0u);
            dfr = __builtin_bit_cast(s16x8, w);
            asm volatile("s_nop 1" : "+v"(dfr));
        }
        SCHED();
#pragma unroll
        for (int n = 0; n < 16; ++n) {
            if (n < 8) mfma_first(acc[n], a0, bd[n]); else mfma_acc(acc[n - 8], dfr, bd[n - 8]);
            SCHED();
            if (n == 0) { rd_k(0, 0); rd_k(0, 1); }
            else if (n == 1) rd_k(0, 9);
            else rd_k(0, n + 8);
            SCHED();
        }
#pragma unroll
        for (int r = 0; r < 8; ++r) {
            int k = 0;
#pragma unroll
            for (int L = 1; L <= 8; ++L) {
#pragma unroll
                for (int n = 7; n >= L - 1; --n) {
                    const int e = n - L + 1;
                    mfma_acc(acc[n], cat(al[r & 1][L - 1], ah[r & 1][L - 1]), (r == 0 && e > 0) ? bd[e - 1] : bf[r & 1][e]);
                    SCHED();
                    if (r < 7 && k < 24) rd_k(r + 1, k);
                    ++k;
                    SCHED();
                }
            }
        }
        MFMA_DONE();
#pragma unroll
        for (int n = 0; n < 8; ++n)
            *(LAS u32x2*)(smem + C1_YOFF + wid * 4096 + (256 * n + 16 * row + 4 * kg) * 2) = (u32x2){pk_bf16(acc[n][0], acc[n][1]), pk_bf16(acc[n][2], acc[n][3])};
        }
    }
    asm volatile("s_waitcnt lgkmcnt(0)" ::: "memory"); BAR();
    {
        u32x2 yc[4];
#pragma unroll
        for (int c = 0; c < 4; ++c) yc[c] = *(const LAS u32x2*)(smem + C1_YOFF + c * 4096 + tid * 8);
        const u32x4 xa = *(const LAS u32x4*)(smem + C1_XOFF + tid * 32), xb = *(const LAS u32x4*)(smem + C1_XOFF + tid * 32 + 16);
        const u32x2 xr[4] = {{xa.x, xa.y}, {xa.z, xa.w}, {xb.x, xb.y}, {xb.z, xb.w}};
        u32x4 s01 = *(const LAS u32x4*)(smem + C1_SOFF + tid * 32), s23 = *(const LAS u32x4*)(smem + C1_SOFF + tid * 32 + 16);
        if (__builtin_expect(!__all(s01.y == tag && s01.w == tag && s23.y == tag && s23.w == tag), 0)) {
            const rsrc_t rs = mk_rsrc(ws + WS_ST, S_ * 8);
            for (int spin = 0; spin < (1 << 20); ++spin) {
                s01 = __builtin_amdgcn_raw_buffer_load_b128(rs, tid * 32, 0, AUX_RD);
                s23 = __builtin_amdgcn_raw_buffer_load_b128(rs, tid * 32 + 16, 0, AUX_RD);
                if (__all(s01.y == tag && s01.w == tag && s23.y == tag && s23.w == tag)) break;
                SLEEP(2);
            }
        }
        const unsigned sv[4] = {s01.x, s01.z, s23.x, s23.z};
        float mm[4], rr[4];
#pragma unroll
        for (int j = 0; j < 4; ++j) { mm[j] = bf_lo(sv[j]); rr[j] = __builtin_amdgcn_rcpf(bf_hi(sv[j])); }
        unsigned hw[8];
#pragma unroll
        for (int j = 0; j < 4; ++j) {
#pragma unroll
            for (int e = 0; e < 2; ++e) {
                const unsigned xw = e ? xr[j].y : xr[j].x;
                const unsigned ya = (j >> 1) ? yc[2 * e].y : yc[2 * e].x, yb = (j >> 1) ? yc[2 * e + 1].y : yc[2 * e + 1].x;
                const float y0 = (j & 1) ? bf_hi(ya) : bf_lo(ya), y1 = (j & 1) ? bf_hi(yb) : bf_lo(yb);
                const float x0 = bf_lo(xw), x1 = bf_hi(xw);
                hw[2 * j + e] = pk_bf16(__builtin_fmaf((x0 - mm[j]) * rr[j], y0, x0), __builtin_fmaf((x1 - mm[j]) * rr[j], y1, x1));
            }
        }
        const rsrc_t rh = mk_rsrc(ws + WS_H, S_ * D_ * 2);
        const unsigned ho = (unsigned)(((tid >> 6) * 144 + cg) * 2048 + (tid & 63) * 32);
        st16_wt(rh, ho, (u32x4){hw[0], hw[1], hw[2], hw[3]});
        st16_wt(rh, ho + 16, (u32x4){hw[4], hw[5], hw[6], hw[7]});
    }
}

constexpr int ST_BYTES = 16384 + 2 * 9216;
constexpr int G_LDS = 4 * ST_BYTES;

struct TokC { float nm, c0, c1, q; };
__device__ __forceinline__ unsigned geglu2(f32x2 ag, f32x2 au, f32x2 csg, f32x2 csu, const TokC& t) {
    const f32x2 nm = {t.nm, t.nm}, c0 = {t.c0, t.c0}, c1 = {t.c1, t.c1}, q = {t.q, t.q};
    const f32x2 g = __builtin_elementwise_fma(nm, csg, ag), u = __builtin_elementwise_fma(nm, csu, au);
    const f32x2 z = g * __builtin_elementwise_fma(g * g, c1, c0);
    const f32x2 e = {__builtin_amdgcn_exp2f(z.x), __builtin_amdgcn_exp2f(z.y)};
    const f32x2 d = __builtin_elementwise_fma(e, q, q);
    const f32x2 r = (g * u) * (f32x2){__builtin_amdgcn_rcpf(d.x), __builtin_amdgcn_rcpf(d.y)};
    return pk_bf16(r.x, r.y);
}
#define LO2(v) __builtin_shufflevector(v, v, 0, 1)
#define HI2(v) __builtin_shufflevector(v, v, 2, 3)
#define WAIT_VI(N) asm volatile("s_waitcnt vmcnt(%0)" :: "i"(N) : "memory")

constexpr int G2_W0 = 71680, G2_FL = 69632;
__device__ __forceinline__ void g2_wpiece(int P, int s, const char* wsrc, unsigned aoff, unsigned mw0) {
    const unsigned m = mw0 + (s % 5) * 18432;
    if (P == 0) dma16<0>(aoff, wsrc + (size_t)s * 18432, m);
    if (P == 1) dma16<1024>(aoff, wsrc + (size_t)s * 18432, m);
    if (P == 2) dma16_q<2048>(aoff, wsrc + (size_t)s * 18432, m);
}
constexpr int g2_n(int S) { return (S + 3 < 18 ? 2 : 0) + (S + 4 < 18 ? 3 : 0) + (S == 4 ? 1 : 0); }
constexpr int g2_vm(int S) {
    return S == 0 ? 3 : (S == 1 ? 1 + g2_n(0) : g2_n(S - 2) - (S + 1 < 18 ? 2 : 0) + g2_n(S - 1));
}
template <int NT>
__device__ __forceinline__ void g2_body(char* ws, u16* __restrict__ out, LAS char* lds, int lane, int wid, int mt, int ks, int nt,
                                        const char* wsrc, unsigned flag2) {
    constexpr int dh = NT == 4;
    const unsigned lds0 = (unsigned)(size_t)lds;
    const int wr = wid & 3;
    const char* asrc = ws + WS_HH + ((size_t)(mt * 4 + ks) * 36) * 8192 + wid * 2048;
    const unsigned aoff = lane * 16;
    const unsigned ma0 = lds0 + wid * 2048, mw0 = lds0 + G2_W0 + wid * 2304;
    auto hpiece = [&](int P, int s) {
        if (P == 0) dma16_sc1<0>(aoff, asrc + (size_t)s * 16384, ma0 + (s & 3) * 16384);
        if (P == 1) dma16_sc1<1024>(aoff, asrc + (size_t)s * 16384, ma0 + (s & 3) * 16384);
    };
    hpiece(0, 0); hpiece(1, 0); hpiece(0, 1); hpiece(1, 1); hpiece(0, 2); hpiece(1, 2);

    const int g4 = lane >> 4;
    const int toff = wr * 2048 + lane * 16;
    const int foff = G2_W0 + dh * 5120 + lane * 16;
    auto rdf = [&](const LAS char* p) -> s16x8 { return *(const LAS s16x8*)p; };
    const rsrc_t rfb = mk_rsrc(ws + WS_FB, 16384);

    f32x4 acc[NT][2];
    s16x8 wf[2 * NT], tf[2][4];
    WAIT_V(4);
    __builtin_amdgcn_raw_buffer_store_b32(1u, rfb, lane == 0 ? flag2 : 0x80000000u, 0, 16);
    BAR();
#pragma unroll
    for (int e = 0; e < 4; ++e) tf[0][e] = rdf(lds + toff + (e >> 1) * 8192 + (e & 1) * 1024);
#pragma unroll
    for (int e = 0; e < 2 * NT; ++e) wf[e] = rdf(lds + foff + (e / NT) * 9216 + (e % NT) * 1024);
    SCHED();

    auto iter = [&](auto sc, s16x8 (&tc)[4], s16x8 (&tn)[4]) {
        constexpr int S = decltype(sc)::v;
        if constexpr (S < 17) {
            WAIT_VI(g2_vm(S));
            BAR();
            if constexpr (S == 7) {
                unsigned fb2 = *(const volatile LAS unsigned*)(lds + G2_FL + wid * 256 + lane * 4);
                if (__builtin_expect(!__all(fb2 == 1u), 0)) {
                    for (int spin = 0; spin < (1 << 20); ++spin) {
                        fb2 = __builtin_amdgcn_raw_buffer_load_b32(rfb, (unsigned)(((mt * 4 + ks) * 2 + 1) * 128 + (lane & 31) * 4), 0, AUX_RD);
                        if (__all(fb2 == 1u)) break;
                        SLEEP(2);
                    }
                }
            }
            SCHED();
        }
        const LAS char* tb = lds + ((S + 1) & 3) * 16384;
        const LAS char* wb = lds + ((S + 1) % 5) * 18432;
#pragma unroll
        for (int e = 0; e < 2 * NT; ++e) {
#pragma unroll
            for (int i = 0; i < 2; ++i) {
                if (S == 0 && e < NT) mfma_first(acc[e % NT][i], wf[e], tc[(e / NT) * 2 + i]);
                else mfma_acc(acc[e % NT][i], wf[e], tc[(e / NT) * 2 + i]);
            }
            SCHED();
            if constexpr (S < 17) {
                wf[e] = rdf(wb + foff + (e / NT) * 9216 + (e % NT) * 1024);
                if (e >= 2 && e < 6) tn[e - 2] = rdf(tb + toff + ((e - 2) >> 1) * 8192 + ((e - 2) & 1) * 1024);
            }
            if constexpr (S + 3 < 18) { if (e < 2) hpiece(e, S + 3); }
            if constexpr (S + 4 < 18) {
                if (e == 3) g2_wpiece(0, S + 4, wsrc, aoff, mw0);
                if (e == 5) g2_wpiece(1, S + 4, wsrc, aoff, mw0);
                if (e == 6) g2_wpiece(2, S + 4, wsrc, aoff, mw0);
            }
            if constexpr (S == 4) {
                if (e == 7) dma4_sc1((unsigned)(lane & 31) * 4, ws + WS_FB + ((mt * 4 + ks) * 2 + 1) * 128, lds0 + G2_FL + wid * 256);
            }
            SCHED();
        }
    };
#define G2_IT2(S) iter(IC<S>{}, tf[0], tf[1]); iter(IC<S + 1>{}, tf[1], tf[0])
    G2_IT2(0); G2_IT2(2); G2_IT2(4); G2_IT2(6); G2_IT2(8); G2_IT2(10); G2_IT2(12);
    G2_IT2(14);
    iter(IC<16>{}, tf[0], tf[1]);
    const int tok0 = mt * 128 + wr * 32 + (lane & 15);
    u32x2 hv[2][5];
    if (wr == ks) {
#pragma unroll
        for (int i = 0; i < 2; ++i) {
            const int t = tok0 + i * 16;
            const char* hp = ws + WS_H + ((size_t)((t >> 8) * 144 + nt * 36 + dh * 20) * 256 + (t & 255)) * 8;
#pragma unroll
            for (int e = 0; e < 4; ++e) hv[i][e] = *(const u32x2*)(hp + ((e >> 1) * 8 + (g4 & 1) * 4 + (g4 >> 1) * 2 + (e & 1)) * 2048);
            if (NT == 5) hv[i][4] = *(const u32x2*)(hp + (16 + g4) * 2048);
        }
    }
    SCHED();
    iter(IC<17>{}, tf[1], tf[0]);
    MFMA_DONE();

    const rsrc_t rx = mk_rsrc(ws + WS_X, X_BYTES);
    const unsigned xt = (unsigned)((mt * 4 + nt) * 4 * 27648 + dh * 4096);
    auto inb = [&](int s) -> unsigned { return xt + (unsigned)(((ks + 1 + s) & 3) * 27648 + (2 - s) * 9216); };
    if (wr != ks) {
        const unsigned o = xt + (unsigned)(ks * 27648 + ((wr - ks - 1) & 3) * 9216);
#pragma unroll
        for (int i = 0; i < 2; ++i) {
            unsigned w[2 * NT];
#pragma unroll
            for (int j = 0; j < NT; ++j) { w[2 * j] = pk_bf16(acc[j][i][0], acc[j][i][1]); w[2 * j + 1] = pk_bf16(acc[j][i][2], acc[j][i][3]); }
#pragma unroll
            for (int e = 0; e < 2 * NT; ++e) w[e] = (w[e] == ~0u) ? 0x7fc07fc0u : w[e];
            if constexpr (NT == 5) st8_wt(rx, o + 8192 + i * 512 + lane * 8, (u32x2){w[8], w[9]});
            st16_wt(rx, o + i * 2048 + lane * 16, (u32x4){w[0], w[1], w[2], w[3]});
            st16_wt(rx, o + i * 2048 + 1024 + lane * 16, (u32x4){w[4], w[5], w[6], w[7]});
        }
    } else {
        u32x4 pa[3][4]; u32x2 pb[3][2];
        SLEEP(16);
        for (int spin = 0; spin < (1 << 18); ++spin) {
            const unsigned t = __builtin_amdgcn_raw_buffer_load_b32(rx, lane < 3 ? inb(lane) + 4092 : 0x80000000u, 0, AUX_RD);
            if (__all(t != ~0u)) {
                unsigned m = 0u;
#pragma unroll
                for (int s = 0; s < 3; ++s) {
#pragma unroll
                    for (int p = 0; p < 4; ++p) pa[s][p] = __builtin_amdgcn_raw_buffer_load_b128(rx, inb(s) + p * 1024 + lane * 16, 0, 16);
                    if constexpr (NT == 5) {
#pragma unroll
                        for (int i = 0; i < 2; ++i) pb[s][i] = __builtin_amdgcn_raw_buffer_load_b64(rx, inb(s) + 8192 + i * 512 + lane * 8, 0, 16);
                    }
                }
#pragma unroll
                for (int s = 0; s < 3; ++s) {
#pragma unroll
                    for (int p = 0; p < 4; ++p) m = max(max(max(m, pa[s][p].x), max(pa[s][p].y, pa[s][p].z)), pa[s][p].w);
                    if constexpr (NT == 5) m = max(max(max(m, pb[s][0].x), max(pb[s][0].y, pb[s][1].x)), pb[s][1].y);
                }
                if (__all(m != ~0u)) break;
            }
            SLEEP(1);
        }
        const rsrc_t ro = mk_rsrc(out, S_ * D_ * 2);
#pragma unroll
        for (int i = 0; i < 2; ++i) {
            const unsigned ob = (unsigned)(((tok0 + i * 16) * D_ + nt * 144 + dh * 80) * 2);
            unsigned r[NT][2];
#pragma unroll
            for (int j = 0; j < NT; ++j) {
                f32x4 t = acc[j][i];
#pragma unroll
                for (int s = 0; s < 3; ++s) {
                    const unsigned w0 = j < 4 ? pa[s][i * 2 + (j >> 1)][(j & 1) * 2] : pb[s][i].x, w1 = j < 4 ? pa[s][i * 2 + (j >> 1)][(j & 1) * 2 + 1] : pb[s][i].y;
                    t += (f32x4){bf_lo(w0), bf_hi(w0), bf_lo(w1), bf_hi(w1)};
                }
                r[j][0] = pk_bf16(t[0], t[1]); r[j][1] = pk_bf16(t[2], t[3]);
            }
            auto addh = [&](unsigned a, unsigned h) -> unsigned { return pk_bf16(bf_lo(h) + bf_lo(a), bf_hi(h) + bf_hi(a)); };
#pragma unroll
            for (int p = 0; p < 2; ++p) {
                auto s0 = __builtin_amdgcn_permlane16_swap(r[2 * p][0], r[2 * p + 1][0], false, false);
                auto s1 = __builtin_amdgcn_permlane16_swap(r[2 * p][1], r[2 * p + 1][1], false, false);
                st16_wt(ro, ob + (unsigned)((p * 32 + (g4 & 1) * 16 + (g4 >> 1) * 8) * 2),
                        (u32x4){addh(s0[0], hv[i][2 * p].x), addh(s1[0], hv[i][2 * p].y), addh(s0[1], hv[i][2 * p + 1].x), addh(s1[1], hv[i][2 * p + 1].y)});
            }
            if constexpr (NT == 5) st8_wt(ro, ob + (unsigned)((64 + g4 * 4) * 2), (u32x2){addh(r[4][0], hv[i][4].x), addh(r[4][1], hv[i][4].y)});
        }
    }
}

constexpr int K3_LDS = 163840;
constexpr int K3_SP = G_LDS;
constexpr int K3_CS = G2_W0 + 4 * 18432;
extern "C" __global__ __launch_bounds__(512, 2) void k3_gemm1(const u16* __restrict__ wg, const u16* __restrict__ wu,
                                                   char* ws, u16* __restrict__ out) {
    extern __shared__ __attribute__((aligned(16))) char smem[];
    LAS char* lds = (LAS char*)smem;
    const unsigned lds0 = (unsigned)(size_t)lds;
    const int tid = threadIdx.x, lane = tid & 63;
    const int wid = __builtin_amdgcn_readfirstlane(tid >> 6);
    const int wr = wid & 3, wc = wid >> 2;
    const int b = blockIdx.x, xcd = b & 7, idx = b >> 3;
    const int rt = (xcd >> 2) * 4 + (idx & 3), ct = (xcd & 3) * 8 + (idx >> 2);
    asm volatile("" :: "s"(wg), "s"(wu), "s"(ws), "s"(out));

    const char* asrc = ws + WS_H + (size_t)rt * 144 * 2048 + wid * 2048;
    const unsigned aoff = (unsigned)(lane ^ ((wid & 2) << 2)) * 16;
    const unsigned ma0 = lds0 + wid * 2048, mw0 = lds0 + 16384 + wc * 9216 + wr * 2304;
    dma16<0>(aoff, asrc, ma0); dma16<1024>(aoff, asrc, ma0);
    const char* wsrc = (const char*)(wc ? wu : wg) + ct * 288 - 2048;
    auto wofs = [&](int i) -> unsigned {
        const int q = lane + 64 * i, lrow = 8 * wr + q / 18, pos = q % 18;
        const int krow = ((lrow >> 2) & 3) * 8 + (lrow >> 4) * 4 + (lrow & 3);
        const int col = pos * 8 + (pos < 8 ? 0 : (pos < 16 ? 8 : (((pos & 1) ^ wc) ? 136 : 64) - pos * 8));
        return (unsigned)(krow * (F_ * 2) + col * 2 + 2048 - 1024 * i);
    };
    const unsigned w0 = wofs(0), w1 = wofs(1), w2 = wofs(2);
    auto piece = [&](int P, int s) {
        const unsigned bo = (s & 3) * ST_BYTES;
        const char* wp = wsrc + (size_t)s * (32 * F_ * 2);
        if (P == 0) dma16<0>(aoff, asrc + (size_t)s * 16384, ma0 + bo);
        if (P == 1) dma16<1024>(aoff, asrc + (size_t)s * 16384, ma0 + bo);
        if (P == 2) dma16<0>(w0, wp, mw0 + bo);
        if (P == 3) dma16<1024>(w1, wp, mw0 + bo);
        if (P == 4) dma16_q<2048>(w2, wp, mw0 + bo);
    };
#define K3_PIECE(P, s) piece(P, s)
#define K3_STAGE(s) do { K3_PIECE(0, s); K3_PIECE(1, s); K3_PIECE(2, s); K3_PIECE(3, s); K3_PIECE(4, s); } while (0)
    K3_PIECE(2, 0); K3_PIECE(3, 0); K3_PIECE(4, 0);
    BAR();
    K3_STAGE(1);
    u32x2 hs[4];
    {
        const char* hsrc = ws + WS_H + (size_t)rt * 144 * 2048 + ct * 64;
        const unsigned o = (unsigned)((wid * 18 + (lane >> 3)) * 2048 + (lane & 7) * 8);
        asm volatile("global_load_dwordx2 %0, %1, %2" : "=v"(hs[0]) : "v"(o), "s"(hsrc) : "memory");
        asm volatile("global_load_dwordx2 %0, %1, %2" : "=v"(hs[1]) : "v"(o + 16384), "s"(hsrc) : "memory");
        asm volatile("global_load_dwordx2 %0, %1, %2" : "=v"(hs[2]) : "v"((unsigned)((wid * 18 + 16 + ((lane >> 3) & 1)) * 2048 + (lane & 7) * 8)), "s"(hsrc) : "memory");
        asm volatile("global_load_dwordx2 %0, %1, %2" : "=v"(hs[3]) : "v"((unsigned)(lane & 7) * 8), "s"(hsrc) : "memory");
    }

    const int g4 = lane >> 4, q4 = (lane & 15) >> 2, p4 = lane & 3;
    const int rowoff = (4 * g4 + q4) * 288;
    const int tbase = g4 * 4096 + wr * 512 + (lane & 15) * 8;
    const int toff0 = tbase + ((2 * wc) ^ (g4 & 1)) * 128, toff1 = tbase + ((2 * wc + 1) ^ (g4 & 1)) * 128;
    const int tstep = wc ? -256 : 256;
    const int foff = 16384 + rowoff + wc * 128 + p4 * 8;
    const int moff = 16384 + rowoff + 256 + (p4 < 2 ? 16 * wc + 8 * p4 : 9216 + 16 * (1 - wc) + 8 * (p4 - 2));

    f32x4 acc[9][4];
    s16x8 wf[9], tf[2][4];
    auto rd_w = [&](const LAS char* sb, int j) -> s16x8 {
        return j < 8 ? tr_frag(sb + foff + (j >> 2) * 9216 + (j & 3) * 32) : tr_frag(sb + moff);
    };
    auto rd_t = [&](const LAS char* sb, int i) -> s16x8 {
        const LAS char* p = sb + ((i & 1) ? toff1 : toff0) + (i >> 1) * tstep;
        const s16x4 lo = *(const volatile LAS s16x4*)p, hi = *(const volatile LAS s16x4*)(p + 2048);
        return __builtin_shufflevector(lo, hi, 0, 1, 2, 3, 4, 5, 6, 7);
    };
    WAIT_V(9); BAR();
    wf[0] = rd_w(lds, 0);
#pragma unroll
    for (int i = 0; i < 4; ++i) tf[0][i] = rd_t(lds, i);
#pragma unroll
    for (int j = 1; j < 9; ++j) wf[j] = rd_w(lds, j);
    SCHED();
    const int mt2 = (xcd >> 2) * 8 + (idx & 7), ks2 = xcd & 3, nt2 = idx >> 3;
    const rsrc_t rfill = mk_rsrc(ws + WS_X + (size_t)((mt2 * 4 + nt2) * 4 + ks2) * 27648, 27648);
    auto fill = [&](int k) { st16_wt(rfill, (unsigned)(k * 8192 + tid * 16), (u32x4){~0u, ~0u, ~0u, ~0u}); };
    unsigned sgr;
    {
        const LAS char* sb = lds + ST_BYTES;
        float hc = 0.f;
#pragma unroll
        for (int j = 0; j < 9; ++j) {
#pragma unroll
            for (int i = 0; i < 4; ++i) mfma_first(acc[j][i], wf[j], tf[0][i]);
            SCHED();
            if (j == 0) { K3_PIECE(0, 2); K3_PIECE(1, 2); }
            if (j == 1) { K3_PIECE(2, 2); K3_PIECE(3, 2); }
            if (j == 2) K3_PIECE(4, 2);
            if (j == 3) {
                asm volatile("s_waitcnt vmcnt(5)" : "+v"(hs[0]), "+v"(hs[1]), "+v"(hs[2]), "+v"(hs[3]) :: "memory");
                hc = bf_lo(hs[3].x);
                float s1 = 0.f, s2 = 0.f;
#pragma unroll
                for (int i = 0; i < 3; ++i) {
                    const float d[4] = {bf_lo(hs[i].x) - hc, bf_hi(hs[i].x) - hc, bf_lo(hs[i].y) - hc, bf_hi(hs[i].y) - hc};
                    float a = (d[0] + d[1]) + (d[2] + d[3]), q = __builtin_fmaf(d[0], d[0], d[1] * d[1]) + __builtin_fmaf(d[2], d[2], d[3] * d[3]);
                    if (i == 2 && lane >= 16) { a = 0.f; q = 0.f; }
                    s1 += a; s2 += q;
                }
                s1 = x32_add(x16_add(dpp_add<0x128>(s1))); s2 = x32_add(x16_add(dpp_add<0x128>(s2)));
                if (lane < 8) *(LAS f32x2*)(lds + K3_SP + (lane * 8 + wid) * 8) = (f32x2){s1, s2};
                asm volatile("s_waitcnt lgkmcnt(0)" ::: "memory");
                BAR();
                wf[0] = rd_w(sb, 0); wf[1] = rd_w(sb, 1);
                tf[1][0] = rd_t(sb, 0); tf[1][1] = rd_t(sb, 1);
            }
            if (j == 4) { tf[1][2] = rd_t(sb, 2); tf[1][3] = rd_t(sb, 3); wf[2] = rd_w(sb, 2); wf[3] = rd_w(sb, 3); }
            if (j >= 4) { wf[j] = rd_w(sb, j); K3_PIECE(j - 4, 3); }
            if (j == 8) {
                const f32x2 pp = *(const LAS f32x2*)(lds + K3_SP + (wid * 8 + (lane & 7)) * 8);
                const float s1 = dpp_add<0x141>(dpp_add<0x4E>(dpp_add<0xB1>(pp.x))) * (1.f / D_);
                const float s2 = dpp_add<0x141>(dpp_add<0x4E>(dpp_add<0xB1>(pp.y))) * (1.f / D_);
                const float c0 = __uint_as_float(__builtin_amdgcn_readlane(__float_as_uint(hc), wid));
                const float sd = ln_sd(__builtin_fmaxf(__builtin_fmaf(-s1, s1, s2), 0.f));
                sgr = pk_bf16(c0 + s1, sd);
                fill(0); fill(1);
            }
            SCHED();
        }
    }

    const rsrc_t rg = mk_rsrc(ws + WS_G2 + (size_t)(rt * 256 + wr * 64) * 8, 512);
    const char* w2src = ws + WS_WT + (size_t)(ks2 * 4 + nt2) * 18 * 18432 + wid * 2304;
    u32x2 gr[4];
    s16x4 tl;
    constexpr int TP = 1, DPJ0 = 0, DPJ1 = 5;
    auto iter = [&](auto mode, auto vm, int s, s16x8 (&tc)[4], s16x8 (&tn)[4]) {
        constexpr int MODE = decltype(mode)::v;
        if constexpr (MODE != 4) {
            if constexpr (MODE == 3) WAIT_V(0); else WAIT_VI(5 + decltype(vm)::v);
            BAR();
            SCHED();
        }
        const LAS char* sb = lds + ((s + 1) & 3) * ST_BYTES;
#pragma unroll
        for (int j = 0; j < 9; ++j) {
            s16x4 wl, wh;
#pragma unroll
            for (int i = 0; i < 4; ++i) {
                mfma_acc(acc[j][i], wf[j], tc[i]);
                SCHED();
                if constexpr (MODE != 4) {
                    if (j >= 1) {
                        const LAS char* p = (j - 1 < 8) ? sb + foff + ((j - 1) >> 2) * 9216 + ((j - 1) & 3) * 32 : sb + moff;
                        if (i == 0) wl = __builtin_amdgcn_ds_read_tr16_b64_v4i16((LAS s16x4*)p);
                        if (i == 1) { wh = __builtin_amdgcn_ds_read_tr16_b64_v4i16((LAS s16x4*)(p + 16 * 288)); wf[j - 1] = __builtin_shufflevector(wl, wh, 0, 1, 2, 3, 4, 5, 6, 7); }
                    }
                    if (j >= TP && j < TP + 4) {
                        const int ti = j - TP;
                        const LAS char* p = sb + ((ti & 1) ? toff1 : toff0) + (ti >> 1) * tstep;
                        if (i == 2) tl = *(const volatile LAS s16x4*)p;
                        if (i == 3) tn[ti] = __builtin_shufflevector(tl, *(const volatile LAS s16x4*)(p + 2048), 0, 1, 2, 3, 4, 5, 6, 7);
                    }
                    if (j == 8 && i == 3) wf[8] = rd_w(sb, 8);
                }
                if constexpr (MODE < 2 || MODE == 5) {
                    if (j == DPJ0 && i == 1) K3_PIECE(0, s + 3);
                    if (j == DPJ0 && i == 3) K3_PIECE(1, s + 3);
                    if (j == DPJ1 && i == 3) K3_PIECE(2, s + 3);
                    if (j == DPJ1 + 1 && i == 3) K3_PIECE(3, s + 3);
                    if (j == DPJ1 + 2 && i == 3) K3_PIECE(4, s + 3);
                }
                SCHED();
            }
            if constexpr (MODE == 0) { if (j == 8) { fill(2); fill(3); } }
            if constexpr (MODE == 5) {
                if (j == 0 && s == 4) st8_wt(mk_rsrc(ws + WS_G2, S_ * 8), lane == 0 ? (unsigned)((rt * 256 + ct * 8 + wid) * 8) : 0x80000000u, (u32x2){sgr, 1u});
            }
            if constexpr (MODE == 2) {
                if (j == 0) {
#pragma unroll
                    for (int i = 0; i < 4; ++i) gr[i] = __builtin_amdgcn_raw_buffer_load_b64(rg, (unsigned)(((i ^ (2 * wc)) * 16 + (lane & 15)) * 8), 0, 16);
                }
            }
            if constexpr (MODE == 3) {
                if (j == 0) {
                    asm volatile("" : "+v"(gr[0]), "+v"(gr[1]), "+v"(gr[2]), "+v"(gr[3]));
                    const unsigned co = (unsigned)((lane >= 18 ? F_ * 4 + (lane - 18) * 16 : lane * 16));
                    dma_m36(co, ws + WS_CS + (size_t)(ct * 144 + wc * 72) * 4, lds0 + K3_CS + wid * 1152);
                    dma_m36(co, ws + WS_CS + (size_t)(2 * F_ + ct * 144 + wc * 72) * 4, lds0 + K3_CS + wid * 1152 + 576);
                }
            }
            if constexpr (MODE == 3 || MODE == 4) {
                if (j >= 2 && j < 8) g2_wpiece((j - 2) % 3, (MODE - 3) * 2 + (j - 2) / 3, w2src, lane * 16, lds0 + G2_W0 + wid * 2304);
            }
            SCHED();
        }
    };
    iter(IC<0>{}, IC<2>{}, 1, tf[1], tf[0]);
    iter(IC<1>{}, IC<4>{}, 2, tf[0], tf[1]);
    iter(IC<1>{}, IC<2>{}, 3, tf[1], tf[0]);
#pragma unroll 1
    for (int s = 4; s < 14; s += 2) {
        iter(IC<5>{}, IC<0>{}, s, tf[0], tf[1]);
        iter(IC<1>{}, IC<0>{}, s + 1, tf[1], tf[0]);
    }
    iter(IC<1>{}, IC<0>{}, 14, tf[0], tf[1]);
    iter(IC<2>{}, IC<0>{}, 15, tf[1], tf[0]);
    iter(IC<3>{}, IC<0>{}, 16, tf[0], tf[1]);
    iter(IC<4>{}, IC<0>{}, 17, tf[1], tf[0]);
    MFMA_DONE();
    WAIT_V(12);
    __builtin_amdgcn_s_setprio(1);
    if (__builtin_expect(!__all((gr[0].y & gr[1].y & gr[2].y & gr[3].y) == 1u), 0)) {
        for (int spin = 0; spin < (1 << 20); ++spin) {
            SLEEP(2);
#pragma unroll
            for (int i = 0; i < 4; ++i) gr[i] = __builtin_amdgcn_raw_buffer_load_b64(rg, (unsigned)(((i ^ (2 * wc)) * 16 + (lane & 15)) * 8), 0, AUX_RD);
            if (__all((gr[0].y & gr[1].y & gr[2].y & gr[3].y) == 1u)) break;
        }
    }
    const int mt = rt * 2 + (wr >> 1), ks = ct >> 3, lct = ct & 7;
    const rsrc_t rhh = mk_rsrc(ws + WS_HH, S_ * F_ * 2);
    const unsigned hb = (unsigned)(((mt * 4 + ks) * 36) * 8192 + ((wr & 1) * 4) * 1024);
    const LAS char* csb = lds + K3_CS + wid * 1152 + 16 * g4;
    TokC tcs[4];
#pragma unroll
    for (int i = 0; i < 4; ++i) {
        const float sd = bf_hi(gr[i].x), r = __builtin_amdgcn_rcpf(sd);
        tcs[i] = (TokC){-bf_lo(gr[i].x), -2.30220819f * r, -0.10294324f * (r * r * r), sd * sd};
    }
    {
        const LAS char* cm = lds + K3_CS + wid * 1152 + 256 + 16 * (g4 & 1);
        const f32x4 cgm = *(const LAS f32x4*)cm + *(const LAS f32x4*)(cm + 576), cum = *(const LAS f32x4*)(cm + 288) + *(const LAS f32x4*)(cm + 864);
#pragma unroll
        for (int ip = 0; ip < 2; ++ip) {
            const int ia = ((2 * ip) ^ (2 * wc)) + (lane >> 5);
            const bool up = lane >= 32;
            const TokC tc = {up ? tcs[2 * ip + 1].nm : tcs[2 * ip].nm, up ? tcs[2 * ip + 1].c0 : tcs[2 * ip].c0,
                             up ? tcs[2 * ip + 1].c1 : tcs[2 * ip].c1, up ? tcs[2 * ip + 1].q : tcs[2 * ip].q};
            f32x4 gm, um;
#pragma unroll
            for (int r = 0; r < 4; ++r) {
                auto sw = __builtin_amdgcn_permlane32_swap(__float_as_uint(acc[8][2 * ip][r]), __float_as_uint(acc[8][2 * ip + 1][r]), false, false);
                gm[r] = __uint_as_float(sw[0]); um[r] = __uint_as_float(sw[1]);
            }
            const int kgs = (lct & 1) * 2 + wc;
            st8_wt(rhh, hb + (unsigned)((lct >> 1) * 8192 + ia * 1024 + (kgs * 16 + (lane & 15)) * 16 + (g4 & 1) * 8),
                   (u32x2){geglu2(LO2(gm), LO2(um), LO2(cgm), LO2(cum), tc), geglu2(HI2(gm), HI2(um), HI2(cgm), HI2(cum), tc)});
        }
    }
    SCHED();
    constexpr int FLAGPOS = 1;
    const rsrc_t rfb = mk_rsrc(ws + WS_FB, 16384);
    const unsigned fbo = (unsigned)((mt * 4 + ks) * 256 + (lct * 4 + (wr & 1) * 2 + wc) * 4);
    u32x4 ob[4];
#pragma unroll
    for (int jp = 0; jp < 2; ++jp) {
        f32x4 cg[2], cu[2];
#pragma unroll
        for (int e = 0; e < 2; ++e) {
            cg[e] = *(const LAS f32x4*)(csb + (2 * jp + e) * 64) + *(const LAS f32x4*)(csb + 576 + (2 * jp + e) * 64);
            cu[e] = *(const LAS f32x4*)(csb + 288 + (2 * jp + e) * 64) + *(const LAS f32x4*)(csb + 864 + (2 * jp + e) * 64);
        }
#pragma unroll
        for (int i = 0; i < 4; ++i) {
            unsigned o[4];
#pragma unroll
            for (int e = 0; e < 2; ++e) {
                const f32x4 g = acc[2 * jp + e][i], u = acc[4 + 2 * jp + e][i];
                o[2 * e] = geglu2(LO2(g), LO2(u), LO2(cg[e]), LO2(cu[e]), tcs[i]);
                o[2 * e + 1] = geglu2(HI2(g), HI2(u), HI2(cg[e]), HI2(cu[e]), tcs[i]);
            }
            ob[i] = (u32x4){o[0], o[1], o[2], o[3]};
            st16_wt(rhh, (jp == 1 && wid == 7) ? 0x80000000u : hb + (unsigned)(((2 + jp * 8 + lct) * 2 + wc) * 8192 + (i ^ (2 * wc)) * 1024 + lane * 16), ob[i]);
            if (jp == 1 && i == FLAGPOS) {
                SCHED();
                WAIT_VI(FLAGPOS + 1);
                __builtin_amdgcn_raw_buffer_store_b32(1u, rfb, lane == 0 ? fbo : 0x80000000u, 0, 16);
                SCHED();
            }
        }
        SCHED();
        if (jp == 0) __builtin_amdgcn_s_setprio(0);
    }
    if (wid == 7) {
        for (int spin = 0; spin < (1 << 20); ++spin) {
            const unsigned f = __builtin_amdgcn_raw_buffer_load_b32(rfb, (unsigned)((mt2 * 4 + ks2) * 256 + (lane & 31) * 4), 0, AUX_RD);
            if (__all(f == 1u)) break;
            SLEEP(4);
        }
#pragma unroll
        for (int i = 0; i < 4; ++i) st16_wt(rhh, hb + (unsigned)(((10 + lct) * 2 + wc) * 8192 + (i ^ (2 * wc)) * 1024 + lane * 16), ob[i]);
    }
    BAR();
    if (wid < 4) g2_body<5>(ws, out, lds, lane, wid, mt2, ks2, nt2, w2src, fbo + 128);
    else g2_body<4>(ws, out, lds, lane, wid, mt2, ks2, nt2, w2src, fbo + 128);
}

extern "C" __global__ void k9_end() {}

extern "C" void kernel_launch(void* const* d_in, const int* in_sizes, int n_in, void* d_out, int out_size, void* d_ws,
                              size_t ws_size, hipStream_t stream) {
    const u16* q = (const u16*)d_in[0];
    const u16* k = (const u16*)d_in[1];
    const u16* v = (const u16*)d_in[2];
    const u16* shift_k = (const u16*)d_in[3];
    const u16* ssm_k = (const u16*)d_in[4];
    const u16* ssm_D = (const u16*)d_in[5];
    const u16* wg = (const u16*)d_in[7];
    const u16* wu = (const u16*)d_in[8];
    const u16* wd = (const u16*)d_in[9];
    char* ws = (char*)d_ws;
    (void)hipFuncSetAttribute((const void*)k1_conv, hipFuncAttributeMaxDynamicSharedMemorySize, C1_LDS);
    (void)hipFuncSetAttribute((const void*)k3_gemm1, hipFuncAttributeMaxDynamicSharedMemorySize, K3_LDS);
    k1_conv<<<256, 512, C1_LDS, stream>>>(q, k, v, shift_k, ssm_k, ssm_D, wg, wu, wd, ws);
    k3_gemm1<<<256, 512, K3_LDS, stream>>>(wg, wu, ws, (u16*)d_out);
}
```
